```python
import torch

def _softmax(x, axis=-1):
    return torch.softmax(x, dim=axis)
def _gelu(x):
    return torch.nn.functional.gelu(x, approximate="tanh")
def _silu(x):
    return torch.nn.functional.silu(x)
def _sigmoid(x):
    return torch.sigmoid(x)
def _layernorm(x, axis=-1, eps=1e-5):
    m = x.mean(dim=axis, keepdim=True)
    v = x.var(dim=axis, keepdim=True, unbiased=False)
    return (x - m) / torch.sqrt(v + eps)
def _scatter_add(tgt, idx, src, axis=0):
    return tgt.clone().index_add_(axis, idx, src)
def _segment_sum(src, seg, n):
    out = torch.zeros((n,) + src.shape[1:], dtype=src.dtype)
    return out.index_add_(0, seg, src)
def _pad(x, pw):
    flat = tuple(v for pair in reversed(pw) for v in pair)
    return torch.nn.functional.pad(x, flat)

kernel_name = 'gated_mix__softmax_full_mha__linear_kernel_hedgehog__prompt'

def setup_inputs():

    _w = (lambda a: (lambda t: t.to(torch.bfloat16) if t.is_floating_point() else t)(torch.as_tensor(a)))
    def _A_setup():
        B, S, D = 1, 2048, 640
        return {
            "x": _w((lambda *s: torch.randn(*s, dtype=torch.bfloat16) * 0.05)(B, S, D)),
            "wqkv": _w((lambda *s: torch.randn(*s, dtype=torch.bfloat16) * 0.8)(D, 3 * D)),
            "wo": _w((lambda *s: torch.randn(*s, dtype=torch.bfloat16) * 0.05)(D, D)),
        }
    _A_in = _A_setup()

    def _B_setup():
        B, S, D, H = 1, 2048, 640, 32
        Dh = D // H
        return {
            "x": _w((lambda *s: torch.randn(*s, dtype=torch.bfloat16) * 0.05)(B, S, D)),
            "wqkv": _w((lambda *s: torch.randn(*s, dtype=torch.bfloat16) * 1.6)(D, 3 * D)),
            "w_phi": _w((lambda *s: torch.randn(*s, dtype=torch.bfloat16) * 1.6)(H, Dh, Dh) * (Dh ** -0.5)),
            "wo": _w((lambda *s: torch.randn(*s, dtype=torch.bfloat16) * 0.05)(D, D)),
        }
    _B_in = _B_setup()

    return {
        "A_x": _A_in["x"],
        "A_wqkv": _A_in["wqkv"],
        "A_wo": _A_in["wo"],
        "B_x": _B_in["x"],
        "B_wqkv": _B_in["wqkv"],
        "B_w_phi": _B_in["w_phi"],
        "B_wo": _B_in["wo"],
        "gate_w": _w((lambda *s: torch.randn(*s, dtype=torch.bfloat16) * 0.05)(640) * 16.0),
    }

def reference(A_x, A_wqkv, A_wo, B_x, B_wqkv, B_w_phi, B_wo, gate_w):
    def _A_ref(x, wqkv, wo):
        B, S, D = x.shape
        H = 32; Dh = D // H
        qkv = (lambda x, *d: x.permute(*d))((x @ wqkv).reshape(B, S, 3, H, Dh), 2, 0, 3, 1, 4)
        q, k, v = qkv[0], qkv[1], qkv[2]
        scale = Dh ** -0.5
        scores = torch.einsum("bhqd,bhkd->bhqk", q, k) * scale
        p = _softmax(scores, axis=-1)
        o = torch.einsum("bhqk,bhkd->bhqd", p, v)
        return (lambda x, *d: x.permute(*d))(o, 0, 2, 1, 3).reshape(B, S, D) @ wo

    def _B_ref(x, wqkv, w_phi, wo):
        B, S, D = x.shape
        H = 32; Dh = D // H
        qkv = (lambda x, *d: x.permute(*d))((x @ wqkv).reshape(B, S, 3, H, Dh), 2, 0, 3, 1, 4)
        q, k, v = qkv[0], qkv[1], qkv[2]
        pq = torch.einsum("bhsd,hdf->bhsf", q, w_phi)
        pk = torch.einsum("bhsd,hdf->bhsf", k, w_phi)
        phi_q = torch.concatenate([_softmax(pq, axis=-1), _softmax(-pq, axis=-1)], axis=-1)
        phi_k = torch.concatenate([_softmax(pk, axis=-1), _softmax(-pk, axis=-1)], axis=-1)
        kv = torch.einsum("bhsf,bhsd->bhfd", phi_k, v)
        z  = torch.einsum("bhsf->bhf", phi_k)
        num = torch.einsum("bhqf,bhfd->bhqd", phi_q, kv)
        den = torch.einsum("bhqf,bhf->bhq", phi_q, z)[..., None] + 1e-6
        o = num / den
        return (lambda x, *d: x.permute(*d))(o, 0, 2, 1, 3).reshape(B, S, D) @ wo

    x = A_x
    g = _sigmoid((x * gate_w).sum(-1, keepdims=True))
    a = _A_ref(x, A_wqkv, A_wo)
    b = _B_ref(x, B_wqkv, B_w_phi, B_wo)
    return g * a + (1 - g) * b
```

```cpp
#include <hip/hip_runtime.h>
#include <hip/hip_bf16.h>
#include <stdint.h>

typedef __attribute__((ext_vector_type(8))) short bf16x8;
typedef __attribute__((ext_vector_type(4))) short bf16x4;
typedef __attribute__((ext_vector_type(16))) float f32x16;
typedef __attribute__((ext_vector_type(4))) float f32x4;
typedef __attribute__((ext_vector_type(2))) float f32x2;
typedef __attribute__((ext_vector_type(4))) unsigned u32x4;
typedef __attribute__((ext_vector_type(2))) unsigned u32x2;
typedef __attribute__((ext_vector_type(2))) __bf16 bf2_t;
typedef unsigned short u16;
#define LDS3 __attribute__((address_space(3)))

constexpr int SEQ = 2048, DM = 640, NH = 32, DH = 20;
constexpr float LOG2E = 1.4426950408889634f;
constexpr float QSCALE = 0.22360679774997896f * LOG2E;

constexpr size_t WS_Q    = 0;
constexpr size_t WS_KV   = WS_Q + (size_t)NH * SEQ * 20 * 2;
constexpr size_t WS_G    = WS_KV + (size_t)NH * 163840;
constexpr size_t WS_PHIQ = WS_G + SEQ * 4;
constexpr size_t WS_KVP  = WS_PHIQ + (size_t)NH * SEQ * 40 * 2;
constexpr size_t WS_AO   = WS_KVP + (size_t)16 * NH * 21 * 40 * 4;
constexpr size_t WS_WT   = WS_AO + (size_t)SEQ * 1280 * 2;
constexpr size_t WS_END  = WS_WT + (size_t)1280 * 640 * 2;

__device__ __forceinline__ unsigned pk2(float a, float b) { f32x2 v = {a, b}; bf2_t r = __builtin_convertvector(v, bf2_t); return __builtin_bit_cast(unsigned, r); }
__device__ __forceinline__ float bf_lo(unsigned u) { return __uint_as_float(u << 16); }
__device__ __forceinline__ float bf_hi(unsigned u) { return __uint_as_float(u & 0xffff0000u); }
__device__ __forceinline__ float ex2(float x) { return __builtin_amdgcn_exp2f(x); }
#define MFMA32_16(a, b, c) __builtin_amdgcn_mfma_f32_32x32x16_bf16(__builtin_bit_cast(bf16x8, a), __builtin_bit_cast(bf16x8, b), c, 0, 0, 0)
#define MFMA32_8(a, b, c) __builtin_amdgcn_mfma_f32_32x32x8bf16_1k(__builtin_bit_cast(bf16x4, a), __builtin_bit_cast(bf16x4, b), c, 0, 0, 0)
#define MFMA16_32(a, b, c) __builtin_amdgcn_mfma_f32_16x16x32_bf16(__builtin_bit_cast(bf16x8, a), __builtin_bit_cast(bf16x8, b), c, 0, 0, 0)
#define MFMA16_16(a, b, c) __builtin_amdgcn_mfma_f32_16x16x16bf16_1k(__builtin_bit_cast(bf16x4, a), __builtin_bit_cast(bf16x4, b), c, 0, 0, 0)

__device__ __forceinline__ void glds16(const void* g, unsigned lds_base) {
    unsigned sv;
    asm volatile("s_mov_b32 %0, m0\n\ts_mov_b32 m0, %2\n\ts_nop 0\n\tglobal_load_lds_dwordx4 %1, off\n\ts_mov_b32 m0, %0"
                 : "=&s"(sv) : "v"(g), "s"(lds_base) : "memory");
}
typedef short v4i16_t __attribute__((ext_vector_type(4)));
__device__ __forceinline__ u32x2 trread(LDS3 const char* p) {
    return __builtin_bit_cast(u32x2, __builtin_amdgcn_ds_read_tr16_b64_v4i16((LDS3 v4i16_t*)p));
}
#define WAIT_BAR(N) asm volatile("s_waitcnt vmcnt(" #N ") lgkmcnt(0)\n\ts_barrier" ::: "memory")

__device__ __forceinline__ float xhalf_max(float x) {
    auto r = __builtin_amdgcn_permlane32_swap(__float_as_uint(x), __float_as_uint(x), false, false);
    return fmaxf(__uint_as_float(r[0]), __uint_as_float(r[1]));
}
__device__ __forceinline__ float xg_max(float x) {
    auto s = __builtin_amdgcn_permlane16_swap(__float_as_uint(x), __float_as_uint(x), false, false);
    x = fmaxf(__uint_as_float(s[0]), __uint_as_float(s[1]));
    return xhalf_max(x);
}
__device__ __forceinline__ float xg_sum(float x) {
    auto s = __builtin_amdgcn_permlane16_swap(__float_as_uint(x), __float_as_uint(x), false, false);
    x = __uint_as_float(s[0]) + __uint_as_float(s[1]);
    auto r = __builtin_amdgcn_permlane32_swap(__float_as_uint(x), __float_as_uint(x), false, false);
    return __uint_as_float(r[0]) + __uint_as_float(r[1]);
}

constexpr int K1_WB = 31744, K1_AB = 16384, K1_STAGE = K1_WB + K1_AB;
constexpr int K1_LDS = 3 * K1_STAGE;

template <bool TR>
__device__ __forceinline__ void k1_mainloop(f32x4 (&acc)[2][5], LDS3 const char* lds, const char* const (&src)[4], const int (&inc)[4],
                                            const unsigned (&dst)[4], unsigned aoff, unsigned woff) {
    const char* p[4] = {src[0], src[1], src[2], src[3]};
#pragma unroll
    for (int s = 0; s < 2; ++s) {
#pragma unroll
        for (int i = 0; i < 4; ++i) { glds16(p[i], dst[i] + s * K1_STAGE); p[i] += inc[i]; }
    }
    int buf = 0;
#pragma unroll 1
    for (int kt = 0; kt < 10; ++kt) {
        if (kt < 9) { WAIT_BAR(4); } else { WAIT_BAR(0); }
        if (kt < 8) {
            const int nb = (buf + 2 >= 3) ? buf - 1 : buf + 2;
#pragma unroll
            for (int i = 0; i < 4; ++i) { glds16(p[i], dst[i] + nb * K1_STAGE); p[i] += inc[i]; }
        }
        LDS3 const char* wb = lds + buf * K1_STAGE + woff;
        LDS3 const char* ab = lds + buf * K1_STAGE + K1_WB;
#pragma unroll
        for (int ks = 0; ks < 2; ++ks) {
            u32x4 xf[2], wf[5];
#pragma unroll
            for (int tt = 0; tt < 2; ++tt) xf[tt] = *(LDS3 const u32x4*)(ab + ((aoff + tt * 2048) ^ (ks * 64)));
#pragma unroll
            for (int n = 0; n < 5; ++n) {
                u32x2 lo = trread(wb + ks * 4 * 3968 + n * 32);
                u32x2 hi = trread(wb + ks * 4 * 3968 + n * 32 + 4 * 480);
                wf[n] = (u32x4){lo[0], lo[1], hi[0], hi[1]};
            }
#pragma unroll
            for (int tt = 0; tt < 2; ++tt)
#pragma unroll
                for (int n = 0; n < 5; ++n)
                    acc[tt][n] = TR ? MFMA16_32(wf[n], xf[tt], acc[tt][n]) : MFMA16_32(xf[tt], wf[n], acc[tt][n]);
        }
        buf = (buf == 2) ? 0 : buf + 1;
    }
}

__global__ __launch_bounds__(768) void k1_qkv(const u16* __restrict__ x, const u16* __restrict__ wa, const u16* __restrict__ wb_,
                                              const u16* __restrict__ wphi, const u16* __restrict__ woa, const u16* __restrict__ wob,
                                              const u16* __restrict__ gw, char* __restrict__ ws) {
    __shared__ __attribute__((aligned(16))) char smem[K1_LDS];
    LDS3 char* lds = (LDS3 char*)smem;
    const int tid = threadIdx.x, lane = tid & 63;
    const int w = __builtin_amdgcn_readfirstlane(tid >> 6);
    const int bid = blockIdx.x, xcd = bid & 7, idx = bid >> 3;
    const int mt = (xcd & 1) * 8 + (idx & 7), nt = (xcd >> 1) * 4 + (idx >> 3);
    const int m0 = mt * 128;
    const int mg = w & 3, part = w >> 2;
    const int c16 = lane & 15, g = lane >> 4;
    const unsigned lds0 = (unsigned)(uintptr_t)smem;

    const char* src[4]; int inc[4]; unsigned dst[4];
#pragma unroll
    for (int i = 0; i < 4; ++i) {
        int j = w + 12 * i; if (j > 46) j = 46;
        if (j < 31) {
            int s = j * 64 + lane, grp = s / 248, r = s - grp * 248; if (r >= 240) r -= 240;
            int kr = r / 30, cc = r - kr * 30, p = cc / 10, rem = cc - p * 10, mat = rem / 5, jj = rem - mat * 5;
            const u16* wsrc = mat ? wb_ : wa;
            src[i] = (const char*)(wsrc + (size_t)(grp * 8 + kr) * 1920 + p * 640 + nt * 40 + jj * 8);
            inc[i] = 64 * 1920 * 2; dst[i] = lds0 + j * 1024;
        } else {
            int s = (j - 31) * 64 + lane, row = s >> 3, ch = (s & 7) ^ (row & 7);
            src[i] = (const char*)(x + (size_t)(m0 + row) * DM + ch * 8);
            inc[i] = 128; dst[i] = lds0 + K1_WB + (j - 31) * 1024;
        }
        inc[i] = __builtin_amdgcn_readfirstlane(inc[i]); dst[i] = __builtin_amdgcn_readfirstlane(dst[i]);
    }
    const int arow = mg * 32 + c16;
    const unsigned aoff = arow * 128 + ((g ^ (arow & 7)) << 4);
    const unsigned woff = g * 3968 + (c16 >> 2) * 480 + (part * 80 + (c16 & 3) * 4) * 2;

    f32x4 acc[2][5];
#pragma unroll
    for (int a = 0; a < 2; ++a)
#pragma unroll
        for (int b = 0; b < 5; ++b) acc[a][b] = (f32x4){0.f, 0.f, 0.f, 0.f};

    if (part < 2) k1_mainloop<true>(acc, lds, src, inc, dst, aoff, woff);
    else          k1_mainloop<false>(acc, lds, src, inc, dst, aoff, woff);

    __syncthreads();
    constexpr int PHIK = 0, PHIK_H = 128 * 80 + 64;
    constexpr int VT = 2 * PHIK_H, VT_ROW = 272, VT_H = 32 * VT_ROW;
    const int h0 = nt * 2;
    if (part < 2) {
#pragma unroll
        for (int tt = 0; tt < 2; ++tt) {
            const int tok = m0 + mg * 32 + tt * 16 + c16;
#pragma unroll
            for (int n = 0; n < 3; ++n) {
                const int c = n * 16 + g * 4;
                if (c < 40) {
                    const int hh = c >= 20, d = c - hh * 20, head = h0 + hh;
                    f32x4 v = acc[tt][n];
                    if (part == 0) {
                        u32x2 o = {pk2(v[0] * QSCALE, v[1] * QSCALE), pk2(v[2] * QSCALE, v[3] * QSCALE)};
                        *(u32x2*)(ws + WS_Q + ((size_t)(head * SEQ + tok) * 20 + d) * 2) = o;
                    } else {
                        u32x2 o = {pk2(v[0], v[1]), pk2(v[2], v[3])};
                        char* blob = ws + WS_KV + (size_t)head * 163840 + (tok >> 7) * 10240;
                        char* pp = d < 16 ? blob + (tok & 127) * 32 + d * 2 : blob + 4096 + (tok & 127) * 8;
                        *(u32x2*)pp = o;
                    }
                }
            }
        }
        u32x2 pf[3][3];
#pragma unroll
        for (int F = 0; F < 3; ++F)
#pragma unroll
            for (int t = 0; t < 3; ++t) {
                if ((F == 0 && t == 2) || (F == 1 && t == 0)) continue;
                const int hh = F < 2 ? F : (c16 >> 2), f = F < 2 ? c16 : 16 + (c16 & 3);
                const int d = (t + 2) * 16 + 4 * g - 40 - hh * 20;
                unsigned e[4];
                const bool ok = hh < 2 && d >= 0 && d < 20;
#pragma unroll
                for (int j = 0; j < 4; ++j) e[j] = ok ? (unsigned)wphi[(size_t)((h0 + hh) * 20 + d + j) * 20 + f] : 0u;
                pf[F][t] = (u32x2){e[0] | (e[1] << 16), e[2] | (e[3] << 16)};
            }
#pragma unroll
        for (int tt = 0; tt < 2; ++tt) {
            u32x2 qb[3];
#pragma unroll
            for (int t = 0; t < 3; ++t) { f32x4 v = acc[tt][t + 2]; qb[t] = (u32x2){pk2(v[0], v[1]), pk2(v[2], v[3])}; }
            f32x4 z = {0.f, 0.f, 0.f, 0.f};
            f32x4 F0 = MFMA16_16(pf[0][0], qb[0], z); F0 = MFMA16_16(pf[0][1], qb[1], F0);
            f32x4 F1 = MFMA16_16(pf[1][1], qb[1], z); F1 = MFMA16_16(pf[1][2], qb[2], F1);
            f32x4 F2 = MFMA16_16(pf[2][0], qb[0], z); F2 = MFMA16_16(pf[2][1], qb[1], F2); F2 = MFMA16_16(pf[2][2], qb[2], F2);
#define RND4(F) { unsigned a_ = pk2(F[0], F[1]), b_ = pk2(F[2], F[3]); F = (f32x4){bf_lo(a_) * LOG2E, bf_hi(a_) * LOG2E, bf_lo(b_) * LOG2E, bf_hi(b_) * LOG2E}; }
            RND4(F0) RND4(F1) RND4(F2)
#undef RND4
            const float tmx = fmaxf(fmaxf(F2[0], F2[1]), fmaxf(F2[2], F2[3])), tmn = fminf(fminf(F2[0], F2[1]), fminf(F2[2], F2[3]));
            float mx0 = fmaxf(fmaxf(F0[0], F0[1]), fmaxf(F0[2], F0[3])), mn0 = fminf(fminf(F0[0], F0[1]), fminf(F0[2], F0[3]));
            float mx1 = fmaxf(fmaxf(F1[0], F1[1]), fmaxf(F1[2], F1[3])), mn1 = fminf(fminf(F1[0], F1[1]), fminf(F1[2], F1[3]));
            if (g == 0) { mx0 = fmaxf(mx0, tmx); mn0 = fminf(mn0, tmn); }
            if (g == 1) { mx1 = fmaxf(mx1, tmx); mn1 = fminf(mn1, tmn); }
            mx0 = xg_max(mx0); mn0 = -xg_max(-mn0); mx1 = xg_max(mx1); mn1 = -xg_max(-mn1);
            const float mxt = g == 0 ? mx0 : mx1, mnt = g == 0 ? mn0 : mn1;
            f32x4 p0, n0, p1, n1, pt, ntl;
#pragma unroll
            for (int j = 0; j < 4; ++j) {
                p0[j] = ex2(F0[j] - mx0); n0[j] = ex2(mn0 - F0[j]);
                p1[j] = ex2(F1[j] - mx1); n1[j] = ex2(mn1 - F1[j]);
                pt[j] = ex2(F2[j] - mxt); ntl[j] = ex2(mnt - F2[j]);
            }
            const float stp = (pt[0] + pt[1]) + (pt[2] + pt[3]), stn = (ntl[0] + ntl[1]) + (ntl[2] + ntl[3]);
            float sp0 = (p0[0] + p0[1]) + (p0[2] + p0[3]) + (g == 0 ? stp : 0.f), sn0 = (n0[0] + n0[1]) + (n0[2] + n0[3]) + (g == 0 ? stn : 0.f);
            float sp1 = (p1[0] + p1[1]) + (p1[2] + p1[3]) + (g == 1 ? stp : 0.f), sn1 = (n1[0] + n1[1]) + (n1[2] + n1[3]) + (g == 1 ? stn : 0.f);
            sp0 = __builtin_amdgcn_rcpf(xg_sum(sp0)); sn0 = __builtin_amdgcn_rcpf(xg_sum(sn0));
            sp1 = __builtin_amdgcn_rcpf(xg_sum(sp1)); sn1 = __builtin_amdgcn_rcpf(xg_sum(sn1));
            const float spt = g == 0 ? sp0 : sp1, snt = g == 0 ? sn0 : sn1;
            u32x4 c0 = {pk2(p0[0] * sp0, p0[1] * sp0), pk2(p0[2] * sp0, p0[3] * sp0), pk2(n0[0] * sn0, n0[1] * sn0), pk2(n0[2] * sn0, n0[3] * sn0)};
            u32x4 c1 = {pk2(p1[0] * sp1, p1[1] * sp1), pk2(p1[2] * sp1, p1[3] * sp1), pk2(n1[0] * sn1, n1[1] * sn1), pk2(n1[2] * sn1, n1[3] * sn1)};
            u32x4 ct = {pk2(pt[0] * spt, pt[1] * spt), pk2(pt[2] * spt, pt[3] * spt), pk2(ntl[0] * snt, ntl[1] * snt), pk2(ntl[2] * snt, ntl[3] * snt)};
            const int tl = mg * 32 + tt * 16 + c16;
            if (part == 0) {
                char* b0 = ws + WS_PHIQ + (size_t)(h0 * SEQ + m0 + tl) * 80;
                char* b1 = b0 + (size_t)SEQ * 80;
                *(u32x4*)(b0 + g * 16) = c0; *(u32x4*)(b1 + g * 16) = c1;
                if (g < 2) *(u32x4*)((g == 0 ? b0 : b1) + 64) = ct;
            } else {
                LDS3 char* b0 = lds + PHIK + tl * 80;
                *(LDS3 u32x4*)(b0 + g * 16) = c0; *(LDS3 u32x4*)(b0 + PHIK_H + g * 16) = c1;
                if (g < 2) *(LDS3 u32x4*)(b0 + g * PHIK_H + 64) = ct;
            }
        }
    } else {
#pragma unroll
        for (int tt = 0; tt < 2; ++tt) {
            const int kb = (m0 + mg * 32 + tt * 16) >> 4;
#pragma unroll
            for (int n = 0; n < 5; ++n) {
                const int c = n * 16 + c16;
                f32x4 v = acc[tt][n];
                u32x2 o = {pk2(v[0], v[1]), pk2(v[2], v[3])};
                if (c < 40) {
                    const int hh = c >= 20, d = c - hh * 20;
                    char* pp = ws + WS_KV + (size_t)(h0 + hh) * 163840 + (kb >> 3) * 10240 + 5120 + (kb & 7) * 640 + (g & 1) * 320 + d * 16 + (g >> 1) * 8;
                    *(u32x2*)pp = o;
                } else {
                    const int hh = c >= 60, d = c - 40 - hh * 20;
                    *(LDS3 u32x2*)(lds + VT + hh * VT_H + d * VT_ROW + (mg * 32 + tt * 16 + g * 4) * 2) = o;
                }
            }
        }
        if (lane < 16) *(LDS3 u32x2*)(lds + VT + (lane >> 3) * VT_H + 20 * VT_ROW + (mg * 32 + (lane & 7) * 4) * 2) = (u32x2){0x3f803f80u, 0x3f803f80u};
#pragma unroll
        for (int rr = 0; rr < 2; ++rr) {
            const int row = m0 + nt * 8 + mg * 2 + rr;
            const u32x4 xa = *(const u32x4*)(x + (size_t)row * DM + lane * 8), ga = *(const u32x4*)(gw + lane * 8);
            float s = 0.f;
#pragma unroll
            for (int j = 0; j < 4; ++j) s += bf_lo(xa[j]) * bf_lo(ga[j]) + bf_hi(xa[j]) * bf_hi(ga[j]);
            if (lane < 16) {
                const u32x4 xb = *(const u32x4*)(x + (size_t)row * DM + 512 + lane * 8), gb = *(const u32x4*)(gw + 512 + lane * 8);
#pragma unroll
                for (int j = 0; j < 4; ++j) s += bf_lo(xb[j]) * bf_lo(gb[j]) + bf_hi(xb[j]) * bf_hi(gb[j]);
            }
#pragma unroll
            for (int o = 32; o >= 1; o >>= 1) s += __shfl_xor(s, o);
            if (lane == 0) ((float*)(ws + WS_G))[row] = 1.f / (1.f + __expf(-s));
        }
        if (bid < 160) {
            const int k0 = bid * 8;
            const u16* wsrc = k0 < 640 ? woa + (size_t)k0 * DM : wob + (size_t)(k0 - 640) * DM;
            const int t4 = mg * 64 + lane;
            for (int n2 = t4; n2 < 320; n2 += 256) {
                unsigned e[8];
#pragma unroll
                for (int j = 0; j < 8; ++j) e[j] = *(const unsigned*)(wsrc + (size_t)j * DM + n2 * 2);
                u32x4 lo = {(e[0] & 0xffffu) | (e[1] << 16), (e[2] & 0xffffu) | (e[3] << 16), (e[4] & 0xffffu) | (e[5] << 16), (e[6] & 0xffffu) | (e[7] << 16)};
                u32x4 hi = {(e[0] >> 16) | (e[1] & 0xffff0000u), (e[2] >> 16) | (e[3] & 0xffff0000u), (e[4] >> 16) | (e[5] & 0xffff0000u), (e[6] >> 16) | (e[7] & 0xffff0000u)};
                const int n = n2 * 2;
                char* dp = ws + WS_WT + ((size_t)((n >> 4) * 40 + (k0 >> 5)) * 64 + ((k0 >> 3) & 3) * 16 + (n & 15)) * 16;
                *(u32x4*)dp = lo; *(u32x4*)(dp + 16) = hi;
            }
        }
    }
    __syncthreads();
    {
        const int hh = w / 6, rem = w - hh * 6, ft = rem >> 1, dt = rem & 1;
        LDS3 const char* ap = lds + PHIK + hh * PHIK_H + (8 * g + (c16 >> 2)) * 80 + (16 * ft + 4 * (c16 & 3)) * 2;
        LDS3 const char* bp = lds + VT + hh * VT_H + (16 * dt + c16) * VT_ROW + g * 16;
        f32x4 kv = {0.f, 0.f, 0.f, 0.f};
#pragma unroll
        for (int ks = 0; ks < 4; ++ks) {
            u32x2 lo = trread(ap + ks * 32 * 80), hi = trread(ap + ks * 32 * 80 + 4 * 80);
            u32x4 a = {lo[0], lo[1], hi[0], hi[1]};
            u32x4 b = *(LDS3 const u32x4*)(bp + ks * 64);
            kv = MFMA16_32(a, b, kv);
        }
        const int d = 16 * dt + c16, sg = 16 * ft + 4 * g;
        if (d <= 20 && sg < 40) *(f32x4*)(ws + WS_KVP + ((size_t)((mt * NH + h0 + hh) * 21 + d) * 40 + sg) * 4) = kv;
    }
}

struct Frag { u32x4 ka; u32x2 kt; };

__global__ __launch_bounds__(1024) void k2_attn(char* __restrict__ ws) {
    __shared__ __attribute__((aligned(16))) char smem[163840];
    LDS3 char* lds = (LDS3 char*)smem;
    const int tid = threadIdx.x, lane = tid & 63, r = lane & 31, h = lane >> 5;
    const int w = __builtin_amdgcn_readfirstlane(tid >> 6);
    const int bid = blockIdx.x;
    const int head = (bid & 7) * 4 + ((bid >> 3) & 3), qb = bid >> 5;
    const int kq = w >> 2, qs = w & 3;
    const unsigned lds0 = (unsigned)(uintptr_t)smem;
    const int q0 = qb * 256 + qs * 64;

    u32x4 qa0, qa1; unsigned qt0, qt1;
    {
        const char* qp = ws + WS_Q + (size_t)(head * SEQ + q0 + r) * 40;
        const char* qm = qp + 16 * h; const char* qt = qp + 32 + 4 * h;
        asm volatile("global_load_dwordx4 %0, %4, off\n\tglobal_load_dwordx4 %1, %4, off offset:1280\n\t"
                     "global_load_dword %2, %5, off\n\tglobal_load_dword %3, %5, off offset:1280"
                     : "=&v"(qa0), "=&v"(qa1), "=&v"(qt0), "=&v"(qt1) : "v"(qm), "v"(qt) : "memory");
    }
    {
        const char* src = ws + WS_KV + (size_t)head * 163840 + kq * 40960 + qs * 1024 + lane * 16;
        const unsigned dst = lds0 + kq * 40960 + qs * 1024;
#pragma unroll
        for (int t = 0; t < 10; ++t) glds16(src + t * 4096, dst + t * 4096);
    }
    LDS3 const char* base = lds + kq * 40960;
    LDS3 const char* kp = base + r * 32 + h * 16;
    LDS3 const char* ktp = base + 4096 + r * 8 + h * 4;
    LDS3 const char* vp = base + 5120 + h * 320 + r * 16;
    const unsigned one = (r == 20) ? 0x3f803f80u : 0u;
    u32x4 v0 = {one, one, one, one}, v1 = v0;
    const f32x16 z16 = {};

    asm volatile("s_waitcnt vmcnt(7)\n\ts_barrier" : "+v"(qa0), "+v"(qa1), "+v"(qt0), "+v"(qt1) :: "memory");

    float m0, m1; u32x2 qT0, qT1;
    {
        u32x4 ka = *(LDS3 const u32x4*)kp;
        u32x2 kt = {*(LDS3 const unsigned*)ktp, 0x3f80u};
        u32x2 a0 = {qt0, 0u}, a1 = {qt1, 0u};
        f32x16 s0 = MFMA32_16(ka, qa0, z16); s0 = MFMA32_8(kt, a0, s0);
        f32x16 s1 = MFMA32_16(ka, qa1, z16); s1 = MFMA32_8(kt, a1, s1);
        float x0 = s0[0], x1 = s1[0];
#pragma unroll
        for (int i = 1; i < 16; ++i) { x0 = fmaxf(x0, s0[i]); x1 = fmaxf(x1, s1[i]); }
        x0 = -xhalf_max(x0); x1 = -xhalf_max(x1);
        const unsigned hh = pk2(x0, x1);
        const float hi0 = bf_lo(hh), hi1 = bf_hi(hh);
        const unsigned ll = pk2(x0 - hi0, x1 - hi1);
        m0 = -(hi0 + bf_lo(ll)); m1 = -(hi1 + bf_hi(ll));
        const unsigned sel = h ? ll : hh;
        qT0 = (u32x2){qt0, sel & 0xffffu}; qT1 = (u32x2){qt1, sel >> 16};
    }
    f32x16 o0 = {}, o1 = {};
#pragma unroll 1
    for (int c = 0; c < 4; ++c) {
        if (c == 1) { WAIT_BAR(5); } else if (c == 2) { WAIT_BAR(0); }
#pragma unroll
        for (int t = 0; t < 4; ++t) {
            u32x4 ka = *(LDS3 const u32x4*)(kp + c * 10240 + t * 1024);
            u32x2 kt = {*(LDS3 const unsigned*)(ktp + c * 10240 + t * 256), 0x3f80u};
            if (r < 20) {
                v0 = *(LDS3 const u32x4*)(vp + c * 10240 + t * 1280);
                v1 = *(LDS3 const u32x4*)(vp + c * 10240 + t * 1280 + 640);
            }
            f32x16 s0 = MFMA32_16(ka, qa0, z16); s0 = MFMA32_8(kt, qT0, s0);
            f32x16 s1 = MFMA32_16(ka, qa1, z16); s1 = MFMA32_8(kt, qT1, s1);
            u32x4 pa, pb;
#pragma unroll
            for (int i = 0; i < 4; ++i) pa[i] = pk2(ex2(s0[2 * i]), ex2(s0[2 * i + 1]));
#pragma unroll
            for (int i = 0; i < 4; ++i) pb[i] = pk2(ex2(s0[8 + 2 * i]), ex2(s0[8 + 2 * i + 1]));
            o0 = MFMA32_16(v0, pa, o0); o0 = MFMA32_16(v1, pb, o0);
#pragma unroll
            for (int i = 0; i < 4; ++i) pa[i] = pk2(ex2(s1[2 * i]), ex2(s1[2 * i + 1]));
#pragma unroll
            for (int i = 0; i < 4; ++i) pb[i] = pk2(ex2(s1[8 + 2 * i]), ex2(s1[8 + 2 * i + 1]));
            o1 = MFMA32_16(v0, pa, o1); o1 = MFMA32_16(v1, pb, o1);
        }
    }
    {
        float chk = 0.f;
#pragma unroll
        for (int i = 0; i < 12; ++i) chk += fabsf(o0[i]) * 0.f + fabsf(o1[i]) * 0.f;
        bool bad = !(chk == 0.f);
        if (h) bad = bad || !(o0[8] >= 0x1p-60f && o0[8] <= 0x1p60f && o1[8] >= 0x1p-60f && o1[8] <= 0x1p60f);
        if (__builtin_expect(__any(bad), 0)) {
            m0 = -INFINITY; m1 = -INFINITY;
#pragma unroll
            for (int i = 0; i < 16; ++i) { o0[i] = 0.f; o1[i] = 0.f; }
            const u32x2 a0 = {qt0, 0u}, a1 = {qt1, 0u};
#pragma unroll 1
            for (int t = 0; t < 16; ++t) {
                const int off = (t >> 2) * 10240, tt = t & 3;
                u32x4 ka = *(LDS3 const u32x4*)(kp + off + tt * 1024);
                u32x2 kt = {*(LDS3 const unsigned*)(ktp + off + tt * 256), 0x3f80u};
                if (r < 20) {
                    v0 = *(LDS3 const u32x4*)(vp + off + tt * 1280);
                    v1 = *(LDS3 const u32x4*)(vp + off + tt * 1280 + 640);
                }
                f32x16 s0 = MFMA32_16(ka, qa0, z16); s0 = MFMA32_8(kt, a0, s0);
                f32x16 s1 = MFMA32_16(ka, qa1, z16); s1 = MFMA32_8(kt, a1, s1);
                float x0 = s0[0], x1 = s1[0];
#pragma unroll
                for (int i = 1; i < 16; ++i) { x0 = fmaxf(x0, s0[i]); x1 = fmaxf(x1, s1[i]); }
                x0 = fmaxf(xhalf_max(x0), m0); x1 = fmaxf(xhalf_max(x1), m1);
                const float f0 = (m0 == x0) ? 1.f : ex2(m0 - x0), f1 = (m1 == x1) ? 1.f : ex2(m1 - x1);
                m0 = x0; m1 = x1;
#pragma unroll
                for (int i = 0; i < 16; ++i) { o0[i] *= f0; o1[i] *= f1; }
                u32x4 pa, pb;
#pragma unroll
                for (int i = 0; i < 4; ++i) pa[i] = pk2(ex2(s0[2 * i] - x0), ex2(s0[2 * i + 1] - x0));
#pragma unroll
                for (int i = 0; i < 4; ++i) pb[i] = pk2(ex2(s0[8 + 2 * i] - x0), ex2(s0[8 + 2 * i + 1] - x0));
                o0 = MFMA32_16(v0, pa, o0); o0 = MFMA32_16(v1, pb, o0);
#pragma unroll
                for (int i = 0; i < 4; ++i) pa[i] = pk2(ex2(s1[2 * i] - x1), ex2(s1[2 * i + 1] - x1));
#pragma unroll
                for (int i = 0; i < 4; ++i) pb[i] = pk2(ex2(s1[8 + 2 * i] - x1), ex2(s1[8 + 2 * i + 1] - x1));
                o1 = MFMA32_16(v0, pa, o1); o1 = MFMA32_16(v1, pb, o1);
            }
        }
    }
    float kvs = 0.f;
    if (tid < 840) {
        const float* kp2 = (const float*)(ws + WS_KVP) + (size_t)head * 840 + tid;
#pragma unroll
        for (int i = 0; i < 16; ++i) kvs += kp2[(size_t)i * NH * 840];
    }
    __syncthreads();
    constexpr int XM = 98304, XKV = XM + 4096, XKV_ROW = 96;
    {
        LDS3 char* xp = lds + ((kq * 4 + qs) * 2) * 3072 + lane * 16;
#pragma unroll
        for (int gq = 0; gq < 3; ++gq) {
            *(LDS3 f32x4*)(xp + gq * 1024) = (f32x4){o0[4 * gq], o0[4 * gq + 1], o0[4 * gq + 2], o0[4 * gq + 3]};
            *(LDS3 f32x4*)(xp + 3072 + gq * 1024) = (f32x4){o1[4 * gq], o1[4 * gq + 1], o1[4 * gq + 2], o1[4 * gq + 3]};
        }
        *(LDS3 float*)(lds + XM + (((kq * 4 + qs) * 2 + h) * 32 + r) * 4) = h ? m1 : m0;
        if (tid < 840) { const int d = tid / 40, sg = tid - d * 40; *(LDS3 u16*)(lds + XKV + d * XKV_ROW + sg * 2) = (u16)(pk2(kvs, 0.f) & 0xffffu); }
    }
    __syncthreads();
    char* ao = ws + WS_AO;
    if (kq < 2) {
        const int qt = kq;
        float mm[4], mx = -INFINITY;
#pragma unroll
        for (int k = 0; k < 4; ++k) { mm[k] = *(LDS3 const float*)(lds + XM + (((k * 4 + qs) * 2 + qt) * 32 + r) * 4); mx = fmaxf(mx, mm[k]); }
        f32x4 a[3] = {{0.f, 0.f, 0.f, 0.f}, {0.f, 0.f, 0.f, 0.f}, {0.f, 0.f, 0.f, 0.f}};
#pragma unroll
        for (int k = 0; k < 4; ++k) {
            const float f = (mm[k] == mx) ? 1.f : ex2(mm[k] - mx);
#pragma unroll
            for (int gq = 0; gq < 3; ++gq) a[gq] += f * *(LDS3 const f32x4*)(lds + ((k * 4 + qs) * 2 + qt) * 3072 + gq * 1024 + lane * 16);
        }
        auto sw = __builtin_amdgcn_permlane32_swap(__float_as_uint(a[2][0]), __float_as_uint(a[2][0]), false, false);
        const float l = __uint_as_float(sw[1]);
        const float rl = 1.f / l;
        const int tok = q0 + qt * 32 + r;
#pragma unroll
        for (int gq = 0; gq < 3; ++gq) {
            const int d = 8 * gq + 4 * h;
            if (d < 20) {
                const int k = head * 20 + d;
                u32x2 o = {pk2(a[gq][0] * rl, a[gq][1] * rl), pk2(a[gq][2] * rl, a[gq][3] * rl)};
                *(u32x2*)(ao + ((size_t)((tok >> 4) * 40 + (k >> 5)) * 64 + ((k >> 3) & 3) * 16 + (tok & 15)) * 16 + (k & 7) * 2) = o;
            }
        }
    } else {
        const int c16 = lane & 15, g = lane >> 4;
        u32x4 a[2][2];
#pragma unroll
        for (int dt = 0; dt < 2; ++dt) {
            LDS3 const char* ap = lds + XKV + (dt * 16 + c16) * XKV_ROW;
            a[dt][0] = *(LDS3 const u32x4*)(ap + g * 16);
            u32x4 t = *(LDS3 const u32x4*)(ap + 64);
            a[dt][1] = g == 0 ? t : (u32x4){0u, 0u, 0u, 0u};
        }
#pragma unroll
        for (int tt = 0; tt < 2; ++tt) {
            const int tok = q0 + (kq - 2) * 32 + tt * 16 + c16;
            const char* pp = ws + WS_PHIQ + (size_t)(head * SEQ + tok) * 80;
            const u32x4 b0 = *(const u32x4*)(pp + g * 16), b1 = *(const u32x4*)(pp + 64);
            f32x4 z = {0.f, 0.f, 0.f, 0.f};
            f32x4 d0 = MFMA16_32(a[0][0], b0, z); d0 = MFMA16_32(a[0][1], b1, d0);
            f32x4 d1 = MFMA16_32(a[1][0], b0, z); d1 = MFMA16_32(a[1][1], b1, d1);
            const float den = __shfl(d1[0], 16 + c16) + 1e-6f;
            const float rd = 1.f / den;
            {
                const int k = 640 + head * 20 + 4 * g;
                u32x2 o = {pk2(d0[0] * rd, d0[1] * rd), pk2(d0[2] * rd, d0[3] * rd)};
                *(u32x2*)(ao + ((size_t)((tok >> 4) * 40 + (k >> 5)) * 64 + ((k >> 3) & 3) * 16 + (tok & 15)) * 16 + (k & 7) * 2) = o;
            }
            if (g == 0) {
                const int k = 640 + head * 20 + 16;
                u32x2 o = {pk2(d1[0] * rd, d1[1] * rd), pk2(d1[2] * rd, d1[3] * rd)};
                *(u32x2*)(ao + ((size_t)((tok >> 4) * 40 + (k >> 5)) * 64 + ((k >> 3) & 3) * 16 + (tok & 15)) * 16 + (k & 7) * 2) = o;
            }
        }
    }
}

__global__ __launch_bounds__(512) void k3_out(const char* __restrict__ ws, u16* __restrict__ out) {
    __shared__ __attribute__((aligned(16))) char smem[8 * 20480];
    LDS3 char* lds = (LDS3 char*)smem;
    const int tid = threadIdx.x, lane = tid & 63, c16 = lane & 15, g = lane >> 4;
    const int w = __builtin_amdgcn_readfirstlane(tid >> 6);
    const int bid = blockIdx.x, xcd = bid & 7, idx = bid >> 3;
    const int mt = xcd * 4 + (idx & 3), nt = idx >> 2;
    const char* ap = ws + WS_AO + ((size_t)(mt * 4) * 40 + w * 5) * 1024 + lane * 16;
    const char* bp = ws + WS_WT + ((size_t)(nt * 5) * 40 + w * 5) * 1024 + lane * 16;
    f32x4 acc[4][5];
#pragma unroll
    for (int a = 0; a < 4; ++a)
#pragma unroll
        for (int b = 0; b < 5; ++b) acc[a][b] = (f32x4){0.f, 0.f, 0.f, 0.f};
#pragma unroll
    for (int ks = 0; ks < 5; ++ks) {
        u32x4 af[4], bf[5];
#pragma unroll
        for (int t = 0; t < 4; ++t) af[t] = *(const u32x4*)(ap + (size_t)t * 40960 + ks * 1024);
#pragma unroll
        for (int n = 0; n < 5; ++n) bf[n] = *(const u32x4*)(bp + (size_t)n * 40960 + ks * 1024);
#pragma unroll
        for (int t = 0; t < 4; ++t)
#pragma unroll
            for (int n = 0; n < 5; ++n) acc[t][n] = MFMA16_32(bf[n], af[t], acc[t][n]);
    }
    const float* gp = (const float*)(ws + WS_G) + mt * 64 + c16;
#pragma unroll
    for (int t = 0; t < 4; ++t) {
        float gt = gp[t * 16]; if (w >= 4) gt = 1.f - gt;
#pragma unroll
        for (int n = 0; n < 5; ++n) *(LDS3 f32x4*)(lds + w * 20480 + (t * 5 + n) * 1024 + lane * 16) = acc[t][n] * gt;
    }
    __syncthreads();
    for (int qd = tid; qd < 1280; qd += 512) {
        f32x4 s = {0.f, 0.f, 0.f, 0.f};
#pragma unroll
        for (int k = 0; k < 8; ++k) s += *(LDS3 const f32x4*)(lds + k * 20480 + qd * 16);
        const int T = qd >> 6, t = T / 5, n = T - t * 5;
        const int tok = mt * 64 + t * 16 + c16, col = nt * 80 + n * 16 + g * 4;
        *(u32x2*)(out + (size_t)tok * DM + col) = (u32x2){pk2(s[0], s[1]), pk2(s[2], s[3])};
    }
}

extern "C" void kernel_launch(void* const* d_in, const int* in_sizes, int n_in, void* d_out, int out_size, void* d_ws, size_t ws_size,
                              hipStream_t stream) {
    const u16* x = (const u16*)d_in[0];
    const u16* wa = (const u16*)d_in[1];
    const u16* woa = (const u16*)d_in[2];
    const u16* wb = (const u16*)d_in[4];
    const u16* wphi = (const u16*)d_in[5];
    const u16* wob = (const u16*)d_in[6];
    const u16* gw = (const u16*)d_in[7];
    char* ws = (char*)d_ws;
    k1_qkv<<<256, 768, 0, stream>>>(x, wa, wb, wphi, woa, wob, gw, ws);
    k2_attn<<<256, 1024, 0, stream>>>(ws);
    k3_out<<<256, 512, 0, stream>>>(ws, (u16*)d_out);
}
```

```cpp
#include <hip/hip_runtime.h>
#include <hip/hip_bf16.h>
#include <stdint.h>

typedef __attribute__((ext_vector_type(8))) short bf16x8;
typedef __attribute__((ext_vector_type(4))) short bf16x4;
typedef __attribute__((ext_vector_type(16))) float f32x16;
typedef __attribute__((ext_vector_type(4))) float f32x4;
typedef __attribute__((ext_vector_type(2))) float f32x2;
typedef __attribute__((ext_vector_type(4))) unsigned u32x4;
typedef __attribute__((ext_vector_type(2))) unsigned u32x2;
typedef __attribute__((ext_vector_type(2))) __bf16 bf2_t;
typedef unsigned short u16;
#define LDS3 __attribute__((address_space(3)))

constexpr int SEQ = 2048, DM = 640, NH = 32, DH = 20;
constexpr float LOG2E = 1.4426950408889634f;
constexpr float QSCALE = 0.22360679774997896f * LOG2E;

constexpr size_t WS_Q    = 0;
constexpr size_t WS_KV   = WS_Q + (size_t)NH * SEQ * 20 * 2;
constexpr size_t WS_G    = WS_KV + (size_t)NH * 163840;
constexpr size_t WS_PHIQ = WS_G + SEQ * 4;
constexpr size_t WS_KVP  = WS_PHIQ + (size_t)NH * SEQ * 40 * 2;
constexpr size_t WS_AO   = WS_KVP + (size_t)16 * NH * 21 * 40 * 4;
constexpr size_t WS_WT   = WS_AO + (size_t)SEQ * 1280 * 2;
constexpr size_t WS_END  = WS_WT + (size_t)1280 * 640 * 2;

#define STAMP_DECL
#define STAMP(i)
#define STAMP_FLUSH(kid)
__device__ __forceinline__ unsigned pk2(float a, float b) { f32x2 v = {a, b}; bf2_t r = __builtin_convertvector(v, bf2_t); return __builtin_bit_cast(unsigned, r); }
__device__ __forceinline__ float bf_lo(unsigned u) { return __uint_as_float(u << 16); }
__device__ __forceinline__ float bf_hi(unsigned u) { return __uint_as_float(u & 0xffff0000u); }
__device__ __forceinline__ float ex2(float x) { return __builtin_amdgcn_exp2f(x); }
#define MFMA32_16(a, b, c) __builtin_amdgcn_mfma_f32_32x32x16_bf16(__builtin_bit_cast(bf16x8, a), __builtin_bit_cast(bf16x8, b), c, 0, 0, 0)
#define MFMA32_8(a, b, c) __builtin_amdgcn_mfma_f32_32x32x8bf16_1k(__builtin_bit_cast(bf16x4, a), __builtin_bit_cast(bf16x4, b), c, 0, 0, 0)
#define MFMA16_32(a, b, c) __builtin_amdgcn_mfma_f32_16x16x32_bf16(__builtin_bit_cast(bf16x8, a), __builtin_bit_cast(bf16x8, b), c, 0, 0, 0)
#define MFMA16_16(a, b, c) __builtin_amdgcn_mfma_f32_16x16x16bf16_1k(__builtin_bit_cast(bf16x4, a), __builtin_bit_cast(bf16x4, b), c, 0, 0, 0)

__device__ __forceinline__ void glds16(const void* g, unsigned lds_base) {
    unsigned sv;
    asm volatile("s_mov_b32 %0, m0\n\ts_mov_b32 m0, %2\n\ts_nop 0\n\tglobal_load_lds_dwordx4 %1, off\n\ts_mov_b32 m0, %0"
                 : "=&s"(sv) : "v"(g), "s"(lds_base) : "memory");
}
typedef short v4i16_t __attribute__((ext_vector_type(4)));
__device__ __forceinline__ u32x2 trread(LDS3 const char* p) {
    return __builtin_bit_cast(u32x2, __builtin_amdgcn_ds_read_tr16_b64_v4i16((LDS3 v4i16_t*)p));
}
#define WAIT_BAR(N) asm volatile("s_waitcnt vmcnt(" #N ") lgkmcnt(0)\n\ts_barrier" ::: "memory")

__device__ __forceinline__ float xhalf_max(float x) {
    auto r = __builtin_amdgcn_permlane32_swap(__float_as_uint(x), __float_as_uint(x), false, false);
    return fmaxf(__uint_as_float(r[0]), __uint_as_float(r[1]));
}
__device__ __forceinline__ float xg_max(float x) {
    auto s = __builtin_amdgcn_permlane16_swap(__float_as_uint(x), __float_as_uint(x), false, false);
    x = fmaxf(__uint_as_float(s[0]), __uint_as_float(s[1]));
    return xhalf_max(x);
}
__device__ __forceinline__ float xg_sum(float x) {
    auto s = __builtin_amdgcn_permlane16_swap(__float_as_uint(x), __float_as_uint(x), false, false);
    x = __uint_as_float(s[0]) + __uint_as_float(s[1]);
    auto r = __builtin_amdgcn_permlane32_swap(__float_as_uint(x), __float_as_uint(x), false, false);
    return __uint_as_float(r[0]) + __uint_as_float(r[1]);
}

constexpr int K1_WB = 31744, K1_AB = 16384, K1_STAGE = K1_WB + K1_AB;
constexpr int K1_LDS = 3 * K1_STAGE;

template <bool TR>
__device__ __forceinline__ void k1_mainloop(f32x4 (&acc)[2][5], LDS3 const char* lds, const char* const (&src)[4], const int (&inc)[4],
                                            const unsigned (&dst)[4], unsigned aoff, unsigned woff) {
    const char* p[4] = {src[0], src[1], src[2], src[3]};
#pragma unroll
    for (int s = 0; s < 2; ++s) {
#pragma unroll
        for (int i = 0; i < 4; ++i) { glds16(p[i], dst[i] + s * K1_STAGE); p[i] += inc[i]; }
    }
    int buf = 0;
#pragma unroll 1
    for (int kt = 0; kt < 10; ++kt) {
        if (kt < 9) { WAIT_BAR(4); } else { WAIT_BAR(0); }
        if (kt < 8) {
            const int nb = (buf + 2 >= 3) ? buf - 1 : buf + 2;
#pragma unroll
            for (int i = 0; i < 4; ++i) { glds16(p[i], dst[i] + nb * K1_STAGE); p[i] += inc[i]; }
        }
        LDS3 const char* wb = lds + buf * K1_STAGE + woff;
        LDS3 const char* ab = lds + buf * K1_STAGE + K1_WB;
#pragma unroll
        for (int ks = 0; ks < 2; ++ks) {
            u32x4 xf[2], wf[5];
#pragma unroll
            for (int tt = 0; tt < 2; ++tt) xf[tt] = *(LDS3 const u32x4*)(ab + ((aoff + tt * 2048) ^ (ks * 64)));
#pragma unroll
            for (int n = 0; n < 5; ++n) {
                u32x2 lo = trread(wb + ks * 4 * 3968 + n * 32);
                u32x2 hi = trread(wb + ks * 4 * 3968 + n * 32 + 4 * 480);
                wf[n] = (u32x4){lo[0], lo[1], hi[0], hi[1]};
            }
#pragma unroll
            for (int tt = 0; tt < 2; ++tt)
#pragma unroll
                for (int n = 0; n < 5; ++n)
                    acc[tt][n] = TR ? MFMA16_32(wf[n], xf[tt], acc[tt][n]) : MFMA16_32(xf[tt], wf[n], acc[tt][n]);
        }
        buf = (buf == 2) ? 0 : buf + 1;
    }
}

__global__ __launch_bounds__(768) void k1_qkv(const u16* __restrict__ x, const u16* __restrict__ wa, const u16* __restrict__ wb_,
                                              const u16* __restrict__ wphi, const u16* __restrict__ woa, const u16* __restrict__ wob,
                                              const u16* __restrict__ gw, char* __restrict__ ws) {
    __shared__ __attribute__((aligned(16))) char smem[K1_LDS];
    LDS3 char* lds = (LDS3 char*)smem;
    const int tid = threadIdx.x, lane = tid & 63;
    const int w = __builtin_amdgcn_readfirstlane(tid >> 6);
    const int bid = blockIdx.x, xcd = bid & 7, idx = bid >> 3;
    const int mt = (xcd & 1) * 8 + (idx & 7), nt = (xcd >> 1) * 4 + (idx >> 3);
    const int m0 = mt * 128;
    const int mg = w & 3, part = w >> 2;
    const int c16 = lane & 15, g = lane >> 4;
    const unsigned lds0 = (unsigned)(uintptr_t)smem;
    STAMP_DECL; STAMP(0);
    const char* src[4]; int inc[4]; unsigned dst[4];
#pragma unroll
    for (int i = 0; i < 4; ++i) {
        int j = w + 12 * i; if (j > 46) j = 46;
        if (j < 31) {
            int s = j * 64 + lane, grp = s / 248, r = s - grp * 248; if (r >= 240) r -= 240;
            int kr = r / 30, cc = r - kr * 30, p = cc / 10, rem = cc - p * 10, mat = rem / 5, jj = rem - mat * 5;
            const u16* wsrc = mat ? wb_ : wa;
            src[i] = (const char*)(wsrc + (size_t)(grp * 8 + kr) * 1920 + p * 640 + nt * 40 + jj * 8);
            inc[i] = 64 * 1920 * 2; dst[i] = lds0 + j * 1024;
        } else {
            int s = (j - 31) * 64 + lane, row = s >> 3, ch = (s & 7) ^ (row & 7);
            src[i] = (const char*)(x + (size_t)(m0 + row) * DM + ch * 8);
            inc[i] = 128; dst[i] = lds0 + K1_WB + (j - 31) * 1024;
        }
        inc[i] = __builtin_amdgcn_readfirstlane(inc[i]); dst[i] = __builtin_amdgcn_readfirstlane(dst[i]);
    }
    const int arow = mg * 32 + c16;
    const unsigned aoff = arow * 128 + ((g ^ (arow & 7)) << 4);
    const unsigned woff = g * 3968 + (c16 >> 2) * 480 + (part * 80 + (c16 & 3) * 4) * 2;

    f32x4 acc[2][5];
#pragma unroll
    for (int a = 0; a < 2; ++a)
#pragma unroll
        for (int b = 0; b < 5; ++b) acc[a][b] = (f32x4){0.f, 0.f, 0.f, 0.f};

    if (part < 2) k1_mainloop<true>(acc, lds, src, inc, dst, aoff, woff);
    else          k1_mainloop<false>(acc, lds, src, inc, dst, aoff, woff);

    STAMP(1);
    __syncthreads();
    constexpr int PHIK = 0, PHIK_H = 128 * 80 + 64;
    constexpr int VT = 2 * PHIK_H, VT_ROW = 272, VT_H = 32 * VT_ROW;
    const int h0 = nt * 2;
    if (part < 2) {
#pragma unroll
        for (int tt = 0; tt < 2; ++tt) {
            const int tok = m0 + mg * 32 + tt * 16 + c16;
#pragma unroll
            for (int n = 0; n < 3; ++n) {
                const int c = n * 16 + g * 4;
                if (c < 40) {
                    const int hh = c >= 20, d = c - hh * 20, head = h0 + hh;
                    f32x4 v = acc[tt][n];
                    if (part == 0) {
                        u32x2 o = {pk2(v[0] * QSCALE, v[1] * QSCALE), pk2(v[2] * QSCALE, v[3] * QSCALE)};
                        *(u32x2*)(ws + WS_Q + ((size_t)(head * SEQ + tok) * 20 + d) * 2) = o;
                    } else {
                        u32x2 o = {pk2(v[0], v[1]), pk2(v[2], v[3])};
                        char* blob = ws + WS_KV + (size_t)head * 163840 + (tok >> 7) * 10240;
                        char* pp = d < 16 ? blob + (tok & 127) * 32 + d * 2 : blob + 4096 + (tok & 127) * 8;
                        *(u32x2*)pp = o;
                    }
                }
            }
        }
        u32x2 pf[3][3];
#pragma unroll
        for (int F = 0; F < 3; ++F)
#pragma unroll
            for (int t = 0; t < 3; ++t) {
                if ((F == 0 && t == 2) || (F == 1 && t == 0)) continue;
                const int hh = F < 2 ? F : (c16 >> 2), f = F < 2 ? c16 : 16 + (c16 & 3);
                const int d = (t + 2) * 16 + 4 * g - 40 - hh * 20;
                unsigned e[4];
                const bool ok = hh < 2 && d >= 0 && d < 20;
#pragma unroll
                for (int j = 0; j < 4; ++j) e[j] = ok ? (unsigned)wphi[(size_t)((h0 + hh) * 20 + d + j) * 20 + f] : 0u;
                pf[F][t] = (u32x2){e[0] | (e[1] << 16), e[2] | (e[3] << 16)};
            }
#pragma unroll
        for (int tt = 0; tt < 2; ++tt) {
            u32x2 qb[3];
#pragma unroll
            for (int t = 0; t < 3; ++t) { f32x4 v = acc[tt][t + 2]; qb[t] = (u32x2){pk2(v[0], v[1]), pk2(v[2], v[3])}; }
            f32x4 z = {0.f, 0.f, 0.f, 0.f};
            f32x4 F0 = MFMA16_16(pf[0][0], qb[0], z); F0 = MFMA16_16(pf[0][1], qb[1], F0);
            f32x4 F1 = MFMA16_16(pf[1][1], qb[1], z); F1 = MFMA16_16(pf[1][2], qb[2], F1);
            f32x4 F2 = MFMA16_16(pf[2][0], qb[0], z); F2 = MFMA16_16(pf[2][1], qb[1], F2); F2 = MFMA16_16(pf[2][2], qb[2], F2);
#define RND4(F) { unsigned a_ = pk2(F[0], F[1]), b_ = pk2(F[2], F[3]); F = (f32x4){bf_lo(a_) * LOG2E, bf_hi(a_) * LOG2E, bf_lo(b_) * LOG2E, bf_hi(b_) * LOG2E}; }
            RND4(F0) RND4(F1) RND4(F2)
#undef RND4
            const float tmx = fmaxf(fmaxf(F2[0], F2[1]), fmaxf(F2[2], F2[3])), tmn = fminf(fminf(F2[0], F2[1]), fminf(F2[2], F2[3]));
            float mx0 = fmaxf(fmaxf(F0[0], F0[1]), fmaxf(F0[2], F0[3])), mn0 = fminf(fminf(F0[0], F0[1]), fminf(F0[2], F0[3]));
            float mx1 = fmaxf(fmaxf(F1[0], F1[1]), fmaxf(F1[2], F1[3])), mn1 = fminf(fminf(F1[0], F1[1]), fminf(F1[2], F1[3]));
            if (g == 0) { mx0 = fmaxf(mx0, tmx); mn0 = fminf(mn0, tmn); }
            if (g == 1) { mx1 = fmaxf(mx1, tmx); mn1 = fminf(mn1, tmn); }
            mx0 = xg_max(mx0); mn0 = -xg_max(-mn0); mx1 = xg_max(mx1); mn1 = -xg_max(-mn1);
            const float mxt = g == 0 ? mx0 : mx1, mnt = g == 0 ? mn0 : mn1;
            f32x4 p0, n0, p1, n1, pt, ntl;
#pragma unroll
            for (int j = 0; j < 4; ++j) {
                p0[j] = ex2(F0[j] - mx0); n0[j] = ex2(mn0 - F0[j]);
                p1[j] = ex2(F1[j] - mx1); n1[j] = ex2(mn1 - F1[j]);
                pt[j] = ex2(F2[j] - mxt); ntl[j] = ex2(mnt - F2[j]);
            }
            const float stp = (pt[0] + pt[1]) + (pt[2] + pt[3]), stn = (ntl[0] + ntl[1]) + (ntl[2] + ntl[3]);
            float sp0 = (p0[0] + p0[1]) + (p0[2] + p0[3]) + (g == 0 ? stp : 0.f), sn0 = (n0[0] + n0[1]) + (n0[2] + n0[3]) + (g == 0 ? stn : 0.f);
            float sp1 = (p1[0] + p1[1]) + (p1[2] + p1[3]) + (g == 1 ? stp : 0.f), sn1 = (n1[0] + n1[1]) + (n1[2] + n1[3]) + (g == 1 ? stn : 0.f);
            sp0 = __builtin_amdgcn_rcpf(xg_sum(sp0)); sn0 = __builtin_amdgcn_rcpf(xg_sum(sn0));
            sp1 = __builtin_amdgcn_rcpf(xg_sum(sp1)); sn1 = __builtin_amdgcn_rcpf(xg_sum(sn1));
            const float spt = g == 0 ? sp0 : sp1, snt = g == 0 ? sn0 : sn1;
            u32x4 c0 = {pk2(p0[0] * sp0, p0[1] * sp0), pk2(p0[2] * sp0, p0[3] * sp0), pk2(n0[0] * sn0, n0[1] * sn0), pk2(n0[2] * sn0, n0[3] * sn0)};
            u32x4 c1 = {pk2(p1[0] * sp1, p1[1] * sp1), pk2(p1[2] * sp1, p1[3] * sp1), pk2(n1[0] * sn1, n1[1] * sn1), pk2(n1[2] * sn1, n1[3] * sn1)};
            u32x4 ct = {pk2(pt[0] * spt, pt[1] * spt), pk2(pt[2] * spt, pt[3] * spt), pk2(ntl[0] * snt, ntl[1] * snt), pk2(ntl[2] * snt, ntl[3] * snt)};
            const int tl = mg * 32 + tt * 16 + c16;
            if (part == 0) {
                char* b0 = ws + WS_PHIQ + (size_t)(h0 * SEQ + m0 + tl) * 80;
                char* b1 = b0 + (size_t)SEQ * 80;
                *(u32x4*)(b0 + g * 16) = c0; *(u32x4*)(b1 + g * 16) = c1;
                if (g < 2) *(u32x4*)((g == 0 ? b0 : b1) + 64) = ct;
            } else {
                LDS3 char* b0 = lds + PHIK + tl * 80;
                *(LDS3 u32x4*)(b0 + g * 16) = c0; *(LDS3 u32x4*)(b0 + PHIK_H + g * 16) = c1;
                if (g < 2) *(LDS3 u32x4*)(b0 + g * PHIK_H + 64) = ct;
            }
        }
    } else {
#pragma unroll
        for (int tt = 0; tt < 2; ++tt) {
            const int kb = (m0 + mg * 32 + tt * 16) >> 4;
#pragma unroll
            for (int n = 0; n < 5; ++n) {
                const int c = n * 16 + c16;
                f32x4 v = acc[tt][n];
                u32x2 o = {pk2(v[0], v[1]), pk2(v[2], v[3])};
                if (c < 40) {
                    const int hh = c >= 20, d = c - hh * 20;
                    char* pp = ws + WS_KV + (size_t)(h0 + hh) * 163840 + (kb >> 3) * 10240 + 5120 + (kb & 7) * 640 + (g & 1) * 320 + d * 16 + (g >> 1) * 8;
                    *(u32x2*)pp = o;
                } else {
                    const int hh = c >= 60, d = c - 40 - hh * 20;
                    *(LDS3 u32x2*)(lds + VT + hh * VT_H + d * VT_ROW + (mg * 32 + tt * 16 + g * 4) * 2) = o;
                }
            }
        }
        if (lane < 16) *(LDS3 u32x2*)(lds + VT + (lane >> 3) * VT_H + 20 * VT_ROW + (mg * 32 + (lane & 7) * 4) * 2) = (u32x2){0x3f803f80u, 0x3f803f80u};
#pragma unroll
        for (int rr = 0; rr < 2; ++rr) {
            const int row = m0 + nt * 8 + mg * 2 + rr;
            const u32x4 xa = *(const u32x4*)(x + (size_t)row * DM + lane * 8), ga = *(const u32x4*)(gw + lane * 8);
            float s = 0.f;
#pragma unroll
            for (int j = 0; j < 4; ++j) s += bf_lo(xa[j]) * bf_lo(ga[j]) + bf_hi(xa[j]) * bf_hi(ga[j]);
            if (lane < 16) {
                const u32x4 xb = *(const u32x4*)(x + (size_t)row * DM + 512 + lane * 8), gb = *(const u32x4*)(gw + 512 + lane * 8);
#pragma unroll
                for (int j = 0; j < 4; ++j) s += bf_lo(xb[j]) * bf_lo(gb[j]) + bf_hi(xb[j]) * bf_hi(gb[j]);
            }
#pragma unroll
            for (int o = 32; o >= 1; o >>= 1) s += __shfl_xor(s, o);
            if (lane == 0) ((float*)(ws + WS_G))[row] = 1.f / (1.f + __expf(-s));
        }
        if (bid < 160) {
            const int k0 = bid * 8;
            const u16* wsrc = k0 < 640 ? woa + (size_t)k0 * DM : wob + (size_t)(k0 - 640) * DM;
            const int t4 = mg * 64 + lane;
            for (int n2 = t4; n2 < 320; n2 += 256) {
                unsigned e[8];
#pragma unroll
                for (int j = 0; j < 8; ++j) e[j] = *(const unsigned*)(wsrc + (size_t)j * DM + n2 * 2);
                u32x4 lo = {(e[0] & 0xffffu) | (e[1] << 16), (e[2] & 0xffffu) | (e[3] << 16), (e[4] & 0xffffu) | (e[5] << 16), (e[6] & 0xffffu) | (e[7] << 16)};
                u32x4 hi = {(e[0] >> 16) | (e[1] & 0xffff0000u), (e[2] >> 16) | (e[3] & 0xffff0000u), (e[4] >> 16) | (e[5] & 0xffff0000u), (e[6] >> 16) | (e[7] & 0xffff0000u)};
                const int n = n2 * 2;
                char* dp = ws + WS_WT + ((size_t)((n >> 4) * 40 + (k0 >> 5)) * 64 + ((k0 >> 3) & 3) * 16 + (n & 15)) * 16;
                *(u32x4*)dp = lo; *(u32x4*)(dp + 16) = hi;
            }
        }
    }
    STAMP(2);
    __syncthreads();
    STAMP(3);
    {
        const int hh = w / 6, rem = w - hh * 6, ft = rem >> 1, dt = rem & 1;
        LDS3 const char* ap = lds + PHIK + hh * PHIK_H + (8 * g + (c16 >> 2)) * 80 + (16 * ft + 4 * (c16 & 3)) * 2;
        LDS3 const char* bp = lds + VT + hh * VT_H + (16 * dt + c16) * VT_ROW + g * 16;
        f32x4 kv = {0.f, 0.f, 0.f, 0.f};
#pragma unroll
        for (int ks = 0; ks < 4; ++ks) {
            u32x2 lo = trread(ap + ks * 32 * 80), hi = trread(ap + ks * 32 * 80 + 4 * 80);
            u32x4 a = {lo[0], lo[1], hi[0], hi[1]};
            u32x4 b = *(LDS3 const u32x4*)(bp + ks * 64);
            kv = MFMA16_32(a, b, kv);
        }
        const int d = 16 * dt + c16, sg = 16 * ft + 4 * g;
        if (d <= 20 && sg < 40) *(f32x4*)(ws + WS_KVP + ((size_t)((mt * NH + h0 + hh) * 21 + d) * 40 + sg) * 4) = kv;
    }
    STAMP(4); STAMP_FLUSH(0);
}

#define SBAR() __builtin_amdgcn_sched_barrier(0)
#define PIN(x) asm volatile("" : "+v"(x))
#define LWAIT3(x) asm volatile("s_waitcnt lgkmcnt(3)" : "+v"(x))
#define DSR128(dst, addr, off) asm volatile("ds_read_b128 %0, %1 offset:%2" : "=v"(dst) : "v"(addr), "i"(off))
#define DSR32(dst, addr, off) asm volatile("ds_read_b32 %0, %1 offset:%2" : "=v"(dst) : "v"(addr), "i"(off))
#define DSR128M(dst, addr, off) asm volatile("s_mov_b64 exec, %3\n\tds_read_b128 %0, %1 offset:%2\n\ts_mov_b64 exec, -1" : "+v"(dst) : "v"(addr), "i"(off), "s"(vmask))

__global__ __launch_bounds__(512) void k2_attn(char* __restrict__ ws) {
    __shared__ __attribute__((aligned(16))) char smem[163840];
    LDS3 char* lds = (LDS3 char*)smem;
    const int tid = threadIdx.x, lane = tid & 63, r = lane & 31, h = lane >> 5;
    const int c16 = lane & 15, g = lane >> 4;
    const int w = __builtin_amdgcn_readfirstlane(tid >> 6);
    const int bid = blockIdx.x;
    const int head = (bid & 7) * 4 + ((bid >> 3) & 3), qb = bid >> 5;
    const unsigned lds0 = (unsigned)(uintptr_t)smem;
    const int q0 = qb * 256 + w * 32;
    constexpr int SCR = 152 * 1024;

    STAMP_DECL; STAMP(0);
    f32x4 kp8[8]; u32x4 ph[4]; u32x4 qa; unsigned qt;
    {
        const int t2 = tid < 420 ? tid : 419, half = t2 >= 210, quad = t2 - half * 210;
        const char* kvp = ws + WS_KVP + ((size_t)(half * 8 * NH + head) * 840 + quad * 4) * 4;
#pragma unroll
        for (int i = 0; i < 8; ++i) { const char* a = kvp + (size_t)i * NH * 840 * 4; asm volatile("global_load_dwordx4 %0, %1, off" : "=v"(kp8[i]) : "v"(a) : "memory"); }
        const char* pp = ws + WS_PHIQ + (size_t)(head * SEQ + q0 + c16) * 80; const char* pg = pp + g * 16;
        asm volatile("global_load_dwordx4 %0, %4, off\n\tglobal_load_dwordx4 %1, %5, off offset:64\n\t"
                     "global_load_dwordx4 %2, %4, off offset:1280\n\tglobal_load_dwordx4 %3, %5, off offset:1344"
                     : "=&v"(ph[0]), "=&v"(ph[1]), "=&v"(ph[2]), "=&v"(ph[3]) : "v"(pg), "v"(pp) : "memory");
        const char* qp = ws + WS_Q + (size_t)(head * SEQ + q0 + r) * 40;
        const char* qm = qp + 16 * h; const char* qq = qp + 32 + 4 * h;
        asm volatile("global_load_dwordx4 %0, %2, off\n\tglobal_load_dword %1, %3, off" : "=&v"(qa), "=&v"(qt) : "v"(qm), "v"(qq) : "memory");
    }
    const char* dsrc = ws + WS_KV + (size_t)head * 163840 + w * 1024 + lane * 16;
    const unsigned ddst = lds0 + w * 1024;
#pragma unroll
    for (int t = 0; t < 19; ++t) glds16(dsrc + t * 8192, ddst + t * 8192);

    asm volatile("s_waitcnt vmcnt(19)" : "+v"(kp8[0]), "+v"(kp8[1]), "+v"(kp8[2]), "+v"(kp8[3]), "+v"(kp8[4]), "+v"(kp8[5]), "+v"(kp8[6]), "+v"(kp8[7]),
                 "+v"(ph[0]), "+v"(ph[1]), "+v"(ph[2]), "+v"(ph[3]), "+v"(qa), "+v"(qt) :: "memory");
    {
        const f32x4 s = ((kp8[0] + kp8[1]) + (kp8[2] + kp8[3])) + ((kp8[4] + kp8[5]) + (kp8[6] + kp8[7]));
        if (tid < 420) *(LDS3 f32x4*)(lds + SCR + tid * 16) = s;
    }
    WAIT_BAR(16);
    STAMP(1);
    char* ao = ws + WS_AO;
    {
        u32x4 a[2][2];
#pragma unroll
        for (int dt = 0; dt < 2; ++dt) {
            int d = dt * 16 + c16; if (d > 20) d = 20;
            LDS3 const char* ap = lds + SCR + d * 160;
            f32x4 x0 = *(LDS3 const f32x4*)(ap + g * 32) + *(LDS3 const f32x4*)(ap + 3360 + g * 32);
            f32x4 x1 = *(LDS3 const f32x4*)(ap + g * 32 + 16) + *(LDS3 const f32x4*)(ap + 3360 + g * 32 + 16);
            f32x4 y0 = *(LDS3 const f32x4*)(ap + 128) + *(LDS3 const f32x4*)(ap + 3360 + 128);
            f32x4 y1 = *(LDS3 const f32x4*)(ap + 144) + *(LDS3 const f32x4*)(ap + 3360 + 144);
            a[dt][0] = (u32x4){pk2(x0[0], x0[1]), pk2(x0[2], x0[3]), pk2(x1[0], x1[1]), pk2(x1[2], x1[3])};
            u32x4 t = {pk2(y0[0], y0[1]), pk2(y0[2], y0[3]), pk2(y1[0], y1[1]), pk2(y1[2], y1[3])};
            a[dt][1] = g == 0 ? t : (u32x4){0u, 0u, 0u, 0u};
        }
#pragma unroll
        for (int tt = 0; tt < 2; ++tt) {
            const int tok = q0 + tt * 16 + c16;
            f32x4 z = {0.f, 0.f, 0.f, 0.f};
            f32x4 d0 = MFMA16_32(a[0][0], ph[2 * tt], z); d0 = MFMA16_32(a[0][1], ph[2 * tt + 1], d0);
            f32x4 d1 = MFMA16_32(a[1][0], ph[2 * tt], z); d1 = MFMA16_32(a[1][1], ph[2 * tt + 1], d1);
            const float den = __shfl(d1[0], 16 + c16) + 1e-6f;
            const float rd = 1.f / den;
            {
                const int k = 640 + head * 20 + 4 * g;
                u32x2 o = {pk2(d0[0] * rd, d0[1] * rd), pk2(d0[2] * rd, d0[3] * rd)};
                *(u32x2*)(ao + ((size_t)((tok >> 4) * 40 + (k >> 5)) * 64 + ((k >> 3) & 3) * 16 + (tok & 15)) * 16 + (k & 7) * 2) = o;
            }
            if (g == 0) {
                const int k = 640 + head * 20 + 16;
                u32x2 o = {pk2(d1[0] * rd, d1[1] * rd), pk2(d1[2] * rd, d1[3] * rd)};
                *(u32x2*)(ao + ((size_t)((tok >> 4) * 40 + (k >> 5)) * 64 + ((k >> 3) & 3) * 16 + (tok & 15)) * 16 + (k & 7) * 2) = o;
            }
        }
    }

    const unsigned laneK = lds0 + r * 32 + h * 16, laneT = lds0 + 4096 + r * 8 + h * 4, laneV = lds0 + 5120 + h * 320 + (r < 20 ? r : 19) * 16;
    const unsigned long long vmask = 0x000fffff000fffffull;
    const unsigned one = (r == 20) ? 0x3f803f80u : 0u;
    u32x4 v0 = {one, one, one, one}, v1 = v0;
    const f32x16 z16 = {};
    float m; u32x2 qT; f32x16 sA, sB;
    {
        u32x4 k0 = *(LDS3 const u32x4*)(lds + r * 32 + h * 16);
        u32x2 t0 = {*(LDS3 const unsigned*)(lds + 4096 + r * 8 + h * 4), 0x3f80u};
        u32x2 a0 = {qt, 0u};
        f32x16 s = MFMA32_16(k0, qa, z16);
        f32x16 s0 = MFMA32_8(t0, a0, s);
        float x0 = s0[0];
#pragma unroll
        for (int i = 1; i < 16; ++i) x0 = fmaxf(x0, s0[i]);
        x0 = -xhalf_max(x0);
        const float hi0 = bf_lo(pk2(x0, 0.f));
        const unsigned hl = pk2(hi0, x0 - hi0);
        m = -(hi0 + bf_hi(hl));
        qT = (u32x2){qt, h ? (hl >> 16) : (hl & 0xffffu)};
        sA = MFMA32_8(t0, qT, s);
    }
    f32x16 o = {};
    u32x4 ka, pa, pb = {0u, 0u, 0u, 0u}; unsigned kt;
    STAMP(2);
    asm volatile("s_waitcnt lgkmcnt(0)" ::: "memory");
    SBAR();
    DSR128(ka, laneK, 1024); DSR128M(v1, laneV, 640); DSR32(kt, laneT, 256); DSR128M(v0, laneV, 0);
#define EX4(S, i, P, j) do { P[j] = pk2(ex2(S[i]), ex2(S[i + 1])); P[j + 1] = pk2(ex2(S[i + 2]), ex2(S[i + 3])); PIN(P); SBAR(); } while (0)
#define UNIT(SC, SN, aK2, oK2, aV1, oV1, aT2, oT2, aV0, oV0) do { \
        LWAIT3(ka); SN = MFMA32_16(ka, qa, z16); SBAR(); DSR128(ka, aK2, oK2); EX4(SC, 0, pa, 0); \
        LWAIT3(v1); o = MFMA32_16(v1, pb, o); SBAR(); DSR128M(v1, aV1, oV1); EX4(SC, 4, pa, 2); \
        LWAIT3(kt); { u32x2 kk_ = {kt, 0x3f80u}; SN = MFMA32_8(kk_, qT, SN); } SBAR(); DSR32(kt, aT2, oT2); EX4(SC, 8, pb, 0); \
        LWAIT3(v0); o = MFMA32_16(v0, pa, o); SBAR(); DSR128M(v0, aV0, oV0); EX4(SC, 12, pb, 2); \
    } while (0)
    unsigned aK = laneK, aT = laneT, aV = laneV;
#pragma unroll 1
    for (int c = 0; c < 16; ++c) {
        if (c == 1) { WAIT_BAR(14); glds16(dsrc + 19 * 8192, ddst + 19 * 8192); }
        else if (c == 3) { WAIT_BAR(0); }
        const unsigned nx = ((c + 1) & 15) * 10240;
        const unsigned nK = laneK + nx, nT = laneT + nx, nV = laneV + nx;
        SBAR();
        UNIT(sA, sB, aK, 2048, aV, 640,  aT, 512, aV, 1280);
        UNIT(sB, sA, aK, 3072, aV, 1920, aT, 768, aV, 2560);
        UNIT(sA, sB, nK, 0,    aV, 3200, nT, 0,   aV, 3840);
        UNIT(sB, sA, nK, 1024, aV, 4480, nT, 256, nV, 0);
        aK = nK; aT = nT; aV = nV;
    }
    asm volatile("s_waitcnt lgkmcnt(0)" : "+v"(v1), "+v"(v0), "+v"(ka), "+v"(kt) :: "memory");
    o = MFMA32_16(v1, pb, o);
#undef UNIT
#undef EX4
    STAMP(3);
    {
        float chk = 0.f;
#pragma unroll
        for (int i = 0; i < 12; ++i) chk += fabsf(o[i]) * 0.f;
        bool bad = !(chk == 0.f);
        if (h) bad = bad || !(o[8] >= 0x1p-60f && o[8] <= 0x1p60f);
        if (__builtin_expect(__any(bad), 0)) {
            m = -INFINITY;
#pragma unroll
            for (int i = 0; i < 16; ++i) o[i] = 0.f;
            const u32x2 a0 = {qt, 0u};
#pragma unroll 1
            for (int t = 0; t < 64; ++t) {
                const int off = (t >> 2) * 10240, tt = t & 3;
                u32x4 k0 = *(LDS3 const u32x4*)(lds + r * 32 + h * 16 + off + tt * 1024);
                u32x2 t0 = {*(LDS3 const unsigned*)(lds + 4096 + r * 8 + h * 4 + off + tt * 256), 0x3f80u};
                if (r < 20) {
                    v0 = *(LDS3 const u32x4*)(lds + 5120 + h * 320 + r * 16 + off + tt * 1280);
                    v1 = *(LDS3 const u32x4*)(lds + 5120 + h * 320 + r * 16 + off + tt * 1280 + 640);
                }
                f32x16 s0 = MFMA32_16(k0, qa, z16); s0 = MFMA32_8(t0, a0, s0);
                float x0 = s0[0];
#pragma unroll
                for (int i = 1; i < 16; ++i) x0 = fmaxf(x0, s0[i]);
                x0 = fmaxf(xhalf_max(x0), m);
                const float f0 = (m == x0) ? 1.f : ex2(m - x0);
                m = x0;
#pragma unroll
                for (int i = 0; i < 16; ++i) o[i] *= f0;
#pragma unroll
                for (int i = 0; i < 4; ++i) pa[i] = pk2(ex2(s0[2 * i] - x0), ex2(s0[2 * i + 1] - x0));
#pragma unroll
                for (int i = 0; i < 4; ++i) pb[i] = pk2(ex2(s0[8 + 2 * i] - x0), ex2(s0[8 + 2 * i + 1] - x0));
                o = MFMA32_16(v0, pa, o); o = MFMA32_16(v1, pb, o);
            }
        }
    }
    {
        auto sw = __builtin_amdgcn_permlane32_swap(__float_as_uint(o[8]), __float_as_uint(o[8]), false, false);
        const float rl = 1.f / __uint_as_float(sw[1]);
        const int tok = q0 + r;
#pragma unroll
        for (int gq = 0; gq < 3; ++gq) {
            const int d = 8 * gq + 4 * h;
            if (d < 20) {
                const int k = head * 20 + d;
                u32x2 ov = {pk2(o[4 * gq] * rl, o[4 * gq + 1] * rl), pk2(o[4 * gq + 2] * rl, o[4 * gq + 3] * rl)};
                *(u32x2*)(ao + ((size_t)((tok >> 4) * 40 + (k >> 5)) * 64 + ((k >> 3) & 3) * 16 + (tok & 15)) * 16 + (k & 7) * 2) = ov;
            }
        }
    }
    STAMP(4); STAMP_FLUSH(1);
}

__global__ __launch_bounds__(512) void k3_out(const char* __restrict__ ws, u16* __restrict__ out) {
    __shared__ __attribute__((aligned(16))) char smem[8 * 20480];
    LDS3 char* lds = (LDS3 char*)smem;
    const int tid = threadIdx.x, lane = tid & 63, c16 = lane & 15, g = lane >> 4;
    const int w = __builtin_amdgcn_readfirstlane(tid >> 6);
    const int bid = blockIdx.x, xcd = bid & 7, idx = bid >> 3;
    const int mt = xcd * 4 + (idx & 3), nt = idx >> 2;
    STAMP_DECL; STAMP(0);
    const char* ap = ws + WS_AO + ((size_t)(mt * 4) * 40 + w * 5) * 1024 + lane * 16;
    const char* bp = ws + WS_WT + ((size_t)(nt * 5) * 40 + w * 5) * 1024 + lane * 16;
    f32x4 acc[4][5];
#pragma unroll
    for (int a = 0; a < 4; ++a)
#pragma unroll
        for (int b = 0; b < 5; ++b) acc[a][b] = (f32x4){0.f, 0.f, 0.f, 0.f};
#pragma unroll
    for (int ks = 0; ks < 5; ++ks) {
        u32x4 af[4], bf[5];
#pragma unroll
        for (int t = 0; t < 4; ++t) af[t] = *(const u32x4*)(ap + (size_t)t * 40960 + ks * 1024);
#pragma unroll
        for (int n = 0; n < 5; ++n) bf[n] = *(const u32x4*)(bp + (size_t)n * 40960 + ks * 1024);
#pragma unroll
        for (int t = 0; t < 4; ++t)
#pragma unroll
            for (int n = 0; n < 5; ++n) acc[t][n] = MFMA16_32(bf[n], af[t], acc[t][n]);
    }
    STAMP(1);
    const float* gp = (const float*)(ws + WS_G) + mt * 64 + c16;
#pragma unroll
    for (int t = 0; t < 4; ++t) {
        float gt = gp[t * 16]; if (w >= 4) gt = 1.f - gt;
#pragma unroll
        for (int n = 0; n < 5; ++n) *(LDS3 f32x4*)(lds + w * 20480 + (t * 5 + n) * 1024 + lane * 16) = acc[t][n] * gt;
    }
    __syncthreads();
    STAMP(2);
    for (int qd = tid; qd < 1280; qd += 512) {
        f32x4 s = {0.f, 0.f, 0.f, 0.f};
#pragma unroll
        for (int k = 0; k < 8; ++k) s += *(LDS3 const f32x4*)(lds + k * 20480 + qd * 16);
        const int T = qd >> 6, t = T / 5, n = T - t * 5;
        const int tok = mt * 64 + t * 16 + c16, col = nt * 80 + n * 16 + g * 4;
        *(u32x2*)(out + (size_t)tok * DM + col) = (u32x2){pk2(s[0], s[1]), pk2(s[2], s[3])};
    }
    STAMP(3); STAMP_FLUSH(2);
}

extern "C" void kernel_launch(void* const* d_in, const int* in_sizes, int n_in, void* d_out, int out_size, void* d_ws, size_t ws_size,
                              hipStream_t stream) {
    const u16* x = (const u16*)d_in[0];
    const u16* wa = (const u16*)d_in[1];
    const u16* woa = (const u16*)d_in[2];
    const u16* wb = (const u16*)d_in[4];
    const u16* wphi = (const u16*)d_in[5];
    const u16* wob = (const u16*)d_in[6];
    const u16* gw = (const u16*)d_in[7];
    char* ws = (char*)d_ws;
    k1_qkv<<<256, 768, 0, stream>>>(x, wa, wb, wphi, woa, wob, gw, ws);
    k2_attn<<<256, 512, 0, stream>>>(ws);
    k3_out<<<256, 512, 0, stream>>>(ws, (u16*)d_out);
}
```

```cpp
#include <hip/hip_runtime.h>
#include <hip/hip_bf16.h>
#include <stdint.h>
#include <type_traits>

#define N_LAUNCH 1

typedef __attribute__((ext_vector_type(8))) short bf16x8;
typedef __attribute__((ext_vector_type(4))) short bf16x4;
typedef __attribute__((ext_vector_type(16))) float f32x16;
typedef __attribute__((ext_vector_type(4))) float f32x4;
typedef __attribute__((ext_vector_type(2))) float f32x2;
typedef __attribute__((ext_vector_type(4))) unsigned u32x4;
typedef __attribute__((ext_vector_type(2))) unsigned u32x2;
typedef __attribute__((ext_vector_type(2))) __bf16 bf2_t;
typedef unsigned short u16;
typedef unsigned long long u64;
#define LDS3 __attribute__((address_space(3)))
typedef __attribute__((address_space(1))) u64 gu64;
#define RLX_AGENT __ATOMIC_RELAXED, __HIP_MEMORY_SCOPE_AGENT

constexpr int SEQ = 2048, DM = 640, NH = 32;
constexpr float LOG2E = 1.4426950408889634f;
constexpr float QSCALE = 0.22360679774997896f * LOG2E;

constexpr unsigned WS_EPOCH = 0;
constexpr unsigned WS_F_G   = 256;
constexpr unsigned WS_F_SD  = WS_F_G + 4096;
constexpr unsigned WS_F_KV  = WS_F_SD + 256;
constexpr unsigned WS_F_OB  = WS_F_KV + 2048;
constexpr unsigned WS_F_OA  = WS_F_OB + 2048;
constexpr unsigned WS_G     = 16384;
constexpr unsigned WS_Q     = 32768;
constexpr unsigned WS_KV    = WS_Q + 2 * NH * 81920;
constexpr unsigned WS_KVP   = WS_KV + 2 * NH * 163840;
constexpr unsigned WS_AO    = WS_KVP + NH * 8 * 1152 * 4;
constexpr unsigned WS_WT    = WS_AO + SEQ * 1280 * 2;
constexpr unsigned WS_END   = WS_WT + 1280 * 640 * 2;

#define STAMP(i)

__device__ __forceinline__ unsigned pk2(float a, float b) { f32x2 v = {a, b}; bf2_t r = __builtin_convertvector(v, bf2_t); return __builtin_bit_cast(unsigned, r); }
__device__ __forceinline__ float bf_lo(unsigned u) { return __uint_as_float(u << 16); }
__device__ __forceinline__ float bf_hi(unsigned u) { return __uint_as_float(u & 0xffff0000u); }
__device__ __forceinline__ float ex2(float x) { return __builtin_amdgcn_exp2f(x); }
#define MFMA32_16(a, b, c) __builtin_amdgcn_mfma_f32_32x32x16_bf16(__builtin_bit_cast(bf16x8, a), __builtin_bit_cast(bf16x8, b), c, 0, 0, 0)
#define MFMA32_8(a, b, c) __builtin_amdgcn_mfma_f32_32x32x8bf16_1k(__builtin_bit_cast(bf16x4, a), __builtin_bit_cast(bf16x4, b), c, 0, 0, 0)
#define MFMA16_32(a, b, c) __builtin_amdgcn_mfma_f32_16x16x32_bf16(__builtin_bit_cast(bf16x8, a), __builtin_bit_cast(bf16x8, b), c, 0, 0, 0)

__device__ __forceinline__ void glds16(unsigned voff, const void* sbase, unsigned lds_base) {
    unsigned sv;
    asm volatile("s_mov_b32 %0, m0\n\ts_mov_b32 m0, %3\n\ts_nop 0\n\tglobal_load_lds_dwordx4 %1, %2\n\ts_mov_b32 m0, %0"
                 : "=&s"(sv) : "v"(voff), "s"(sbase), "s"(lds_base) : "memory");
}
typedef short v4i16_t __attribute__((ext_vector_type(4)));
__device__ __forceinline__ u32x2 trread(LDS3 const char* p) {
    return __builtin_bit_cast(u32x2, __builtin_amdgcn_ds_read_tr16_b64_v4i16((LDS3 v4i16_t*)p));
}
#define WAIT_BAR(N) asm volatile("s_waitcnt vmcnt(" #N ") lgkmcnt(0)\n\ts_barrier" ::: "memory")
#define DRAIN() asm volatile("s_waitcnt vmcnt(0)" ::: "memory")
#define SBAR() __builtin_amdgcn_sched_barrier(0)
#define PIN(x) asm volatile("" : "+v"(x))

__device__ __forceinline__ float xhalf_max(float x) {
    auto r = __builtin_amdgcn_permlane32_swap(__float_as_uint(x), __float_as_uint(x), false, false);
    return fmaxf(__uint_as_float(r[0]), __uint_as_float(r[1]));
}
__device__ __forceinline__ float xhalf_sum(float x) {
    auto r = __builtin_amdgcn_permlane32_swap(__float_as_uint(x), __float_as_uint(x), false, false);
    return __uint_as_float(r[0]) + __uint_as_float(r[1]);
}
__device__ __forceinline__ float upper_half(float x) {
    auto r = __builtin_amdgcn_permlane32_swap(__float_as_uint(x), __float_as_uint(x), false, false);
    return __uint_as_float(r[1]);
}

typedef __amdgpu_buffer_rsrc_t rsrc_t;
#define ST8(v, off)  __builtin_amdgcn_raw_buffer_store_b64(v, rs, (int)(off), 0, 16)
#define ST16(v, off) __builtin_amdgcn_raw_buffer_store_b128(v, rs, (int)(off), 0, 16)
#define ST4(v, off)  __builtin_amdgcn_raw_buffer_store_b32(v, rs, (int)(off), 0, 16)
#define LD16(off) __builtin_amdgcn_raw_buffer_load_b128(rs, (int)(off), 0, 16)
#define LD4(off)  __builtin_amdgcn_raw_buffer_load_b32(rs, (int)(off), 0, 16)

__device__ __forceinline__ void wait_flags(const char* ws, unsigned off0, unsigned off1, u64 tag) {
    gu64* p0 = (gu64*)(ws + off0); gu64* p1 = (gu64*)(ws + off1);
#pragma unroll 1
    for (unsigned spins = 0; spins < (1u << 16); ++spins) {
        const u64 a = __hip_atomic_load(p0, RLX_AGENT), b = __hip_atomic_load(p1, RLX_AGENT);
        if (__all(a == tag && b == tag)) break;
        __builtin_amdgcn_s_sleep(2);
    }
    __builtin_amdgcn_fence(__ATOMIC_ACQUIRE, "wavefront");
}
__device__ __forceinline__ void set_flag(char* ws, unsigned off, u64 tag) { __hip_atomic_store((gu64*)(ws + off), tag, RLX_AGENT); }

struct Params { const u16* x; const u16* wa; const u16* wb; const u16* wphi; const u16* woa; const u16* wob; const u16* gw; char* ws; u16* out; };

__device__ __forceinline__ void feature_map(const u32x4& pm, unsigned pt, const u32x4& tm, unsigned tt, int h, u32x4& fp, u32x4& fn, u32x4& ft) {
    const f32x16 z16 = {};
    const u32x2 a8 = {pt, 0u}, b8 = {tt, 0u};
    f32x16 s = MFMA32_16(pm, tm, z16); s = MFMA32_8(a8, b8, s);
    float v[12];
#pragma unroll
    for (int i = 0; i < 12; i += 2) { const unsigned u = pk2(s[i], s[i + 1]); v[i] = bf_lo(u) * LOG2E; v[i + 1] = bf_hi(u) * LOG2E; }
    float mx = v[0], mn = v[0];
#pragma unroll
    for (int i = 1; i < 8; ++i) { mx = fmaxf(mx, v[i]); mn = fminf(mn, v[i]); }
    const float tx = fmaxf(fmaxf(v[8], v[9]), fmaxf(v[10], v[11])), tn = fminf(fminf(v[8], v[9]), fminf(v[10], v[11]));
    if (!h) { mx = fmaxf(mx, tx); mn = fminf(mn, tn); }
    mx = xhalf_max(mx); mn = -xhalf_max(-mn);
    float ep[12], en[12], sp = 0.f, sn = 0.f;
#pragma unroll
    for (int i = 0; i < 12; ++i) {
        ep[i] = ex2(v[i] - mx); en[i] = ex2(mn - v[i]);
        if (i >= 8 && h) { ep[i] = 0.f; en[i] = 0.f; }
        sp += ep[i]; sn += en[i];
    }
    sp = __builtin_amdgcn_rcpf(xhalf_sum(sp)); sn = __builtin_amdgcn_rcpf(xhalf_sum(sn));
#pragma unroll
    for (int i = 0; i < 4; ++i) { fp[i] = pk2(ep[2 * i] * sp, ep[2 * i + 1] * sp); fn[i] = pk2(en[2 * i] * sn, en[2 * i + 1] * sn); }
    ft = (u32x4){pk2(ep[8] * sp, ep[9] * sp), pk2(ep[10] * sp, ep[11] * sp), pk2(en[8] * sn, en[9] * sn), pk2(en[10] * sn, en[11] * sn)};
}

__device__ __forceinline__ void store_o(rsrc_t rs, const float (&o)[12], float sc, int tok, int kbase, int h) {
#pragma unroll
    for (int gq = 0; gq < 3; ++gq) {
        const int d = 8 * gq + 4 * h;
        if (d < 20) {
            const int k = kbase + d;
            u32x2 ov = {pk2(o[4 * gq] * sc, o[4 * gq + 1] * sc), pk2(o[4 * gq + 2] * sc, o[4 * gq + 3] * sc)};
            ST8(ov, WS_AO + (((tok >> 4) * 40 + (k >> 5)) * 64 + ((k >> 3) & 3) * 16 + (tok & 15)) * 16 + (k & 7) * 2);
        }
    }
}

#define DSR128(dst, addr, off) asm volatile("ds_read_b128 %0, %1 offset:%2" : "=v"(dst) : "v"(addr), "i"(off))
#define DSR32(dst, addr, off) asm volatile("ds_read_b32 %0, %1 offset:%2" : "=v"(dst) : "v"(addr), "i"(off))
#define DSR128M2(d0, a0, o0, d1, a1, o1) asm volatile("s_mov_b64 exec, %6\n\tds_read_b128 %0, %2 offset:%3\n\tds_read_b128 %1, %4 offset:%5\n\ts_mov_b64 exec, -1" \
                                                      : "+v"(d0), "+v"(d1) : "v"(a0), "i"(o0), "v"(a1), "i"(o1), "s"(vmask))
#define LWAIT(n) do { asm volatile("s_waitcnt lgkmcnt(" #n ")" ::: "memory"); SBAR(); } while (0)

template <int PH>
__global__ __launch_bounds__(512) void mega(Params P) {
    __shared__ __attribute__((aligned(16))) char smem[163840];
    LDS3 char* lds = (LDS3 char*)smem;
    char* ws = P.ws;
    const int tid = threadIdx.x, lane = tid & 63, r = lane & 31, h = lane >> 5;
    const int w = __builtin_amdgcn_readfirstlane(tid >> 6);
    const int bid = blockIdx.x;
    const unsigned lds0 = (unsigned)(uintptr_t)smem;
    const rsrc_t rs = __builtin_amdgcn_make_buffer_rsrc(ws, 0, WS_END, 0x00020000);
    STAMP(0);
    u64 tag = 0;
#define GET_TAG() do { asm volatile("s_load_dwordx2 %0, %1, 0x0\n\ts_waitcnt lgkmcnt(0)" : "=s"(tag) : "s"(ws) : "memory"); tag += 1; } while (0)
    const int head = (bid & 7) * 4 + ((bid >> 3) & 3), qb = bid >> 5;
    const int nlo = (head * 20) >> 7, nhi = (head * 20 + 19) >> 7;

    if constexpr (PH & 1) {
        if (bid < 240) {
            const int idx = bid >> 3, hi15 = idx >= 15;
            const int mt = (bid & 7) * 2 + hi15, nt = idx - 15 * hi15;
            const int m0 = mt * 128, part = nt / 5, c0 = (nt - part * 5) * 128;
            const int rot = (nt * 3 + mt * 5 + (mt >> 1)) % 10;
            const int wm = w & 1, wn = w >> 1;
            unsigned xo[2], wo[2];
#pragma unroll
            for (int i = 0; i < 2; ++i) {
                const int s = (w + 8 * i) * 64 + lane;
                { const int row = s >> 3, ch = (s & 7) ^ ((row >> 1) & 7); xo[i] = (m0 + row) * 1280 + ch * 16; }
                { const int row = s >> 4, ch = (s & 15) ^ (((row & 3) << 2) | ((row >> 2) & 3)); wo[i] = row * 3840 + nt * 256 + ch * 16; }
            }
            const unsigned dX = lds0 + w * 1024, dW = lds0 + 16384 + w * 1024;
            auto issue = [&](int s) {
                int kt = (s >= 10 ? s - 10 : s) + rot; if (kt >= 10) kt -= 10;
                const char* xb = (const char*)P.x + kt * 128;
                const char* wb = (const char*)(s >= 10 ? P.wb : P.wa) + (size_t)kt * (64 * 3840);
                const unsigned sl = (s & 3) * 32768;
                glds16(xo[0], xb, dX + sl); glds16(wo[0], wb, dW + sl); glds16(xo[1], xb, dX + sl + 8192); glds16(wo[1], wb, dW + sl + 8192);
            };
            const int xr = wm * 64 + r;
            const unsigned xa = xr * 128 + ((h ^ ((xr >> 1) & 7)) << 4);
            unsigned wt[2];
#pragma unroll
            for (int t = 0; t < 2; ++t) {
                const int q = (lane & 15) >> 2, p = lane & 3, blk = (lane >> 4) & 1, row = 8 * h + 4 * t + q;
                wt[t] = 16384 + row * 256 + (((4 * wn + 2 * blk + (p >> 1)) ^ ((q << 2) | (2 * h + t))) << 4) + 8 * (p & 1);
            }
            issue(0); issue(1); issue(2);
            GET_TAG();
            auto gbody = [&](auto trc) {
            constexpr bool TR = decltype(trc)::value;
#pragma unroll 1
            for (int mat = 0; mat < 2; ++mat) {
                f32x16 acc0 = {}, acc1 = {};
#pragma unroll 1
                for (int kk = 0; kk < 10; ++kk) {
                    const int s = mat * 10 + kk;
                    if (s >= 10 && s <= 12) { WAIT_BAR(12); } else if (s <= 17) { WAIT_BAR(8); } else if (s == 18) { WAIT_BAR(4); } else { WAIT_BAR(0); }
                    if (s + 3 < 20) issue(s + 3);
                    if (s == 13 && tid == 0) set_flag(ws, WS_F_G + (mt * 15 + nt) * 8, tag);
                    LDS3 const char* sb = lds + (s & 3) * 32768;
#pragma unroll
                    for (int ks = 0; ks < 4; ++ks) {
                        const u32x4 x0 = *(LDS3 const u32x4*)(sb + (xa ^ (ks * 32)));
                        const u32x4 x1 = *(LDS3 const u32x4*)(sb + ((xa + 4096) ^ (ks * 32)));
                        const u32x2 lo = trread(sb + wt[0] + ks * 4096), hi = trread(sb + wt[1] + ks * 4096);
                        const u32x4 wf = {lo[0], lo[1], hi[0], hi[1]};
                        if constexpr (TR) { acc0 = MFMA32_16(wf, x0, acc0); acc1 = MFMA32_16(wf, x1, acc1); }
                        else              { acc0 = MFMA32_16(x0, wf, acc0); acc1 = MFMA32_16(x1, wf, acc1); }
                    }
                }
                if constexpr (TR) {
                    const float sc = (part == 0 && mat == 0) ? QSCALE : 1.f;
                    const unsigned base = part == 0 ? WS_Q + mat * NH * 81920 : WS_KV + mat * NH * 163840;
                    const unsigned hs = part == 0 ? 81920 : 163840, cs = part == 0 ? 5120 : 10240;
#pragma unroll
                    for (int gq = 0; gq < 4; ++gq) {
                        const int n = c0 + 32 * wn + 8 * gq + 4 * h, hd = n / 20, d = n - hd * 20;
#pragma unroll
                        for (int i = 0; i < 2; ++i) {
                            const f32x16& a = i ? acc1 : acc0;
                            const int tl = wm * 64 + i * 32 + r;
                            const unsigned off = base + hd * hs + mt * cs + (d < 16 ? (tl >> 5) * 1024 + (d >> 3) * 512 + r * 16 + (d & 4) * 2 : 4096 + tl * 8);
                            u32x2 ov = {pk2(a[4 * gq] * sc, a[4 * gq + 1] * sc), pk2(a[4 * gq + 2] * sc, a[4 * gq + 3] * sc)};
                            ST8(ov, off);
                        }
                    }
                } else {
                    const int n = c0 + 32 * wn + r, hd = n / 20, d = n - hd * 20;
                    const unsigned base = WS_KV + (mat * NH + hd) * 163840 + mt * 10240 + 5120 + h * 320 + d * 16;
#pragma unroll
                    for (int i = 0; i < 2; ++i)
#pragma unroll
                        for (int s2 = 0; s2 < 2; ++s2) {
                            const f32x16& a = i ? acc1 : acc0;
                            u32x4 ov = {pk2(a[8 * s2], a[8 * s2 + 1]), pk2(a[8 * s2 + 2], a[8 * s2 + 3]), pk2(a[8 * s2 + 4], a[8 * s2 + 5]), pk2(a[8 * s2 + 6], a[8 * s2 + 7])};
                            ST16(ov, base + (wm * 4 + i * 2 + s2) * 640);
                        }
                }
                if (mat == 0) STAMP(1);
            }
            };
            if (part < 2) gbody(std::true_type{}); else gbody(std::false_type{});
            STAMP(2);
            WAIT_BAR(0);
            if (tid == 0) set_flag(ws, WS_F_G + 2048 + (mt * 15 + nt) * 8, tag);
        } else {
            const int sj = bid - 240;
            GET_TAG();
            const u32x4 ga = *(const u32x4*)(P.gw + lane * 8), gb = *(const u32x4*)(P.gw + 512 + (lane & 15) * 8);
#pragma unroll 4
            for (int rr = 0; rr < 16; ++rr) {
                const int row = sj * 128 + w * 16 + rr;
                const u32x4 xa = *(const u32x4*)(P.x + (size_t)row * DM + lane * 8), xb = *(const u32x4*)(P.x + (size_t)row * DM + 512 + (lane & 15) * 8);
                float s = 0.f, s2 = 0.f;
#pragma unroll
                for (int j = 0; j < 4; ++j) { s += bf_lo(xa[j]) * bf_lo(ga[j]) + bf_hi(xa[j]) * bf_hi(ga[j]); s2 += bf_lo(xb[j]) * bf_lo(gb[j]) + bf_hi(xb[j]) * bf_hi(gb[j]); }
                if (lane < 16) s += s2;
#pragma unroll
                for (int o = 32; o >= 1; o >>= 1) s += __shfl_xor(s, o);
                if (lane == 0) ST4(__float_as_uint(1.f / (1.f + __expf(-s))), WS_G + row * 4);
            }
            if (tid < 320) {
#pragma unroll 2
                for (int u = 0; u < 10; ++u) {
                    const int k0 = (sj * 10 + u) * 8;
                    const u16* wsrc = (k0 < 640 ? P.woa + (size_t)k0 * DM : P.wob + (size_t)(k0 - 640) * DM) + tid * 2;
                    unsigned e[8];
#pragma unroll
                    for (int j = 0; j < 8; ++j) e[j] = *(const unsigned*)(wsrc + (size_t)j * DM);
                    u32x4 lo = {(e[0] & 0xffffu) | (e[1] << 16), (e[2] & 0xffffu) | (e[3] << 16), (e[4] & 0xffffu) | (e[5] << 16), (e[6] & 0xffffu) | (e[7] << 16)};
                    u32x4 hi = {(e[0] >> 16) | (e[1] & 0xffff0000u), (e[2] >> 16) | (e[3] & 0xffff0000u), (e[4] >> 16) | (e[5] & 0xffff0000u), (e[6] >> 16) | (e[7] & 0xffff0000u)};
                    const int n = tid * 2;
                    const unsigned dp = WS_WT + (((n >> 4) * 40 + (k0 >> 5)) * 64 + ((k0 >> 3) & 3) * 16 + (n & 15)) * 16;
                    ST16(lo, dp); ST16(hi, dp + 16);
                }
            }
            WAIT_BAR(0);
            if (tid == 0) set_flag(ws, WS_F_SD + sj * 8, tag);
        }
    }
    STAMP(3);

    if constexpr (PH & 2) {
        const int qs = w & 3, kh = w >> 2;
        const int q0 = qb * 256 + qs * 64;
        if constexpr (!(PH & 1)) GET_TAG();
        u32x4 phm; unsigned pht;
        {
            const u16* wp = P.wphi + head * 400 + (r < 20 ? r : 0);
            unsigned e[10];
#pragma unroll
            for (int j = 0; j < 8; ++j) e[j] = wp[(8 * h + j) * 20];
            e[8] = wp[(16 + 2 * h) * 20]; e[9] = wp[(17 + 2 * h) * 20];
            phm = (u32x4){e[0] | (e[1] << 16), e[2] | (e[3] << 16), e[4] | (e[5] << 16), e[6] | (e[7] << 16)}; pht = e[8] | (e[9] << 16);
            if (r >= 20) { phm = (u32x4){0u, 0u, 0u, 0u}; pht = 0u; }
            PIN(phm); PIN(pht);
        }
        if (w == 0) {
            const unsigned f0 = WS_F_G + ((lane & 15) * 15 + (lane < 32 ? 5 : 10) + ((lane & 16) ? nhi : nlo)) * 8;
            const unsigned f1 = lane < 16 ? WS_F_SD + lane * 8 : WS_F_G + ((qb * 2 + (lane & 1)) * 15 + ((lane & 2) ? nhi : nlo)) * 8;
            wait_flags(ws, f0, f1, tag);
            __builtin_amdgcn_fence(__ATOMIC_ACQUIRE, "agent");
        }
        WAIT_BAR(0);
        STAMP(4);
        u32x4 qA, qB; unsigned qtA, qtB;
        {
            const char* qbase = ws + WS_Q + head * 81920 + (q0 >> 7) * 5120;
            const unsigned om = ((q0 >> 5) & 3) * 1024 + h * 512 + r * 16, ot = 4096 + ((q0 & 127) + r) * 8 + 4 * h;
            asm volatile("global_load_dwordx4 %0, %4, %6\n\tglobal_load_dwordx4 %1, %4, %6 offset:1024\n\t"
                         "global_load_dword %2, %5, %6\n\tglobal_load_dword %3, %5, %6 offset:256"
                         : "=&v"(qA), "=&v"(qB), "=&v"(qtA), "=&v"(qtB) : "v"(om), "v"(ot), "s"(qbase) : "memory");
        }
        {
            const char* kvb = ws + WS_KV + head * 163840;
#pragma unroll
            for (int t = 0; t < 20; ++t) {
                const int n = w + 8 * t, pr = n / 20, rem = n - pr * 20;
                const unsigned pc = (pr * 10 + (rem >= 10 ? rem + 70 : rem)) * 1024;
                glds16(lane * 16 + pc, kvb, lds0 + pc);
            }
        }
        const unsigned hb = lds0 + kh * 81920;
        const unsigned laneK = hb + h * 512 + r * 16, laneT = hb + 4096 + r * 8 + h * 4, laneV = hb + 5120 + h * 320 + (r < 20 ? r : 19) * 16;
        const u64 vmask = 0x000fffff000fffffull;
        const unsigned one = (r == 20) ? 0x3f803f80u : 0u;
        u32x4 v0a = {one, one, one, one}, v0b = v0a, v1 = v0a;
        const f32x16 z16 = {};
        asm volatile("s_waitcnt vmcnt(15) lgkmcnt(0)\n\ts_barrier" : "+v"(qA), "+v"(qB), "+v"(qtA), "+v"(qtB) :: "memory");
        STAMP(5);
        float mA, mB; u32x2 qTA, qTB; f32x16 sA, sB; u32x4 ka; unsigned kt;
        {
            DSR128(ka, laneK, 0); DSR32(kt, laneT, 0); DSR128M2(v0a, laneV, 0, v1, laneV, 640);
            asm volatile("s_waitcnt lgkmcnt(0)" : "+v"(ka), "+v"(kt), "+v"(v0a), "+v"(v1) :: "memory");
            const u32x2 t0 = {kt, 0x3f80u}, a0 = {qtA, 0u}, b0 = {qtB, 0u};
            const f32x16 s = MFMA32_16(ka, qA, z16);
            const f32x16 s0 = MFMA32_8(t0, a0, s);
            f32x16 s1 = MFMA32_16(ka, qB, z16); s1 = MFMA32_8(t0, b0, s1);
            float x0 = s0[0], x1 = s1[0];
#pragma unroll
            for (int i = 1; i < 16; ++i) { x0 = fmaxf(x0, s0[i]); x1 = fmaxf(x1, s1[i]); }
            x0 = -xhalf_max(x0); x1 = -xhalf_max(x1);
            const unsigned hh = pk2(x0, x1);
            const unsigned ll = pk2(x0 - bf_lo(hh), x1 - bf_hi(hh));
            mA = -(bf_lo(hh) + bf_lo(ll)); mB = -(bf_hi(hh) + bf_hi(ll));
            const unsigned sel = h ? ll : hh;
            qTA = (u32x2){qtA, sel & 0xffffu}; qTB = (u32x2){qtB, sel >> 16};
            sA = MFMA32_8(t0, qTA, s);
        }
        f32x16 oA = {}, oB = {};
        u32x4 pa, pb = {0u, 0u, 0u, 0u};
        constexpr int X_ACC = 0, X_CNT = 4608, X_KVF = 4864, X_O = 8192;
#define EX4(S, i, Pv, j) do { Pv[j] = pk2(ex2(S[i]), ex2(S[i + 1])); Pv[j + 1] = pk2(ex2(S[i + 2]), ex2(S[i + 3])); PIN(Pv); SBAR(); } while (0)
#define KT2 ((u32x2){kt, 0x3f80u})
#define TILE(V0C, V0N, aK1, oK1, aT1, oT1, oV1, aV0, oV0) do { \
            sB = MFMA32_16(ka, qB, z16); SBAR(); DSR128(ka, aK1, oK1); EX4(sA, 0, pa, 0); \
            oB = MFMA32_16(v1, pb, oB); SBAR(); DSR128M2(v1, aV, oV1, V0N, aV0, oV0); EX4(sA, 4, pa, 2); \
            sB = MFMA32_8(KT2, qTB, sB); SBAR(); DSR32(kt, aT1, oT1); EX4(sA, 8, pb, 0); \
            oA = MFMA32_16(V0C, pa, oA); SBAR(); EX4(sA, 12, pb, 2); \
            LWAIT(1); sA = MFMA32_16(ka, qA, z16); SBAR(); EX4(sB, 0, pa, 0); \
            oA = MFMA32_16(v1, pb, oA); SBAR(); EX4(sB, 4, pa, 2); \
            LWAIT(0); sA = MFMA32_8(KT2, qTA, sA); SBAR(); EX4(sB, 8, pb, 0); \
            oB = MFMA32_16(V0C, pa, oB); SBAR(); EX4(sB, 12, pb, 2); \
        } while (0)
        unsigned aK = laneK, aT = laneT, aV = laneV;
        const int tokS = qb * 256 + w * 32;
#pragma unroll 1
        for (int c = 0; c < 8; ++c) {
            if (c == 1) {
                WAIT_BAR(10);
                if (tid < 304) *(LDS3 f32x4*)(lds + X_ACC + tid * 16) = (f32x4){0.f, 0.f, 0.f, 0.f};
            } else if (c == 2) {
                WAIT_BAR(0);
                wait_flags(ws, WS_F_G + 2048 + ((tokS >> 7) * 15 + ((lane & 1) ? nhi : nlo) + ((lane & 2) ? 5 : 10)) * 8,
                           WS_F_G + 2048 + ((tokS >> 7) * 15 + ((lane & 1) ? nhi : nlo)) * 8, tag);
                const unsigned kb = WS_KV + (NH + head) * 163840 + (tokS >> 7) * 10240, t4 = (tokS >> 5) & 3;
                const u32x4 tm = LD16(kb + t4 * 1024 + h * 512 + r * 16);
                const unsigned tt = LD4(kb + 4096 + (t4 * 32 + r) * 8 + 4 * h);
                u32x4 w0 = LD16(kb + 5120 + t4 * 1280 + h * 320 + (r < 20 ? r : 19) * 16), w1 = LD16(kb + 5120 + t4 * 1280 + 640 + h * 320 + (r < 20 ? r : 19) * 16);
                if (r >= 20) { w0 = (u32x4){one, one, one, one}; w1 = w0; }
                u32x4 fp, fn, ft;
                feature_map(phm, pht, tm, tt, h, fp, fn, ft);
                u32x4 i0, i1;
#pragma unroll
                for (int j = 0; j < 4; ++j) {
                    const int e = r - 8 * h - 2 * j;
                    i0[j] = e == 0 ? 0x3f80u : (e == 1 ? 0x3f800000u : 0u);
                    i1[j] = e == 16 ? 0x3f80u : (e == 17 ? 0x3f800000u : 0u);
                }
                f32x16 T0 = MFMA32_16(fp, i0, z16); T0 = MFMA32_16(fn, i1, T0);
                f32x16 T1 = MFMA32_16(ft, i0, z16);
                u32x4 b00, b01, b10, b11;
#pragma unroll
                for (int j = 0; j < 4; ++j) {
                    b00[j] = pk2(T0[2 * j], T0[2 * j + 1]); b01[j] = pk2(T0[8 + 2 * j], T0[9 + 2 * j]);
                    b10[j] = pk2(T1[2 * j], T1[2 * j + 1]); b11[j] = pk2(T1[8 + 2 * j], T1[9 + 2 * j]);
                }
                f32x16 k0 = MFMA32_16(w0, b00, z16); k0 = MFMA32_16(w1, b01, k0);
                f32x16 k1 = MFMA32_16(w0, b10, z16); k1 = MFMA32_16(w1, b11, k1);
                LDS3 float* acc = (LDS3 float*)(lds + X_ACC);
#pragma unroll
                for (int i = 0; i < 12; ++i) {
                    const int d = (i & 3) + 8 * (i >> 2) + 4 * h;
                    __hip_atomic_fetch_add(acc + d * 48 + r, k0[i], __ATOMIC_RELAXED, __HIP_MEMORY_SCOPE_WORKGROUP);
                    if (r < 16) __hip_atomic_fetch_add(acc + d * 48 + 32 + r, k1[i], __ATOMIC_RELAXED, __HIP_MEMORY_SCOPE_WORKGROUP);
                }
                asm volatile("s_waitcnt lgkmcnt(0)" ::: "memory");
                unsigned arr = 0;
                if (lane == 0) arr = __hip_atomic_fetch_add((LDS3 unsigned*)(lds + X_CNT), 1u, __ATOMIC_RELAXED, __HIP_MEMORY_SCOPE_WORKGROUP);
                if (__builtin_amdgcn_readfirstlane(arr) == 7) {
#pragma unroll
                    for (int i = 0; i < 5; ++i) {
                        const int qd = lane + 64 * i;
                        if (qd < 288) ST16(__builtin_bit_cast(u32x4, *(LDS3 const f32x4*)(lds + X_ACC + qd * 16)), WS_KVP + (head * 8 + qb) * 4608 + qd * 16);
                    }
                    DRAIN();
                    if (lane == 0) set_flag(ws, WS_F_KV + (head * 8 + qb) * 8, tag);
                }
            } else if (c == 5) {
                if (w == 7) {
                    wait_flags(ws, WS_F_KV + (head * 8 + (lane & 7)) * 8, WS_F_KV + (head * 8 + (lane & 7)) * 8, tag);
#pragma unroll 1
                    for (int i = 0; i < 5; ++i) {
                        const int qd = lane + 64 * i;
                        if (qd < 288) {
                            f32x4 s = {0.f, 0.f, 0.f, 0.f};
#pragma unroll
                            for (int p = 0; p < 8; ++p) s += __builtin_bit_cast(f32x4, LD16(WS_KVP + (head * 8 + p) * 4608 + qd * 16));
                            *(LDS3 u32x2*)(lds + X_KVF + qd * 8) = (u32x2){pk2(s[0], s[1]), pk2(s[2], s[3])};
                        }
                    }
                    asm volatile("s_waitcnt lgkmcnt(0)" ::: "memory");
                    if (lane == 0) __hip_atomic_store((LDS3 unsigned*)(lds + X_CNT + 4), 1u, __ATOMIC_RELAXED, __HIP_MEMORY_SCOPE_WORKGROUP);
                }
            } else if (c == 6) {
                const unsigned qbb = WS_Q + (NH + head) * 81920 + (tokS >> 7) * 5120, t4 = (tokS >> 5) & 3;
                const u32x4 tm = LD16(qbb + t4 * 1024 + h * 512 + r * 16);
                const unsigned tt = LD4(qbb + 4096 + (t4 * 32 + r) * 8 + 4 * h);
                const float gt = __uint_as_float(LD4(WS_G + (tokS + r) * 4));
                u32x4 fp, fn, ft;
                feature_map(phm, pht, tm, tt, h, fp, fn, ft);
#pragma unroll 1
                for (unsigned spins = 0; spins < (1u << 20); ++spins) {
                    if (__builtin_amdgcn_readfirstlane(__hip_atomic_load((LDS3 unsigned*)(lds + X_CNT + 4), __ATOMIC_RELAXED, __HIP_MEMORY_SCOPE_WORKGROUP))) break;
                    __builtin_amdgcn_s_sleep(1);
                }
                __builtin_amdgcn_fence(__ATOMIC_ACQUIRE, "wavefront");
                LDS3 const char* kp = lds + X_KVF + (r < 24 ? r : 23) * 96 + h * 16;
                f32x16 d = MFMA32_16(*(LDS3 const u32x4*)kp, fp, z16);
                d = MFMA32_16(*(LDS3 const u32x4*)(kp + 32), fn, d);
                d = MFMA32_16(*(LDS3 const u32x4*)(kp + 64), ft, d);
                const float den = upper_half(d[8]) + 1e-6f;
                float o[12];
#pragma unroll
                for (int i = 0; i < 12; ++i) o[i] = d[i];
                store_o(rs, o, (1.f - gt) / den, tokS + r, 640 + head * 20, h);
            } else if (c == 7) {
                DRAIN();
                unsigned arr = 0;
                if (lane == 0) arr = __hip_atomic_fetch_add((LDS3 unsigned*)(lds + X_CNT + 8), 1u, __ATOMIC_RELAXED, __HIP_MEMORY_SCOPE_WORKGROUP);
                if (__builtin_amdgcn_readfirstlane(arr) == 7 && lane == 0) set_flag(ws, WS_F_OB + (qb * 32 + head) * 8, tag);
            }
            const unsigned nx = ((c + 1) & 7) * 10240;
            const unsigned nK = laneK + nx, nT = laneT + nx, nV = laneV + nx;
            asm volatile("s_waitcnt lgkmcnt(0)" ::: "memory");
            SBAR();
            TILE(v0a, v0b, aK, 1024, aT, 256, 640,  aV, 1280);
            TILE(v0b, v0a, aK, 2048, aT, 512, 1920, aV, 2560);
            TILE(v0a, v0b, aK, 3072, aT, 768, 3200, aV, 3840);
            TILE(v0b, v0a, nK, 0,    nT, 0,   4480, nV, 0);
            aK = nK; aT = nT; aV = nV;
        }
        oB = MFMA32_16(v1, pb, oB);
#undef TILE
#undef EX4
        STAMP(6);
        {
            float chk = 0.f;
#pragma unroll
            for (int i = 0; i < 12; ++i) chk += fabsf(oA[i]) * 0.f + fabsf(oB[i]) * 0.f;
            bool bad = !(chk == 0.f);
            if (h) bad = bad || !(oA[8] >= 0x1p-60f && oA[8] <= 0x1p60f && oB[8] >= 0x1p-60f && oB[8] <= 0x1p60f);
            if (__builtin_expect(__any(bad), 0)) {
                mA = -INFINITY; mB = -INFINITY;
#pragma unroll
                for (int i = 0; i < 16; ++i) { oA[i] = 0.f; oB[i] = 0.f; }
                const u32x2 a0 = {qtA, 0u}, b0 = {qtB, 0u};
#pragma unroll 1
                for (int t = 0; t < 32; ++t) {
                    const unsigned kb = WS_KV + head * 163840 + (kh * 8 + (t >> 2)) * 10240, t4 = t & 3;
                    const u32x4 k0 = LD16(kb + t4 * 1024 + h * 512 + r * 16);
                    const u32x2 t0 = {LD4(kb + 4096 + (t4 * 32 + r) * 8 + 4 * h), 0u};
                    u32x4 w0 = LD16(kb + 5120 + t4 * 1280 + h * 320 + (r < 20 ? r : 19) * 16), w1 = LD16(kb + 5120 + t4 * 1280 + 640 + h * 320 + (r < 20 ? r : 19) * 16);
                    if (r >= 20) { w0 = (u32x4){one, one, one, one}; w1 = w0; }
                    f32x16 s0 = MFMA32_16(k0, qA, z16); s0 = MFMA32_8(t0, a0, s0);
                    f32x16 s1 = MFMA32_16(k0, qB, z16); s1 = MFMA32_8(t0, b0, s1);
                    float x0 = s0[0], x1 = s1[0];
#pragma unroll
                    for (int i = 1; i < 16; ++i) { x0 = fmaxf(x0, s0[i]); x1 = fmaxf(x1, s1[i]); }
                    x0 = fmaxf(xhalf_max(x0), mA); x1 = fmaxf(xhalf_max(x1), mB);
                    const float f0 = (mA == x0) ? 1.f : ex2(mA - x0), f1 = (mB == x1) ? 1.f : ex2(mB - x1);
                    mA = x0; mB = x1;
#pragma unroll
                    for (int i = 0; i < 16; ++i) { oA[i] *= f0; oB[i] *= f1; }
#pragma unroll
                    for (int i = 0; i < 4; ++i) { pa[i] = pk2(ex2(s0[2 * i] - x0), ex2(s0[2 * i + 1] - x0)); pb[i] = pk2(ex2(s0[8 + 2 * i] - x0), ex2(s0[9 + 2 * i] - x0)); }
                    oA = MFMA32_16(w0, pa, oA); oA = MFMA32_16(w1, pb, oA);
#pragma unroll
                    for (int i = 0; i < 4; ++i) { pa[i] = pk2(ex2(s1[2 * i] - x1), ex2(s1[2 * i + 1] - x1)); pb[i] = pk2(ex2(s1[8 + 2 * i] - x1), ex2(s1[9 + 2 * i] - x1)); }
                    oB = MFMA32_16(w0, pa, oB); oB = MFMA32_16(w1, pb, oB);
                }
            }
        }
        const float gt = __uint_as_float(LD4(WS_G + (q0 + kh * 32 + r) * 4));
        {
            LDS3 char* xp = lds + kh * 81920 + X_O + qs * 3072 + lane * 16;
            const f32x16& s = kh ? oA : oB;
            f32x4 t2 = {s[8], s[9], s[10], s[11]};
            if (h) t2[1] = kh ? mA : mB;
            *(LDS3 f32x4*)(xp) = (f32x4){s[0], s[1], s[2], s[3]};
            *(LDS3 f32x4*)(xp + 1024) = (f32x4){s[4], s[5], s[6], s[7]};
            *(LDS3 f32x4*)(xp + 2048) = t2;
        }
        WAIT_BAR(63);
        {
            LDS3 const char* xp = lds + (kh ^ 1) * 81920 + X_O + qs * 3072 + lane * 16;
            const f32x4 p0 = *(LDS3 const f32x4*)(xp), p1 = *(LDS3 const f32x4*)(xp + 1024), p2 = *(LDS3 const f32x4*)(xp + 2048);
            const f32x16& s = kh ? oB : oA;
            const float mo = kh ? mB : mA, mp = upper_half(p2[1]);
            const float mx = fmaxf(mo, mp);
            const float fo = (mo == mx) ? 1.f : ex2(mo - mx), fq = (mp == mx) ? 1.f : ex2(mp - mx);
            float o[12];
#pragma unroll
            for (int i = 0; i < 4; ++i) { o[i] = s[i] * fo + p0[i] * fq; o[4 + i] = s[4 + i] * fo + p1[i] * fq; o[8 + i] = s[8 + i] * fo + p2[i] * fq; }
            const float l = upper_half(o[8]);
            store_o(rs, o, gt / l, q0 + kh * 32 + r, head * 20, h);
        }
        WAIT_BAR(0);
        if (tid == 0) set_flag(ws, WS_F_OA + (qb * 32 + head) * 8, tag);
    }
    STAMP(7);

    if constexpr (PH & 4) {
        const int c16 = lane & 15, g = lane >> 4;
        const int mt = (bid & 7) * 4 + ((bid >> 3) & 3), nt = bid >> 5;
        if constexpr (!(PH & 2)) {
            GET_TAG();
            if (w == 0) { wait_flags(ws, WS_F_SD + (lane & 15) * 8, WS_F_SD + (lane & 15) * 8, tag); __builtin_amdgcn_fence(__ATOMIC_ACQUIRE, "agent"); }
            WAIT_BAR(0);
        }
        {
            const char* wsrc = ws + WS_WT + (size_t)nt * 5 * 40960;
#pragma unroll
            for (int t = 0; t < 13; ++t) {
                int pc = w + 8 * t; if (pc > 99) pc = 99;
                const int n = pc / 20, ks = pc - n * 20;
                glds16(lane * 16 + (n * 40 + ks) * 1024, wsrc, lds0 + pc * 1024);
            }
        }
        const int mh = (w >> 1) & 1, kg = (w & 1) + 2 * (w >> 2), tt = w & 3;
        f32x4 acc[2][5];
#pragma unroll
        for (int a = 0; a < 2; ++a)
#pragma unroll
            for (int b = 0; b < 5; ++b) acc[a][b] = (f32x4){0.f, 0.f, 0.f, 0.f};
        wait_flags(ws, WS_F_OB + ((mt >> 2) * 32 + r) * 8, WS_F_OB + ((mt >> 2) * 32 + r) * 8, tag);
        {
            const unsigned ab = WS_AO + ((mt * 4 + mh * 2) * 40 + 20 + kg * 5) * 1024 + lane * 16;
            const char* bb = ws + WS_WT + ((size_t)(nt * 5) * 40 + 20 + kg * 5) * 1024 + lane * 16;
#pragma unroll
            for (int ks = 0; ks < 5; ++ks) {
                u32x4 af[2], bf[5];
#pragma unroll
                for (int t = 0; t < 2; ++t) af[t] = LD16(ab + t * 40960 + ks * 1024);
#pragma unroll
                for (int n = 0; n < 5; ++n) bf[n] = *(const u32x4*)(bb + (size_t)n * 40960 + ks * 1024);
#pragma unroll
                for (int t = 0; t < 2; ++t)
#pragma unroll
                    for (int n = 0; n < 5; ++n) acc[t][n] = MFMA16_32(bf[n], af[t], acc[t][n]);
            }
        }
        constexpr int XO = 102400;
        f32x4 c[5];
#pragma unroll
        for (int n = 0; n < 5; ++n) {
            *(LDS3 f32x4*)(lds + XO + w * 5120 + n * 1024 + lane * 16) = (tt & 1) ? acc[0][n] : acc[1][n];
            c[n] = (tt & 1) ? acc[1][n] : acc[0][n];
        }
        WAIT_BAR(0);
#pragma unroll
        for (int n = 0; n < 5; ++n) c[n] += *(LDS3 const f32x4*)(lds + XO + (w ^ 1) * 5120 + n * 1024 + lane * 16);
        STAMP(8);
        wait_flags(ws, WS_F_OA + ((mt >> 2) * 32 + r) * 8, WS_F_OA + ((mt >> 2) * 32 + r) * 8, tag);
        STAMP(9);
        {
            const int k0 = (w >> 2) * 10;
            const unsigned ab = WS_AO + ((mt * 4 + tt) * 40 + k0) * 1024 + lane * 16;
            u32x4 af[10];
#pragma unroll
            for (int ks = 0; ks < 10; ++ks) af[ks] = LD16(ab + ks * 1024);
#pragma unroll
            for (int ks = 0; ks < 10; ++ks)
#pragma unroll
                for (int n = 0; n < 5; ++n) c[n] = MFMA16_32(*(LDS3 const u32x4*)(lds + (n * 20 + k0 + ks) * 1024 + lane * 16), af[ks], c[n]);
        }
        {
            LDS3 char* xp = lds + XO + 40960 + tt * 5120 + lane * 16;
            if (w < 4) { *(LDS3 f32x4*)(xp + 3072) = c[3]; *(LDS3 f32x4*)(xp + 4096) = c[4]; }
            else { *(LDS3 f32x4*)(xp) = c[0]; *(LDS3 f32x4*)(xp + 1024) = c[1]; *(LDS3 f32x4*)(xp + 2048) = c[2]; }
            WAIT_BAR(63);
            u16* op = P.out + (size_t)(mt * 64 + tt * 16 + c16) * DM + nt * 80 + g * 4;
#pragma unroll
            for (int n = 0; n < 5; ++n) {
                if ((w < 4) == (n < 3)) {
                    const f32x4 s = c[n] + *(LDS3 const f32x4*)(xp + n * 1024);
                    *(u32x2*)(op + n * 16) = (u32x2){pk2(s[0], s[1]), pk2(s[2], s[3])};
                }
            }
        }
        if (bid == 0 && tid == 0) set_flag(ws, WS_EPOCH, tag);
    }
    STAMP(10);
}

extern "C" void kernel_launch(void* const* d_in, const int* in_sizes, int n_in, void* d_out, int out_size, void* d_ws, size_t ws_size,
                              hipStream_t stream) {
    Params P;
    P.x = (const u16*)d_in[0]; P.wa = (const u16*)d_in[1]; P.woa = (const u16*)d_in[2]; P.wb = (const u16*)d_in[4];
    P.wphi = (const u16*)d_in[5]; P.wob = (const u16*)d_in[6]; P.gw = (const u16*)d_in[7]; P.ws = (char*)d_ws; P.out = (u16*)d_out;
    mega<7><<<256, 512, 0, stream>>>(P);
}
```

```cpp
#include <hip/hip_runtime.h>
#include <hip/hip_bf16.h>
#include <stdint.h>
#include <type_traits>

typedef __attribute__((ext_vector_type(8))) short bf16x8;
typedef __attribute__((ext_vector_type(4))) short bf16x4;
typedef __attribute__((ext_vector_type(16))) float f32x16;
typedef __attribute__((ext_vector_type(4))) float f32x4;
typedef __attribute__((ext_vector_type(2))) float f32x2;
typedef __attribute__((ext_vector_type(4))) unsigned u32x4;
typedef __attribute__((ext_vector_type(2))) unsigned u32x2;
typedef __attribute__((ext_vector_type(2))) __bf16 bf2_t;
typedef unsigned short u16;
typedef unsigned long long u64;
#define LDS3 __attribute__((address_space(3)))
typedef __attribute__((address_space(1))) u64 gu64;
#define RLX_AGENT __ATOMIC_RELAXED, __HIP_MEMORY_SCOPE_AGENT

constexpr int SEQ = 2048, DM = 640, NH = 32;
constexpr float LOG2E = 1.4426950408889634f;
constexpr float QSCALE = 0.22360679774997896f * LOG2E;

constexpr unsigned WS_EPOCH = 0;
constexpr unsigned WS_F_G   = 256;
constexpr unsigned WS_F_SD  = WS_F_G + 4096;
constexpr unsigned WS_F_KV  = WS_F_SD + 256;
constexpr unsigned WS_F_OB  = WS_F_KV + 2048;
constexpr unsigned WS_F_OA  = WS_F_OB + 2048;
constexpr unsigned WS_G     = 16384;
constexpr unsigned WS_Q     = 32768;
constexpr unsigned WS_KV    = WS_Q + 2 * NH * 81920;
constexpr unsigned WS_KVP   = WS_KV + 2 * NH * 163840;
constexpr unsigned WS_AO    = WS_KVP + NH * 8 * 4096;
constexpr unsigned WS_WT    = WS_AO + SEQ * 1280 * 2;
constexpr unsigned WS_END   = WS_WT + 1280 * 640 * 2;

#define STAMP(i)

__device__ __forceinline__ unsigned pk2(float a, float b) { f32x2 v = {a, b}; bf2_t r = __builtin_convertvector(v, bf2_t); return __builtin_bit_cast(unsigned, r); }
__device__ __forceinline__ float bf_lo(unsigned u) { return __uint_as_float(u << 16); }
__device__ __forceinline__ float bf_hi(unsigned u) { return __uint_as_float(u & 0xffff0000u); }
__device__ __forceinline__ float ex2(float x) { return __builtin_amdgcn_exp2f(x); }
#define MFMA32_16(a, b, c) __builtin_amdgcn_mfma_f32_32x32x16_bf16(__builtin_bit_cast(bf16x8, a), __builtin_bit_cast(bf16x8, b), c, 0, 0, 0)
#define MFMA32_8(a, b, c) __builtin_amdgcn_mfma_f32_32x32x8bf16_1k(__builtin_bit_cast(bf16x4, a), __builtin_bit_cast(bf16x4, b), c, 0, 0, 0)
#define MFMA16_32(a, b, c) __builtin_amdgcn_mfma_f32_16x16x32_bf16(__builtin_bit_cast(bf16x8, a), __builtin_bit_cast(bf16x8, b), c, 0, 0, 0)

__device__ __forceinline__ void glds16(unsigned voff, const void* sbase, unsigned lds_base) {
    unsigned sv;
    asm volatile("s_mov_b32 %0, m0\n\ts_mov_b32 m0, %3\n\ts_nop 0\n\tglobal_load_lds_dwordx4 %1, %2\n\ts_mov_b32 m0, %0"
                 : "=&s"(sv) : "v"(voff), "s"(sbase), "s"(__builtin_amdgcn_readfirstlane(lds_base)) : "memory");
}
__device__ __forceinline__ void glds16s(unsigned voff, const void* sbase, unsigned lds_base) {
    unsigned sv;
    asm volatile("s_mov_b32 %0, m0\n\ts_mov_b32 m0, %3\n\ts_nop 0\n\tglobal_load_lds_dwordx4 %1, %2 sc1\n\ts_mov_b32 m0, %0"
                 : "=&s"(sv) : "v"(voff), "s"(sbase), "s"(__builtin_amdgcn_readfirstlane(lds_base)) : "memory");
}
__device__ __forceinline__ void glds4s(unsigned voff, const void* sbase, unsigned lds_base) {
    unsigned sv;
    asm volatile("s_mov_b32 %0, m0\n\ts_mov_b32 m0, %3\n\ts_nop 0\n\tglobal_load_lds_dword %1, %2 sc1\n\ts_mov_b32 m0, %0"
                 : "=&s"(sv) : "v"(voff), "s"(sbase), "s"(__builtin_amdgcn_readfirstlane(lds_base)) : "memory");
}
typedef short v4i16_t __attribute__((ext_vector_type(4)));
__device__ __forceinline__ u32x2 trread(LDS3 const char* p) {
    return __builtin_bit_cast(u32x2, __builtin_amdgcn_ds_read_tr16_b64_v4i16((LDS3 v4i16_t*)p));
}
#define WAIT_BAR(N) asm volatile("s_waitcnt vmcnt(" #N ") lgkmcnt(0)\n\ts_barrier" ::: "memory")
#define DRAIN() asm volatile("s_waitcnt vmcnt(0)" ::: "memory")
#define SBAR() __builtin_amdgcn_sched_barrier(0)
#define PIN(x) asm volatile("" : "+v"(x))

__device__ __forceinline__ float xhalf_max(float x) {
    auto r = __builtin_amdgcn_permlane32_swap(__float_as_uint(x), __float_as_uint(x), false, false);
    return fmaxf(__uint_as_float(r[0]), __uint_as_float(r[1]));
}
__device__ __forceinline__ float xhalf_sum(float x) {
    auto r = __builtin_amdgcn_permlane32_swap(__float_as_uint(x), __float_as_uint(x), false, false);
    return __uint_as_float(r[0]) + __uint_as_float(r[1]);
}
__device__ __forceinline__ float upper_half(float x) {
    auto r = __builtin_amdgcn_permlane32_swap(__float_as_uint(x), __float_as_uint(x), false, false);
    return __uint_as_float(r[1]);
}

typedef __amdgpu_buffer_rsrc_t rsrc_t;
#define ST8(v, off)  __builtin_amdgcn_raw_buffer_store_b64(v, rs, (int)(off), 0, 16)
#define ST16(v, off) __builtin_amdgcn_raw_buffer_store_b128(v, rs, (int)(off), 0, 16)
#define ST4(v, off)  __builtin_amdgcn_raw_buffer_store_b32(v, rs, (int)(off), 0, 16)
#define LD16(off) __builtin_amdgcn_raw_buffer_load_b128(rs, (int)(off), 0, 16)
#define LD4(off)  __builtin_amdgcn_raw_buffer_load_b32(rs, (int)(off), 0, 16)

__device__ __forceinline__ void wait_flags(const char* ws, unsigned off0, unsigned off1, u64 tag) {
    gu64* p0 = (gu64*)(ws + off0); gu64* p1 = (gu64*)(ws + off1);
#pragma unroll 1
    for (unsigned spins = 0; spins < (1u << 16); ++spins) {
        const u64 a = __hip_atomic_load(p0, RLX_AGENT), b = __hip_atomic_load(p1, RLX_AGENT);
        if (__all(a == tag && b == tag)) break;
        __builtin_amdgcn_s_sleep(2);
    }
    __builtin_amdgcn_fence(__ATOMIC_ACQUIRE, "wavefront");
}
__device__ __forceinline__ void set_flag(char* ws, unsigned off, u64 tag) { __hip_atomic_store((gu64*)(ws + off), tag, RLX_AGENT); }

struct Params { const u16* x; const u16* wa; const u16* wb; const u16* wphi; const u16* woa; const u16* wob; const u16* gw; char* ws; u16* out; };

__device__ __forceinline__ void feature_map(const u32x4& pm, unsigned pt, const u32x4& tm, unsigned tt, int h, u32x4& fp, u32x4& fn, u32x4& ft) {
    const f32x16 z16 = {};
    const u32x2 a8 = {pt, 0u}, b8 = {tt, 0u};
    f32x16 s = MFMA32_16(pm, tm, z16); s = MFMA32_8(a8, b8, s);
    float v[12];
#pragma unroll
    for (int i = 0; i < 12; i += 2) { const unsigned u = pk2(s[i], s[i + 1]); v[i] = bf_lo(u) * LOG2E; v[i + 1] = bf_hi(u) * LOG2E; }
    float mx = v[0], mn = v[0];
#pragma unroll
    for (int i = 1; i < 8; ++i) { mx = fmaxf(mx, v[i]); mn = fminf(mn, v[i]); }
    const float tx = fmaxf(fmaxf(v[8], v[9]), fmaxf(v[10], v[11])), tn = fminf(fminf(v[8], v[9]), fminf(v[10], v[11]));
    if (!h) { mx = fmaxf(mx, tx); mn = fminf(mn, tn); }
    mx = xhalf_max(mx); mn = -xhalf_max(-mn);
    float ep[12], en[12], sp = 0.f, sn = 0.f;
#pragma unroll
    for (int i = 0; i < 12; ++i) {
        ep[i] = ex2(v[i] - mx); en[i] = ex2(mn - v[i]);
        if (i >= 8 && h) { ep[i] = 0.f; en[i] = 0.f; }
        sp += ep[i]; sn += en[i];
    }
    sp = __builtin_amdgcn_rcpf(xhalf_sum(sp)); sn = __builtin_amdgcn_rcpf(xhalf_sum(sn));
#pragma unroll
    for (int i = 0; i < 4; ++i) { fp[i] = pk2(ep[2 * i] * sp, ep[2 * i + 1] * sp); fn[i] = pk2(en[2 * i] * sn, en[2 * i + 1] * sn); }
    ft = (u32x4){pk2(ep[8] * sp, ep[9] * sp), pk2(ep[10] * sp, ep[11] * sp), pk2(en[8] * sn, en[9] * sn), pk2(en[10] * sn, en[11] * sn)};
}

__device__ __forceinline__ void store_o(rsrc_t rs, const float (&o)[12], float sc, int tok, int kbase, int h) {
#pragma unroll
    for (int gq = 0; gq < 3; ++gq) {
        const int d = 8 * gq + 4 * h;
        if (d < 20) {
            const int k = kbase + d;
            u32x2 ov = {pk2(o[4 * gq] * sc, o[4 * gq + 1] * sc), pk2(o[4 * gq + 2] * sc, o[4 * gq + 3] * sc)};
            ST8(ov, WS_AO + (((tok >> 4) * 40 + (k >> 5)) * 64 + ((k >> 3) & 3) * 16 + (tok & 15)) * 16 + (k & 7) * 2);
        }
    }
}

#define DSR128(dst, addr, off) asm volatile("ds_read_b128 %0, %1 offset:%2" : "=v"(dst) : "v"(addr), "i"(off))
#define DSR32(dst, addr, off) asm volatile("ds_read_b32 %0, %1 offset:%2" : "=v"(dst) : "v"(addr), "i"(off))
#define DSR128M2(d0, a0, o0, d1, a1, o1) asm volatile("s_mov_b64 exec, %6\n\tds_read_b128 %0, %2 offset:%3\n\tds_read_b128 %1, %4 offset:%5\n\ts_mov_b64 exec, -1" \
                                                      : "+v"(d0), "+v"(d1) : "v"(a0), "i"(o0), "v"(a1), "i"(o1), "s"(vmask))
#define LWAIT(n) do { asm volatile("s_waitcnt lgkmcnt(" #n ")" ::: "memory"); SBAR(); } while (0)

#define BAR() asm volatile("s_barrier" ::: "memory")
#define LDS_CNT_ADD(off) __hip_atomic_fetch_add((LDS3 unsigned*)(lds + (off)), 1u, __ATOMIC_RELAXED, __HIP_MEMORY_SCOPE_WORKGROUP)

__device__ __forceinline__ void lds_wait(LDS3 char* p, unsigned want) {
#pragma unroll 1
    for (unsigned spins = 0; spins < (1u << 20); ++spins) {
        if ((unsigned)__builtin_amdgcn_readfirstlane(__hip_atomic_load((LDS3 unsigned*)p, __ATOMIC_RELAXED, __HIP_MEMORY_SCOPE_WORKGROUP)) >= want) break;
        __builtin_amdgcn_s_sleep(1);
    }
    __builtin_amdgcn_fence(__ATOMIC_ACQUIRE, "wavefront");
}

__global__ __launch_bounds__(512) void mega(Params P) {
    __shared__ __attribute__((aligned(16))) char smem[163840];
    LDS3 char* lds = (LDS3 char*)smem;
    char* ws = P.ws;
    const int tid = threadIdx.x, lane = tid & 63, r = lane & 31, h = lane >> 5;
    const int w = __builtin_amdgcn_readfirstlane(tid >> 6);
    const int bid = blockIdx.x;
    const unsigned lds0 = (unsigned)(uintptr_t)smem;
    const rsrc_t rs = __builtin_amdgcn_make_buffer_rsrc(ws, 0, WS_END, 0x00020000);
    STAMP(0);
    u64 tag = 0;
#define GET_TAG() do { asm volatile("s_load_dwordx2 %0, %1, 0x0\n\ts_waitcnt lgkmcnt(0)" : "=s"(tag) : "s"(ws) : "memory"); tag += 1; } while (0)
    const int head = (bid & 7) * 4 + ((bid >> 3) & 3), qb = bid >> 5;
    const int nlo = (head * 20) >> 7, nhi = (head * 20 + 19) >> 7;
    const int qs = w & 3, kh = w >> 2;
    const int q0 = qb * 256 + qs * 64;
    const unsigned fA0 = WS_F_G + ((lane & 15) * 15 + (lane < 32 ? 5 : 10) + ((lane & 16) ? nhi : nlo)) * 8;
    const unsigned fA1 = WS_F_G + ((qb * 2 + (lane & 1)) * 15 + ((lane & 2) ? nhi : nlo)) * 8;
    u32x4 qA, qB; unsigned qtA, qtB;
    auto kv_issue = [&](int grp) {
        const char* kvb = ws + WS_KV + head * 163840;
#pragma unroll
        for (int t = 0; t < 4; ++t) { const unsigned pc = (grp * 32 + w + 8 * t) * 1024; glds16s(lane * 16 + pc, kvb, lds0 + pc); }
    };
    constexpr int L_Q = 65536;
    auto q_issue = [&]() {
        const char* qbase = ws + WS_Q + head * 81920 + qb * 10240;
        const unsigned p1 = (w < 2 ? w + 8 : 9) * 1024;
        glds16s(lane * 16 + w * 1024, qbase, lds0 + L_Q + w * 1024); glds16s(lane * 16 + p1, qbase, lds0 + L_Q + p1);
    };
    auto q_read = [&]() {
        LDS3 const char* qp = lds + L_Q + (qs >> 1) * 5120;
        const unsigned om = (qs & 1) * 2048 + h * 512 + r * 16, ot = 4096 + ((qs & 1) * 64 + r) * 8 + 4 * h;
        qA = *(LDS3 const u32x4*)(qp + om); qB = *(LDS3 const u32x4*)(qp + om + 1024);
        qtA = *(LDS3 const unsigned*)(qp + ot); qtB = *(LDS3 const unsigned*)(qp + ot + 256);
    };
    auto fl_issue = [&](unsigned o0, unsigned o1, unsigned lo) {
        glds4s(o0, ws, lds0 + lo); glds4s(o0 + 4, ws, lds0 + lo + 256); glds4s(o1, ws, lds0 + lo + 512); glds4s(o1 + 4, ws, lds0 + lo + 768);
    };
    auto fl_ok = [&](unsigned lo, int ln) -> bool {
        LDS3 const unsigned* f = (LDS3 const unsigned*)(lds + lo) + ln;
        const unsigned tl = (unsigned)tag, th = (unsigned)(tag >> 32);
        return __all(f[0] == tl && f[64] == th && f[128] == tl && f[192] == th);
    };
    constexpr int L_PHI = 3072;
    auto phi_frag = [&](int r_, int h_, u32x4& phm, unsigned& pht) {
        LDS3 const u16* wp = (LDS3 const u16*)(lds + L_PHI) + (r_ < 20 ? r_ : 0);
        unsigned e[10];
#pragma unroll
        for (int j = 0; j < 8; ++j) e[j] = wp[(8 * h_ + j) * 20];
        e[8] = wp[(16 + 2 * h_) * 20]; e[9] = wp[(17 + 2 * h_) * 20];
        phm = (u32x4){e[0] | (e[1] << 16), e[2] | (e[3] << 16), e[4] | (e[5] << 16), e[6] | (e[7] << 16)}; pht = e[8] | (e[9] << 16);
        if (r_ >= 20) { phm = (u32x4){0u, 0u, 0u, 0u}; pht = 0u; }
    };
    unsigned fGB = 0;

    if (bid < 240) {
        const int idx = bid >> 3, hi15 = idx >= 15;
        const int mt = (bid & 7) * 2 + hi15, nt = idx - 15 * hi15;
        const int m0 = mt * 128, part = nt / 5, c0 = (nt - part * 5) * 128;
        const int rot = (nt * 3 + mt * 5 + (mt >> 1)) % 10;
        const int wm = w & 1, wn = w >> 1;
        constexpr int RB = 32768;
        fGB = WS_F_G + 2048 + (mt * 15 + nt) * 8;
        unsigned xo[2], wo[2];
#pragma unroll
        for (int i = 0; i < 2; ++i) {
            const int s = (w + 8 * i) * 64 + lane;
            { const int row = s >> 3, ch = (s & 7) ^ ((row >> 1) & 7); xo[i] = (m0 + row) * 1280 + ch * 16; }
            { const int row = s >> 4, ch = (s & 15) ^ (((row & 3) << 2) | ((row >> 2) & 3)); wo[i] = row * 3840 + nt * 256 + ch * 16; }
        }
        const unsigned dX = lds0 + RB + w * 1024, dW = dX + 16384;
        auto issue = [&](int s) {
            int kt = (s >= 10 ? s - 10 : s) + rot; if (kt >= 10) kt -= 10;
            const char* xb = (const char*)P.x + kt * 128;
            const char* wb = (const char*)(s >= 10 ? P.wb : P.wa) + (size_t)kt * (64 * 3840);
            const unsigned sl = (s & 3) * 32768;
            glds16(xo[0], xb, dX + sl); glds16(wo[0], wb, dW + sl); glds16(xo[1], xb, dX + sl + 8192); glds16(wo[1], wb, dW + sl + 8192);
        };
        const int xr = wm * 64 + r;
        const unsigned xa = RB + xr * 128 + ((h ^ ((xr >> 1) & 7)) << 4);
        unsigned wt[2];
#pragma unroll
        for (int t = 0; t < 2; ++t) {
            const int q = (lane & 15) >> 2, p = lane & 3, blk = (lane >> 4) & 1, row = 8 * h + 4 * t + q;
            wt[t] = RB + 16384 + row * 256 + (((4 * wn + 2 * blk + (p >> 1)) ^ ((q << 2) | (2 * h + t))) << 4) + 8 * (p & 1);
        }
        issue(0); issue(1); issue(2); issue(3);
        GET_TAG();
        auto gbody = [&](auto trc) {
            constexpr bool TR = decltype(trc)::value;
            u32x4 xf[2][8], wf[2][4];
            auto rd = [&](auto bc, int s) {
                constexpr int B = decltype(bc)::value;
                LDS3 const char* sb = lds + (s & 3) * 32768;
#pragma unroll
                for (int ks = 0; ks < 4; ++ks) {
                    xf[B][2 * ks] = *(LDS3 const u32x4*)(sb + (xa ^ (ks * 32)));
                    xf[B][2 * ks + 1] = *(LDS3 const u32x4*)(sb + ((xa + 4096) ^ (ks * 32)));
                    const u32x2 lo = trread(sb + wt[0] + ks * 4096), hi = trread(sb + wt[1] + ks * 4096);
                    wf[B][ks] = (u32x4){lo[0], lo[1], hi[0], hi[1]};
                }
            };
            f32x16 acc0, acc1;
            auto step = [&](auto bc, int s) {
                constexpr int B = decltype(bc)::value;
                if (s == 10 || s == 11) { WAIT_BAR(12); }
                else if (s <= 16) { WAIT_BAR(8); }
                else if (s == 17) {
                    asm volatile("s_waitcnt vmcnt(4) lgkmcnt(0)" ::: "memory");
                    if (w == 0 && !fl_ok(0, lane)) wait_flags(ws, fA0, fA1, tag);
                    WAIT_BAR(63);
                }
                else if (s == 18) { WAIT_BAR(10); }
                else { WAIT_BAR(4); }
                if (s == 15 && w == 0) fl_issue(fA0, fA1, 0);
                if (s < 16) issue(s + 4);
                else if (s == 17) { kv_issue(0); kv_issue(1); q_issue(); }
                else if (s == 18) kv_issue(3);
                else if (s == 19) { kv_issue(4); q_read(); }
                if (s == 12 && tid == 0) set_flag(ws, fGB - 2048, tag);
                if (s < 19) rd(std::integral_constant<int, B ^ 1>{}, s + 1);
#pragma unroll
                for (int ks = 0; ks < 4; ++ks) {
                    if constexpr (TR) { acc0 = MFMA32_16(wf[B][ks], xf[B][2 * ks], acc0); acc1 = MFMA32_16(wf[B][ks], xf[B][2 * ks + 1], acc1); }
                    else              { acc0 = MFMA32_16(xf[B][2 * ks], wf[B][ks], acc0); acc1 = MFMA32_16(xf[B][2 * ks + 1], wf[B][ks], acc1); }
                }
            };
            WAIT_BAR(12);
            rd(std::integral_constant<int, 0>{}, 0);
#pragma unroll 1
            for (int mat = 0; mat < 2; ++mat) {
                acc0 = (f32x16){}; acc1 = (f32x16){};
#pragma unroll 1
                for (int kk = 0; kk < 10; kk += 2) {
                    step(std::integral_constant<int, 0>{}, mat * 10 + kk);
                    step(std::integral_constant<int, 1>{}, mat * 10 + kk + 1);
                }
                const int cpos = 2 * (mt & 7) + (mt >> 3);
                if constexpr (TR) {
                    const float sc = (part == 0 && mat == 0) ? QSCALE : 1.f;
                    const unsigned base = part == 0 ? WS_Q + mat * NH * 81920 + mt * 5120 : WS_KV + mat * NH * 163840 + cpos * 10240;
                    const unsigned hs = part == 0 ? 81920 : 163840;
#pragma unroll
                    for (int gq = 0; gq < 4; ++gq) {
                        const int n = c0 + 32 * wn + 8 * gq + 4 * h, hd = n / 20, d = n - hd * 20;
#pragma unroll
                        for (int i = 0; i < 2; ++i) {
                            const f32x16& a = i ? acc1 : acc0;
                            const int tl = wm * 64 + i * 32 + r;
                            const unsigned off = base + hd * hs + (d < 16 ? (tl >> 5) * 1024 + (d >> 3) * 512 + r * 16 + (d & 4) * 2 : 4096 + tl * 8);
                            u32x2 ov = {pk2(a[4 * gq] * sc, a[4 * gq + 1] * sc), pk2(a[4 * gq + 2] * sc, a[4 * gq + 3] * sc)};
                            ST8(ov, off);
                        }
                    }
                } else {
                    const int n = c0 + 32 * wn + r, hd = n / 20, d = n - hd * 20;
                    const unsigned base = WS_KV + (mat * NH + hd) * 163840 + cpos * 10240 + 5120 + h * 320 + d * 16;
#pragma unroll
                    for (int i = 0; i < 2; ++i)
#pragma unroll
                        for (int s2 = 0; s2 < 2; ++s2) {
                            const f32x16& a = i ? acc1 : acc0;
                            u32x4 ov = {pk2(a[8 * s2], a[8 * s2 + 1]), pk2(a[8 * s2 + 2], a[8 * s2 + 3]), pk2(a[8 * s2 + 4], a[8 * s2 + 5]), pk2(a[8 * s2 + 6], a[8 * s2 + 7])};
                            ST16(ov, base + (wm * 4 + i * 2 + s2) * 640);
                        }
                }
                if (mat == 0) STAMP(1);
            }
        };
        if (part < 2) gbody(std::true_type{}); else gbody(std::false_type{});
        STAMP(2);
    } else {
        const int sj = bid - 240;
        GET_TAG();
        {
            const u32x4 ga = *(const u32x4*)(P.gw + lane * 8), gb = *(const u32x4*)(P.gw + 512 + (lane & 15) * 8);
            u32x4 xa[16], xb[4];
            const u16* xr = P.x + (size_t)(sj * 128 + w * 16) * DM;
#pragma unroll
            for (int rr = 0; rr < 16; ++rr) xa[rr] = *(const u32x4*)(xr + rr * DM + lane * 8);
#pragma unroll
            for (int i = 0; i < 4; ++i) xb[i] = *(const u32x4*)(xr + (i * 4 + (lane >> 4)) * DM + 512 + (lane & 15) * 8);
            float res = 0.f;
#pragma unroll
            for (int rr = 0; rr < 16; ++rr) {
                float s = 0.f, s2 = 0.f;
#pragma unroll
                for (int j = 0; j < 4; ++j) { s += bf_lo(xa[rr][j]) * bf_lo(ga[j]) + bf_hi(xa[rr][j]) * bf_hi(ga[j]); s2 += bf_lo(xb[rr >> 2][j]) * bf_lo(gb[j]) + bf_hi(xb[rr >> 2][j]) * bf_hi(gb[j]); }
                if ((lane >> 4) == (rr & 3)) s += s2;
#pragma unroll
                for (int o = 32; o >= 1; o >>= 1) s += __shfl_xor(s, o);
                if (lane == rr) res = s;
            }
            if (lane < 16) ST4(__float_as_uint(1.f / (1.f + __expf(-res))), WS_G + (sj * 128 + w * 16 + lane) * 4);
        }
#pragma unroll 1
        for (int it = 0; it < 2; ++it) {
            unsigned e[4][8];
#pragma unroll
            for (int q = 0; q < 4; ++q) {
                int item = tid + 512 * (it * 4 + q); if (item >= 3200) item = 3199;
                const int u = item / 320, n2 = item - u * 320, k0 = (sj * 10 + u) * 8;
                const u16* wsrc = (k0 < 640 ? P.woa + (size_t)k0 * DM : P.wob + (size_t)(k0 - 640) * DM) + n2 * 2;
#pragma unroll
                for (int j = 0; j < 8; ++j) e[q][j] = *(const unsigned*)(wsrc + (size_t)j * DM);
            }
#pragma unroll
            for (int q = 0; q < 4; ++q) {
                int item = tid + 512 * (it * 4 + q); if (item >= 3200) item = 3199;
                const int u = item / 320, n = (item - u * 320) * 2, k0 = (sj * 10 + u) * 8;
                const unsigned* f = e[q];
                u32x4 lo = {(f[0] & 0xffffu) | (f[1] << 16), (f[2] & 0xffffu) | (f[3] << 16), (f[4] & 0xffffu) | (f[5] << 16), (f[6] & 0xffffu) | (f[7] << 16)};
                u32x4 hi = {(f[0] >> 16) | (f[1] & 0xffff0000u), (f[2] >> 16) | (f[3] & 0xffff0000u), (f[4] >> 16) | (f[5] & 0xffff0000u), (f[6] >> 16) | (f[7] & 0xffff0000u)};
                const unsigned dp = WS_WT + (((n >> 4) * 40 + (k0 >> 5)) * 64 + ((k0 >> 3) & 3) * 16 + (n & 15)) * 16;
                ST16(lo, dp); ST16(hi, dp + 16);
            }
        }
        WAIT_BAR(0);
        if (tid == 0) set_flag(ws, WS_F_SD + sj * 8, tag);
        STAMP(2);
        if (w == 0) wait_flags(ws, fA0, fA1, tag);
        WAIT_BAR(0);
        kv_issue(0); kv_issue(1); q_issue(); kv_issue(3); kv_issue(4);
        WAIT_BAR(8);
        q_read();
    }

    const int mtO = (bid & 7) * 4 + ((bid >> 3) & 3), ntO = bid >> 5;
    constexpr int L_WA = 24576;
    {
        const unsigned hb = lds0 + kh * 10240;
        const unsigned laneK = hb + h * 512 + r * 16, laneT = hb + 4096 + r * 8 + h * 4, laneV = hb + 5120 + h * 320 + (r < 20 ? r : 19) * 16;
        const u64 vmask = 0x000fffff000fffffull;
        const unsigned one = (r == 20) ? 0x3f803f80u : 0u;
        u32x4 v0a = {one, one, one, one}, v0b = v0a, v1 = v0a;
        const f32x16 z16 = {};
        WAIT_BAR(63);
        kv_issue(2);
        STAMP(5);
        float mA, mB; u32x2 qTA, qTB; f32x16 sA, sB; u32x4 ka; unsigned kt;
        {
            DSR128(ka, laneK, 0); DSR32(kt, laneT, 0); DSR128M2(v0a, laneV, 0, v1, laneV, 640);
            asm volatile("s_waitcnt lgkmcnt(0)" : "+v"(ka), "+v"(kt), "+v"(v0a), "+v"(v1) :: "memory");
            const u32x2 t0 = {kt, 0x3f80u}, a0 = {qtA, 0u}, b0 = {qtB, 0u};
            const f32x16 s = MFMA32_16(ka, qA, z16);
            const f32x16 s0 = MFMA32_8(t0, a0, s);
            f32x16 s1 = MFMA32_16(ka, qB, z16); s1 = MFMA32_8(t0, b0, s1);
            float x0 = s0[0], x1 = s1[0];
#pragma unroll
            for (int i = 1; i < 16; ++i) { x0 = fmaxf(x0, s0[i]); x1 = fmaxf(x1, s1[i]); }
            x0 = -xhalf_max(x0); x1 = -xhalf_max(x1);
            const unsigned hh = pk2(x0, x1);
            const unsigned ll = pk2(x0 - bf_lo(hh), x1 - bf_hi(hh));
            mA = -(bf_lo(hh) + bf_lo(ll)); mB = -(bf_hi(hh) + bf_hi(ll));
            const unsigned sel = h ? ll : hh;
            qTA = (u32x2){qtA, sel & 0xffffu}; qTB = (u32x2){qtB, sel >> 16};
            sA = MFMA32_8(t0, qTA, s);
        }
        f32x16 oA = {}, oB = {};
        u32x4 pa, pb = {0u, 0u, 0u, 0u};
        constexpr int X_CNT = 0, X_KVF = 256, X_SLOT = 8192;
#define EX4(S, i, Pv, j) do { Pv[j] = pk2(ex2(S[i]), ex2(S[i + 1])); Pv[j + 1] = pk2(ex2(S[i + 2]), ex2(S[i + 3])); PIN(Pv); SBAR(); } while (0)
#define KT2 ((u32x2){kt, 0x3f80u})
#define TILE(V0C, V0N, aK1, oK1, aT1, oT1, oV1, aV0, oV0) do { \
            sB = MFMA32_16(ka, qB, z16); SBAR(); DSR128(ka, aK1, oK1); EX4(sA, 0, pa, 0); \
            oB = MFMA32_16(v1, pb, oB); SBAR(); DSR128M2(v1, aV, oV1, V0N, aV0, oV0); EX4(sA, 4, pa, 2); \
            sB = MFMA32_8(KT2, qTB, sB); SBAR(); DSR32(kt, aT1, oT1); EX4(sA, 8, pb, 0); \
            oA = MFMA32_16(V0C, pa, oA); SBAR(); EX4(sA, 12, pb, 2); \
            LWAIT(1); sA = MFMA32_16(ka, qA, z16); SBAR(); EX4(sB, 0, pa, 0); \
            oA = MFMA32_16(v1, pb, oA); SBAR(); EX4(sB, 4, pa, 2); \
            LWAIT(0); sA = MFMA32_8(KT2, qTA, sA); SBAR(); EX4(sB, 8, pb, 0); \
            oB = MFMA32_16(V0C, pa, oB); SBAR(); EX4(sB, 12, pb, 2); \
        } while (0)
        unsigned aK = laneK, aT = laneT, aV = laneV;
        const int tokS = qb * 256 + w * 32;
        bool last = false; float gO = 0.f;
        const unsigned slot = X_SLOT + w * 4096;
#pragma unroll 1
        for (int hc = 0; hc < 16; ++hc) {
            int ln = lane; asm volatile("" : "+v"(ln));
            const int r_ = ln & 31, h_ = ln >> 5;
            if (hc == 2) {
                WAIT_BAR(0);
                if (tid == 0 && fGB) set_flag(ws, fGB, tag);
                if (w < 4) *(LDS3 u32x4*)(lds + (w * 64 + ln) * 16) = (u32x4){0u, 0u, 0u, 0u};
            } else if (hc == 3) {
                const unsigned tb = WS_F_G + 2048 + ((tokS >> 7) * 15 + ((ln & 1) ? nhi : nlo)) * 8;
                fl_issue(tb + ((ln & 2) ? 40 : 80), ln < 16 ? WS_F_SD + ln * 8 : tb, 4096 + w * 1024);
                if (w == 3) glds16((ln < 50 ? ln : 49) * 16, (const char*)P.wphi + head * 800, lds0 + L_PHI);
            } else if (hc == 4) {
                DRAIN();
                if (!fl_ok(4096 + w * 1024, ln)) {
                    const unsigned tb = WS_F_G + 2048 + ((tokS >> 7) * 15 + ((ln & 1) ? nhi : nlo)) * 8;
                    wait_flags(ws, tb + ((ln & 2) ? 40 : 80), ln < 16 ? WS_F_SD + ln * 8 : tb, tag);
                }
                WAIT_BAR(63);
                const int m7 = tokS >> 7, t4 = (tokS >> 5) & 3;
                const char* kb = ws + WS_KV + (NH + head) * 163840 + (2 * (m7 & 7) + (m7 >> 3)) * 10240;
                const unsigned ov = 5120 + t4 * 1280 + h_ * 320 + (r_ < 20 ? r_ : 19) * 16;
                glds16s(t4 * 1024 + ln * 16, kb, lds0 + slot); glds16s(ov, kb, lds0 + slot + 1024); glds16s(ov + 640, kb, lds0 + slot + 2048);
                glds4s(4096 + (t4 * 32 + r_) * 8 + 4 * h_, kb, lds0 + slot + 3072);
            } else if (hc == 5) {
                DRAIN();
                LDS3 char* sp = lds + slot;
                const u32x4 e0 = *(LDS3 const u32x4*)(sp + ln * 16);
                u32x4 w0 = *(LDS3 const u32x4*)(sp + 1024 + ln * 16), w1 = *(LDS3 const u32x4*)(sp + 2048 + ln * 16);
                const unsigned et = *(LDS3 const unsigned*)(sp + 3072 + ln * 4);
                if (r_ >= 20) { w0 = (u32x4){one, one, one, one}; w1 = w0; }
                u32x4 fp, fn, ft, phm; unsigned pht;
                phi_frag(r_, h_, phm, pht);
                feature_map(phm, pht, e0, et, h_, fp, fn, ft);
                u32x4 i0, i1;
#pragma unroll
                for (int j = 0; j < 4; ++j) {
                    const int e = r_ - 8 * h_ - 2 * j;
                    i0[j] = e == 0 ? 0x3f80u : (e == 1 ? 0x3f800000u : 0u);
                    i1[j] = e == 16 ? 0x3f80u : (e == 17 ? 0x3f800000u : 0u);
                }
                f32x16 T0 = MFMA32_16(fp, i0, z16); T0 = MFMA32_16(fn, i1, T0);
                f32x16 T1 = MFMA32_16(ft, i0, z16);
                u32x4 b00, b01, b10, b11;
#pragma unroll
                for (int j = 0; j < 4; ++j) {
                    b00[j] = pk2(T0[2 * j], T0[2 * j + 1]); b01[j] = pk2(T0[8 + 2 * j], T0[9 + 2 * j]);
                    b10[j] = pk2(T1[2 * j], T1[2 * j + 1]); b11[j] = pk2(T1[8 + 2 * j], T1[9 + 2 * j]);
                }
                f32x16 k0 = MFMA32_16(w0, b00, z16); k0 = MFMA32_16(w1, b01, k0);
                f32x16 k1 = MFMA32_16(w0, b10, z16); k1 = MFMA32_16(w1, b11, k1);
#pragma unroll
                for (int gq = 0; gq < 3; ++gq) {
                    *(LDS3 f32x4*)(sp + gq * 1024 + ln * 16) = (f32x4){k0[4 * gq], k0[4 * gq + 1], k0[4 * gq + 2], k0[4 * gq + 3]};
                    if (r_ < 8) *(LDS3 f32x4*)(sp + 3072 + (gq * 16 + h_ * 8 + r_) * 16) = (f32x4){k1[4 * gq], k1[4 * gq + 1], k1[4 * gq + 2], k1[4 * gq + 3]};
                }
                asm volatile("s_waitcnt lgkmcnt(0)" ::: "memory");
                unsigned arr = 0;
                if (ln == 0) arr = LDS_CNT_ADD(X_CNT);
                last = __builtin_amdgcn_readfirstlane(arr) == 7;
                if (last) {
                    __builtin_amdgcn_fence(__ATOMIC_ACQUIRE, "wavefront");
#pragma unroll
                    for (int i = 0; i < 4; ++i) {
                        const int qd = ln + 64 * i;
                        f32x4 s = {0.f, 0.f, 0.f, 0.f};
#pragma unroll
                        for (int p = 0; p < 8; ++p) s += *(LDS3 const f32x4*)(lds + X_SLOT + p * 4096 + qd * 16);
                        if (qd < 240) ST16(__builtin_bit_cast(u32x4, s), WS_KVP + (head * 8 + qb) * 4096 + qd * 16);
                    }
                }
            } else if (hc == 6) {
                if (last) { DRAIN(); if (ln == 0) set_flag(ws, WS_F_KV + (head * 8 + qb) * 8, tag); }
            } else if (hc == 9) {
                const unsigned fo = WS_F_KV + (head * 8 + (ln & 7)) * 8;
                glds4s(fo, ws, lds0 + 4096 + w * 512); glds4s(fo + 4, ws, lds0 + 4096 + w * 512 + 256);
            } else if (hc == 10) {
                DRAIN();
                {
                    LDS3 const unsigned* f = (LDS3 const unsigned*)(lds + 4096 + w * 512) + ln;
                    const unsigned fo = WS_F_KV + (head * 8 + (ln & 7)) * 8;
                    if (!__all(f[0] == (unsigned)tag && f[64] == (unsigned)(tag >> 32))) wait_flags(ws, fo, fo, tag);
                }
                const unsigned o = WS_KVP + (head * 8 + 4 * h_) * 4096 + (w * 30 + (r_ < 30 ? r_ : 29)) * 16;
#pragma unroll
                for (int i = 0; i < 4; ++i) glds16s(o + i * 4096, ws, lds0 + slot + i * 1024);
            } else if (hc == 11) {
                DRAIN();
                LDS3 const char* sp = lds + slot + ln * 16;
                const f32x4 s4 = (*(LDS3 const f32x4*)sp + *(LDS3 const f32x4*)(sp + 1024)) + (*(LDS3 const f32x4*)(sp + 2048) + *(LDS3 const f32x4*)(sp + 3072));
                const int qd = w * 30 + r_;
                const int gq = qd < 192 ? qd >> 6 : (qd - 192) >> 4, hs = qd < 192 ? (qd >> 5) & 1 : (qd >> 3) & 1, sg = qd < 192 ? qd & 31 : 32 + (qd & 7);
                const unsigned a = pk2(xhalf_sum(s4[0]), xhalf_sum(s4[1])), b = pk2(xhalf_sum(s4[2]), xhalf_sum(s4[3]));
                if (ln < 30) {
                    LDS3 u16* kp = (LDS3 u16*)(lds + X_KVF + (8 * gq + 4 * hs) * 96 + sg * 2);
                    kp[0] = (u16)a; kp[48] = (u16)(a >> 16); kp[96] = (u16)b; kp[144] = (u16)(b >> 16);
                }
                asm volatile("s_waitcnt lgkmcnt(0)" ::: "memory");
                if (ln == 0) LDS_CNT_ADD(X_CNT + 4);
                const int t4 = (tokS >> 5) & 3;
                const char* qbb = ws + WS_Q + (NH + head) * 81920 + (tokS >> 7) * 5120;
                glds16s(t4 * 1024 + ln * 16, qbb, lds0 + slot); glds4s(4096 + (t4 * 32 + r_) * 8 + 4 * h_, qbb, lds0 + slot + 1024);
                glds4s(WS_G + (h_ ? q0 + kh * 32 + r_ : tokS + r_) * 4, ws, lds0 + slot + 1280);
            } else if (hc == 12) {
                DRAIN();
                LDS3 const char* sp = lds + slot;
                const u32x4 eq = *(LDS3 const u32x4*)(sp + ln * 16);
                const unsigned eqt = *(LDS3 const unsigned*)(sp + 1024 + ln * 4);
                const float gS = *(LDS3 const float*)(sp + 1280 + r_ * 4);
                gO = *(LDS3 const float*)(sp + 1408 + r_ * 4);
                u32x4 fp, fn, ft, phm; unsigned pht;
                phi_frag(r_, h_, phm, pht);
                feature_map(phm, pht, eq, eqt, h_, fp, fn, ft);
                lds_wait(lds + X_CNT + 4, 8);
                LDS3 const char* kp = lds + X_KVF + (r_ < 24 ? r_ : 23) * 96 + h_ * 16;
                f32x16 d = MFMA32_16(*(LDS3 const u32x4*)kp, fp, z16);
                d = MFMA32_16(*(LDS3 const u32x4*)(kp + 32), fn, d);
                d = MFMA32_16(*(LDS3 const u32x4*)(kp + 64), ft, d);
                const float den = upper_half(d[8]) + 1e-6f;
                float o[12];
#pragma unroll
                for (int i = 0; i < 12; ++i) o[i] = d[i];
                store_o(rs, o, (1.f - gS) / den, tokS + r_, 640 + head * 20, h_);
            } else if (hc == 13) {
                DRAIN();
                unsigned arr = 0;
                if (ln == 0) arr = LDS_CNT_ADD(X_CNT + 8);
                if (__builtin_amdgcn_readfirstlane(arr) == 7 && ln == 0) set_flag(ws, WS_F_OB + (qb * 32 + head) * 8, tag);
            } else if (hc == 14) {
                lds_wait(lds + X_CNT + 8, 8);
                const char* wsrc = ws + WS_WT + (size_t)ntO * 5 * 40960;
#pragma unroll 1
                for (int t = 0; t < 13; ++t) {
                    int pc = w + 8 * t; if (pc > 97) pc = 97;
                    const int n = pc / 20, ks = pc - n * 20;
                    glds16s(ln * 16 + (n * 40 + ks) * 1024, wsrc, lds0 + L_WA + pc * 1024);
                }
            }
            const int h1 = (hc + 1) & 15;
            const unsigned cb = (h1 >> 1) * 20480;
            const unsigned nK = laneK + cb + (h1 & 1) * 2048, nT = laneT + cb + (h1 & 1) * 512, nV = laneV + cb + (h1 & 1) * 2560;
            asm volatile("s_waitcnt lgkmcnt(0)" ::: "memory");
            SBAR();
            TILE(v0a, v0b, aK, 1024, aT, 256, 640,  aV, 1280);
            TILE(v0b, v0a, nK, 0,    nT, 0,   1920, nV, 0);
            aK = nK; aT = nT; aV = nV;
        }
        oB = MFMA32_16(v1, pb, oB);
#undef TILE
#undef EX4
        STAMP(6);
        {
            float chk = 0.f;
#pragma unroll
            for (int i = 0; i < 12; ++i) chk += fabsf(oA[i]) * 0.f + fabsf(oB[i]) * 0.f;
            bool bad = !(chk == 0.f);
            if (h) bad = bad || !(oA[8] >= 0x1p-60f && oA[8] <= 0x1p60f && oB[8] >= 0x1p-60f && oB[8] <= 0x1p60f);
            if (__builtin_expect(__any(bad), 0)) {
                mA = -INFINITY; mB = -INFINITY;
#pragma unroll
                for (int i = 0; i < 16; ++i) { oA[i] = 0.f; oB[i] = 0.f; }
                const u32x2 a0 = {qtA, 0u}, b0 = {qtB, 0u};
#pragma unroll 1
                for (int t = 0; t < 32; ++t) {
                    const unsigned kb = WS_KV + head * 163840 + (2 * (t >> 2) + kh) * 10240, t4 = t & 3;
                    const u32x4 k0 = LD16(kb + t4 * 1024 + h * 512 + r * 16);
                    const u32x2 t0 = {LD4(kb + 4096 + (t4 * 32 + r) * 8 + 4 * h), 0u};
                    u32x4 w0 = LD16(kb + 5120 + t4 * 1280 + h * 320 + (r < 20 ? r : 19) * 16), w1 = LD16(kb + 5120 + t4 * 1280 + 640 + h * 320 + (r < 20 ? r : 19) * 16);
                    if (r >= 20) { w0 = (u32x4){one, one, one, one}; w1 = w0; }
                    f32x16 s0 = MFMA32_16(k0, qA, z16); s0 = MFMA32_8(t0, a0, s0);
                    f32x16 s1 = MFMA32_16(k0, qB, z16); s1 = MFMA32_8(t0, b0, s1);
                    float x0 = s0[0], x1 = s1[0];
#pragma unroll
                    for (int i = 1; i < 16; ++i) { x0 = fmaxf(x0, s0[i]); x1 = fmaxf(x1, s1[i]); }
                    x0 = fmaxf(xhalf_max(x0), mA); x1 = fmaxf(xhalf_max(x1), mB);
                    const float g0 = (mA == x0) ? 1.f : ex2(mA - x0), g1 = (mB == x1) ? 1.f : ex2(mB - x1);
                    mA = x0; mB = x1;
#pragma unroll
                    for (int i = 0; i < 16; ++i) { oA[i] *= g0; oB[i] *= g1; }
#pragma unroll
                    for (int i = 0; i < 4; ++i) { pa[i] = pk2(ex2(s0[2 * i] - x0), ex2(s0[2 * i + 1] - x0)); pb[i] = pk2(ex2(s0[8 + 2 * i] - x0), ex2(s0[9 + 2 * i] - x0)); }
                    oA = MFMA32_16(w0, pa, oA); oA = MFMA32_16(w1, pb, oA);
#pragma unroll
                    for (int i = 0; i < 4; ++i) { pa[i] = pk2(ex2(s1[2 * i] - x1), ex2(s1[2 * i + 1] - x1)); pb[i] = pk2(ex2(s1[8 + 2 * i] - x1), ex2(s1[9 + 2 * i] - x1)); }
                    oB = MFMA32_16(w0, pa, oB); oB = MFMA32_16(w1, pb, oB);
                }
            }
        }
        {
            LDS3 char* xp = lds + 256 + w * 2816;
            const f32x16& s = kh ? oA : oB;
            *(LDS3 f32x4*)(xp + lane * 16) = (f32x4){s[0], s[1], s[2], s[3]};
            *(LDS3 f32x4*)(xp + 1024 + lane * 16) = (f32x4){s[4], s[5], s[6], s[7]};
            if (!h) *(LDS3 f32x4*)(xp + 2048 + r * 16) = (f32x4){s[8], s[9], s[10], s[11]};
            else *(LDS3 f32x2*)(xp + 2560 + r * 8) = (f32x2){s[8], kh ? mA : mB};
        }
        WAIT_BAR(63);
        if (w < 2) glds16s(lane * 16 + (4 * 40 + 18 + w) * 1024, ws + WS_WT + (size_t)ntO * 5 * 40960, lds0 + L_WA + (98 + w) * 1024);
        {
            LDS3 const char* xp = lds + 256 + (w ^ 4) * 2816;
            const f32x4 p0 = *(LDS3 const f32x4*)(xp + lane * 16), p1 = *(LDS3 const f32x4*)(xp + 1024 + lane * 16), p2 = *(LDS3 const f32x4*)(xp + 2048 + r * 16);
            const f32x2 lm = *(LDS3 const f32x2*)(xp + 2560 + r * 8);
            const f32x16& s = kh ? oB : oA;
            const float mo = kh ? mB : mA, mp = lm[1];
            const float mx = fmaxf(mo, mp);
            const float fo = (mo == mx) ? 1.f : ex2(mo - mx), fq = (mp == mx) ? 1.f : ex2(mp - mx);
            float o[12];
#pragma unroll
            for (int i = 0; i < 4; ++i) { o[i] = s[i] * fo + p0[i] * fq; o[4 + i] = s[4 + i] * fo + p1[i] * fq; o[8 + i] = s[8 + i] * fo + p2[i] * fq; }
            const float l = upper_half(s[8]) * fo + lm[0] * fq;
            store_o(rs, o, gO / l, q0 + kh * 32 + r, head * 20, h);
        }
    }
    STAMP(7);

    {
        const int c16 = lane & 15, g = lane >> 4;
        const int mt = mtO, nt = ntO;
        const int mh = (w >> 1) & 1, kg = (w & 1) + 2 * (w >> 2), tt = w & 3;
        f32x4 acc[2][5];
#pragma unroll
        for (int a = 0; a < 2; ++a)
#pragma unroll
            for (int b = 0; b < 5; ++b) acc[a][b] = (f32x4){0.f, 0.f, 0.f, 0.f};
        u32x4 bf[5][5];
        asm volatile("" ::: "memory");
        {
            const unsigned bb = WS_WT + ((nt * 5) * 40 + 20 + kg * 5) * 1024 + lane * 16;
#pragma unroll
            for (int ks = 0; ks < 5; ++ks)
#pragma unroll
                for (int n = 0; n < 5; ++n) bf[ks][n] = LD16(bb + n * 40960 + ks * 1024);
        }
        asm volatile("s_waitcnt vmcnt(25) lgkmcnt(0)\n\ts_barrier" ::: "memory");
        if (tid == 0) set_flag(ws, WS_F_OA + (qb * 32 + head) * 8, tag);
        wait_flags(ws, WS_F_OB + ((mt >> 2) * 32 + r) * 8, WS_F_OB + ((mt >> 2) * 32 + r) * 8, tag);
        {
            const unsigned ab = WS_AO + ((mt * 4 + mh * 2) * 40 + 20 + kg * 5) * 1024 + lane * 16;
            u32x4 af[5][2];
#pragma unroll
            for (int ks = 0; ks < 5; ++ks)
#pragma unroll
                for (int t = 0; t < 2; ++t) af[ks][t] = LD16(ab + t * 40960 + ks * 1024);
#pragma unroll
            for (int ks = 0; ks < 5; ++ks)
#pragma unroll
                for (int t = 0; t < 2; ++t)
#pragma unroll
                    for (int n = 0; n < 5; ++n) acc[t][n] = MFMA16_32(bf[ks][n], af[ks][t], acc[t][n]);
        }
        const int XO = w < 4 ? 0 : 143360 - 20480;
        f32x4 c[5];
#pragma unroll
        for (int n = 0; n < 5; ++n) {
            *(LDS3 f32x4*)(lds + XO + w * 5120 + n * 1024 + lane * 16) = (tt & 1) ? acc[0][n] : acc[1][n];
            c[n] = (tt & 1) ? acc[1][n] : acc[0][n];
        }
        WAIT_BAR(0);
#pragma unroll
        for (int n = 0; n < 5; ++n) c[n] += *(LDS3 const f32x4*)(lds + XO + (w ^ 1) * 5120 + n * 1024 + lane * 16);
        STAMP(8);
        wait_flags(ws, WS_F_OA + ((mt >> 2) * 32 + r) * 8, WS_F_OA + ((mt >> 2) * 32 + r) * 8, tag);
        STAMP(9);
        {
            const int k0 = (w >> 2) * 10;
            const unsigned ab = WS_AO + ((mt * 4 + tt) * 40 + k0) * 1024 + lane * 16;
            u32x4 af[10];
#pragma unroll
            for (int ks = 0; ks < 10; ++ks) af[ks] = LD16(ab + ks * 1024);
#pragma unroll
            for (int ks = 0; ks < 10; ++ks)
#pragma unroll
                for (int n = 0; n < 5; ++n) c[n] = MFMA16_32(*(LDS3 const u32x4*)(lds + L_WA + (n * 20 + k0 + ks) * 1024 + lane * 16), af[ks], c[n]);
        }
        {
            LDS3 char* xp = lds + 126976 + tt * 4096 + lane * 16;
            LDS3 char* xq = lds + 20480 + tt * 1024 + lane * 16;
            if (w < 4) { *(LDS3 f32x4*)(xp + 3072) = c[3]; *(LDS3 f32x4*)(xq) = c[4]; }
            else { *(LDS3 f32x4*)(xp) = c[0]; *(LDS3 f32x4*)(xp + 1024) = c[1]; *(LDS3 f32x4*)(xp + 2048) = c[2]; }
            WAIT_BAR(63);
            u16* op = P.out + (size_t)(mt * 64 + tt * 16 + c16) * DM + nt * 80 + g * 4;
#pragma unroll
            for (int n = 0; n < 5; ++n) {
                if ((w < 4) == (n < 3)) {
                    const f32x4 s = c[n] + *(LDS3 const f32x4*)(n < 4 ? xp + n * 1024 : xq);
                    *(u32x2*)(op + n * 16) = (u32x2){pk2(s[0], s[1]), pk2(s[2], s[3])};
                }
            }
        }
        if (bid == 0 && tid == 0) set_flag(ws, WS_EPOCH, tag);
    }
    STAMP(10);
}

extern "C" void kernel_launch(void* const* d_in, const int* in_sizes, int n_in, void* d_out, int out_size, void* d_ws, size_t ws_size,
                              hipStream_t stream) {
    Params P;
    P.x = (const u16*)d_in[0]; P.wa = (const u16*)d_in[1]; P.woa = (const u16*)d_in[2]; P.wb = (const u16*)d_in[4];
    P.wphi = (const u16*)d_in[5]; P.wob = (const u16*)d_in[6]; P.gw = (const u16*)d_in[7]; P.ws = (char*)d_ws; P.out = (u16*)d_out;
    mega<<<256, 512, 0, stream>>>(P);
}
```

```cpp
#include <hip/hip_runtime.h>
#include <hip/hip_bf16.h>
#include <stdint.h>
#include <type_traits>

typedef __attribute__((ext_vector_type(8))) short bf16x8;
typedef __attribute__((ext_vector_type(4))) short bf16x4;
typedef __attribute__((ext_vector_type(16))) float f32x16;
typedef __attribute__((ext_vector_type(4))) float f32x4;
typedef __attribute__((ext_vector_type(2))) float f32x2;
typedef __attribute__((ext_vector_type(4))) unsigned u32x4;
typedef __attribute__((ext_vector_type(2))) unsigned u32x2;
typedef __attribute__((ext_vector_type(2))) __bf16 bf2_t;
typedef unsigned short u16;
typedef unsigned long long u64;
#define LDS3 __attribute__((address_space(3)))
typedef __attribute__((address_space(1))) u64 gu64;
#define RLX_AGENT __ATOMIC_RELAXED, __HIP_MEMORY_SCOPE_AGENT

constexpr int SEQ = 2048, DM = 640, NH = 32;
constexpr float LOG2E = 1.4426950408889634f;
constexpr float QSCALE = 0.22360679774997896f * LOG2E;

constexpr unsigned WS_EPOCH = 0;
constexpr unsigned WS_F_G   = 256;
constexpr unsigned WS_F_SD  = WS_F_G + 4096;
constexpr unsigned WS_F_KV  = WS_F_SD + 256;
constexpr unsigned WS_F_OB  = WS_F_KV + 2048;
constexpr unsigned WS_F_OA  = WS_F_OB + 2048;
constexpr unsigned WS_G     = 16384;
constexpr unsigned WS_Q     = 32768;
constexpr unsigned WS_KV    = WS_Q + 2 * NH * 81920;
constexpr unsigned WS_KVP   = WS_KV + 2 * NH * 163840;
constexpr unsigned WS_AO    = WS_KVP + NH * 8 * 4096;
constexpr unsigned WS_WT    = WS_AO + SEQ * 1280 * 2;
constexpr unsigned WS_END   = WS_WT + 1280 * 640 * 2;

#define STAMP(i)

__device__ __forceinline__ unsigned pk2(float a, float b) { f32x2 v = {a, b}; bf2_t r = __builtin_convertvector(v, bf2_t); return __builtin_bit_cast(unsigned, r); }
__device__ __forceinline__ float bf_lo(unsigned u) { return __uint_as_float(u << 16); }
__device__ __forceinline__ float bf_hi(unsigned u) { return __uint_as_float(u & 0xffff0000u); }
__device__ __forceinline__ float ex2(float x) { return __builtin_amdgcn_exp2f(x); }
#define MFMA32_16(a, b, c) __builtin_amdgcn_mfma_f32_32x32x16_bf16(__builtin_bit_cast(bf16x8, a), __builtin_bit_cast(bf16x8, b), c, 0, 0, 0)
#define MFMA32_8(a, b, c) __builtin_amdgcn_mfma_f32_32x32x8bf16_1k(__builtin_bit_cast(bf16x4, a), __builtin_bit_cast(bf16x4, b), c, 0, 0, 0)
#define MFMA16_32(a, b, c) __builtin_amdgcn_mfma_f32_16x16x32_bf16(__builtin_bit_cast(bf16x8, a), __builtin_bit_cast(bf16x8, b), c, 0, 0, 0)

__device__ __forceinline__ void glds16(unsigned voff, const void* sbase, unsigned lds_base) {
    unsigned sv;
    asm volatile("s_mov_b32 %0, m0\n\ts_mov_b32 m0, %3\n\ts_nop 0\n\tglobal_load_lds_dwordx4 %1, %2\n\ts_mov_b32 m0, %0"
                 : "=&s"(sv) : "v"(voff), "s"(sbase), "s"(__builtin_amdgcn_readfirstlane(lds_base)) : "memory");
}
__device__ __forceinline__ void glds16s(unsigned voff, const void* sbase, unsigned lds_base) {
    unsigned sv;
    asm volatile("s_mov_b32 %0, m0\n\ts_mov_b32 m0, %3\n\ts_nop 0\n\tglobal_load_lds_dwordx4 %1, %2 sc1\n\ts_mov_b32 m0, %0"
                 : "=&s"(sv) : "v"(voff), "s"(sbase), "s"(__builtin_amdgcn_readfirstlane(lds_base)) : "memory");
}
__device__ __forceinline__ void glds4s(unsigned voff, const void* sbase, unsigned lds_base) {
    unsigned sv;
    asm volatile("s_mov_b32 %0, m0\n\ts_mov_b32 m0, %3\n\ts_nop 0\n\tglobal_load_lds_dword %1, %2 sc1\n\ts_mov_b32 m0, %0"
                 : "=&s"(sv) : "v"(voff), "s"(sbase), "s"(__builtin_amdgcn_readfirstlane(lds_base)) : "memory");
}
typedef short v4i16_t __attribute__((ext_vector_type(4)));
__device__ __forceinline__ u32x2 trread(LDS3 const char* p) {
    return __builtin_bit_cast(u32x2, __builtin_amdgcn_ds_read_tr16_b64_v4i16((LDS3 v4i16_t*)p));
}
#define WAIT_BAR(N) asm volatile("s_waitcnt vmcnt(" #N ") lgkmcnt(0)\n\ts_barrier" ::: "memory")
#define DRAIN() asm volatile("s_waitcnt vmcnt(0)" ::: "memory")
#define SBAR() __builtin_amdgcn_sched_barrier(0)
#define PIN(x) asm volatile("" : "+v"(x))

__device__ __forceinline__ float xhalf_max(float x) {
    auto r = __builtin_amdgcn_permlane32_swap(__float_as_uint(x), __float_as_uint(x), false, false);
    return fmaxf(__uint_as_float(r[0]), __uint_as_float(r[1]));
}
__device__ __forceinline__ float xhalf_sum(float x) {
    auto r = __builtin_amdgcn_permlane32_swap(__float_as_uint(x), __float_as_uint(x), false, false);
    return __uint_as_float(r[0]) + __uint_as_float(r[1]);
}
__device__ __forceinline__ float upper_half(float x) {
    auto r = __builtin_amdgcn_permlane32_swap(__float_as_uint(x), __float_as_uint(x), false, false);
    return __uint_as_float(r[1]);
}

typedef __amdgpu_buffer_rsrc_t rsrc_t;
#define ST8(v, off)  __builtin_amdgcn_raw_buffer_store_b64(v, rs, (int)(off), 0, 16)
#define ST16(v, off) __builtin_amdgcn_raw_buffer_store_b128(v, rs, (int)(off), 0, 16)
#define ST4(v, off)  __builtin_amdgcn_raw_buffer_store_b32(v, rs, (int)(off), 0, 16)
#define LD16(off) __builtin_amdgcn_raw_buffer_load_b128(rs, (int)(off), 0, 16)
#define LD4(off)  __builtin_amdgcn_raw_buffer_load_b32(rs, (int)(off), 0, 16)

__device__ __forceinline__ void wait_flags(const char* ws, unsigned off0, unsigned off1, u64 tag) {
    gu64* p0 = (gu64*)(ws + off0); gu64* p1 = (gu64*)(ws + off1);
#pragma unroll 1
    for (unsigned spins = 0; spins < (1u << 16); ++spins) {
        const u64 a = __hip_atomic_load(p0, RLX_AGENT), b = __hip_atomic_load(p1, RLX_AGENT);
        if (__all(a == tag && b == tag)) break;
        __builtin_amdgcn_s_sleep(2);
    }
    __builtin_amdgcn_fence(__ATOMIC_ACQUIRE, "wavefront");
}
__device__ __forceinline__ void set_flag(char* ws, unsigned off, u64 tag) { __hip_atomic_store((gu64*)(ws + off), tag, RLX_AGENT); }

struct Params { const u16* x; const u16* wa; const u16* wb; const u16* wphi; const u16* woa; const u16* wob; const u16* gw; char* ws; u16* out; };

__device__ __forceinline__ void feature_map(const u32x4& pm, unsigned pt, const u32x4& tm, unsigned tt, int h, u32x4& fp, u32x4& fn, u32x4& ft) {
    const f32x16 z16 = {};
    const u32x2 a8 = {pt, 0u}, b8 = {tt, 0u};
    f32x16 s = MFMA32_16(pm, tm, z16); s = MFMA32_8(a8, b8, s);
    float v[12];
#pragma unroll
    for (int i = 0; i < 12; i += 2) { const unsigned u = pk2(s[i], s[i + 1]); v[i] = bf_lo(u) * LOG2E; v[i + 1] = bf_hi(u) * LOG2E; }
    float mx = v[0], mn = v[0];
#pragma unroll
    for (int i = 1; i < 8; ++i) { mx = fmaxf(mx, v[i]); mn = fminf(mn, v[i]); }
    const float tx = fmaxf(fmaxf(v[8], v[9]), fmaxf(v[10], v[11])), tn = fminf(fminf(v[8], v[9]), fminf(v[10], v[11]));
    if (!h) { mx = fmaxf(mx, tx); mn = fminf(mn, tn); }
    mx = xhalf_max(mx); mn = -xhalf_max(-mn);
    float ep[12], en[12], sp = 0.f, sn = 0.f;
#pragma unroll
    for (int i = 0; i < 12; ++i) {
        ep[i] = ex2(v[i] - mx); en[i] = ex2(mn - v[i]);
        if (i >= 8 && h) { ep[i] = 0.f; en[i] = 0.f; }
        sp += ep[i]; sn += en[i];
    }
    sp = __builtin_amdgcn_rcpf(xhalf_sum(sp)); sn = __builtin_amdgcn_rcpf(xhalf_sum(sn));
#pragma unroll
    for (int i = 0; i < 4; ++i) { fp[i] = pk2(ep[2 * i] * sp, ep[2 * i + 1] * sp); fn[i] = pk2(en[2 * i] * sn, en[2 * i + 1] * sn); }
    ft = (u32x4){pk2(ep[8] * sp, ep[9] * sp), pk2(ep[10] * sp, ep[11] * sp), pk2(en[8] * sn, en[9] * sn), pk2(en[10] * sn, en[11] * sn)};
}

__device__ __forceinline__ void store_o(rsrc_t rs, const float (&o)[12], float sc, int tok, int kbase, int h) {
#pragma unroll
    for (int gq = 0; gq < 3; ++gq) {
        const int d = 8 * gq + 4 * h;
        if (d < 20) {
            const int k = kbase + d;
            u32x2 ov = {pk2(o[4 * gq] * sc, o[4 * gq + 1] * sc), pk2(o[4 * gq + 2] * sc, o[4 * gq + 3] * sc)};
            ST8(ov, WS_AO + (((tok >> 4) * 40 + (k >> 5)) * 64 + ((k >> 3) & 3) * 16 + (tok & 15)) * 16 + (k & 7) * 2);
        }
    }
}

#define DSR128(dst, addr, off) asm volatile("ds_read_b128 %0, %1 offset:%2" : "=v"(dst) : "v"(addr), "i"(off))
#define DSR32(dst, addr, off) asm volatile("ds_read_b32 %0, %1 offset:%2" : "=v"(dst) : "v"(addr), "i"(off))
#define DSR128M2(d0, a0, o0, d1, a1, o1) asm volatile("s_mov_b64 exec, %6\n\tds_read_b128 %0, %2 offset:%3\n\tds_read_b128 %1, %4 offset:%5\n\ts_mov_b64 exec, -1" \
                                                      : "+v"(d0), "+v"(d1) : "v"(a0), "i"(o0), "v"(a1), "i"(o1), "s"(vmask))
#define LWAIT(n) do { asm volatile("s_waitcnt lgkmcnt(" #n ")" ::: "memory"); SBAR(); } while (0)

#define BAR() asm volatile("s_barrier" ::: "memory")
#define LDS_CNT_ADD(off) __hip_atomic_fetch_add((LDS3 unsigned*)(lds + (off)), 1u, __ATOMIC_RELAXED, __HIP_MEMORY_SCOPE_WORKGROUP)

__device__ __forceinline__ void lds_wait(LDS3 char* p, unsigned want) {
#pragma unroll 1
    for (unsigned spins = 0; spins < (1u << 20); ++spins) {
        if ((unsigned)__builtin_amdgcn_readfirstlane(__hip_atomic_load((LDS3 unsigned*)p, __ATOMIC_RELAXED, __HIP_MEMORY_SCOPE_WORKGROUP)) >= want) break;
        __builtin_amdgcn_s_sleep(1);
    }
    __builtin_amdgcn_fence(__ATOMIC_ACQUIRE, "wavefront");
}

constexpr int C_KV = 0, C_KVF = 4, C_OB = 8, C_PROG = 12, C_WA = 16, C_OA = 20, C_MRG = 32  , C_FIN = 64  , C_BP = 96  ;
constexpr int X_KVF = 1024, X_SLOT = 4096;
constexpr int X_MRG = 1024;
constexpr int L_WA = 24576;
constexpr int L_Q = 65536;
constexpr int X_FIN = 126976;

__global__ __launch_bounds__(768) void mega(Params P) {
    __shared__ __attribute__((aligned(16))) char smem[163840];
    LDS3 char* lds = (LDS3 char*)smem;
    char* ws = P.ws;
    const int tid = threadIdx.x, lane = tid & 63, r = lane & 31, h = lane >> 5;
    const int w = __builtin_amdgcn_readfirstlane(tid >> 6);
    const bool helper = w >= 8;
    const int hw = w - 8;
    const int bid = blockIdx.x;
    const unsigned lds0 = (unsigned)(uintptr_t)smem;
    const rsrc_t rs = __builtin_amdgcn_make_buffer_rsrc(ws, 0, WS_END, 0x00020000);
    STAMP(0);
    u64 tag = 0;
#define GET_TAG() do { asm volatile("s_load_dwordx2 %0, %1, 0x0\n\ts_waitcnt lgkmcnt(0)" : "=s"(tag) : "s"(ws) : "memory"); tag += 1; } while (0)
    const int head = (bid & 7) * 4 + ((bid >> 3) & 3), qb = bid >> 5;
    const int nlo = (head * 20) >> 7, nhi = (head * 20 + 19) >> 7;
    const int mtO = (bid & 7) * 4 + ((bid >> 3) & 3), ntO = bid >> 5;
    const int lin = (bid & 7) * 30 + (bid >> 3), lh = lin >= 120, lrem = lin - 120 * lh;
    const int mt = lh * 8 + (lrem & 7), nt = lrem >> 3;
    const unsigned fG = WS_F_G + (mt * 15 + nt) * 8;
    const unsigned one = (r == 20) ? 0x3f803f80u : 0u;
    const f32x16 z16 = {};

    if (helper) {
        const unsigned fA0 = WS_F_G + ((lane & 15) * 15 + (lane < 32 ? 5 : 10) + ((lane & 16) ? nhi : nlo)) * 8;
        const unsigned fA1 = WS_F_G + ((qb * 2 + (lane & 1)) * 15 + ((lane & 2) ? nhi : nlo)) * 8;
        auto kv_issue = [&](int grp) {
            const char* kvb = ws + WS_KV + head * 163840;
#pragma unroll
            for (int t = 0; t < 8; ++t) { const unsigned pc = (grp * 32 + hw + 4 * t) * 1024; glds16s(lane * 16 + pc, kvb, lds0 + pc); }
        };
        auto q_issue = [&]() {
            const char* qbase = ws + WS_Q + head * 81920 + qb * 10240;
#pragma unroll
            for (int t = 0; t < 3; ++t) { const unsigned pc = (hw + 4 * t < 10 ? hw + 4 * t : 9) * 1024; glds16s(lane * 16 + pc, qbase, lds0 + L_Q + pc); }
        };
        if (bid < 240) {
            const int rot = (nt * 3 + mt * 5 + (mt >> 1)) % 10;
            unsigned xo[4], wo[4];
#pragma unroll
            for (int i = 0; i < 4; ++i) {
                const int s = (hw + 4 * i) * 64 + lane;
                { const int row = s >> 3, ch = (s & 7) ^ ((row >> 1) & 7); xo[i] = (mt * 128 + row) * 1280 + ch * 16; }
                { const int row = s >> 4, ch = (s & 15) ^ (((row & 3) << 2) | ((row >> 2) & 3)); wo[i] = row * 3840 + nt * 256 + ch * 16; }
            }
            const unsigned dX = lds0 + 32768 + hw * 1024, dW = dX + 16384;
            auto issue = [&](int s) {
                int kt = (s >= 10 ? s - 10 : s) + rot; if (kt >= 10) kt -= 10;
                const char* xb = (const char*)P.x + kt * 128;
                const char* wb = (const char*)(s >= 10 ? P.wb : P.wa) + (size_t)kt * (64 * 3840);
                const unsigned sl = (s & 3) * 32768;
#pragma unroll
                for (int i = 0; i < 4; ++i) { glds16(xo[i], xb, dX + sl + i * 4096); glds16(wo[i], wb, dW + sl + i * 4096); }
            };
            issue(0); issue(1); issue(2); issue(3);
            GET_TAG();
            WAIT_BAR(24);
#pragma unroll 1
            for (int s = 0; s < 16; ++s) {
                WAIT_BAR(16);
                if (s == 12 && tid == 512) set_flag(ws, fG, tag);
                if (s == 15 && hw == 0) {
                    glds4s(fA0, ws, lds0); glds4s(fA0 + 4, ws, lds0 + 256); glds4s(fA1, ws, lds0 + 512); glds4s(fA1 + 4, ws, lds0 + 768);
                }
                issue(s + 4);
            }
            WAIT_BAR(16);
            asm volatile("s_waitcnt vmcnt(8)" ::: "memory");
            if (hw == 0) {
                LDS3 const unsigned* f = (LDS3 const unsigned*)lds + lane;
                const unsigned tl = (unsigned)tag, th = (unsigned)(tag >> 32);
                if (!__all(f[0] == tl && f[64] == th && f[128] == tl && f[192] == th)) wait_flags(ws, fA0, fA1, tag);
            }
            WAIT_BAR(63);
            kv_issue(0); kv_issue(1); q_issue();
            WAIT_BAR(19);
            kv_issue(3);
            WAIT_BAR(8);
            kv_issue(4);
        } else {
            GET_TAG();
            WAIT_BAR(0);
            if (tid == 512) set_flag(ws, WS_F_SD + (bid - 240) * 8, tag);
            if (hw == 0) wait_flags(ws, fA0, fA1, tag);
            WAIT_BAR(0);
            kv_issue(0); kv_issue(1); q_issue(); kv_issue(3); kv_issue(4);
            WAIT_BAR(16);
        }
        WAIT_BAR(63);
        kv_issue(2);
        if (hw == 0) *(LDS3 u32x4*)(lds + lane * 16) = (u32x4){0u, 0u, 0u, 0u};
        u32x4 phm; unsigned pht;
        {
            const u16* wp = P.wphi + head * 400 + (r < 20 ? r : 0);
            unsigned e[10];
#pragma unroll
            for (int j = 0; j < 8; ++j) e[j] = wp[(8 * h + j) * 20];
            e[8] = wp[(16 + 2 * h) * 20]; e[9] = wp[(17 + 2 * h) * 20];
            phm = (u32x4){e[0] | (e[1] << 16), e[2] | (e[3] << 16), e[4] | (e[5] << 16), e[6] | (e[7] << 16)}; pht = e[8] | (e[9] << 16);
            if (r >= 20) { phm = (u32x4){0u, 0u, 0u, 0u}; pht = 0u; }
        }
        WAIT_BAR(0);
        if (tid == 512 && bid < 240) set_flag(ws, fG + 2048, tag);
        STAMP(3);
        if (hw < 3) *(LDS3 u32x4*)(lds + X_KVF + (hw * 64 + lane) * 16) = (u32x4){0u, 0u, 0u, 0u};
        const int tokS = qb * 256 + hw * 64, m7 = tokS >> 7, t4 = (tokS >> 5) & 3;
        {
            const unsigned tb = WS_F_G + 2048 + (m7 * 15 + ((lane & 1) ? nhi : nlo)) * 8;
            wait_flags(ws, tb + ((lane & 2) ? 40 : 80), lane < 16 ? WS_F_SD + lane * 8 : tb, tag);
        }
        const unsigned om = t4 * 1024 + lane * 16, ot = 4096 + (t4 * 32 + r) * 8 + 4 * h;
        u32x4 qf[2][3]; float gS[2];
        {
            const unsigned kb = WS_KV + (NH + head) * 163840 + (2 * (m7 & 7) + (m7 >> 3)) * 10240, qbb = WS_Q + (NH + head) * 81920 + m7 * 5120;
            const unsigned ov = 5120 + t4 * 1280 + h * 320 + (r < 20 ? r : 19) * 16;
            u32x4 e0[2], w0[2], w1[2], eq[2]; unsigned et[2], eqt[2];
#pragma unroll
            for (int i = 0; i < 2; ++i) {
                e0[i] = LD16(kb + om + i * 1024); et[i] = LD4(kb + ot + i * 256); w0[i] = LD16(kb + ov + i * 1280); w1[i] = LD16(kb + ov + i * 1280 + 640);
                eq[i] = LD16(qbb + om + i * 1024); eqt[i] = LD4(qbb + ot + i * 256); gS[i] = __uint_as_float(LD4(WS_G + (tokS + i * 32 + r) * 4));
            }
            u32x4 i0, i1;
#pragma unroll
            for (int j = 0; j < 4; ++j) {
                const int e = r - 8 * h - 2 * j;
                i0[j] = e == 0 ? 0x3f80u : (e == 1 ? 0x3f800000u : 0u);
                i1[j] = e == 16 ? 0x3f80u : (e == 17 ? 0x3f800000u : 0u);
            }
            f32x16 k0 = {}, k1 = {};
#pragma unroll
            for (int i = 0; i < 2; ++i) {
                if (r >= 20) { w0[i] = (u32x4){one, one, one, one}; w1[i] = w0[i]; }
                u32x4 fp, fn, ft;
                feature_map(phm, pht, e0[i], et[i], h, fp, fn, ft);
                f32x16 T0 = MFMA32_16(fp, i0, z16); T0 = MFMA32_16(fn, i1, T0);
                f32x16 T1 = MFMA32_16(ft, i0, z16);
                u32x4 b00, b01, b10, b11;
#pragma unroll
                for (int j = 0; j < 4; ++j) {
                    b00[j] = pk2(T0[2 * j], T0[2 * j + 1]); b01[j] = pk2(T0[8 + 2 * j], T0[9 + 2 * j]);
                    b10[j] = pk2(T1[2 * j], T1[2 * j + 1]); b11[j] = pk2(T1[8 + 2 * j], T1[9 + 2 * j]);
                }
                k0 = MFMA32_16(w0[i], b00, k0); k0 = MFMA32_16(w1[i], b01, k0);
                k1 = MFMA32_16(w0[i], b10, k1); k1 = MFMA32_16(w1[i], b11, k1);
            }
            LDS3 char* sp = lds + X_SLOT + hw * 4096;
#pragma unroll
            for (int gq = 0; gq < 3; ++gq) {
                *(LDS3 f32x4*)(sp + gq * 1024 + lane * 16) = (f32x4){k0[4 * gq], k0[4 * gq + 1], k0[4 * gq + 2], k0[4 * gq + 3]};
                if (r < 8) *(LDS3 f32x4*)(sp + 3072 + (gq * 16 + h * 8 + r) * 16) = (f32x4){k1[4 * gq], k1[4 * gq + 1], k1[4 * gq + 2], k1[4 * gq + 3]};
            }
            asm volatile("s_waitcnt lgkmcnt(0)" ::: "memory");
            unsigned arr = 0;
            if (lane == 0) arr = LDS_CNT_ADD(C_KV);
            if (__builtin_amdgcn_readfirstlane(arr) == 3) {
                __builtin_amdgcn_fence(__ATOMIC_ACQUIRE, "wavefront");
#pragma unroll
                for (int i = 0; i < 4; ++i) {
                    const int qd = lane + 64 * i;
                    LDS3 const char* s0 = lds + X_SLOT + qd * 16;
                    const f32x4 s = (*(LDS3 const f32x4*)s0 + *(LDS3 const f32x4*)(s0 + 4096)) + (*(LDS3 const f32x4*)(s0 + 8192) + *(LDS3 const f32x4*)(s0 + 12288));
                    if (qd < 240) ST16(__builtin_bit_cast(u32x4, s), WS_KVP + (head * 8 + qb) * 4096 + qd * 16);
                }
                DRAIN();
                if (lane == 0) set_flag(ws, WS_F_KV + (head * 8 + qb) * 8, tag);
            }
#pragma unroll
            for (int i = 0; i < 2; ++i) feature_map(phm, pht, eq[i], eqt[i], h, qf[i][0], qf[i][1], qf[i][2]);
        }
        STAMP(4);
        wait_flags(ws, WS_F_KV + (head * 8 + (lane & 7)) * 8, WS_F_KV + (head * 8 + (lane & 7)) * 8, tag);
        {
            const int qd = hw * 60 + (lane < 60 ? lane : 59);
            f32x4 pp[8];
#pragma unroll
            for (int p = 0; p < 8; ++p) pp[p] = __builtin_bit_cast(f32x4, LD16(WS_KVP + (head * 8 + p) * 4096 + qd * 16));
            const f32x4 s4 = ((pp[0] + pp[1]) + (pp[2] + pp[3])) + ((pp[4] + pp[5]) + (pp[6] + pp[7]));
            const int gq = qd < 192 ? qd >> 6 : (qd - 192) >> 4, hs = qd < 192 ? (qd >> 5) & 1 : (qd >> 3) & 1, sg = qd < 192 ? qd & 31 : 32 + (qd & 7);
            const unsigned a = pk2(s4[0], s4[1]), b = pk2(s4[2], s4[3]);
            LDS3 u16* kp = (LDS3 u16*)(lds + X_KVF + (8 * gq + 4 * hs) * 96 + sg * 2);
            kp[0] = (u16)a; kp[48] = (u16)(a >> 16); kp[96] = (u16)b; kp[144] = (u16)(b >> 16);
            asm volatile("s_waitcnt lgkmcnt(0)" ::: "memory");
            if (lane == 0) LDS_CNT_ADD(C_KVF);
            lds_wait(lds + C_KVF, 4);
        }
        {
            LDS3 const char* kp = lds + X_KVF + (r < 24 ? r : 23) * 96 + h * 16;
            const u32x4 a0 = *(LDS3 const u32x4*)kp, a1 = *(LDS3 const u32x4*)(kp + 32), a2 = *(LDS3 const u32x4*)(kp + 64);
#pragma unroll
            for (int i = 0; i < 2; ++i) {
                f32x16 d = MFMA32_16(a0, qf[i][0], z16); d = MFMA32_16(a1, qf[i][1], d); d = MFMA32_16(a2, qf[i][2], d);
                const float den = upper_half(d[8]) + 1e-6f;
                float o[12];
#pragma unroll
                for (int j = 0; j < 12; ++j) o[j] = d[j];
                store_o(rs, o, (1.f - gS[i]) / den, tokS + i * 32 + r, 640 + head * 20, h);
            }
            DRAIN();
            unsigned arr = 0;
            if (lane == 0) arr = LDS_CNT_ADD(C_OB);
            if (__builtin_amdgcn_readfirstlane(arr) == 3 && lane == 0) set_flag(ws, WS_F_OB + (qb * 32 + head) * 8, tag);
        }
        STAMP(5);
        f32x4 c[5];
#pragma unroll
        for (int n = 0; n < 5; ++n) c[n] = (f32x4){0.f, 0.f, 0.f, 0.f};
        wait_flags(ws, WS_F_OB + ((mtO >> 2) * 32 + r) * 8, WS_F_OB + ((mtO >> 2) * 32 + r) * 8, tag);
        {
            const unsigned ab = WS_AO + ((mtO * 4 + hw) * 40 + 20) * 1024 + lane * 16, bb = WS_WT + ((ntO * 5) * 40 + 20) * 1024 + lane * 16;
#pragma unroll 1
            for (int kq = 0; kq < 5; ++kq) {
                u32x4 af[4], bf[4][5];
#pragma unroll
                for (int ks = 0; ks < 4; ++ks) {
                    af[ks] = LD16(ab + (kq * 4 + ks) * 1024);
#pragma unroll
                    for (int n = 0; n < 5; ++n) bf[ks][n] = LD16(bb + n * 40960 + (kq * 4 + ks) * 1024);
                }
#pragma unroll
                for (int ks = 0; ks < 4; ++ks)
#pragma unroll
                    for (int n = 0; n < 5; ++n) c[n] = MFMA16_32(bf[ks][n], af[ks], c[n]);
            }
        }
        STAMP(6);
        lds_wait(lds + C_PROG, 8);
        lds_wait(lds + C_OB, 4);
        {
            const char* wsrc = ws + WS_WT + (size_t)ntO * 5 * 40960;
#pragma unroll 1
            for (int n = 0; n < 5; ++n)
#pragma unroll
                for (int t = 0; t < 5; ++t) { const int ks = hw + 4 * t; glds16s(lane * 16 + (n * 40 + ks) * 1024, wsrc, lds0 + L_WA + (n * 20 + ks) * 1024); }
#pragma unroll
            for (int n = 0; n < 3; ++n) *(LDS3 f32x4*)(lds + X_FIN + hw * 3072 + n * 1024 + lane * 16) = c[n];
            DRAIN();
            asm volatile("s_waitcnt lgkmcnt(0)" ::: "memory");
            if (lane == 0) LDS_CNT_ADD(C_WA);
            lds_wait(lds + C_OA, 8);
#pragma unroll
            for (int n = 3; n < 5; ++n) *(LDS3 f32x4*)(lds + 143360 + hw * 2048 + (n - 3) * 1024 + lane * 16) = c[n];
            asm volatile("s_waitcnt lgkmcnt(0)" ::: "memory");
            if (lane == 0) __hip_atomic_store((LDS3 unsigned*)(lds + C_BP + hw * 4), 1u, __ATOMIC_RELAXED, __HIP_MEMORY_SCOPE_WORKGROUP);
        }
        STAMP(7);
    } else {
        const int qs = w & 3, kh = w >> 2;
        const int q0 = qb * 256 + qs * 64;
        GET_TAG();
        if (bid < 240) {
            const int part = nt / 5, c0 = (nt - part * 5) * 128;
            const int wm = w & 1, wn = w >> 1;
            constexpr int RB = 32768;
            const int xr = wm * 64 + r;
            const unsigned xa = RB + xr * 128 + ((h ^ ((xr >> 1) & 7)) << 4);
            unsigned wt[2];
#pragma unroll
            for (int t = 0; t < 2; ++t) {
                const int q = (lane & 15) >> 2, p = lane & 3, blk = (lane >> 4) & 1, row = 8 * h + 4 * t + q;
                wt[t] = RB + 16384 + row * 256 + (((4 * wn + 2 * blk + (p >> 1)) ^ ((q << 2) | (2 * h + t))) << 4) + 8 * (p & 1);
            }
            auto gbody = [&](auto trc) {
                constexpr bool TR = decltype(trc)::value;
                u32x4 xf[2][8], wf[2][4];
                auto rd = [&](auto bc, int s) {
                    constexpr int B = decltype(bc)::value;
                    LDS3 const char* sb = lds + (s & 3) * 32768;
#pragma unroll
                    for (int ks = 0; ks < 4; ++ks) {
                        xf[B][2 * ks] = *(LDS3 const u32x4*)(sb + (xa ^ (ks * 32)));
                        xf[B][2 * ks + 1] = *(LDS3 const u32x4*)(sb + ((xa + 4096) ^ (ks * 32)));
                        const u32x2 lo = trread(sb + wt[0] + ks * 4096), hi = trread(sb + wt[1] + ks * 4096);
                        wf[B][ks] = (u32x4){lo[0], lo[1], hi[0], hi[1]};
                    }
                };
                f32x16 acc0, acc1;
                auto step = [&](auto bc, int s) {
                    constexpr int B = decltype(bc)::value;
                    if (s == 12) { WAIT_BAR(0); } else { WAIT_BAR(63); }
                    rd(std::integral_constant<int, B ^ 1>{}, s < 19 ? s + 1 : 18);
#pragma unroll
                    for (int ks = 0; ks < 4; ++ks) {
                        if constexpr (TR) { acc0 = MFMA32_16(wf[B][ks], xf[B][2 * ks], acc0); acc1 = MFMA32_16(wf[B][ks], xf[B][2 * ks + 1], acc1); }
                        else              { acc0 = MFMA32_16(xf[B][2 * ks], wf[B][ks], acc0); acc1 = MFMA32_16(xf[B][2 * ks + 1], wf[B][ks], acc1); }
                    }
                };
                WAIT_BAR(63);
                rd(std::integral_constant<int, 0>{}, 0);
#pragma unroll 1
                for (int mat = 0; mat < 2; ++mat) {
                    acc0 = (f32x16){}; acc1 = (f32x16){};
#pragma unroll 1
                    for (int kk = 0; kk < 10; kk += 2) {
                        step(std::integral_constant<int, 0>{}, mat * 10 + kk);
                        step(std::integral_constant<int, 1>{}, mat * 10 + kk + 1);
                    }
                    const int cpos = 2 * (mt & 7) + (mt >> 3);
                    if constexpr (TR) {
                        const float sc = (part == 0 && mat == 0) ? QSCALE : 1.f;
                        const unsigned base = part == 0 ? WS_Q + mat * NH * 81920 + mt * 5120 : WS_KV + mat * NH * 163840 + cpos * 10240;
                        const unsigned hs = part == 0 ? 81920 : 163840;
#pragma unroll
                        for (int gq = 0; gq < 4; ++gq) {
                            const int n = c0 + 32 * wn + 8 * gq + 4 * h, hd = n / 20, d = n - hd * 20;
#pragma unroll
                            for (int i = 0; i < 2; ++i) {
                                const f32x16& a = i ? acc1 : acc0;
                                const int tl = wm * 64 + i * 32 + r;
                                const unsigned off = base + hd * hs + (d < 16 ? (tl >> 5) * 1024 + (d >> 3) * 512 + r * 16 + (d & 4) * 2 : 4096 + tl * 8);
                                u32x2 ov = {pk2(a[4 * gq] * sc, a[4 * gq + 1] * sc), pk2(a[4 * gq + 2] * sc, a[4 * gq + 3] * sc)};
                                ST8(ov, off);
                            }
                        }
                    } else {
                        const int n = c0 + 32 * wn + r, hd = n / 20, d = n - hd * 20;
                        const unsigned base = WS_KV + (mat * NH + hd) * 163840 + cpos * 10240 + 5120 + h * 320 + d * 16;
#pragma unroll
                        for (int i = 0; i < 2; ++i)
#pragma unroll
                            for (int s2 = 0; s2 < 2; ++s2) {
                                const f32x16& a = i ? acc1 : acc0;
                                u32x4 ov = {pk2(a[8 * s2], a[8 * s2 + 1]), pk2(a[8 * s2 + 2], a[8 * s2 + 3]), pk2(a[8 * s2 + 4], a[8 * s2 + 5]), pk2(a[8 * s2 + 6], a[8 * s2 + 7])};
                                ST16(ov, base + (wm * 4 + i * 2 + s2) * 640);
                            }
                    }
                    if (mat == 0) STAMP(1);
                }
            };
            if (part < 2) gbody(std::true_type{}); else gbody(std::false_type{});
        } else {
            const int sj = bid - 240;
            {
                const u32x4 ga = *(const u32x4*)(P.gw + lane * 8), gb = *(const u32x4*)(P.gw + 512 + (lane & 15) * 8);
                u32x4 xa[16], xb[4];
                const u16* xr = P.x + (size_t)(sj * 128 + w * 16) * DM;
#pragma unroll
                for (int rr = 0; rr < 16; ++rr) xa[rr] = *(const u32x4*)(xr + rr * DM + lane * 8);
#pragma unroll
                for (int i = 0; i < 4; ++i) xb[i] = *(const u32x4*)(xr + (i * 4 + (lane >> 4)) * DM + 512 + (lane & 15) * 8);
                float res = 0.f;
#pragma unroll
                for (int rr = 0; rr < 16; ++rr) {
                    float s = 0.f, s2 = 0.f;
#pragma unroll
                    for (int j = 0; j < 4; ++j) { s += bf_lo(xa[rr][j]) * bf_lo(ga[j]) + bf_hi(xa[rr][j]) * bf_hi(ga[j]); s2 += bf_lo(xb[rr >> 2][j]) * bf_lo(gb[j]) + bf_hi(xb[rr >> 2][j]) * bf_hi(gb[j]); }
                    if ((lane >> 4) == (rr & 3)) s += s2;
#pragma unroll
                    for (int o = 32; o >= 1; o >>= 1) s += __shfl_xor(s, o);
                    if (lane == rr) res = s;
                }
                if (lane < 16) ST4(__float_as_uint(1.f / (1.f + __expf(-res))), WS_G + (sj * 128 + w * 16 + lane) * 4);
            }
#pragma unroll 1
            for (int it = 0; it < 2; ++it) {
                unsigned e[4][8];
#pragma unroll
                for (int q = 0; q < 4; ++q) {
                    int item = tid + 512 * (it * 4 + q); if (item >= 3200) item = 3199;
                    const int u = item / 320, n2 = item - u * 320, k0 = (sj * 10 + u) * 8;
                    const u16* wsrc = (k0 < 640 ? P.woa + (size_t)k0 * DM : P.wob + (size_t)(k0 - 640) * DM) + n2 * 2;
#pragma unroll
                    for (int j = 0; j < 8; ++j) e[q][j] = *(const unsigned*)(wsrc + (size_t)j * DM);
                }
#pragma unroll
                for (int q = 0; q < 4; ++q) {
                    int item = tid + 512 * (it * 4 + q); if (item >= 3200) item = 3199;
                    const int u = item / 320, n = (item - u * 320) * 2, k0 = (sj * 10 + u) * 8;
                    const unsigned* f = e[q];
                    u32x4 lo = {(f[0] & 0xffffu) | (f[1] << 16), (f[2] & 0xffffu) | (f[3] << 16), (f[4] & 0xffffu) | (f[5] << 16), (f[6] & 0xffffu) | (f[7] << 16)};
                    u32x4 hi = {(f[0] >> 16) | (f[1] & 0xffff0000u), (f[2] >> 16) | (f[3] & 0xffff0000u), (f[4] >> 16) | (f[5] & 0xffff0000u), (f[6] >> 16) | (f[7] & 0xffff0000u)};
                    const unsigned dp = WS_WT + (((n >> 4) * 40 + (k0 >> 5)) * 64 + ((k0 >> 3) & 3) * 16 + (n & 15)) * 16;
                    ST16(lo, dp); ST16(hi, dp + 16);
                }
            }
            WAIT_BAR(0); WAIT_BAR(63); WAIT_BAR(63);
        }
        STAMP(2);
        const unsigned hb = lds0 + kh * 10240;
        const unsigned laneK = hb + lane * 16, laneT = hb + 4096 + r * 8 + h * 4, laneV = hb + 5120 + h * 320 + (r < 20 ? r : 19) * 16;
        const u64 vmask = 0x000fffff000fffffull;
        u32x4 v0a = {one, one, one, one}, v0b = v0a, v1 = v0a;
        u32x4 qA, qB, ka; unsigned qtA, qtB, kt;
        {
            LDS3 const char* qp = lds + L_Q + (qs >> 1) * 5120;
            const unsigned om = (qs & 1) * 2048 + lane * 16, ot = 4096 + ((qs & 1) * 64 + r) * 8 + 4 * h;
            qA = *(LDS3 const u32x4*)(qp + om); qB = *(LDS3 const u32x4*)(qp + om + 1024);
            qtA = *(LDS3 const unsigned*)(qp + ot); qtB = *(LDS3 const unsigned*)(qp + ot + 256);
            DSR128(ka, laneK, 0); DSR32(kt, laneT, 0); DSR128M2(v0a, laneV, 0, v1, laneV, 640);
            asm volatile("s_waitcnt lgkmcnt(0)\n\ts_barrier" : "+v"(ka), "+v"(kt), "+v"(v0a), "+v"(v1), "+v"(qA), "+v"(qB), "+v"(qtA), "+v"(qtB) :: "memory");
        }
        STAMP(5);
        float mA, mB; u32x2 qTA, qTB; f32x16 sA, sB;
        {
            const u32x2 t0 = {kt, 0x3f80u}, a0 = {qtA, 0u}, b0 = {qtB, 0u};
            const f32x16 s = MFMA32_16(ka, qA, z16);
            const f32x16 s0 = MFMA32_8(t0, a0, s);
            f32x16 s1 = MFMA32_16(ka, qB, z16); s1 = MFMA32_8(t0, b0, s1);
            float x0 = s0[0], x1 = s1[0];
#pragma unroll
            for (int i = 1; i < 16; ++i) { x0 = fmaxf(x0, s0[i]); x1 = fmaxf(x1, s1[i]); }
            x0 = -xhalf_max(x0); x1 = -xhalf_max(x1);
            const unsigned hh = pk2(x0, x1);
            const unsigned ll = pk2(x0 - bf_lo(hh), x1 - bf_hi(hh));
            mA = -(bf_lo(hh) + bf_lo(ll)); mB = -(bf_hi(hh) + bf_hi(ll));
            const unsigned sel = h ? ll : hh;
            qTA = (u32x2){qtA, sel & 0xffffu}; qTB = (u32x2){qtB, sel >> 16};
            sA = MFMA32_8(t0, qTA, s);
        }
        f32x16 oA = {}, oB = {};
        u32x4 pa, pb = {0u, 0u, 0u, 0u};
#define EX4(S, i, Pv, j) do { Pv[j] = pk2(ex2(S[i]), ex2(S[i + 1])); Pv[j + 1] = pk2(ex2(S[i + 2]), ex2(S[i + 3])); PIN(Pv); SBAR(); } while (0)
#define KT2 ((u32x2){kt, 0x3f80u})
#define TILE(V0C, V0N, aK1, oK1, aT1, oT1, oV1, aV0, oV0) do { \
            sB = MFMA32_16(ka, qB, z16); SBAR(); DSR128(ka, aK1, oK1); EX4(sA, 0, pa, 0); \
            oB = MFMA32_16(v1, pb, oB); SBAR(); DSR128M2(v1, aV, oV1, V0N, aV0, oV0); EX4(sA, 4, pa, 2); \
            sB = MFMA32_8(KT2, qTB, sB); SBAR(); DSR32(kt, aT1, oT1); EX4(sA, 8, pb, 0); \
            oA = MFMA32_16(V0C, pa, oA); SBAR(); EX4(sA, 12, pb, 2); \
            LWAIT(1); sA = MFMA32_16(ka, qA, z16); SBAR(); EX4(sB, 0, pa, 0); \
            oA = MFMA32_16(v1, pb, oA); SBAR(); EX4(sB, 4, pa, 2); \
            LWAIT(0); sA = MFMA32_8(KT2, qTA, sA); SBAR(); EX4(sB, 8, pb, 0); \
            oB = MFMA32_16(V0C, pa, oB); SBAR(); EX4(sB, 12, pb, 2); \
        } while (0)
        unsigned aK = laneK, aT = laneT, aV = laneV;
#pragma unroll 1
        for (int c = 0; c < 8; ++c) {
            if (c == 1) { WAIT_BAR(0); }
            else if (c == 7) { if (lane == 0) LDS_CNT_ADD(C_PROG); asm volatile("s_waitcnt lgkmcnt(0)" ::: "memory"); }
            const unsigned nx = (c < 7 ? c + 1 : 7) * 20480;
            const unsigned nK = laneK + nx, nT = laneT + nx, nV = laneV + nx;
            SBAR();
            TILE(v0a, v0b, aK, 1024, aT, 256, 640,  aV, 1280);
            TILE(v0b, v0a, aK, 2048, aT, 512, 1920, aV, 2560);
            TILE(v0a, v0b, aK, 3072, aT, 768, 3200, aV, 3840);
            TILE(v0b, v0a, nK, 0,    nT, 0,   4480, nV, 0);
            aK = nK; aT = nT; aV = nV;
        }
        oB = MFMA32_16(v1, pb, oB);
#undef TILE
#undef EX4
        STAMP(6);
        {
            float chk = 0.f;
#pragma unroll
            for (int i = 0; i < 12; ++i) chk += fabsf(oA[i]) * 0.f + fabsf(oB[i]) * 0.f;
            bool bad = !(chk == 0.f);
            if (h) bad = bad || !(oA[8] >= 0x1p-60f && oA[8] <= 0x1p60f && oB[8] >= 0x1p-60f && oB[8] <= 0x1p60f);
            if (__builtin_expect(__any(bad), 0)) {
                mA = -INFINITY; mB = -INFINITY;
#pragma unroll
                for (int i = 0; i < 16; ++i) { oA[i] = 0.f; oB[i] = 0.f; }
                const u32x2 a0 = {qtA, 0u}, b0 = {qtB, 0u};
#pragma unroll 1
                for (int t = 0; t < 32; ++t) {
                    const unsigned kb = WS_KV + head * 163840 + (2 * (t >> 2) + kh) * 10240, t4 = t & 3;
                    const u32x4 k0 = LD16(kb + t4 * 1024 + lane * 16);
                    const u32x2 t0 = {LD4(kb + 4096 + (t4 * 32 + r) * 8 + 4 * h), 0u};
                    u32x4 w0 = LD16(kb + 5120 + t4 * 1280 + h * 320 + (r < 20 ? r : 19) * 16), w1 = LD16(kb + 5120 + t4 * 1280 + 640 + h * 320 + (r < 20 ? r : 19) * 16);
                    if (r >= 20) { w0 = (u32x4){one, one, one, one}; w1 = w0; }
                    f32x16 s0 = MFMA32_16(k0, qA, z16); s0 = MFMA32_8(t0, a0, s0);
                    f32x16 s1 = MFMA32_16(k0, qB, z16); s1 = MFMA32_8(t0, b0, s1);
                    float x0 = s0[0], x1 = s1[0];
#pragma unroll
                    for (int i = 1; i < 16; ++i) { x0 = fmaxf(x0, s0[i]); x1 = fmaxf(x1, s1[i]); }
                    x0 = fmaxf(xhalf_max(x0), mA); x1 = fmaxf(xhalf_max(x1), mB);
                    const float g0 = (mA == x0) ? 1.f : ex2(mA - x0), g1 = (mB == x1) ? 1.f : ex2(mB - x1);
                    mA = x0; mB = x1;
#pragma unroll
                    for (int i = 0; i < 16; ++i) { oA[i] *= g0; oB[i] *= g1; }
#pragma unroll
                    for (int i = 0; i < 4; ++i) { pa[i] = pk2(ex2(s0[2 * i] - x0), ex2(s0[2 * i + 1] - x0)); pb[i] = pk2(ex2(s0[8 + 2 * i] - x0), ex2(s0[9 + 2 * i] - x0)); }
                    oA = MFMA32_16(w0, pa, oA); oA = MFMA32_16(w1, pb, oA);
#pragma unroll
                    for (int i = 0; i < 4; ++i) { pa[i] = pk2(ex2(s1[2 * i] - x1), ex2(s1[2 * i + 1] - x1)); pb[i] = pk2(ex2(s1[8 + 2 * i] - x1), ex2(s1[9 + 2 * i] - x1)); }
                    oB = MFMA32_16(w0, pa, oB); oB = MFMA32_16(w1, pb, oB);
                }
            }
        }
        lds_wait(lds + C_OB, 4);
        lds_wait(lds + C_PROG, 8);
        const float gO = __uint_as_float(LD4(WS_G + (q0 + kh * 32 + r) * 4));
        {
            LDS3 char* xp = lds + X_MRG + w * 2816;
            const f32x16& s = kh ? oA : oB;
            *(LDS3 f32x4*)(xp + lane * 16) = (f32x4){s[0], s[1], s[2], s[3]};
            *(LDS3 f32x4*)(xp + 1024 + lane * 16) = (f32x4){s[4], s[5], s[6], s[7]};
            if (!h) *(LDS3 f32x4*)(xp + 2048 + r * 16) = (f32x4){s[8], s[9], s[10], s[11]};
            else *(LDS3 f32x2*)(xp + 2560 + r * 8) = (f32x2){s[8], kh ? mA : mB};
            asm volatile("s_waitcnt lgkmcnt(0)" ::: "memory");
            if (lane == 0) __hip_atomic_store((LDS3 unsigned*)(lds + C_MRG + w * 4), 1u, __ATOMIC_RELAXED, __HIP_MEMORY_SCOPE_WORKGROUP);
        }
        lds_wait(lds + C_MRG + (w ^ 4) * 4, 1);
        {
            LDS3 const char* xp = lds + X_MRG + (w ^ 4) * 2816;
            const f32x4 p0 = *(LDS3 const f32x4*)(xp + lane * 16), p1 = *(LDS3 const f32x4*)(xp + 1024 + lane * 16), p2 = *(LDS3 const f32x4*)(xp + 2048 + r * 16);
            const f32x2 lm = *(LDS3 const f32x2*)(xp + 2560 + r * 8);
            const f32x16& s = kh ? oB : oA;
            const float mo = kh ? mB : mA, mp = lm[1];
            const float mx = fmaxf(mo, mp);
            const float fo = (mo == mx) ? 1.f : ex2(mo - mx), fq = (mp == mx) ? 1.f : ex2(mp - mx);
            float o[12];
#pragma unroll
            for (int i = 0; i < 4; ++i) { o[i] = s[i] * fo + p0[i] * fq; o[4 + i] = s[4 + i] * fo + p1[i] * fq; o[8 + i] = s[8 + i] * fo + p2[i] * fq; }
            const float l = upper_half(s[8]) * fo + lm[0] * fq;
            store_o(rs, o, gO / l, q0 + kh * 32 + r, head * 20, h);
            DRAIN();
            unsigned arr = 0;
            if (lane == 0) arr = LDS_CNT_ADD(C_OA);
            if (__builtin_amdgcn_readfirstlane(arr) == 7 && lane == 0) set_flag(ws, WS_F_OA + (qb * 32 + head) * 8, tag);
        }
        STAMP(7);
        {
            const int c16 = lane & 15, g = lane >> 4, tt = w & 3, k0 = kh * 10;
            wait_flags(ws, WS_F_OA + ((mtO >> 2) * 32 + r) * 8, WS_F_OA + ((mtO >> 2) * 32 + r) * 8, tag);
            STAMP(9);
            const unsigned ab = WS_AO + ((mtO * 4 + tt) * 40 + k0) * 1024 + lane * 16;
            u32x4 af[10];
#pragma unroll
            for (int ks = 0; ks < 10; ++ks) af[ks] = LD16(ab + ks * 1024);
            lds_wait(lds + C_WA, 4);
            lds_wait(lds + C_OA, 8);
            f32x4 c[5];
#pragma unroll
            for (int n = 0; n < 5; ++n) c[n] = (f32x4){0.f, 0.f, 0.f, 0.f};
#pragma unroll
            for (int ks = 0; ks < 10; ++ks)
#pragma unroll
                for (int n = 0; n < 5; ++n) c[n] = MFMA16_32(*(LDS3 const u32x4*)(lds + L_WA + (n * 20 + k0 + ks) * 1024 + lane * 16), af[ks], c[n]);
            LDS3 char* xp = lds + 1024 + tt * 5120 + lane * 16;
            if (w < 4) { *(LDS3 f32x4*)(xp + 3072) = c[3]; *(LDS3 f32x4*)(xp + 4096) = c[4]; }
            else { *(LDS3 f32x4*)(xp) = c[0]; *(LDS3 f32x4*)(xp + 1024) = c[1]; *(LDS3 f32x4*)(xp + 2048) = c[2]; }
            asm volatile("s_waitcnt lgkmcnt(0)" ::: "memory");
            if (lane == 0) __hip_atomic_store((LDS3 unsigned*)(lds + C_FIN + w * 4), 1u, __ATOMIC_RELAXED, __HIP_MEMORY_SCOPE_WORKGROUP);
            lds_wait(lds + C_FIN + (w ^ 4) * 4, 1);
            lds_wait(lds + C_BP + tt * 4, 1);
            u16* op = P.out + (size_t)(mtO * 64 + tt * 16 + c16) * DM + ntO * 80 + g * 4;
#pragma unroll
            for (int n = 0; n < 5; ++n) {
                if ((w < 4) == (n < 3)) {
                    const f32x4 bp = *(LDS3 const f32x4*)(n < 3 ? lds + X_FIN + tt * 3072 + n * 1024 + lane * 16 : lds + 143360 + tt * 2048 + (n - 3) * 1024 + lane * 16);
                    const f32x4 s = c[n] + *(LDS3 const f32x4*)(xp + n * 1024) + bp;
                    *(u32x2*)(op + n * 16) = (u32x2){pk2(s[0], s[1]), pk2(s[2], s[3])};
                }
            }
            if (bid == 0 && tid == 0) set_flag(ws, WS_EPOCH, tag);
        }
        STAMP(10);
    }
}

extern "C" void kernel_launch(void* const* d_in, const int* in_sizes, int n_in, void* d_out, int out_size, void* d_ws, size_t ws_size,
                              hipStream_t stream) {
    Params P;
    P.x = (const u16*)d_in[0]; P.wa = (const u16*)d_in[1]; P.woa = (const u16*)d_in[2]; P.wb = (const u16*)d_in[4];
    P.wphi = (const u16*)d_in[5]; P.wob = (const u16*)d_in[6]; P.gw = (const u16*)d_in[7]; P.ws = (char*)d_ws; P.out = (u16*)d_out;
    mega<<<256, 768, 0, stream>>>(P);
}
```

```cpp
#include <hip/hip_runtime.h>
#include <hip/hip_bf16.h>
#include <stdint.h>
#include <type_traits>

typedef __attribute__((ext_vector_type(8))) short bf16x8;
typedef __attribute__((ext_vector_type(4))) short bf16x4;
typedef __attribute__((ext_vector_type(16))) float f32x16;
typedef __attribute__((ext_vector_type(4))) float f32x4;
typedef __attribute__((ext_vector_type(2))) float f32x2;
typedef __attribute__((ext_vector_type(4))) unsigned u32x4;
typedef __attribute__((ext_vector_type(2))) unsigned u32x2;
typedef __attribute__((ext_vector_type(2))) __bf16 bf2_t;
typedef unsigned short u16;
typedef unsigned long long u64;
#define LDS3 __attribute__((address_space(3)))
typedef __attribute__((address_space(1))) u64 gu64;
#define RLX_AGENT __ATOMIC_RELAXED, __HIP_MEMORY_SCOPE_AGENT

constexpr int SEQ = 2048, DM = 640, NH = 32;
constexpr float LOG2E = 1.4426950408889634f;
constexpr float QSCALE = 0.22360679774997896f * LOG2E;

constexpr unsigned WS_EPOCH = 0;
constexpr unsigned WS_F_G   = 256;
constexpr unsigned WS_F_SD  = WS_F_G + 4096;
constexpr unsigned WS_F_KV  = WS_F_SD + 256;
constexpr unsigned WS_F_OB  = WS_F_KV + 2048;
constexpr unsigned WS_F_OA  = WS_F_OB + 2048;
constexpr unsigned WS_G     = 16384;
constexpr unsigned WS_Q     = 32768;
constexpr unsigned WS_KV    = WS_Q + 2 * NH * 81920;
constexpr unsigned WS_KVP   = WS_KV + 2 * NH * 163840;
constexpr unsigned WS_AO    = WS_KVP + NH * 8 * 4096;
constexpr unsigned WS_WT    = WS_AO + SEQ * 1280 * 2;
constexpr unsigned WS_END   = WS_WT + 1280 * 640 * 2;

#define STAMP(i)

__device__ __forceinline__ unsigned pk2(float a, float b) { f32x2 v = {a, b}; bf2_t r = __builtin_convertvector(v, bf2_t); return __builtin_bit_cast(unsigned, r); }
__device__ __forceinline__ float bf_lo(unsigned u) { return __uint_as_float(u << 16); }
__device__ __forceinline__ float bf_hi(unsigned u) { return __uint_as_float(u & 0xffff0000u); }
__device__ __forceinline__ float ex2(float x) { return __builtin_amdgcn_exp2f(x); }
#define MFMA32_16(a, b, c) __builtin_amdgcn_mfma_f32_32x32x16_bf16(__builtin_bit_cast(bf16x8, a), __builtin_bit_cast(bf16x8, b), c, 0, 0, 0)
#define MFMA32_8(a, b, c) __builtin_amdgcn_mfma_f32_32x32x8bf16_1k(__builtin_bit_cast(bf16x4, a), __builtin_bit_cast(bf16x4, b), c, 0, 0, 0)
#define MFMA16_32(a, b, c) __builtin_amdgcn_mfma_f32_16x16x32_bf16(__builtin_bit_cast(bf16x8, a), __builtin_bit_cast(bf16x8, b), c, 0, 0, 0)

__device__ __forceinline__ void glds16(unsigned voff, const void* sbase, unsigned lds_base) {
    unsigned sv;
    asm volatile("s_mov_b32 %0, m0\n\ts_mov_b32 m0, %3\n\ts_nop 0\n\tglobal_load_lds_dwordx4 %1, %2\n\ts_mov_b32 m0, %0"
                 : "=&s"(sv) : "v"(voff), "s"(sbase), "s"(__builtin_amdgcn_readfirstlane(lds_base)) : "memory");
}
__device__ __forceinline__ void glds16s(unsigned voff, const void* sbase, unsigned lds_base) {
    unsigned sv;
    asm volatile("s_mov_b32 %0, m0\n\ts_mov_b32 m0, %3\n\ts_nop 0\n\tglobal_load_lds_dwordx4 %1, %2 sc1\n\ts_mov_b32 m0, %0"
                 : "=&s"(sv) : "v"(voff), "s"(sbase), "s"(__builtin_amdgcn_readfirstlane(lds_base)) : "memory");
}
__device__ __forceinline__ void glds4s(unsigned voff, const void* sbase, unsigned lds_base) {
    unsigned sv;
    asm volatile("s_mov_b32 %0, m0\n\ts_mov_b32 m0, %3\n\ts_nop 0\n\tglobal_load_lds_dword %1, %2 sc1\n\ts_mov_b32 m0, %0"
                 : "=&s"(sv) : "v"(voff), "s"(sbase), "s"(__builtin_amdgcn_readfirstlane(lds_base)) : "memory");
}
typedef short v4i16_t __attribute__((ext_vector_type(4)));
__device__ __forceinline__ u32x2 trread(LDS3 const char* p) {
    return __builtin_bit_cast(u32x2, __builtin_amdgcn_ds_read_tr16_b64_v4i16((LDS3 v4i16_t*)p));
}
#define WAIT_BAR(N) asm volatile("s_waitcnt vmcnt(" #N ") lgkmcnt(0)\n\ts_barrier" ::: "memory")
#define DRAIN() asm volatile("s_waitcnt vmcnt(0)" ::: "memory")
#define SBAR() __builtin_amdgcn_sched_barrier(0)
#define PIN(x) asm volatile("" : "+v"(x))

__device__ __forceinline__ float xhalf_max(float x) {
    auto r = __builtin_amdgcn_permlane32_swap(__float_as_uint(x), __float_as_uint(x), false, false);
    return fmaxf(__uint_as_float(r[0]), __uint_as_float(r[1]));
}
__device__ __forceinline__ float xhalf_sum(float x) {
    auto r = __builtin_amdgcn_permlane32_swap(__float_as_uint(x), __float_as_uint(x), false, false);
    return __uint_as_float(r[0]) + __uint_as_float(r[1]);
}
__device__ __forceinline__ float upper_half(float x) {
    auto r = __builtin_amdgcn_permlane32_swap(__float_as_uint(x), __float_as_uint(x), false, false);
    return __uint_as_float(r[1]);
}

typedef __amdgpu_buffer_rsrc_t rsrc_t;
#define ST8(v, off)  __builtin_amdgcn_raw_buffer_store_b64(v, rs, (int)(off), 0, 16)
#define ST16(v, off) __builtin_amdgcn_raw_buffer_store_b128(v, rs, (int)(off), 0, 16)
#define ST4(v, off)  __builtin_amdgcn_raw_buffer_store_b32(v, rs, (int)(off), 0, 16)
#define LD16(off) __builtin_amdgcn_raw_buffer_load_b128(rs, (int)(off), 0, 16)
#define LD4(off)  __builtin_amdgcn_raw_buffer_load_b32(rs, (int)(off), 0, 16)

__device__ __forceinline__ void wait_flags(const char* ws, unsigned off0, unsigned off1, u64 tag) {
    gu64* p0 = (gu64*)(ws + off0); gu64* p1 = (gu64*)(ws + off1);
#pragma unroll 1
    for (unsigned spins = 0; spins < (1u << 16); ++spins) {
        const u64 a = __hip_atomic_load(p0, RLX_AGENT), b = __hip_atomic_load(p1, RLX_AGENT);
        if (__all(a == tag && b == tag)) break;
        __builtin_amdgcn_s_sleep(2);
    }
    __builtin_amdgcn_fence(__ATOMIC_ACQUIRE, "wavefront");
}
__device__ __forceinline__ void set_flag(char* ws, unsigned off, u64 tag) { __hip_atomic_store((gu64*)(ws + off), tag, RLX_AGENT); }

struct Params { const u16* x; const u16* wa; const u16* wb; const u16* wphi; const u16* woa; const u16* wob; const u16* gw; char* ws; u16* out; };

__device__ __forceinline__ void feature_map(const u32x4& pm, unsigned pt, const u32x4& tm, unsigned tt, int h, u32x4& fp, u32x4& fn, u32x4& ft) {
    const f32x16 z16 = {};
    const u32x2 a8 = {pt, 0u}, b8 = {tt, 0u};
    f32x16 s = MFMA32_16(pm, tm, z16); s = MFMA32_8(a8, b8, s);
    float v[12];
#pragma unroll
    for (int i = 0; i < 12; i += 2) { const unsigned u = pk2(s[i], s[i + 1]); v[i] = bf_lo(u) * LOG2E; v[i + 1] = bf_hi(u) * LOG2E; }
    float mx = v[0], mn = v[0];
#pragma unroll
    for (int i = 1; i < 8; ++i) { mx = fmaxf(mx, v[i]); mn = fminf(mn, v[i]); }
    const float tx = fmaxf(fmaxf(v[8], v[9]), fmaxf(v[10], v[11])), tn = fminf(fminf(v[8], v[9]), fminf(v[10], v[11]));
    if (!h) { mx = fmaxf(mx, tx); mn = fminf(mn, tn); }
    mx = xhalf_max(mx); mn = -xhalf_max(-mn);
    float ep[12], en[12], sp = 0.f, sn = 0.f;
#pragma unroll
    for (int i = 0; i < 12; ++i) {
        ep[i] = ex2(v[i] - mx); en[i] = ex2(mn - v[i]);
        if (i >= 8 && h) { ep[i] = 0.f; en[i] = 0.f; }
        sp += ep[i]; sn += en[i];
    }
    sp = __builtin_amdgcn_rcpf(xhalf_sum(sp)); sn = __builtin_amdgcn_rcpf(xhalf_sum(sn));
#pragma unroll
    for (int i = 0; i < 4; ++i) { fp[i] = pk2(ep[2 * i] * sp, ep[2 * i + 1] * sp); fn[i] = pk2(en[2 * i] * sn, en[2 * i + 1] * sn); }
    ft = (u32x4){pk2(ep[8] * sp, ep[9] * sp), pk2(ep[10] * sp, ep[11] * sp), pk2(en[8] * sn, en[9] * sn), pk2(en[10] * sn, en[11] * sn)};
}

__device__ __forceinline__ void store_o(rsrc_t rs, const float (&o)[12], float sc, int tok, int kbase, int h) {
#pragma unroll
    for (int gq = 0; gq < 3; ++gq) {
        const int d = 8 * gq + 4 * h;
        if (d < 20) {
            const int k = kbase + d;
            u32x2 ov = {pk2(o[4 * gq] * sc, o[4 * gq + 1] * sc), pk2(o[4 * gq + 2] * sc, o[4 * gq + 3] * sc)};
            ST8(ov, WS_AO + (((tok >> 4) * 40 + (k >> 5)) * 64 + ((k >> 3) & 3) * 16 + (tok & 15)) * 16 + (k & 7) * 2);
        }
    }
}

#define DSR128(dst, addr, off) asm volatile("ds_read_b128 %0, %1 offset:%2" : "=v"(dst) : "v"(addr), "i"(off))
#define DSR32(dst, addr, off) asm volatile("ds_read_b32 %0, %1 offset:%2" : "=v"(dst) : "v"(addr), "i"(off))
#define DSR128M2(d0, a0, o0, d1, a1, o1) asm volatile("s_mov_b64 exec, %6\n\tds_read_b128 %0, %2 offset:%3\n\tds_read_b128 %1, %4 offset:%5\n\ts_mov_b64 exec, -1" \
                                                      : "+v"(d0), "+v"(d1) : "v"(a0), "i"(o0), "v"(a1), "i"(o1), "s"(vmask))
#define LWAIT(n) do { asm volatile("s_waitcnt lgkmcnt(" #n ")" ::: "memory"); SBAR(); } while (0)

#define BAR() asm volatile("s_barrier" ::: "memory")
#define LDS_CNT_ADD(off) __hip_atomic_fetch_add((LDS3 unsigned*)(lds + (off)), 1u, __ATOMIC_RELAXED, __HIP_MEMORY_SCOPE_WORKGROUP)

__device__ __forceinline__ void lds_wait(LDS3 char* p, unsigned want) {
#pragma unroll 1
    for (unsigned spins = 0; spins < (1u << 20); ++spins) {
        if ((unsigned)__builtin_amdgcn_readfirstlane(__hip_atomic_load((LDS3 unsigned*)p, __ATOMIC_RELAXED, __HIP_MEMORY_SCOPE_WORKGROUP)) >= want) break;
        __builtin_amdgcn_s_sleep(1);
    }
    __builtin_amdgcn_fence(__ATOMIC_ACQUIRE, "wavefront");
}

constexpr int C_KV = 0, C_KVF = 4, C_OB = 8, C_PROG = 12, C_WA = 16, C_OA = 20, C_MRG = 32  , C_FIN = 64  , C_BP = 96  ;
constexpr int C_PROG2 = 24, C_BX = 28;
constexpr int G_W = 31744;
constexpr int G_FULL = G_W, G_FREE = G_W + 16, G_Q = G_W + 32, G_KV0 = G_W + 36, G_ST = G_W + 40;
constexpr int X_KVF = 1024, X_SLOT = 4096;
constexpr int X_MRG = 1024;
constexpr int L_WA = 24576;
constexpr int L_Q = 65536;
constexpr int X_FIN = 126976;

__global__ __launch_bounds__(768) void mega(Params P) {
    __shared__ __attribute__((aligned(16))) char smem[163840];
    LDS3 char* lds = (LDS3 char*)smem;
    char* ws = P.ws;
    const int tid = threadIdx.x, lane = tid & 63, r = lane & 31, h = lane >> 5;
    const int hw = __builtin_amdgcn_readfirstlane(tid >> 6);
    const bool helper = hw < 4;
    const int w = hw - 4, mtid = tid - 256;
    const int bid = blockIdx.x;
    const unsigned lds0 = (unsigned)(uintptr_t)smem;
    const rsrc_t rs = __builtin_amdgcn_make_buffer_rsrc(ws, 0, WS_END, 0x00020000);
    STAMP(0);
    u64 tag = 0;
#define GET_TAG() do { asm volatile("s_load_dwordx2 %0, %1, 0x0\n\ts_waitcnt lgkmcnt(0)" : "=s"(tag) : "s"(ws) : "memory"); tag += 1; } while (0)
    const int head = (bid & 7) * 4 + ((bid >> 3) & 3), qb = bid >> 5;
    const int nlo = (head * 20) >> 7, nhi = (head * 20 + 19) >> 7;
    const int mtO = (bid & 7) * 4 + ((bid >> 3) & 3), ntO = bid >> 5;
    const int lin = (bid & 7) * 30 + (bid >> 3), lh = lin >= 120, lrem = lin - 120 * lh;
    const int mt = lh * 8 + (lrem & 7), nt = lrem >> 3;
    const unsigned fG = WS_F_G + (mt * 15 + nt) * 8;
    const unsigned one = (r == 20) ? 0x3f803f80u : 0u;
    const f32x16 z16 = {};

    if (helper) {
        const unsigned fA0 = WS_F_G + ((lane & 15) * 15 + (lane < 32 ? 5 : 10) + ((lane & 16) ? nhi : nlo)) * 8;
        const unsigned fA1 = WS_F_G + ((qb * 2 + (lane & 1)) * 15 + ((lane & 2) ? nhi : nlo)) * 8;
        const char* kvb = ws + WS_KV + head * 163840;
        auto kv_issue = [&](int p0, int n) {
#pragma unroll 1
            for (int t = 0; t < n; ++t) glds16s(lane * 16, kvb + (p0 + t) * 1024, lds0 + (p0 + t) * 1024);
        };
        if (bid < 240) {
            const int rot = (nt * 3 + mt * 5 + (mt >> 1)) % 10;
            unsigned xo[2], wo[4];
#pragma unroll
            for (int i = 0; i < 2; ++i) xo[i] = (mt * 128 + (lane >> 3)) * 1280 + (((lane & 7) ^ ((4 * i + (lane >> 4)) & 7)) << 4);
#pragma unroll
            for (int i = 0; i < 4; ++i) wo[i] = (lane >> 4) * 3840 + nt * 256 + (((lane & 15) ^ (((lane >> 4) << 2) | i)) << 4);
            const unsigned dX = lds0 + 32768 + hw * 32768;
#pragma unroll 1
            for (int i = 0; i < 5; ++i) {
                const int s = hw + 4 * i;
                int kt = (s >= 10 ? s - 10 : s) + rot; if (kt >= 10) kt -= 10;
                const char* xb = (const char*)P.x + kt * 128;
                const char* wb = (const char*)(s >= 10 ? P.wb : P.wa) + (size_t)kt * (64 * 3840);
                if (i) lds_wait(lds + G_FREE + hw * 4, 8 * i);
#pragma unroll 1
                for (int p4 = 0; p4 < 4; ++p4) {
#pragma unroll
                    for (int j = 0; j < 4; ++j) {
                        const int pc = p4 * 4 + j;
                        glds16(xo[j & 1], xb + pc * 10240, dX + pc * 1024);
                        glds16(wo[j], wb + pc * 15360, dX + 16384 + pc * 1024);
                    }
                    if (i == 0 && p4 == 1) { if (hw == 0 && lane < 16) *(LDS3 unsigned*)(lds + G_W + lane * 4) = 0u; GET_TAG(); WAIT_BAR(63); }
                }
                DRAIN();
                if (lane == 0) __hip_atomic_store((LDS3 unsigned*)(lds + G_FULL + hw * 4), (unsigned)(i + 1), __ATOMIC_RELAXED, __HIP_MEMORY_SCOPE_WORKGROUP);
            }
            wait_flags(ws, fA0, fA1, tag);
            if (hw == 0) kv_issue(0, 31);
            lds_wait(lds + G_FREE + hw * 4, 40);
        } else {
            if (hw == 0 && lane < 16) *(LDS3 unsigned*)(lds + G_W + lane * 4) = 0u;
            GET_TAG();
            WAIT_BAR(63);
            wait_flags(ws, fA0, fA1, tag);
            if (hw == 0) kv_issue(0, 31);
        }
        if (hw == 1) {
            const char* qbase = ws + WS_Q + head * 81920 + qb * 10240;
#pragma unroll 1
            for (int t = 0; t < 10; ++t) glds16s(lane * 16, qbase + t * 1024, lds0 + L_Q + t * 1024);
            DRAIN();
            if (lane == 0) __hip_atomic_store((LDS3 unsigned*)(lds + G_Q), 1u, __ATOMIC_RELAXED, __HIP_MEMORY_SCOPE_WORKGROUP);
        } else {
            kv_issue(32 * (hw + 1), 32);
            if (hw == 0) {
                asm volatile("s_waitcnt vmcnt(24)" ::: "memory");
                if (lane == 0) __hip_atomic_store((LDS3 unsigned*)(lds + G_KV0), 1u, __ATOMIC_RELAXED, __HIP_MEMORY_SCOPE_WORKGROUP);
            }
        }
        WAIT_BAR(63);
        if (hw == 1) { kv_issue(31, 1); kv_issue(64, 32); }
        if (hw == 0) *(LDS3 u32x4*)(lds + lane * 16) = (u32x4){0u, 0u, 0u, 0u};
        u32x4 phm; unsigned pht;
        {
            const u16* wp = P.wphi + head * 400 + (r < 20 ? r : 0);
            unsigned e[10];
#pragma unroll
            for (int j = 0; j < 8; ++j) e[j] = wp[(8 * h + j) * 20];
            e[8] = wp[(16 + 2 * h) * 20]; e[9] = wp[(17 + 2 * h) * 20];
            phm = (u32x4){e[0] | (e[1] << 16), e[2] | (e[3] << 16), e[4] | (e[5] << 16), e[6] | (e[7] << 16)}; pht = e[8] | (e[9] << 16);
            if (r >= 20) { phm = (u32x4){0u, 0u, 0u, 0u}; pht = 0u; }
        }
        WAIT_BAR(0);
        if (tid == 0 && bid < 240) set_flag(ws, fG + 2048, tag);
        STAMP(3);
        if (hw < 3) *(LDS3 u32x4*)(lds + X_KVF + (hw * 64 + lane) * 16) = (u32x4){0u, 0u, 0u, 0u};
        const int tokS = qb * 256 + hw * 64, m7 = tokS >> 7, t4 = (tokS >> 5) & 3;
        {
            const unsigned tb = WS_F_G + 2048 + (m7 * 15 + ((lane & 1) ? nhi : nlo)) * 8;
            wait_flags(ws, tb + ((lane & 2) ? 40 : 80), lane < 16 ? WS_F_SD + lane * 8 : tb, tag);
        }
        const unsigned om = t4 * 1024 + lane * 16, ot = 4096 + (t4 * 32 + r) * 8 + 4 * h;
        u32x4 qf[2][3]; float gS[2];
        {
            const unsigned kb = WS_KV + (NH + head) * 163840 + (2 * (m7 & 7) + (m7 >> 3)) * 10240, qbb = WS_Q + (NH + head) * 81920 + m7 * 5120;
            const unsigned ov = 5120 + t4 * 1280 + h * 320 + (r < 20 ? r : 19) * 16;
            u32x4 e0[2], w0[2], w1[2], eq[2]; unsigned et[2], eqt[2];
#pragma unroll
            for (int i = 0; i < 2; ++i) {
                e0[i] = LD16(kb + om + i * 1024); et[i] = LD4(kb + ot + i * 256); w0[i] = LD16(kb + ov + i * 1280); w1[i] = LD16(kb + ov + i * 1280 + 640);
                eq[i] = LD16(qbb + om + i * 1024); eqt[i] = LD4(qbb + ot + i * 256); gS[i] = __uint_as_float(LD4(WS_G + (tokS + i * 32 + r) * 4));
            }
            u32x4 i0, i1;
#pragma unroll
            for (int j = 0; j < 4; ++j) {
                const int e = r - 8 * h - 2 * j;
                i0[j] = e == 0 ? 0x3f80u : (e == 1 ? 0x3f800000u : 0u);
                i1[j] = e == 16 ? 0x3f80u : (e == 17 ? 0x3f800000u : 0u);
            }
            f32x16 k0 = {}, k1 = {};
#pragma unroll
            for (int i = 0; i < 2; ++i) {
                if (r >= 20) { w0[i] = (u32x4){one, one, one, one}; w1[i] = w0[i]; }
                u32x4 fp, fn, ft;
                feature_map(phm, pht, e0[i], et[i], h, fp, fn, ft);
                f32x16 T0 = MFMA32_16(fp, i0, z16); T0 = MFMA32_16(fn, i1, T0);
                f32x16 T1 = MFMA32_16(ft, i0, z16);
                u32x4 b00, b01, b10, b11;
#pragma unroll
                for (int j = 0; j < 4; ++j) {
                    b00[j] = pk2(T0[2 * j], T0[2 * j + 1]); b01[j] = pk2(T0[8 + 2 * j], T0[9 + 2 * j]);
                    b10[j] = pk2(T1[2 * j], T1[2 * j + 1]); b11[j] = pk2(T1[8 + 2 * j], T1[9 + 2 * j]);
                }
                k0 = MFMA32_16(w0[i], b00, k0); k0 = MFMA32_16(w1[i], b01, k0);
                k1 = MFMA32_16(w0[i], b10, k1); k1 = MFMA32_16(w1[i], b11, k1);
            }
            LDS3 char* sp = lds + X_SLOT + hw * 4096;
#pragma unroll
            for (int gq = 0; gq < 3; ++gq) {
                *(LDS3 f32x4*)(sp + gq * 1024 + lane * 16) = (f32x4){k0[4 * gq], k0[4 * gq + 1], k0[4 * gq + 2], k0[4 * gq + 3]};
                if (r < 8) *(LDS3 f32x4*)(sp + 3072 + (gq * 16 + h * 8 + r) * 16) = (f32x4){k1[4 * gq], k1[4 * gq + 1], k1[4 * gq + 2], k1[4 * gq + 3]};
            }
            asm volatile("s_waitcnt lgkmcnt(0)" ::: "memory");
            unsigned arr = 0;
            if (lane == 0) arr = LDS_CNT_ADD(C_KV);
            if (__builtin_amdgcn_readfirstlane(arr) == 3) {
                __builtin_amdgcn_fence(__ATOMIC_ACQUIRE, "wavefront");
#pragma unroll
                for (int i = 0; i < 4; ++i) {
                    const int qd = lane + 64 * i;
                    LDS3 const char* s0 = lds + X_SLOT + qd * 16;
                    const f32x4 s = (*(LDS3 const f32x4*)s0 + *(LDS3 const f32x4*)(s0 + 4096)) + (*(LDS3 const f32x4*)(s0 + 8192) + *(LDS3 const f32x4*)(s0 + 12288));
                    if (qd < 240) ST16(__builtin_bit_cast(u32x4, s), WS_KVP + (head * 8 + qb) * 4096 + qd * 16);
                }
                DRAIN();
                if (lane == 0) set_flag(ws, WS_F_KV + (head * 8 + qb) * 8, tag);
            }
#pragma unroll
            for (int i = 0; i < 2; ++i) feature_map(phm, pht, eq[i], eqt[i], h, qf[i][0], qf[i][1], qf[i][2]);
        }
        STAMP(4);
        wait_flags(ws, WS_F_KV + (head * 8 + (lane & 7)) * 8, WS_F_KV + (head * 8 + (lane & 7)) * 8, tag);
        {
            const int qd = hw * 60 + (lane < 60 ? lane : 59);
            f32x4 pp[8];
#pragma unroll
            for (int p = 0; p < 8; ++p) pp[p] = __builtin_bit_cast(f32x4, LD16(WS_KVP + (head * 8 + p) * 4096 + qd * 16));
            const f32x4 s4 = ((pp[0] + pp[1]) + (pp[2] + pp[3])) + ((pp[4] + pp[5]) + (pp[6] + pp[7]));
            const int gq = qd < 192 ? qd >> 6 : (qd - 192) >> 4, hs = qd < 192 ? (qd >> 5) & 1 : (qd >> 3) & 1, sg = qd < 192 ? qd & 31 : 32 + (qd & 7);
            const unsigned a = pk2(s4[0], s4[1]), b = pk2(s4[2], s4[3]);
            LDS3 u16* kp = (LDS3 u16*)(lds + X_KVF + (8 * gq + 4 * hs) * 96 + sg * 2);
            kp[0] = (u16)a; kp[48] = (u16)(a >> 16); kp[96] = (u16)b; kp[144] = (u16)(b >> 16);
            asm volatile("s_waitcnt lgkmcnt(0)" ::: "memory");
            if (lane == 0) LDS_CNT_ADD(C_KVF);
            lds_wait(lds + C_KVF, 4);
        }
        {
            LDS3 const char* kp = lds + X_KVF + (r < 24 ? r : 23) * 96 + h * 16;
            const u32x4 a0 = *(LDS3 const u32x4*)kp, a1 = *(LDS3 const u32x4*)(kp + 32), a2 = *(LDS3 const u32x4*)(kp + 64);
#pragma unroll
            for (int i = 0; i < 2; ++i) {
                f32x16 d = MFMA32_16(a0, qf[i][0], z16); d = MFMA32_16(a1, qf[i][1], d); d = MFMA32_16(a2, qf[i][2], d);
                const float den = upper_half(d[8]) + 1e-6f;
                float o[12];
#pragma unroll
                for (int j = 0; j < 12; ++j) o[j] = d[j];
                store_o(rs, o, (1.f - gS[i]) / den, tokS + i * 32 + r, 640 + head * 20, h);
            }
            DRAIN();
            unsigned arr = 0;
            if (lane == 0) arr = LDS_CNT_ADD(C_OB);
            if (__builtin_amdgcn_readfirstlane(arr) == 3 && lane == 0) set_flag(ws, WS_F_OB + (qb * 32 + head) * 8, tag);
        }
        STAMP(5);
        f32x4 c[5];
        wait_flags(ws, WS_F_OB + ((mtO >> 2) * 32 + r) * 8, WS_F_OB + ((mtO >> 2) * 32 + r) * 8, tag);
        {
            f32x4 acc[4][5];
#pragma unroll
            for (int t = 0; t < 4; ++t)
#pragma unroll
                for (int n = 0; n < 5; ++n) acc[t][n] = (f32x4){0.f, 0.f, 0.f, 0.f};
            const unsigned ab = WS_AO + ((mtO * 4) * 40 + 20 + 5 * hw) * 1024 + lane * 16, bb = WS_WT + ((ntO * 5) * 40 + 20 + 5 * hw) * 1024 + lane * 16;
#pragma unroll
            for (int ks = 0; ks < 5; ++ks) {
                u32x4 af[4], bf[5];
#pragma unroll
                for (int t = 0; t < 4; ++t) af[t] = LD16(ab + ((t + hw) & 3) * 40960 + ks * 1024);
#pragma unroll
                for (int n = 0; n < 5; ++n) bf[n] = LD16(bb + n * 40960 + ks * 1024);
#pragma unroll
                for (int t = 0; t < 4; ++t)
#pragma unroll
                    for (int n = 0; n < 5; ++n) acc[t][n] = MFMA16_32(bf[n], af[t], acc[t][n]);
            }
            lds_wait(lds + C_PROG2, 8);
#pragma unroll
            for (int t = 1; t < 4; ++t)
#pragma unroll
                for (int n = 0; n < 5; ++n) *(LDS3 f32x4*)(lds + 24576 + (((t + hw) & 3) * 3 + t - 1) * 5120 + n * 1024 + lane * 16) = acc[t][n];
            asm volatile("s_waitcnt lgkmcnt(0)" ::: "memory");
            if (lane == 0) LDS_CNT_ADD(C_BX);
            lds_wait(lds + C_BX, 4);
#pragma unroll
            for (int n = 0; n < 5; ++n) {
                LDS3 const char* xp = lds + 24576 + hw * 15360 + n * 1024 + lane * 16;
                c[n] = (acc[0][n] + *(LDS3 const f32x4*)xp) + (*(LDS3 const f32x4*)(xp + 5120) + *(LDS3 const f32x4*)(xp + 10240));
            }
            asm volatile("s_waitcnt lgkmcnt(0)" ::: "memory");
            if (lane == 0) LDS_CNT_ADD(C_BX);
            lds_wait(lds + C_BX, 8);
        }
        STAMP(6);
        lds_wait(lds + C_PROG, 8);
        lds_wait(lds + C_OB, 4);
        {
            const char* wsrc = ws + WS_WT + (size_t)ntO * 5 * 40960;
#pragma unroll 1
            for (int n = 0; n < 5; ++n)
#pragma unroll
                for (int t = 0; t < 5; ++t) { const int ks = hw + 4 * t; glds16s(lane * 16 + (n * 40 + ks) * 1024, wsrc, lds0 + L_WA + (n * 20 + ks) * 1024); }
#pragma unroll
            for (int n = 0; n < 3; ++n) *(LDS3 f32x4*)(lds + X_FIN + hw * 3072 + n * 1024 + lane * 16) = c[n];
            DRAIN();
            asm volatile("s_waitcnt lgkmcnt(0)" ::: "memory");
            if (lane == 0) LDS_CNT_ADD(C_WA);
            lds_wait(lds + C_OA, 8);
#pragma unroll
            for (int n = 3; n < 5; ++n) *(LDS3 f32x4*)(lds + 143360 + hw * 2048 + (n - 3) * 1024 + lane * 16) = c[n];
            asm volatile("s_waitcnt lgkmcnt(0)" ::: "memory");
            if (lane == 0) __hip_atomic_store((LDS3 unsigned*)(lds + C_BP + hw * 4), 1u, __ATOMIC_RELAXED, __HIP_MEMORY_SCOPE_WORKGROUP);
        }
        STAMP(7);
    } else {
        const int qs = w & 3, kh = w >> 2;
        const int q0 = qb * 256 + qs * 64;
        GET_TAG();
        if (bid < 240) {
            const int part = nt / 5, c0 = (nt - part * 5) * 128;
            const int wm = w & 1, wn = w >> 1;
            constexpr int RB = 32768;
            const int xr = wm * 64 + r;
            const unsigned xa = RB + xr * 128 + ((h ^ ((xr >> 1) & 7)) << 4);
            unsigned wt[2];
#pragma unroll
            for (int t = 0; t < 2; ++t) {
                const int q = (lane & 15) >> 2, p = lane & 3, blk = (lane >> 4) & 1, row = 8 * h + 4 * t + q;
                wt[t] = RB + 16384 + row * 256 + (((4 * wn + 2 * blk + (p >> 1)) ^ ((q << 2) | (2 * h + t))) << 4) + 8 * (p & 1);
            }
            auto gbody = [&](auto trc) {
                constexpr bool TR = decltype(trc)::value;
                u32x4 xf[2][8], wf[2][4];
                auto rd = [&](auto bc, int s) {
                    constexpr int B = decltype(bc)::value;
                    LDS3 const char* sb = lds + (s & 3) * 32768;
#pragma unroll
                    for (int ks = 0; ks < 4; ++ks) {
                        xf[B][2 * ks] = *(LDS3 const u32x4*)(sb + (xa ^ (ks * 32)));
                        xf[B][2 * ks + 1] = *(LDS3 const u32x4*)(sb + ((xa + 4096) ^ (ks * 32)));
                        const u32x2 lo = trread(sb + wt[0] + ks * 4096), hi = trread(sb + wt[1] + ks * 4096);
                        wf[B][ks] = (u32x4){lo[0], lo[1], hi[0], hi[1]};
                    }
                };
                f32x16 acc0, acc1;
                auto step = [&](auto bc, int s) {
                    constexpr int B = decltype(bc)::value;
                    if (s == 12) {
                        DRAIN();
                        unsigned arr = 0;
                        if (lane == 0) arr = LDS_CNT_ADD(G_ST);
                        if (__builtin_amdgcn_readfirstlane(arr) == 7 && lane == 0) set_flag(ws, fG, tag);
                    }
                    if (s < 19) lds_wait(lds + G_FULL + ((s + 1) & 3) * 4, ((s + 1) >> 2) + 1);
                    rd(std::integral_constant<int, B ^ 1>{}, s + 1);
#pragma unroll
                    for (int ks = 0; ks < 4; ++ks) {
                        if constexpr (TR) { acc0 = MFMA32_16(wf[B][ks], xf[B][2 * ks], acc0); acc1 = MFMA32_16(wf[B][ks], xf[B][2 * ks + 1], acc1); }
                        else              { acc0 = MFMA32_16(xf[B][2 * ks], wf[B][ks], acc0); acc1 = MFMA32_16(xf[B][2 * ks + 1], wf[B][ks], acc1); }
                    }
                    SBAR();
                    asm volatile("s_waitcnt lgkmcnt(0)" ::: "memory");
                    if (s < 19 && lane == 0) LDS_CNT_ADD(G_FREE + ((s + 1) & 3) * 4);
                };
                WAIT_BAR(63);
                lds_wait(lds + G_FULL, 1);
                rd(std::integral_constant<int, 0>{}, 0);
                asm volatile("s_waitcnt lgkmcnt(0)" ::: "memory");
                if (lane == 0) LDS_CNT_ADD(G_FREE);
#pragma unroll 1
                for (int mat = 0; mat < 2; ++mat) {
                    acc0 = (f32x16){}; acc1 = (f32x16){};
#pragma unroll 1
                    for (int kk = 0; kk < 10; kk += 2) {
                        step(std::integral_constant<int, 0>{}, mat * 10 + kk);
                        step(std::integral_constant<int, 1>{}, mat * 10 + kk + 1);
                    }
                    const int cpos = 2 * (mt & 7) + (mt >> 3);
                    if constexpr (TR) {
                        const float sc = (part == 0 && mat == 0) ? QSCALE : 1.f;
                        const unsigned base = part == 0 ? WS_Q + mat * NH * 81920 + mt * 5120 : WS_KV + mat * NH * 163840 + cpos * 10240;
                        const unsigned hs = part == 0 ? 81920 : 163840;
#pragma unroll
                        for (int gq = 0; gq < 4; ++gq) {
                            const int n = c0 + 32 * wn + 8 * gq + 4 * h, hd = n / 20, d = n - hd * 20;
#pragma unroll
                            for (int i = 0; i < 2; ++i) {
                                const f32x16& a = i ? acc1 : acc0;
                                const int tl = wm * 64 + i * 32 + r;
                                const unsigned off = base + hd * hs + (d < 16 ? (tl >> 5) * 1024 + (d >> 3) * 512 + r * 16 + (d & 4) * 2 : 4096 + tl * 8);
                                u32x2 ov = {pk2(a[4 * gq] * sc, a[4 * gq + 1] * sc), pk2(a[4 * gq + 2] * sc, a[4 * gq + 3] * sc)};
                                ST8(ov, off);
                            }
                        }
                    } else {
                        const int n = c0 + 32 * wn + r, hd = n / 20, d = n - hd * 20;
                        const unsigned base = WS_KV + (mat * NH + hd) * 163840 + cpos * 10240 + 5120 + h * 320 + d * 16;
#pragma unroll
                        for (int i = 0; i < 2; ++i)
#pragma unroll
                            for (int s2 = 0; s2 < 2; ++s2) {
                                const f32x16& a = i ? acc1 : acc0;
                                u32x4 ov = {pk2(a[8 * s2], a[8 * s2 + 1]), pk2(a[8 * s2 + 2], a[8 * s2 + 3]), pk2(a[8 * s2 + 4], a[8 * s2 + 5]), pk2(a[8 * s2 + 6], a[8 * s2 + 7])};
                                ST16(ov, base + (wm * 4 + i * 2 + s2) * 640);
                            }
                    }
                    if (mat == 0) STAMP(1);
                }
            };
            if (part < 2) gbody(std::true_type{}); else gbody(std::false_type{});
        } else {
            const int sj = bid - 240;
            WAIT_BAR(63);
            {
                const u32x4 ga = *(const u32x4*)(P.gw + lane * 8), gb = *(const u32x4*)(P.gw + 512 + (lane & 15) * 8);
                u32x4 xa[16], xb[4];
                const u16* xr = P.x + (size_t)(sj * 128 + w * 16) * DM;
#pragma unroll
                for (int rr = 0; rr < 16; ++rr) xa[rr] = *(const u32x4*)(xr + rr * DM + lane * 8);
#pragma unroll
                for (int i = 0; i < 4; ++i) xb[i] = *(const u32x4*)(xr + (i * 4 + (lane >> 4)) * DM + 512 + (lane & 15) * 8);
                float res = 0.f;
#pragma unroll
                for (int rr = 0; rr < 16; ++rr) {
                    float s = 0.f, s2 = 0.f;
#pragma unroll
                    for (int j = 0; j < 4; ++j) { s += bf_lo(xa[rr][j]) * bf_lo(ga[j]) + bf_hi(xa[rr][j]) * bf_hi(ga[j]); s2 += bf_lo(xb[rr >> 2][j]) * bf_lo(gb[j]) + bf_hi(xb[rr >> 2][j]) * bf_hi(gb[j]); }
                    if ((lane >> 4) == (rr & 3)) s += s2;
#pragma unroll
                    for (int o = 32; o >= 1; o >>= 1) s += __shfl_xor(s, o);
                    if (lane == rr) res = s;
                }
                if (lane < 16) ST4(__float_as_uint(1.f / (1.f + __expf(-res))), WS_G + (sj * 128 + w * 16 + lane) * 4);
            }
#pragma unroll 1
            for (int it = 0; it < 2; ++it) {
                unsigned e[4][8];
#pragma unroll
                for (int q = 0; q < 4; ++q) {
                    int item = mtid + 512 * (it * 4 + q); if (item >= 3200) item = 3199;
                    const int u = item / 320, n2 = item - u * 320, k0 = (sj * 10 + u) * 8;
                    const u16* wsrc = (k0 < 640 ? P.woa + (size_t)k0 * DM : P.wob + (size_t)(k0 - 640) * DM) + n2 * 2;
#pragma unroll
                    for (int j = 0; j < 8; ++j) e[q][j] = *(const unsigned*)(wsrc + (size_t)j * DM);
                }
#pragma unroll
                for (int q = 0; q < 4; ++q) {
                    int item = mtid + 512 * (it * 4 + q); if (item >= 3200) item = 3199;
                    const int u = item / 320, n = (item - u * 320) * 2, k0 = (sj * 10 + u) * 8;
                    const unsigned* f = e[q];
                    u32x4 lo = {(f[0] & 0xffffu) | (f[1] << 16), (f[2] & 0xffffu) | (f[3] << 16), (f[4] & 0xffffu) | (f[5] << 16), (f[6] & 0xffffu) | (f[7] << 16)};
                    u32x4 hi = {(f[0] >> 16) | (f[1] & 0xffff0000u), (f[2] >> 16) | (f[3] & 0xffff0000u), (f[4] >> 16) | (f[5] & 0xffff0000u), (f[6] >> 16) | (f[7] & 0xffff0000u)};
                    const unsigned dp = WS_WT + (((n >> 4) * 40 + (k0 >> 5)) * 64 + ((k0 >> 3) & 3) * 16 + (n & 15)) * 16;
                    ST16(lo, dp); ST16(hi, dp + 16);
                }
            }
            DRAIN();
            unsigned arr = 0;
            if (lane == 0) arr = LDS_CNT_ADD(G_ST);
            if (__builtin_amdgcn_readfirstlane(arr) == 7 && lane == 0) set_flag(ws, WS_F_SD + sj * 8, tag);
        }
        STAMP(2);
        const unsigned hb = lds0 + kh * 10240;
        const unsigned laneK = hb + lane * 16, laneT = hb + 4096 + r * 8 + h * 4, laneV = hb + 5120 + h * 320 + (r < 20 ? r : 19) * 16;
        const u64 vmask = 0x000fffff000fffffull;
        u32x4 v0a = {one, one, one, one}, v0b = v0a, v1 = v0a;
        u32x4 qA, qB, ka; unsigned qtA, qtB, kt;
        {
            lds_wait(lds + G_Q, 1); lds_wait(lds + G_KV0, 1);
            LDS3 const char* qp = lds + L_Q + (qs >> 1) * 5120;
            const unsigned om = (qs & 1) * 2048 + lane * 16, ot = 4096 + ((qs & 1) * 64 + r) * 8 + 4 * h;
            qA = *(LDS3 const u32x4*)(qp + om); qB = *(LDS3 const u32x4*)(qp + om + 1024);
            qtA = *(LDS3 const unsigned*)(qp + ot); qtB = *(LDS3 const unsigned*)(qp + ot + 256);
            DSR128(ka, laneK, 0); DSR32(kt, laneT, 0); DSR128M2(v0a, laneV, 0, v1, laneV, 640);
            asm volatile("s_waitcnt lgkmcnt(0)\n\ts_barrier" : "+v"(ka), "+v"(kt), "+v"(v0a), "+v"(v1), "+v"(qA), "+v"(qB), "+v"(qtA), "+v"(qtB) :: "memory");
        }
        STAMP(5);
        float mA, mB; u32x2 qTA, qTB; f32x16 sA, sB;
        {
            const u32x2 t0 = {kt, 0x3f80u}, a0 = {qtA, 0u}, b0 = {qtB, 0u};
            const f32x16 s = MFMA32_16(ka, qA, z16);
            const f32x16 s0 = MFMA32_8(t0, a0, s);
            f32x16 s1 = MFMA32_16(ka, qB, z16); s1 = MFMA32_8(t0, b0, s1);
            float x0 = s0[0], x1 = s1[0];
#pragma unroll
            for (int i = 1; i < 16; ++i) { x0 = fmaxf(x0, s0[i]); x1 = fmaxf(x1, s1[i]); }
            x0 = -xhalf_max(x0); x1 = -xhalf_max(x1);
            const unsigned hh = pk2(x0, x1);
            const unsigned ll = pk2(x0 - bf_lo(hh), x1 - bf_hi(hh));
            mA = -(bf_lo(hh) + bf_lo(ll)); mB = -(bf_hi(hh) + bf_hi(ll));
            const unsigned sel = h ? ll : hh;
            qTA = (u32x2){qtA, sel & 0xffffu}; qTB = (u32x2){qtB, sel >> 16};
            sA = MFMA32_8(t0, qTA, s);
        }
        f32x16 oA = {}, oB = {};
        u32x4 pa, pb = {0u, 0u, 0u, 0u};
#define EX4(S, i, Pv, j) do { Pv[j] = pk2(ex2(S[i]), ex2(S[i + 1])); Pv[j + 1] = pk2(ex2(S[i + 2]), ex2(S[i + 3])); PIN(Pv); SBAR(); } while (0)
#define KT2 ((u32x2){kt, 0x3f80u})
#define TILE(V0C, V0N, aK1, oK1, aT1, oT1, oV1, aV0, oV0) do { \
            sB = MFMA32_16(ka, qB, z16); SBAR(); DSR128(ka, aK1, oK1); EX4(sA, 0, pa, 0); \
            oB = MFMA32_16(v1, pb, oB); SBAR(); DSR128M2(v1, aV, oV1, V0N, aV0, oV0); EX4(sA, 4, pa, 2); \
            sB = MFMA32_8(KT2, qTB, sB); SBAR(); DSR32(kt, aT1, oT1); EX4(sA, 8, pb, 0); \
            oA = MFMA32_16(V0C, pa, oA); SBAR(); EX4(sA, 12, pb, 2); \
            LWAIT(1); sA = MFMA32_16(ka, qA, z16); SBAR(); EX4(sB, 0, pa, 0); \
            oA = MFMA32_16(v1, pb, oA); SBAR(); EX4(sB, 4, pa, 2); \
            LWAIT(0); sA = MFMA32_8(KT2, qTA, sA); SBAR(); EX4(sB, 8, pb, 0); \
            oB = MFMA32_16(V0C, pa, oB); SBAR(); EX4(sB, 12, pb, 2); \
        } while (0)
        unsigned aK = laneK, aT = laneT, aV = laneV;
#pragma unroll 1
        for (int c = 0; c < 8; ++c) {
            if (c == 1) { WAIT_BAR(0); }
            else if (c == 5 || c == 7) { if (lane == 0) LDS_CNT_ADD(c == 5 ? C_PROG2 : C_PROG); asm volatile("s_waitcnt lgkmcnt(0)" ::: "memory"); }
            const unsigned nx = (c < 7 ? c + 1 : 7) * 20480;
            const unsigned nK = laneK + nx, nT = laneT + nx, nV = laneV + nx;
            SBAR();
            TILE(v0a, v0b, aK, 1024, aT, 256, 640,  aV, 1280);
            TILE(v0b, v0a, aK, 2048, aT, 512, 1920, aV, 2560);
            TILE(v0a, v0b, aK, 3072, aT, 768, 3200, aV, 3840);
            TILE(v0b, v0a, nK, 0,    nT, 0,   4480, nV, 0);
            aK = nK; aT = nT; aV = nV;
        }
        oB = MFMA32_16(v1, pb, oB);
#undef TILE
#undef EX4
        STAMP(6);
        {
            float chk = 0.f;
#pragma unroll
            for (int i = 0; i < 12; ++i) chk += fabsf(oA[i]) * 0.f + fabsf(oB[i]) * 0.f;
            bool bad = !(chk == 0.f);
            if (h) bad = bad || !(oA[8] >= 0x1p-60f && oA[8] <= 0x1p60f && oB[8] >= 0x1p-60f && oB[8] <= 0x1p60f);
            if (__builtin_expect(__any(bad), 0)) {
                mA = -INFINITY; mB = -INFINITY;
#pragma unroll
                for (int i = 0; i < 16; ++i) { oA[i] = 0.f; oB[i] = 0.f; }
                const u32x2 a0 = {qtA, 0u}, b0 = {qtB, 0u};
#pragma unroll 1
                for (int t = 0; t < 32; ++t) {
                    const unsigned kb = WS_KV + head * 163840 + (2 * (t >> 2) + kh) * 10240, t4 = t & 3;
                    const u32x4 k0 = LD16(kb + t4 * 1024 + lane * 16);
                    const u32x2 t0 = {LD4(kb + 4096 + (t4 * 32 + r) * 8 + 4 * h), 0u};
                    u32x4 w0 = LD16(kb + 5120 + t4 * 1280 + h * 320 + (r < 20 ? r : 19) * 16), w1 = LD16(kb + 5120 + t4 * 1280 + 640 + h * 320 + (r < 20 ? r : 19) * 16);
                    if (r >= 20) { w0 = (u32x4){one, one, one, one}; w1 = w0; }
                    f32x16 s0 = MFMA32_16(k0, qA, z16); s0 = MFMA32_8(t0, a0, s0);
                    f32x16 s1 = MFMA32_16(k0, qB, z16); s1 = MFMA32_8(t0, b0, s1);
                    float x0 = s0[0], x1 = s1[0];
#pragma unroll
                    for (int i = 1; i < 16; ++i) { x0 = fmaxf(x0, s0[i]); x1 = fmaxf(x1, s1[i]); }
                    x0 = fmaxf(xhalf_max(x0), mA); x1 = fmaxf(xhalf_max(x1), mB);
                    const float g0 = (mA == x0) ? 1.f : ex2(mA - x0), g1 = (mB == x1) ? 1.f : ex2(mB - x1);
                    mA = x0; mB = x1;
#pragma unroll
                    for (int i = 0; i < 16; ++i) { oA[i] *= g0; oB[i] *= g1; }
#pragma unroll
                    for (int i = 0; i < 4; ++i) { pa[i] = pk2(ex2(s0[2 * i] - x0), ex2(s0[2 * i + 1] - x0)); pb[i] = pk2(ex2(s0[8 + 2 * i] - x0), ex2(s0[9 + 2 * i] - x0)); }
                    oA = MFMA32_16(w0, pa, oA); oA = MFMA32_16(w1, pb, oA);
#pragma unroll
                    for (int i = 0; i < 4; ++i) { pa[i] = pk2(ex2(s1[2 * i] - x1), ex2(s1[2 * i + 1] - x1)); pb[i] = pk2(ex2(s1[8 + 2 * i] - x1), ex2(s1[9 + 2 * i] - x1)); }
                    oB = MFMA32_16(w0, pa, oB); oB = MFMA32_16(w1, pb, oB);
                }
            }
        }
        lds_wait(lds + C_OB, 4);
        lds_wait(lds + C_PROG, 8);
        const float gO = __uint_as_float(LD4(WS_G + (q0 + kh * 32 + r) * 4));
        {
            LDS3 char* xp = lds + X_MRG + w * 2816;
            const f32x16& s = kh ? oA : oB;
            *(LDS3 f32x4*)(xp + lane * 16) = (f32x4){s[0], s[1], s[2], s[3]};
            *(LDS3 f32x4*)(xp + 1024 + lane * 16) = (f32x4){s[4], s[5], s[6], s[7]};
            if (!h) *(LDS3 f32x4*)(xp + 2048 + r * 16) = (f32x4){s[8], s[9], s[10], s[11]};
            else *(LDS3 f32x2*)(xp + 2560 + r * 8) = (f32x2){s[8], kh ? mA : mB};
            asm volatile("s_waitcnt lgkmcnt(0)" ::: "memory");
            if (lane == 0) __hip_atomic_store((LDS3 unsigned*)(lds + C_MRG + w * 4), 1u, __ATOMIC_RELAXED, __HIP_MEMORY_SCOPE_WORKGROUP);
        }
        lds_wait(lds + C_MRG + (w ^ 4) * 4, 1);
        {
            LDS3 const char* xp = lds + X_MRG + (w ^ 4) * 2816;
            const f32x4 p0 = *(LDS3 const f32x4*)(xp + lane * 16), p1 = *(LDS3 const f32x4*)(xp + 1024 + lane * 16), p2 = *(LDS3 const f32x4*)(xp + 2048 + r * 16);
            const f32x2 lm = *(LDS3 const f32x2*)(xp + 2560 + r * 8);
            const f32x16& s = kh ? oB : oA;
            const float mo = kh ? mB : mA, mp = lm[1];
            const float mx = fmaxf(mo, mp);
            const float fo = (mo == mx) ? 1.f : ex2(mo - mx), fq = (mp == mx) ? 1.f : ex2(mp - mx);
            float o[12];
#pragma unroll
            for (int i = 0; i < 4; ++i) { o[i] = s[i] * fo + p0[i] * fq; o[4 + i] = s[4 + i] * fo + p1[i] * fq; o[8 + i] = s[8 + i] * fo + p2[i] * fq; }
            const float l = upper_half(s[8]) * fo + lm[0] * fq;
            store_o(rs, o, gO / l, q0 + kh * 32 + r, head * 20, h);
            DRAIN();
            unsigned arr = 0;
            if (lane == 0) arr = LDS_CNT_ADD(C_OA);
            if (__builtin_amdgcn_readfirstlane(arr) == 7 && lane == 0) set_flag(ws, WS_F_OA + (qb * 32 + head) * 8, tag);
        }
        STAMP(7);
        {
            const int c16 = lane & 15, g = lane >> 4, tt = w & 3, k0 = kh * 10;
            wait_flags(ws, WS_F_OA + ((mtO >> 2) * 32 + r) * 8, WS_F_OA + ((mtO >> 2) * 32 + r) * 8, tag);
            STAMP(9);
            const unsigned ab = WS_AO + ((mtO * 4 + tt) * 40 + k0) * 1024 + lane * 16;
            u32x4 af[10];
#pragma unroll
            for (int ks = 0; ks < 10; ++ks) af[ks] = LD16(ab + ks * 1024);
            lds_wait(lds + C_WA, 4);
            lds_wait(lds + C_OA, 8);
            f32x4 c[5];
#pragma unroll
            for (int n = 0; n < 5; ++n) c[n] = (f32x4){0.f, 0.f, 0.f, 0.f};
#pragma unroll
            for (int ks = 0; ks < 10; ++ks)
#pragma unroll
                for (int n = 0; n < 5; ++n) c[n] = MFMA16_32(*(LDS3 const u32x4*)(lds + L_WA + (n * 20 + k0 + ks) * 1024 + lane * 16), af[ks], c[n]);
            LDS3 char* xp = lds + 1024 + tt * 5120 + lane * 16;
            if (w < 4) { *(LDS3 f32x4*)(xp + 3072) = c[3]; *(LDS3 f32x4*)(xp + 4096) = c[4]; }
            else { *(LDS3 f32x4*)(xp) = c[0]; *(LDS3 f32x4*)(xp + 1024) = c[1]; *(LDS3 f32x4*)(xp + 2048) = c[2]; }
            asm volatile("s_waitcnt lgkmcnt(0)" ::: "memory");
            if (lane == 0) __hip_atomic_store((LDS3 unsigned*)(lds + C_FIN + w * 4), 1u, __ATOMIC_RELAXED, __HIP_MEMORY_SCOPE_WORKGROUP);
            lds_wait(lds + C_FIN + (w ^ 4) * 4, 1);
            lds_wait(lds + C_BP + tt * 4, 1);
            u16* op = P.out + (size_t)(mtO * 64 + tt * 16 + c16) * DM + ntO * 80 + g * 4;
#pragma unroll
            for (int n = 0; n < 5; ++n) {
                if ((w < 4) == (n < 3)) {
                    const f32x4 bp = *(LDS3 const f32x4*)(n < 3 ? lds + X_FIN + tt * 3072 + n * 1024 + lane * 16 : lds + 143360 + tt * 2048 + (n - 3) * 1024 + lane * 16);
                    const f32x4 s = c[n] + *(LDS3 const f32x4*)(xp + n * 1024) + bp;
                    *(u32x2*)(op + n * 16) = (u32x2){pk2(s[0], s[1]), pk2(s[2], s[3])};
                }
            }
            if (bid == 0 && mtid == 0) set_flag(ws, WS_EPOCH, tag);
        }
        STAMP(10);
    }
}

extern "C" void kernel_launch(void* const* d_in, const int* in_sizes, int n_in, void* d_out, int out_size, void* d_ws, size_t ws_size,
                              hipStream_t stream) {
    Params P;
    P.x = (const u16*)d_in[0]; P.wa = (const u16*)d_in[1]; P.woa = (const u16*)d_in[2]; P.wb = (const u16*)d_in[4];
    P.wphi = (const u16*)d_in[5]; P.wob = (const u16*)d_in[6]; P.gw = (const u16*)d_in[7]; P.ws = (char*)d_ws; P.out = (u16*)d_out;
    mega<<<256, 768, 0, stream>>>(P);
}
```

```cpp
#include <hip/hip_runtime.h>
#include <hip/hip_bf16.h>
#include <stdint.h>
#include <type_traits>

typedef __attribute__((ext_vector_type(8))) short bf16x8;
typedef __attribute__((ext_vector_type(4))) short bf16x4;
typedef __attribute__((ext_vector_type(16))) float f32x16;
typedef __attribute__((ext_vector_type(4))) float f32x4;
typedef __attribute__((ext_vector_type(2))) float f32x2;
typedef __attribute__((ext_vector_type(4))) unsigned u32x4;
typedef __attribute__((ext_vector_type(2))) unsigned u32x2;
typedef __attribute__((ext_vector_type(2))) __bf16 bf2_t;
typedef unsigned short u16;
typedef unsigned long long u64;
#define LDS3 __attribute__((address_space(3)))
typedef __attribute__((address_space(1))) u64 gu64;
#define RLX_AGENT __ATOMIC_RELAXED, __HIP_MEMORY_SCOPE_AGENT

constexpr int SEQ = 2048, DM = 640, NH = 32;
constexpr float LOG2E = 1.4426950408889634f;
constexpr float QSCALE = 0.22360679774997896f * LOG2E;

constexpr unsigned WS_EPOCH = 0;
constexpr unsigned WS_F_G   = 256;
constexpr unsigned WS_F_SD  = WS_F_G + 4096;
constexpr unsigned WS_F_KV  = WS_F_SD + 256;
constexpr unsigned WS_F_OB  = WS_F_KV + 2048;
constexpr unsigned WS_F_OA  = WS_F_OB + 2048;
constexpr unsigned WS_G     = 16384;
constexpr unsigned WS_Q     = 32768;
constexpr unsigned WS_KV    = WS_Q + 2 * NH * 81920;
constexpr unsigned WS_KVP   = WS_KV + 2 * NH * 163840;
constexpr unsigned WS_AO    = WS_KVP + NH * 8 * 4096;
constexpr unsigned WS_WT    = WS_AO + SEQ * 1280 * 2;
constexpr unsigned WS_END   = WS_WT + 1280 * 640 * 2;

#define STAMP(i)

__device__ __forceinline__ unsigned pk2(float a, float b) { f32x2 v = {a, b}; bf2_t r = __builtin_convertvector(v, bf2_t); return __builtin_bit_cast(unsigned, r); }
__device__ __forceinline__ float bf_lo(unsigned u) { return __uint_as_float(u << 16); }
__device__ __forceinline__ float bf_hi(unsigned u) { return __uint_as_float(u & 0xffff0000u); }
__device__ __forceinline__ float ex2(float x) { return __builtin_amdgcn_exp2f(x); }
#define MFMA32_16(a, b, c) __builtin_amdgcn_mfma_f32_32x32x16_bf16(__builtin_bit_cast(bf16x8, a), __builtin_bit_cast(bf16x8, b), c, 0, 0, 0)
#define MFMA32_8(a, b, c) __builtin_amdgcn_mfma_f32_32x32x8bf16_1k(__builtin_bit_cast(bf16x4, a), __builtin_bit_cast(bf16x4, b), c, 0, 0, 0)
#define MFMA16_32(a, b, c) __builtin_amdgcn_mfma_f32_16x16x32_bf16(__builtin_bit_cast(bf16x8, a), __builtin_bit_cast(bf16x8, b), c, 0, 0, 0)

__device__ __forceinline__ void glds16(unsigned voff, const void* sbase, unsigned lds_base) {
    unsigned sv;
    asm volatile("s_mov_b32 %0, m0\n\ts_mov_b32 m0, %3\n\ts_nop 0\n\tglobal_load_lds_dwordx4 %1, %2\n\ts_mov_b32 m0, %0"
                 : "=&s"(sv) : "v"(voff), "s"(sbase), "s"(__builtin_amdgcn_readfirstlane(lds_base)) : "memory");
}
__device__ __forceinline__ void glds16s(unsigned voff, const void* sbase, unsigned lds_base) {
    unsigned sv;
    asm volatile("s_mov_b32 %0, m0\n\ts_mov_b32 m0, %3\n\ts_nop 0\n\tglobal_load_lds_dwordx4 %1, %2 sc1\n\ts_mov_b32 m0, %0"
                 : "=&s"(sv) : "v"(voff), "s"(sbase), "s"(__builtin_amdgcn_readfirstlane(lds_base)) : "memory");
}
__device__ __forceinline__ void glds4s(unsigned voff, const void* sbase, unsigned lds_base) {
    unsigned sv;
    asm volatile("s_mov_b32 %0, m0\n\ts_mov_b32 m0, %3\n\ts_nop 0\n\tglobal_load_lds_dword %1, %2 sc1\n\ts_mov_b32 m0, %0"
                 : "=&s"(sv) : "v"(voff), "s"(sbase), "s"(__builtin_amdgcn_readfirstlane(lds_base)) : "memory");
}
typedef short v4i16_t __attribute__((ext_vector_type(4)));
__device__ __forceinline__ u32x2 trread(LDS3 const char* p) {
    return __builtin_bit_cast(u32x2, __builtin_amdgcn_ds_read_tr16_b64_v4i16((LDS3 v4i16_t*)p));
}
#define WAIT_BAR(N) asm volatile("s_waitcnt vmcnt(" #N ") lgkmcnt(0)\n\ts_barrier" ::: "memory")
#define DRAIN() asm volatile("s_waitcnt vmcnt(0)" ::: "memory")
#define SBAR() __builtin_amdgcn_sched_barrier(0)
#define PIN(x) asm volatile("" : "+v"(x))

__device__ __forceinline__ float xhalf_max(float x) {
    auto r = __builtin_amdgcn_permlane32_swap(__float_as_uint(x), __float_as_uint(x), false, false);
    return fmaxf(__uint_as_float(r[0]), __uint_as_float(r[1]));
}
__device__ __forceinline__ float xhalf_sum(float x) {
    auto r = __builtin_amdgcn_permlane32_swap(__float_as_uint(x), __float_as_uint(x), false, false);
    return __uint_as_float(r[0]) + __uint_as_float(r[1]);
}
__device__ __forceinline__ float upper_half(float x) {
    auto r = __builtin_amdgcn_permlane32_swap(__float_as_uint(x), __float_as_uint(x), false, false);
    return __uint_as_float(r[1]);
}

typedef __amdgpu_buffer_rsrc_t rsrc_t;
#define ST8(v, off)  __builtin_amdgcn_raw_buffer_store_b64(v, rs, (int)(off), 0, 16)
#define ST16(v, off) __builtin_amdgcn_raw_buffer_store_b128(v, rs, (int)(off), 0, 16)
#define ST4(v, off)  __builtin_amdgcn_raw_buffer_store_b32(v, rs, (int)(off), 0, 16)
#define LD16(off) __builtin_amdgcn_raw_buffer_load_b128(rs, (int)(off), 0, 16)
#define LD4(off)  __builtin_amdgcn_raw_buffer_load_b32(rs, (int)(off), 0, 16)

__device__ __forceinline__ void wait_flags(const char* ws, unsigned off0, unsigned off1, u64 tag) {
    gu64* p0 = (gu64*)(ws + off0); gu64* p1 = (gu64*)(ws + off1);
#pragma unroll 1
    for (unsigned spins = 0; spins < (1u << 16); ++spins) {
        const u64 a = __hip_atomic_load(p0, RLX_AGENT), b = __hip_atomic_load(p1, RLX_AGENT);
        if (__all(a == tag && b == tag)) break;
        __builtin_amdgcn_s_sleep(2);
    }
    __builtin_amdgcn_fence(__ATOMIC_ACQUIRE, "wavefront");
}
__device__ __forceinline__ void set_flag(char* ws, unsigned off, u64 tag) { __hip_atomic_store((gu64*)(ws + off), tag, RLX_AGENT); }

struct Params { const u16* x; const u16* wa; const u16* wb; const u16* wphi; const u16* woa; const u16* wob; const u16* gw; char* ws; u16* out; };

__device__ __forceinline__ void feature_map(const u32x4& pm, unsigned pt, const u32x4& tm, unsigned tt, int h, u32x4& fp, u32x4& fn, u32x4& ft) {
    const f32x16 z16 = {};
    const u32x2 a8 = {pt, 0u}, b8 = {tt, 0u};
    f32x16 s = MFMA32_16(pm, tm, z16); s = MFMA32_8(a8, b8, s);
    float v[12];
#pragma unroll
    for (int i = 0; i < 12; i += 2) { const unsigned u = pk2(s[i], s[i + 1]); v[i] = bf_lo(u) * LOG2E; v[i + 1] = bf_hi(u) * LOG2E; }
    float mx = v[0], mn = v[0];
#pragma unroll
    for (int i = 1; i < 8; ++i) { mx = fmaxf(mx, v[i]); mn = fminf(mn, v[i]); }
    const float tx = fmaxf(fmaxf(v[8], v[9]), fmaxf(v[10], v[11])), tn = fminf(fminf(v[8], v[9]), fminf(v[10], v[11]));
    if (!h) { mx = fmaxf(mx, tx); mn = fminf(mn, tn); }
    mx = xhalf_max(mx); mn = -xhalf_max(-mn);
    float ep[12], en[12], sp = 0.f, sn = 0.f;
#pragma unroll
    for (int i = 0; i < 12; ++i) {
        ep[i] = ex2(v[i] - mx); en[i] = ex2(mn - v[i]);
        if (i >= 8 && h) { ep[i] = 0.f; en[i] = 0.f; }
        sp += ep[i]; sn += en[i];
    }
    sp = __builtin_amdgcn_rcpf(xhalf_sum(sp)); sn = __builtin_amdgcn_rcpf(xhalf_sum(sn));
#pragma unroll
    for (int i = 0; i < 4; ++i) { fp[i] = pk2(ep[2 * i] * sp, ep[2 * i + 1] * sp); fn[i] = pk2(en[2 * i] * sn, en[2 * i + 1] * sn); }
    ft = (u32x4){pk2(ep[8] * sp, ep[9] * sp), pk2(ep[10] * sp, ep[11] * sp), pk2(en[8] * sn, en[9] * sn), pk2(en[10] * sn, en[11] * sn)};
}

__device__ __forceinline__ void store_o(rsrc_t rs, const float (&o)[12], float sc, int tok, int kbase, int h) {
#pragma unroll
    for (int gq = 0; gq < 3; ++gq) {
        const int d = 8 * gq + 4 * h;
        if (d < 20) {
            const int k = kbase + ((kbase & 4) ? (d == 16 ? 0 : d + 4) : d);
            u32x2 ov = {pk2(o[4 * gq] * sc, o[4 * gq + 1] * sc), pk2(o[4 * gq + 2] * sc, o[4 * gq + 3] * sc)};
            ST8(ov, WS_AO + (((tok >> 4) * 40 + (k >> 5)) * 64 + ((k >> 3) & 3) * 16 + (tok & 15)) * 16 + (k & 7) * 2);
        }
    }
}

#define DSR128(dst, addr, off) asm volatile("ds_read_b128 %0, %1 offset:%2" : "=v"(dst) : "v"(addr), "i"(off))
#define DSR32(dst, addr, off) asm volatile("ds_read_b32 %0, %1 offset:%2" : "=v"(dst) : "v"(addr), "i"(off))
#define DSR128M2(d0, a0, o0, d1, a1, o1) asm volatile("s_mov_b64 exec, %6\n\tds_read_b128 %0, %2 offset:%3\n\tds_read_b128 %1, %4 offset:%5\n\ts_mov_b64 exec, -1" \
                                                      : "+v"(d0), "+v"(d1) : "v"(a0), "i"(o0), "v"(a1), "i"(o1), "s"(vmask))
#define LWAIT(n) do { asm volatile("s_waitcnt lgkmcnt(" #n ")" ::: "memory"); SBAR(); } while (0)

#define BAR() asm volatile("s_barrier" ::: "memory")
#define LDS_CNT_ADD(off) __hip_atomic_fetch_add((LDS3 unsigned*)(lds + (off)), 1u, __ATOMIC_RELAXED, __HIP_MEMORY_SCOPE_WORKGROUP)

__device__ __forceinline__ void lds_wait(LDS3 char* p, unsigned want) {
#pragma unroll 1
    for (unsigned spins = 0; spins < (1u << 20); ++spins) {
        if ((unsigned)__builtin_amdgcn_readfirstlane(__hip_atomic_load((LDS3 unsigned*)p, __ATOMIC_RELAXED, __HIP_MEMORY_SCOPE_WORKGROUP)) >= want) break;
        __builtin_amdgcn_s_sleep(1);
    }
    __builtin_amdgcn_fence(__ATOMIC_ACQUIRE, "wavefront");
}

constexpr int C_KV = 0, C_KVF = 4, C_OB = 8, C_PROG = 12, C_WA = 16, C_OA = 20, C_MRG = 32  , C_FIN = 64  , C_BP = 96  ;
constexpr int C_PROG2 = 24, C_BX = 28;
constexpr int G_W = 31744;
constexpr int G_FULL = G_W, G_FREE = G_W + 16, G_Q = G_W + 32, G_KV0 = G_W + 36, G_ST = G_W + 40;
constexpr int X_KVF = 1024, X_SLOT = 4096;
constexpr int X_MRG = 1024;
constexpr int L_WA = 24576;
constexpr int L_Q = 65536;
constexpr int X_FIN = 126976;

__global__ __launch_bounds__(768) void mega(Params P) {
    __shared__ __attribute__((aligned(16))) char smem[163840];
    LDS3 char* lds = (LDS3 char*)smem;
    char* ws = P.ws;
    const int tid = threadIdx.x, lane = tid & 63, r = lane & 31, h = lane >> 5;
    const int hw = __builtin_amdgcn_readfirstlane(tid >> 6);
    const bool helper = hw < 4;
    const int w = hw - 4, mtid = tid - 256;
    const int bid = blockIdx.x;
    const unsigned lds0 = (unsigned)(uintptr_t)smem;
    const rsrc_t rs = __builtin_amdgcn_make_buffer_rsrc(ws, 0, WS_END, 0x00020000);
    STAMP(0);
    u64 tag = 0;
#define GET_TAG() do { asm volatile("s_load_dwordx2 %0, %1, 0x0\n\ts_waitcnt lgkmcnt(0)" : "=s"(tag) : "s"(ws) : "memory"); tag += 1; } while (0)
    const int head = (bid & 7) * 4 + ((bid >> 3) & 3), qb = bid >> 5;
    const int nlo = (head * 20) >> 7, nhi = (head * 20 + 19) >> 7;
    const int mtO = (bid & 7) * 4 + ((bid >> 3) & 3), ntO = bid >> 5;
    const int lin = (bid & 7) * 30 + (bid >> 3), lh = lin >= 120, lrem = lin - 120 * lh;
    const int mt = lh * 8 + (lrem & 7), nt = lrem >> 3;
    const unsigned fG = WS_F_G + (mt * 15 + nt) * 8;
    const unsigned one = (r == 20) ? 0x3f803f80u : 0u;
    const f32x16 z16 = {};

    if (helper) {
        const unsigned fA0 = WS_F_G + ((lane & 15) * 15 + (lane < 32 ? 5 : 10) + ((lane & 16) ? nhi : nlo)) * 8;
        const unsigned fA1 = WS_F_G + ((qb * 2 + (lane & 1)) * 15 + ((lane & 2) ? nhi : nlo)) * 8;
        const char* kvb = ws + WS_KV + head * 163840;
        auto kv_issue = [&](int grp) {
#pragma unroll
            for (int t = 0; t < 8; ++t) { int pc = grp * 32 + hw + 4 * t; if (pc == 31) pc = 30; glds16s(lane * 16, kvb + pc * 1024, lds0 + pc * 1024); }
        };
        auto q_issue = [&]() {
            const char* qbase = ws + WS_Q + head * 81920 + qb * 10240;
#pragma unroll
            for (int t = 0; t < 3; ++t) { const int pc = hw + 4 * t < 10 ? hw + 4 * t : 9; glds16s(lane * 16, qbase + pc * 1024, lds0 + L_Q + pc * 1024); }
        };
        if (bid < 240) {
            const int rot = (nt * 3 + mt * 5 + (mt >> 1)) % 10;
            const unsigned xo = (mt * 128 + 8 * hw + (lane >> 3)) * 1280 + (((lane & 7) ^ ((4 * hw + (lane >> 4)) & 7)) << 4);
            const unsigned wo = (4 * hw + (lane >> 4)) * 3840 + nt * 256 + (((lane & 15) ^ (((lane >> 4) << 2) | hw)) << 4);
            const unsigned dX = lds0 + 32768 + hw * 1024;
            auto issue = [&](int s) {
                int kt = (s >= 10 ? s - 10 : s) + rot; if (kt >= 10) kt -= 10;
                const char* xb = (const char*)P.x + kt * 128;
                const char* wb = (const char*)(s >= 10 ? P.wb : P.wa) + (size_t)kt * (64 * 3840);
                const unsigned d = dX + (s & 3) * 32768;
#pragma unroll
                for (int i = 0; i < 4; ++i) { glds16(xo, xb + i * 40960, d + i * 4096); glds16(wo, wb + i * 61440, d + 16384 + i * 4096); }
            };
#pragma unroll 1
            for (int n = 0; n < 20; ++n) {
                if (n == 2) { if (hw == 0 && lane < 16) *(LDS3 unsigned*)(lds + G_W + lane * 4) = 0u; GET_TAG(); WAIT_BAR(8); }
                else if (n == 3) { WAIT_BAR(8); }
                else if (n >= 5) { WAIT_BAR(16); }
                if (n == 16 && tid == 0) set_flag(ws, fG, tag);
                if (n == 19 && hw == 0) {
                    glds4s(fA0, ws, lds0); glds4s(fA0 + 4, ws, lds0 + 256); glds4s(fA1, ws, lds0 + 512); glds4s(fA1 + 4, ws, lds0 + 768);
                }
                issue(n);
            }
            WAIT_BAR(16);
            asm volatile("s_waitcnt vmcnt(8)" ::: "memory");
            if (hw == 0) {
                LDS3 const unsigned* f = (LDS3 const unsigned*)lds + lane;
                const unsigned tl = (unsigned)tag, th = (unsigned)(tag >> 32);
                if (!__all(f[0] == tl && f[64] == th && f[128] == tl && f[192] == th)) wait_flags(ws, fA0, fA1, tag);
            }
            WAIT_BAR(63);
            kv_issue(0); q_issue();
            WAIT_BAR(11);
            kv_issue(1);
            WAIT_BAR(63);
        } else {
            if (hw == 0 && lane < 16) *(LDS3 unsigned*)(lds + G_W + lane * 4) = 0u;
            GET_TAG();
            WAIT_BAR(63);
            {
                const int l0 = (bid & 7) * 30, n0 = (l0 - (l0 >= 120 ? 120 : 0)) >> 3;
                const unsigned po = (lane >> 4) * 3840 + (lane & 15) * 16;
                __builtin_amdgcn_s_sleep(64);
#pragma unroll 1
                for (int t = 0; t < 100; ++t) {
                    const int pc = hw + 4 * t, c = pc / 80, k4 = pc - c * 80, nn = n0 + c < 15 ? n0 + c : 14;
                    glds16(po, (const char*)P.wb + (size_t)(((bid >> 3) & 1) * 320 + k4 * 4) * 3840 + nn * 256, lds0 + 32768 + (pc & 63) * 1024);
                    if ((t & 15) == 15) asm volatile("s_waitcnt vmcnt(16)" ::: "memory");
                }
            }
            wait_flags(ws, fA0, fA1, tag);
            asm volatile("s_waitcnt vmcnt(0) lgkmcnt(0)" ::: "memory");
            if (lane == 0) LDS_CNT_ADD(G_FREE);
            lds_wait(lds + G_FREE, 4);
            kv_issue(0); q_issue(); kv_issue(1);
        }
        asm volatile("s_waitcnt vmcnt(6)" ::: "memory");
        if (lane == 0) LDS_CNT_ADD(G_Q);
        kv_issue(3); kv_issue(4);
        WAIT_BAR(63);
        if (hw == 3) glds16s(lane * 16, kvb + 31 * 1024, lds0 + 31 * 1024);
        kv_issue(2);
        if (hw == 0) *(LDS3 u32x4*)(lds + lane * 16) = (u32x4){0u, 0u, 0u, 0u};
        u32x4 phm; unsigned pht;
        {
            const u16* wp = P.wphi + head * 400 + (r < 20 ? r : 0);
            unsigned e[10];
#pragma unroll
            for (int j = 0; j < 8; ++j) e[j] = wp[(8 * h + j + 4 * (head & 1)) * 20];
            e[8] = wp[((head & 1 ? 0 : 16) + 2 * h) * 20]; e[9] = wp[((head & 1 ? 1 : 17) + 2 * h) * 20];
            phm = (u32x4){e[0] | (e[1] << 16), e[2] | (e[3] << 16), e[4] | (e[5] << 16), e[6] | (e[7] << 16)}; pht = e[8] | (e[9] << 16);
            if (r >= 20) { phm = (u32x4){0u, 0u, 0u, 0u}; pht = 0u; }
        }
        WAIT_BAR(0);
        if (tid == 0 && bid < 240) set_flag(ws, fG + 2048, tag);
        STAMP(3);
        if (hw < 3) *(LDS3 u32x4*)(lds + X_KVF + (hw * 64 + lane) * 16) = (u32x4){0u, 0u, 0u, 0u};
        const int tokS = qb * 256 + hw * 64, m7 = tokS >> 7, t4 = (tokS >> 5) & 3;
        {
            const unsigned tb = WS_F_G + 2048 + (m7 * 15 + ((lane & 1) ? nhi : nlo)) * 8;
            wait_flags(ws, tb + ((lane & 2) ? 40 : 80), lane < 16 ? WS_F_SD + lane * 8 : tb, tag);
        }
        const unsigned om = t4 * 1024 + lane * 16, ot = 4096 + (t4 * 32 + r) * 8 + 4 * h;
        u32x4 qf[2][3]; float gS[2];
        {
            const unsigned kb = WS_KV + (NH + head) * 163840 + (2 * (m7 & 7) + (m7 >> 3)) * 10240, qbb = WS_Q + (NH + head) * 81920 + m7 * 5120;
            const unsigned ov = 5120 + t4 * 1280 + h * 320 + (r < 20 ? r : 19) * 16;
            u32x4 e0[2], w0[2], w1[2], eq[2]; unsigned et[2], eqt[2];
#pragma unroll
            for (int i = 0; i < 2; ++i) {
                e0[i] = LD16(kb + om + i * 1024); et[i] = LD4(kb + ot + i * 256); w0[i] = LD16(kb + ov + i * 1280); w1[i] = LD16(kb + ov + i * 1280 + 640);
                eq[i] = LD16(qbb + om + i * 1024); eqt[i] = LD4(qbb + ot + i * 256); gS[i] = __uint_as_float(LD4(WS_G + (tokS + i * 32 + r) * 4));
            }
            u32x4 i0, i1;
#pragma unroll
            for (int j = 0; j < 4; ++j) {
                const int e = r - 8 * h - 2 * j;
                i0[j] = e == 0 ? 0x3f80u : (e == 1 ? 0x3f800000u : 0u);
                i1[j] = e == 16 ? 0x3f80u : (e == 17 ? 0x3f800000u : 0u);
            }
            f32x16 k0 = {}, k1 = {};
#pragma unroll
            for (int i = 0; i < 2; ++i) {
                if (r >= 20) { w0[i] = (u32x4){one, one, one, one}; w1[i] = w0[i]; }
                u32x4 fp, fn, ft;
                feature_map(phm, pht, e0[i], et[i], h, fp, fn, ft);
                f32x16 T0 = MFMA32_16(fp, i0, z16); T0 = MFMA32_16(fn, i1, T0);
                f32x16 T1 = MFMA32_16(ft, i0, z16);
                u32x4 b00, b01, b10, b11;
#pragma unroll
                for (int j = 0; j < 4; ++j) {
                    b00[j] = pk2(T0[2 * j], T0[2 * j + 1]); b01[j] = pk2(T0[8 + 2 * j], T0[9 + 2 * j]);
                    b10[j] = pk2(T1[2 * j], T1[2 * j + 1]); b11[j] = pk2(T1[8 + 2 * j], T1[9 + 2 * j]);
                }
                k0 = MFMA32_16(w0[i], b00, k0); k0 = MFMA32_16(w1[i], b01, k0);
                k1 = MFMA32_16(w0[i], b10, k1); k1 = MFMA32_16(w1[i], b11, k1);
            }
            LDS3 char* sp = lds + X_SLOT + hw * 4096;
#pragma unroll
            for (int gq = 0; gq < 3; ++gq) {
                *(LDS3 f32x4*)(sp + gq * 1024 + lane * 16) = (f32x4){k0[4 * gq], k0[4 * gq + 1], k0[4 * gq + 2], k0[4 * gq + 3]};
                if (r < 8) *(LDS3 f32x4*)(sp + 3072 + (gq * 16 + h * 8 + r) * 16) = (f32x4){k1[4 * gq], k1[4 * gq + 1], k1[4 * gq + 2], k1[4 * gq + 3]};
            }
            asm volatile("s_waitcnt lgkmcnt(0)" ::: "memory");
            unsigned arr = 0;
            if (lane == 0) arr = LDS_CNT_ADD(C_KV);
            if (__builtin_amdgcn_readfirstlane(arr) == 3) {
                __builtin_amdgcn_fence(__ATOMIC_ACQUIRE, "wavefront");
#pragma unroll
                for (int i = 0; i < 4; ++i) {
                    const int qd = lane + 64 * i;
                    LDS3 const char* s0 = lds + X_SLOT + qd * 16;
                    const f32x4 s = (*(LDS3 const f32x4*)s0 + *(LDS3 const f32x4*)(s0 + 4096)) + (*(LDS3 const f32x4*)(s0 + 8192) + *(LDS3 const f32x4*)(s0 + 12288));
                    if (qd < 240) ST16(__builtin_bit_cast(u32x4, s), WS_KVP + (head * 8 + qb) * 4096 + qd * 16);
                }
                DRAIN();
                if (lane == 0) set_flag(ws, WS_F_KV + (head * 8 + qb) * 8, tag);
            }
#pragma unroll
            for (int i = 0; i < 2; ++i) feature_map(phm, pht, eq[i], eqt[i], h, qf[i][0], qf[i][1], qf[i][2]);
        }
        STAMP(4);
        wait_flags(ws, WS_F_KV + (head * 8 + (lane & 7)) * 8, WS_F_KV + (head * 8 + (lane & 7)) * 8, tag);
        {
            const int qd = hw * 60 + (lane < 60 ? lane : 59);
            f32x4 pp[8];
#pragma unroll
            for (int p = 0; p < 8; ++p) pp[p] = __builtin_bit_cast(f32x4, LD16(WS_KVP + (head * 8 + p) * 4096 + qd * 16));
            const f32x4 s4 = ((pp[0] + pp[1]) + (pp[2] + pp[3])) + ((pp[4] + pp[5]) + (pp[6] + pp[7]));
            const int gq = qd < 192 ? qd >> 6 : (qd - 192) >> 4, hs = qd < 192 ? (qd >> 5) & 1 : (qd >> 3) & 1, sg = qd < 192 ? qd & 31 : 32 + (qd & 7);
            const unsigned a = pk2(s4[0], s4[1]), b = pk2(s4[2], s4[3]);
            LDS3 u16* kp = (LDS3 u16*)(lds + X_KVF + (8 * gq + 4 * hs) * 96 + sg * 2);
            kp[0] = (u16)a; kp[48] = (u16)(a >> 16); kp[96] = (u16)b; kp[144] = (u16)(b >> 16);
            asm volatile("s_waitcnt lgkmcnt(0)" ::: "memory");
            if (lane == 0) LDS_CNT_ADD(C_KVF);
            lds_wait(lds + C_KVF, 4);
        }
        {
            LDS3 const char* kp = lds + X_KVF + (r < 24 ? r : 23) * 96 + h * 16;
            const u32x4 a0 = *(LDS3 const u32x4*)kp, a1 = *(LDS3 const u32x4*)(kp + 32), a2 = *(LDS3 const u32x4*)(kp + 64);
#pragma unroll
            for (int i = 0; i < 2; ++i) {
                f32x16 d = MFMA32_16(a0, qf[i][0], z16); d = MFMA32_16(a1, qf[i][1], d); d = MFMA32_16(a2, qf[i][2], d);
                const float den = upper_half(d[8]) + 1e-6f;
                float o[12];
#pragma unroll
                for (int j = 0; j < 12; ++j) o[j] = d[j];
                store_o(rs, o, (1.f - gS[i]) / den, tokS + i * 32 + r, 640 + head * 20, h);
            }
            DRAIN();
            unsigned arr = 0;
            if (lane == 0) arr = LDS_CNT_ADD(C_OB);
            if (__builtin_amdgcn_readfirstlane(arr) == 3 && lane == 0) set_flag(ws, WS_F_OB + (qb * 32 + head) * 8, tag);
        }
        STAMP(5);
        f32x4 c[5];
        wait_flags(ws, WS_F_OB + ((mtO >> 2) * 32 + r) * 8, WS_F_OB + ((mtO >> 2) * 32 + r) * 8, tag);
        {
            f32x4 acc[4][5];
#pragma unroll
            for (int t = 0; t < 4; ++t)
#pragma unroll
                for (int n = 0; n < 5; ++n) acc[t][n] = (f32x4){0.f, 0.f, 0.f, 0.f};
            const unsigned ab = WS_AO + ((mtO * 4) * 40 + 20 + 5 * hw) * 1024 + lane * 16, bb = WS_WT + ((ntO * 5) * 40 + 20 + 5 * hw) * 1024 + lane * 16;
#pragma unroll
            for (int ks = 0; ks < 5; ++ks) {
                u32x4 af[4], bf[5];
#pragma unroll
                for (int t = 0; t < 4; ++t) af[t] = LD16(ab + ((t + hw) & 3) * 40960 + ks * 1024);
#pragma unroll
                for (int n = 0; n < 5; ++n) bf[n] = LD16(bb + n * 40960 + ks * 1024);
#pragma unroll
                for (int t = 0; t < 4; ++t)
#pragma unroll
                    for (int n = 0; n < 5; ++n) acc[t][n] = MFMA16_32(bf[n], af[t], acc[t][n]);
            }
            lds_wait(lds + C_PROG2, 8);
#pragma unroll
            for (int t = 1; t < 4; ++t)
#pragma unroll
                for (int n = 0; n < 5; ++n) *(LDS3 f32x4*)(lds + 24576 + (((t + hw) & 3) * 3 + t - 1) * 5120 + n * 1024 + lane * 16) = acc[t][n];
            asm volatile("s_waitcnt lgkmcnt(0)" ::: "memory");
            if (lane == 0) LDS_CNT_ADD(C_BX);
            lds_wait(lds + C_BX, 4);
#pragma unroll
            for (int n = 0; n < 5; ++n) {
                LDS3 const char* xp = lds + 24576 + hw * 15360 + n * 1024 + lane * 16;
                c[n] = (acc[0][n] + *(LDS3 const f32x4*)xp) + (*(LDS3 const f32x4*)(xp + 5120) + *(LDS3 const f32x4*)(xp + 10240));
            }
            asm volatile("s_waitcnt lgkmcnt(0)" ::: "memory");
            if (lane == 0) LDS_CNT_ADD(C_BX);
            lds_wait(lds + C_BX, 8);
        }
        STAMP(6);
        lds_wait(lds + C_PROG, 8);
        lds_wait(lds + C_OB, 4);
        {
            const char* wsrc = ws + WS_WT + (size_t)ntO * 5 * 40960;
#pragma unroll 1
            for (int n = 0; n < 5; ++n)
#pragma unroll
                for (int t = 0; t < 5; ++t) { const int ks = hw + 4 * t; glds16s(lane * 16 + (n * 40 + ks) * 1024, wsrc, lds0 + L_WA + (n * 20 + ks) * 1024); }
#pragma unroll
            for (int n = 0; n < 3; ++n) *(LDS3 f32x4*)(lds + X_FIN + hw * 3072 + n * 1024 + lane * 16) = c[n];
            DRAIN();
            asm volatile("s_waitcnt lgkmcnt(0)" ::: "memory");
            if (lane == 0) LDS_CNT_ADD(C_WA);
            lds_wait(lds + C_OA, 8);
#pragma unroll
            for (int n = 3; n < 5; ++n) *(LDS3 f32x4*)(lds + 143360 + hw * 2048 + (n - 3) * 1024 + lane * 16) = c[n];
            asm volatile("s_waitcnt lgkmcnt(0)" ::: "memory");
            if (lane == 0) __hip_atomic_store((LDS3 unsigned*)(lds + C_BP + hw * 4), 1u, __ATOMIC_RELAXED, __HIP_MEMORY_SCOPE_WORKGROUP);
        }
        STAMP(7);
    } else {
        const int qs = w & 3, kh = w >> 2;
        const int q0 = qb * 256 + qs * 64;
        GET_TAG();
        if (bid < 240) {
            const int part = nt / 5, c0 = (nt - part * 5) * 128;
            const int wm = w & 1, wn = w >> 1;
            constexpr int RB = 32768;
            const int xr = wm * 64 + r;
            const unsigned xa = RB + xr * 128 + ((h ^ ((xr >> 1) & 7)) << 4);
            unsigned wt[2];
#pragma unroll
            for (int t = 0; t < 2; ++t) {
                const int q = (lane & 15) >> 2, p = lane & 3, blk = (lane >> 4) & 1, row = 8 * h + 4 * t + q;
                wt[t] = RB + 16384 + row * 256 + (((4 * wn + 2 * blk + (p >> 1)) ^ ((q << 2) | (2 * h + t))) << 4) + 8 * (p & 1);
            }
            auto gbody = [&](auto trc) {
                constexpr bool TR = decltype(trc)::value;
                u32x4 xf[2][8], wf[2][4];
                auto rd = [&](auto bc, int s) {
                    constexpr int B = decltype(bc)::value;
                    LDS3 const char* sb = lds + (s & 3) * 32768;
#pragma unroll
                    for (int ks = 0; ks < 4; ++ks) {
                        xf[B][2 * ks] = *(LDS3 const u32x4*)(sb + (xa ^ (ks * 32)));
                        xf[B][2 * ks + 1] = *(LDS3 const u32x4*)(sb + ((xa + 4096) ^ (ks * 32)));
                        const u32x2 lo = trread(sb + wt[0] + ks * 4096), hi = trread(sb + wt[1] + ks * 4096);
                        wf[B][ks] = (u32x4){lo[0], lo[1], hi[0], hi[1]};
                    }
                };
                f32x16 acc0, acc1;
                auto step = [&](auto bc, int s) {
                    constexpr int B = decltype(bc)::value;
                    if (s == 12) DRAIN();
                    __builtin_amdgcn_s_waitcnt(0xC07F);
                    BAR(); SBAR();
                    rd(std::integral_constant<int, B ^ 1>{}, s + 1);
#pragma unroll
                    for (int ks = 0; ks < 4; ++ks) {
                        if constexpr (TR) { acc0 = MFMA32_16(wf[B][ks], xf[B][2 * ks], acc0); acc1 = MFMA32_16(wf[B][ks], xf[B][2 * ks + 1], acc1); }
                        else              { acc0 = MFMA32_16(xf[B][2 * ks], wf[B][ks], acc0); acc1 = MFMA32_16(xf[B][2 * ks + 1], wf[B][ks], acc1); }
                    }
#pragma unroll
                    for (int i = 0; i < 4; ++i) { __builtin_amdgcn_sched_group_barrier(0x8, 1, 0); __builtin_amdgcn_sched_group_barrier(0x2, 2, 0); __builtin_amdgcn_sched_group_barrier(0x100, 4, 0); }
                    __builtin_amdgcn_sched_group_barrier(0x8, 4, 0);
                    SBAR();
                };
                WAIT_BAR(63);
                rd(std::integral_constant<int, 0>{}, 0);
#pragma unroll 1
                for (int mat = 0; mat < 2; ++mat) {
                    acc0 = (f32x16){}; acc1 = (f32x16){};
#pragma unroll 1
                    for (int kk = 0; kk < 10; kk += 2) {
                        step(std::integral_constant<int, 0>{}, mat * 10 + kk);
                        step(std::integral_constant<int, 1>{}, mat * 10 + kk + 1);
                    }
                    const int cpos = 2 * (mt & 7) + (mt >> 3);
                    if constexpr (TR) {
                        const float sc = (part == 0 && mat == 0) ? QSCALE : 1.f;
                        const unsigned base = part == 0 ? WS_Q + mat * NH * 81920 + mt * 5120 : WS_KV + mat * NH * 163840 + cpos * 10240;
                        const unsigned hs = part == 0 ? 81920 : 163840;
#pragma unroll
                        for (int pr = 0; pr < 2; ++pr) {
                            const int G8 = ((c0 + 32 * wn) >> 3) + 2 * pr + h, pp = G8 / 5, gi = G8 - pp * 5;
                            const unsigned hb0 = base + (2 * pp + (gi > 2)) * hs;
#pragma unroll
                            for (int i = 0; i < 2; ++i) {
                                const f32x16& a = i ? acc1 : acc0;
                                const int tl = wm * 64 + i * 32 + r;
                                const auto s0 = __builtin_amdgcn_permlane32_swap(pk2(a[8 * pr] * sc, a[8 * pr + 1] * sc), pk2(a[8 * pr + 4] * sc, a[8 * pr + 5] * sc), false, false);
                                const auto s1 = __builtin_amdgcn_permlane32_swap(pk2(a[8 * pr + 2] * sc, a[8 * pr + 3] * sc), pk2(a[8 * pr + 6] * sc, a[8 * pr + 7] * sc), false, false);
                                if (gi != 2) { const u32x4 ov = {s0[0], s1[0], s0[1], s1[1]}; ST16(ov, hb0 + (tl >> 5) * 1024 + (gi > 2 ? gi - 3 : gi) * 512 + r * 16); }
                                else { const u32x2 o0 = {s0[0], s1[0]}, o1 = {s0[1], s1[1]}; ST8(o0, hb0 + 4096 + tl * 8); ST8(o1, hb0 + hs + 4096 + tl * 8); }
                            }
                        }
                    } else {
                        const int n = c0 + 32 * wn + r, hd = n / 20, d = n - hd * 20, rho = (hd & 1) ? (d < 4 ? d + 16 : d - 4) : d;
                        const unsigned base = WS_KV + (mat * NH + hd) * 163840 + cpos * 10240 + 5120 + h * 320 + rho * 16;
#pragma unroll
                        for (int i = 0; i < 2; ++i)
#pragma unroll
                            for (int s2 = 0; s2 < 2; ++s2) {
                                const f32x16& a = i ? acc1 : acc0;
                                u32x4 ov = {pk2(a[8 * s2], a[8 * s2 + 1]), pk2(a[8 * s2 + 2], a[8 * s2 + 3]), pk2(a[8 * s2 + 4], a[8 * s2 + 5]), pk2(a[8 * s2 + 6], a[8 * s2 + 7])};
                                ST16(ov, base + (wm * 4 + i * 2 + s2) * 640);
                            }
                    }
                    if (mat == 0) STAMP(1);
                }
            };
            if (part < 2) gbody(std::true_type{}); else gbody(std::false_type{});
        } else {
            const int sj = bid - 240;
            WAIT_BAR(63);
            {
                const u32x4 ga = *(const u32x4*)(P.gw + lane * 8), gb = *(const u32x4*)(P.gw + 512 + (lane & 15) * 8);
                u32x4 xa[16], xb[4];
                const u16* xr = P.x + (size_t)(sj * 128 + w * 16) * DM;
#pragma unroll
                for (int rr = 0; rr < 16; ++rr) xa[rr] = *(const u32x4*)(xr + rr * DM + lane * 8);
#pragma unroll
                for (int i = 0; i < 4; ++i) xb[i] = *(const u32x4*)(xr + (i * 4 + (lane >> 4)) * DM + 512 + (lane & 15) * 8);
                float res = 0.f;
#pragma unroll
                for (int rr = 0; rr < 16; ++rr) {
                    float s = 0.f, s2 = 0.f;
#pragma unroll
                    for (int j = 0; j < 4; ++j) { s += bf_lo(xa[rr][j]) * bf_lo(ga[j]) + bf_hi(xa[rr][j]) * bf_hi(ga[j]); s2 += bf_lo(xb[rr >> 2][j]) * bf_lo(gb[j]) + bf_hi(xb[rr >> 2][j]) * bf_hi(gb[j]); }
                    if ((lane >> 4) == (rr & 3)) s += s2;
#pragma unroll
                    for (int o = 32; o >= 1; o >>= 1) s += __shfl_xor(s, o);
                    if (lane == rr) res = s;
                }
                if (lane < 16) ST4(__float_as_uint(1.f / (1.f + __expf(-res))), WS_G + (sj * 128 + w * 16 + lane) * 4);
            }
#pragma unroll 1
            for (int it = 0; it < 2; ++it) {
                unsigned e[4][8];
#pragma unroll
                for (int q = 0; q < 4; ++q) {
                    int item = mtid + 512 * (it * 4 + q); if (item >= 3200) item = 3199;
                    const int u = item / 320, n2 = item - u * 320, k0 = (sj * 10 + u) * 8;
                    const u16* wsrc = (k0 < 640 ? P.woa + (size_t)k0 * DM : P.wob + (size_t)(k0 - 640) * DM) + n2 * 2;
#pragma unroll
                    for (int j = 0; j < 8; ++j) e[q][j] = *(const unsigned*)(wsrc + (size_t)j * DM);
                }
#pragma unroll
                for (int q = 0; q < 4; ++q) {
                    int item = mtid + 512 * (it * 4 + q); if (item >= 3200) item = 3199;
                    const int u = item / 320, n = (item - u * 320) * 2, k0 = (sj * 10 + u) * 8;
                    const unsigned* f = e[q];
                    u32x4 lo = {(f[0] & 0xffffu) | (f[1] << 16), (f[2] & 0xffffu) | (f[3] << 16), (f[4] & 0xffffu) | (f[5] << 16), (f[6] & 0xffffu) | (f[7] << 16)};
                    u32x4 hi = {(f[0] >> 16) | (f[1] & 0xffff0000u), (f[2] >> 16) | (f[3] & 0xffff0000u), (f[4] >> 16) | (f[5] & 0xffff0000u), (f[6] >> 16) | (f[7] & 0xffff0000u)};
                    const unsigned dp = WS_WT + (((n >> 4) * 40 + (k0 >> 5)) * 64 + ((k0 >> 3) & 3) * 16 + (n & 15)) * 16;
                    ST16(lo, dp); ST16(hi, dp + 16);
                }
            }
            DRAIN();
            unsigned arr = 0;
            if (lane == 0) arr = LDS_CNT_ADD(G_ST);
            if (__builtin_amdgcn_readfirstlane(arr) == 7 && lane == 0) set_flag(ws, WS_F_SD + sj * 8, tag);
        }
        STAMP(2);
        const unsigned hb = lds0 + kh * 10240;
        const unsigned laneK = hb + lane * 16, laneT = hb + 4096 + r * 8 + h * 4, laneV = hb + 5120 + h * 320 + (r < 20 ? r : 19) * 16;
        const u64 vmask = 0x000fffff000fffffull;
        u32x4 v0a = {one, one, one, one}, v0b = v0a, v1 = v0a;
        u32x4 qA, qB, ka; unsigned qtA, qtB, kt;
        {
            lds_wait(lds + G_Q, 4);
            LDS3 const char* qp = lds + L_Q + (qs >> 1) * 5120;
            const unsigned om = (qs & 1) * 2048 + lane * 16, ot = 4096 + ((qs & 1) * 64 + r) * 8 + 4 * h;
            qA = *(LDS3 const u32x4*)(qp + om); qB = *(LDS3 const u32x4*)(qp + om + 1024);
            qtA = *(LDS3 const unsigned*)(qp + ot); qtB = *(LDS3 const unsigned*)(qp + ot + 256);
            DSR128(ka, laneK, 0); DSR32(kt, laneT, 0); DSR128M2(v0a, laneV, 0, v1, laneV, 640);
            asm volatile("s_waitcnt lgkmcnt(0)\n\ts_barrier" : "+v"(ka), "+v"(kt), "+v"(v0a), "+v"(v1), "+v"(qA), "+v"(qB), "+v"(qtA), "+v"(qtB) :: "memory");
        }
        STAMP(5);
        float mA, mB; u32x2 qTA, qTB; f32x16 sA, sB;
        {
            const u32x2 t0 = {kt, 0x3f80u}, a0 = {qtA, 0u}, b0 = {qtB, 0u};
            const f32x16 s = MFMA32_16(ka, qA, z16);
            const f32x16 s0 = MFMA32_8(t0, a0, s);
            f32x16 s1 = MFMA32_16(ka, qB, z16); s1 = MFMA32_8(t0, b0, s1);
            float x0 = s0[0], x1 = s1[0];
#pragma unroll
            for (int i = 1; i < 16; ++i) { x0 = fmaxf(x0, s0[i]); x1 = fmaxf(x1, s1[i]); }
            x0 = -xhalf_max(x0); x1 = -xhalf_max(x1);
            const unsigned hh = pk2(x0, x1);
            const unsigned ll = pk2(x0 - bf_lo(hh), x1 - bf_hi(hh));
            mA = -(bf_lo(hh) + bf_lo(ll)); mB = -(bf_hi(hh) + bf_hi(ll));
            const unsigned sel = h ? ll : hh;
            qTA = (u32x2){qtA, sel & 0xffffu}; qTB = (u32x2){qtB, sel >> 16};
            sA = MFMA32_8(t0, qTA, s);
        }
        f32x16 oA = {}, oB = {};
        u32x4 pa, pb = {0u, 0u, 0u, 0u};
#define EX4(S, i, Pv, j) do { Pv[j] = pk2(ex2(S[i]), ex2(S[i + 1])); Pv[j + 1] = pk2(ex2(S[i + 2]), ex2(S[i + 3])); PIN(Pv); SBAR(); } while (0)
#define KT2 ((u32x2){kt, 0x3f80u})
#define TILE(V0C, V0N, aK1, oK1, aT1, oT1, oV1, aV0, oV0) do { \
            sB = MFMA32_16(ka, qB, z16); SBAR(); DSR128(ka, aK1, oK1); EX4(sA, 0, pa, 0); \
            oB = MFMA32_16(v1, pb, oB); SBAR(); DSR128M2(v1, aV, oV1, V0N, aV0, oV0); EX4(sA, 4, pa, 2); \
            sB = MFMA32_8(KT2, qTB, sB); SBAR(); DSR32(kt, aT1, oT1); EX4(sA, 8, pb, 0); \
            oA = MFMA32_16(V0C, pa, oA); SBAR(); EX4(sA, 12, pb, 2); \
            LWAIT(1); sA = MFMA32_16(ka, qA, z16); SBAR(); EX4(sB, 0, pa, 0); \
            oA = MFMA32_16(v1, pb, oA); SBAR(); EX4(sB, 4, pa, 2); \
            LWAIT(0); sA = MFMA32_8(KT2, qTA, sA); SBAR(); EX4(sB, 8, pb, 0); \
            oB = MFMA32_16(V0C, pa, oB); SBAR(); EX4(sB, 12, pb, 2); \
        } while (0)
        unsigned aK = laneK, aT = laneT, aV = laneV;
#pragma unroll 1
        for (int c = 0; c < 8; ++c) {
            if (c == 1) { WAIT_BAR(0); }
            else if (c == 5 || c == 7) { if (lane == 0) LDS_CNT_ADD(c == 5 ? C_PROG2 : C_PROG); asm volatile("s_waitcnt lgkmcnt(0)" ::: "memory"); }
            const unsigned nx = (c < 7 ? c + 1 : 7) * 20480;
            const unsigned nK = laneK + nx, nT = laneT + nx, nV = laneV + nx;
            SBAR();
            TILE(v0a, v0b, aK, 1024, aT, 256, 640,  aV, 1280);
            TILE(v0b, v0a, aK, 2048, aT, 512, 1920, aV, 2560);
            TILE(v0a, v0b, aK, 3072, aT, 768, 3200, aV, 3840);
            TILE(v0b, v0a, nK, 0,    nT, 0,   4480, nV, 0);
            aK = nK; aT = nT; aV = nV;
        }
        oB = MFMA32_16(v1, pb, oB);
#undef TILE
#undef EX4
        STAMP(6);
        {
            float chk = 0.f;
#pragma unroll
            for (int i = 0; i < 12; ++i) chk += fabsf(oA[i]) * 0.f + fabsf(oB[i]) * 0.f;
            bool bad = !(chk == 0.f);
            if (h) bad = bad || !(oA[8] >= 0x1p-60f && oA[8] <= 0x1p60f && oB[8] >= 0x1p-60f && oB[8] <= 0x1p60f);
            if (__builtin_expect(__any(bad), 0)) {
                mA = -INFINITY; mB = -INFINITY;
#pragma unroll
                for (int i = 0; i < 16; ++i) { oA[i] = 0.f; oB[i] = 0.f; }
                const u32x2 a0 = {qtA, 0u}, b0 = {qtB, 0u};
#pragma unroll 1
                for (int t = 0; t < 32; ++t) {
                    const unsigned kb = WS_KV + head * 163840 + (2 * (t >> 2) + kh) * 10240, t4 = t & 3;
                    const u32x4 k0 = LD16(kb + t4 * 1024 + lane * 16);
                    const u32x2 t0 = {LD4(kb + 4096 + (t4 * 32 + r) * 8 + 4 * h), 0u};
                    u32x4 w0 = LD16(kb + 5120 + t4 * 1280 + h * 320 + (r < 20 ? r : 19) * 16), w1 = LD16(kb + 5120 + t4 * 1280 + 640 + h * 320 + (r < 20 ? r : 19) * 16);
                    if (r >= 20) { w0 = (u32x4){one, one, one, one}; w1 = w0; }
                    f32x16 s0 = MFMA32_16(k0, qA, z16); s0 = MFMA32_8(t0, a0, s0);
                    f32x16 s1 = MFMA32_16(k0, qB, z16); s1 = MFMA32_8(t0, b0, s1);
                    float x0 = s0[0], x1 = s1[0];
#pragma unroll
                    for (int i = 1; i < 16; ++i) { x0 = fmaxf(x0, s0[i]); x1 = fmaxf(x1, s1[i]); }
                    x0 = fmaxf(xhalf_max(x0), mA); x1 = fmaxf(xhalf_max(x1), mB);
                    const float g0 = (mA == x0) ? 1.f : ex2(mA - x0), g1 = (mB == x1) ? 1.f : ex2(mB - x1);
                    mA = x0; mB = x1;
#pragma unroll
                    for (int i = 0; i < 16; ++i) { oA[i] *= g0; oB[i] *= g1; }
#pragma unroll
                    for (int i = 0; i < 4; ++i) { pa[i] = pk2(ex2(s0[2 * i] - x0), ex2(s0[2 * i + 1] - x0)); pb[i] = pk2(ex2(s0[8 + 2 * i] - x0), ex2(s0[9 + 2 * i] - x0)); }
                    oA = MFMA32_16(w0, pa, oA); oA = MFMA32_16(w1, pb, oA);
#pragma unroll
                    for (int i = 0; i < 4; ++i) { pa[i] = pk2(ex2(s1[2 * i] - x1), ex2(s1[2 * i + 1] - x1)); pb[i] = pk2(ex2(s1[8 + 2 * i] - x1), ex2(s1[9 + 2 * i] - x1)); }
                    oB = MFMA32_16(w0, pa, oB); oB = MFMA32_16(w1, pb, oB);
                }
            }
        }
        lds_wait(lds + C_OB, 4);
        lds_wait(lds + C_PROG, 8);
        const float gO = __uint_as_float(LD4(WS_G + (q0 + kh * 32 + r) * 4));
        {
            LDS3 char* xp = lds + X_MRG + w * 2816;
            const f32x16& s = kh ? oA : oB;
            *(LDS3 f32x4*)(xp + lane * 16) = (f32x4){s[0], s[1], s[2], s[3]};
            *(LDS3 f32x4*)(xp + 1024 + lane * 16) = (f32x4){s[4], s[5], s[6], s[7]};
            if (!h) *(LDS3 f32x4*)(xp + 2048 + r * 16) = (f32x4){s[8], s[9], s[10], s[11]};
            else *(LDS3 f32x2*)(xp + 2560 + r * 8) = (f32x2){s[8], kh ? mA : mB};
            asm volatile("s_waitcnt lgkmcnt(0)" ::: "memory");
            if (lane == 0) __hip_atomic_store((LDS3 unsigned*)(lds + C_MRG + w * 4), 1u, __ATOMIC_RELAXED, __HIP_MEMORY_SCOPE_WORKGROUP);
        }
        lds_wait(lds + C_MRG + (w ^ 4) * 4, 1);
        {
            LDS3 const char* xp = lds + X_MRG + (w ^ 4) * 2816;
            const f32x4 p0 = *(LDS3 const f32x4*)(xp + lane * 16), p1 = *(LDS3 const f32x4*)(xp + 1024 + lane * 16), p2 = *(LDS3 const f32x4*)(xp + 2048 + r * 16);
            const f32x2 lm = *(LDS3 const f32x2*)(xp + 2560 + r * 8);
            const f32x16& s = kh ? oB : oA;
            const float mo = kh ? mB : mA, mp = lm[1];
            const float mx = fmaxf(mo, mp);
            const float fo = (mo == mx) ? 1.f : ex2(mo - mx), fq = (mp == mx) ? 1.f : ex2(mp - mx);
            float o[12];
#pragma unroll
            for (int i = 0; i < 4; ++i) { o[i] = s[i] * fo + p0[i] * fq; o[4 + i] = s[4 + i] * fo + p1[i] * fq; o[8 + i] = s[8 + i] * fo + p2[i] * fq; }
            const float l = upper_half(s[8]) * fo + lm[0] * fq;
            store_o(rs, o, gO / l, q0 + kh * 32 + r, head * 20, h);
            DRAIN();
            unsigned arr = 0;
            if (lane == 0) arr = LDS_CNT_ADD(C_OA);
            if (__builtin_amdgcn_readfirstlane(arr) == 7 && lane == 0) set_flag(ws, WS_F_OA + (qb * 32 + head) * 8, tag);
        }
        STAMP(7);
        {
            const int c16 = lane & 15, g = lane >> 4, tt = w & 3, k0 = kh * 10;
            wait_flags(ws, WS_F_OA + ((mtO >> 2) * 32 + r) * 8, WS_F_OA + ((mtO >> 2) * 32 + r) * 8, tag);
            STAMP(9);
            const unsigned ab = WS_AO + ((mtO * 4 + tt) * 40 + k0) * 1024 + lane * 16;
            u32x4 af[10];
#pragma unroll
            for (int ks = 0; ks < 10; ++ks) af[ks] = LD16(ab + ks * 1024);
            lds_wait(lds + C_WA, 4);
            lds_wait(lds + C_OA, 8);
            f32x4 c[5];
#pragma unroll
            for (int n = 0; n < 5; ++n) c[n] = (f32x4){0.f, 0.f, 0.f, 0.f};
#pragma unroll
            for (int ks = 0; ks < 10; ++ks)
#pragma unroll
                for (int n = 0; n < 5; ++n) c[n] = MFMA16_32(*(LDS3 const u32x4*)(lds + L_WA + (n * 20 + k0 + ks) * 1024 + lane * 16), af[ks], c[n]);
            LDS3 char* xp = lds + 1024 + tt * 5120 + lane * 16;
            if (w < 4) { *(LDS3 f32x4*)(xp + 3072) = c[3]; *(LDS3 f32x4*)(xp + 4096) = c[4]; }
            else { *(LDS3 f32x4*)(xp) = c[0]; *(LDS3 f32x4*)(xp + 1024) = c[1]; *(LDS3 f32x4*)(xp + 2048) = c[2]; }
            asm volatile("s_waitcnt lgkmcnt(0)" ::: "memory");
            if (lane == 0) __hip_atomic_store((LDS3 unsigned*)(lds + C_FIN + w * 4), 1u, __ATOMIC_RELAXED, __HIP_MEMORY_SCOPE_WORKGROUP);
            lds_wait(lds + C_FIN + (w ^ 4) * 4, 1);
            lds_wait(lds + C_BP + tt * 4, 1);
            u16* op = P.out + (size_t)(mtO * 64 + tt * 16 + c16) * DM + ntO * 80 + g * 4;
#pragma unroll
            for (int n = 0; n < 5; ++n) {
                if ((w < 4) == (n < 3)) {
                    const f32x4 bp = *(LDS3 const f32x4*)(n < 3 ? lds + X_FIN + tt * 3072 + n * 1024 + lane * 16 : lds + 143360 + tt * 2048 + (n - 3) * 1024 + lane * 16);
                    const f32x4 s = c[n] + *(LDS3 const f32x4*)(xp + n * 1024) + bp;
                    *(u32x2*)(op + n * 16) = (u32x2){pk2(s[0], s[1]), pk2(s[2], s[3])};
                }
            }
            if (bid == 0 && mtid == 0) set_flag(ws, WS_EPOCH, tag);
        }
        STAMP(10);
    }
}

extern "C" void kernel_launch(void* const* d_in, const int* in_sizes, int n_in, void* d_out, int out_size, void* d_ws, size_t ws_size,
                              hipStream_t stream) {
    Params P;
    P.x = (const u16*)d_in[0]; P.wa = (const u16*)d_in[1]; P.woa = (const u16*)d_in[2]; P.wb = (const u16*)d_in[4];
    P.wphi = (const u16*)d_in[5]; P.wob = (const u16*)d_in[6]; P.gw = (const u16*)d_in[7]; P.ws = (char*)d_ws; P.out = (u16*)d_out;
    mega<<<256, 768, 0, stream>>>(P);
}
```

```cpp
#include <hip/hip_runtime.h>
#include <hip/hip_bf16.h>
#include <stdint.h>
#include <type_traits>

typedef __attribute__((ext_vector_type(8))) short bf16x8;
typedef __attribute__((ext_vector_type(4))) short bf16x4;
typedef __attribute__((ext_vector_type(16))) float f32x16;
typedef __attribute__((ext_vector_type(4))) float f32x4;
typedef __attribute__((ext_vector_type(2))) float f32x2;
typedef __attribute__((ext_vector_type(4))) unsigned u32x4;
typedef __attribute__((ext_vector_type(2))) unsigned u32x2;
typedef __attribute__((ext_vector_type(2))) __bf16 bf2_t;
typedef unsigned short u16;
typedef unsigned long long u64;
#define LDS3 __attribute__((address_space(3)))
typedef __attribute__((address_space(1))) u64 gu64;
#define RLX_AGENT __ATOMIC_RELAXED, __HIP_MEMORY_SCOPE_AGENT

#define PRE_SLEEP 2
constexpr int SEQ = 2048, DM = 640, NH = 32;
constexpr float LOG2E = 1.4426950408889634f;
constexpr float QSCALE = 0.22360679774997896f * LOG2E;

constexpr unsigned WS_EPOCH = 0;
constexpr unsigned WS_F_G   = 256;
constexpr unsigned WS_F_SD  = WS_F_G + 4096;
constexpr unsigned WS_F_KV  = WS_F_SD + 256;
constexpr unsigned WS_F_OB  = WS_F_KV + 2048;
constexpr unsigned WS_F_OA  = WS_F_OB + 2048;
constexpr unsigned WS_G     = 16384;
constexpr unsigned WS_Q     = 32768;
constexpr unsigned WS_KV    = WS_Q + 2 * NH * 81920;
constexpr unsigned WS_KVP   = WS_KV + 2 * NH * 163840;
constexpr unsigned WS_AO    = WS_KVP + NH * 8 * 4096;
constexpr unsigned WS_WT    = WS_AO + SEQ * 1280 * 2;
constexpr unsigned WS_END   = WS_WT + 1280 * 640 * 2;

#define STAMP(i)

__device__ __forceinline__ unsigned pk2(float a, float b) { f32x2 v = {a, b}; bf2_t r = __builtin_convertvector(v, bf2_t); return __builtin_bit_cast(unsigned, r); }
__device__ __forceinline__ float bf_lo(unsigned u) { return __uint_as_float(u << 16); }
__device__ __forceinline__ float bf_hi(unsigned u) { return __uint_as_float(u & 0xffff0000u); }
__device__ __forceinline__ float ex2(float x) { return __builtin_amdgcn_exp2f(x); }
#define MFMA32_16(a, b, c) __builtin_amdgcn_mfma_f32_32x32x16_bf16(__builtin_bit_cast(bf16x8, a), __builtin_bit_cast(bf16x8, b), c, 0, 0, 0)
#define MFMA32_8(a, b, c) __builtin_amdgcn_mfma_f32_32x32x8bf16_1k(__builtin_bit_cast(bf16x4, a), __builtin_bit_cast(bf16x4, b), c, 0, 0, 0)
#define MFMA16_32(a, b, c) __builtin_amdgcn_mfma_f32_16x16x32_bf16(__builtin_bit_cast(bf16x8, a), __builtin_bit_cast(bf16x8, b), c, 0, 0, 0)

__device__ __forceinline__ void glds16(unsigned voff, const void* sbase, unsigned lds_base) {
    unsigned sv;
    asm volatile("s_mov_b32 %0, m0\n\ts_mov_b32 m0, %3\n\ts_nop 0\n\tglobal_load_lds_dwordx4 %1, %2\n\ts_mov_b32 m0, %0"
                 : "=&s"(sv) : "v"(voff), "s"(sbase), "s"(__builtin_amdgcn_readfirstlane(lds_base)) : "memory");
}
__device__ __forceinline__ void glds16s(unsigned voff, const void* sbase, unsigned lds_base) {
    unsigned sv;
    asm volatile("s_mov_b32 %0, m0\n\ts_mov_b32 m0, %3\n\ts_nop 0\n\tglobal_load_lds_dwordx4 %1, %2 sc1\n\ts_mov_b32 m0, %0"
                 : "=&s"(sv) : "v"(voff), "s"(sbase), "s"(__builtin_amdgcn_readfirstlane(lds_base)) : "memory");
}
__device__ __forceinline__ void glds4s(unsigned voff, const void* sbase, unsigned lds_base) {
    unsigned sv;
    asm volatile("s_mov_b32 %0, m0\n\ts_mov_b32 m0, %3\n\ts_nop 0\n\tglobal_load_lds_dword %1, %2 sc1\n\ts_mov_b32 m0, %0"
                 : "=&s"(sv) : "v"(voff), "s"(sbase), "s"(__builtin_amdgcn_readfirstlane(lds_base)) : "memory");
}
typedef short v4i16_t __attribute__((ext_vector_type(4)));
__device__ __forceinline__ u32x2 trread(LDS3 const char* p) {
    return __builtin_bit_cast(u32x2, __builtin_amdgcn_ds_read_tr16_b64_v4i16((LDS3 v4i16_t*)p));
}
#define WAIT_BAR(N) asm volatile("s_waitcnt vmcnt(" #N ") lgkmcnt(0)\n\ts_barrier" ::: "memory")
#define DRAIN() asm volatile("s_waitcnt vmcnt(0)" ::: "memory")
#define SBAR() __builtin_amdgcn_sched_barrier(0)
#define PIN(x) asm volatile("" : "+v"(x))

__device__ __forceinline__ float xhalf_max(float x) {
    auto r = __builtin_amdgcn_permlane32_swap(__float_as_uint(x), __float_as_uint(x), false, false);
    return fmaxf(__uint_as_float(r[0]), __uint_as_float(r[1]));
}
__device__ __forceinline__ float xhalf_sum(float x) {
    auto r = __builtin_amdgcn_permlane32_swap(__float_as_uint(x), __float_as_uint(x), false, false);
    return __uint_as_float(r[0]) + __uint_as_float(r[1]);
}
__device__ __forceinline__ float upper_half(float x) {
    auto r = __builtin_amdgcn_permlane32_swap(__float_as_uint(x), __float_as_uint(x), false, false);
    return __uint_as_float(r[1]);
}

typedef __amdgpu_buffer_rsrc_t rsrc_t;
#define ST8(v, off)  __builtin_amdgcn_raw_buffer_store_b64(v, rs, (int)(off), 0, 16)
#define ST16(v, off) __builtin_amdgcn_raw_buffer_store_b128(v, rs, (int)(off), 0, 16)
#define ST4(v, off)  __builtin_amdgcn_raw_buffer_store_b32(v, rs, (int)(off), 0, 16)
#define LD16(off) __builtin_amdgcn_raw_buffer_load_b128(rs, (int)(off), 0, 16)
#define LD4(off)  __builtin_amdgcn_raw_buffer_load_b32(rs, (int)(off), 0, 16)

template <int SLP = 2>
__device__ __forceinline__ void wait_flags(const char* ws, unsigned off0, unsigned off1, u64 tag) {
    gu64* p0 = (gu64*)(ws + off0); gu64* p1 = (gu64*)(ws + off1);
#pragma unroll 1
    for (unsigned spins = 0; spins < (1u << 16); ++spins) {
        const u64 a = __hip_atomic_load(p0, RLX_AGENT), b = __hip_atomic_load(p1, RLX_AGENT);
        if (__all(a == tag && b == tag)) break;
        __builtin_amdgcn_s_sleep(SLP);
    }
    __builtin_amdgcn_fence(__ATOMIC_ACQUIRE, "wavefront");
}
__device__ __forceinline__ void set_flag(char* ws, unsigned off, u64 tag) { __hip_atomic_store((gu64*)(ws + off), tag, RLX_AGENT); }

struct Params { const u16* x; const u16* wa; const u16* wb; const u16* wphi; const u16* woa; const u16* wob; const u16* gw; char* ws; u16* out; };

__device__ __forceinline__ void feature_map(const u32x4& pm, unsigned pt, const u32x4& tm, unsigned tt, int h, u32x4& fp, u32x4& fn, u32x4& ft) {
    const f32x16 z16 = {};
    const u32x2 a8 = {pt, 0u}, b8 = {tt, 0u};
    f32x16 s = MFMA32_16(pm, tm, z16); s = MFMA32_8(a8, b8, s);
    float v[12];
#pragma unroll
    for (int i = 0; i < 12; i += 2) { const unsigned u = pk2(s[i], s[i + 1]); v[i] = bf_lo(u) * LOG2E; v[i + 1] = bf_hi(u) * LOG2E; }
    float mx = v[0], mn = v[0];
#pragma unroll
    for (int i = 1; i < 8; ++i) { mx = fmaxf(mx, v[i]); mn = fminf(mn, v[i]); }
    const float tx = fmaxf(fmaxf(v[8], v[9]), fmaxf(v[10], v[11])), tn = fminf(fminf(v[8], v[9]), fminf(v[10], v[11]));
    if (!h) { mx = fmaxf(mx, tx); mn = fminf(mn, tn); }
    mx = xhalf_max(mx); mn = -xhalf_max(-mn);
    float ep[12], en[12], sp = 0.f, sn = 0.f;
#pragma unroll
    for (int i = 0; i < 12; ++i) {
        ep[i] = ex2(v[i] - mx); en[i] = ex2(mn - v[i]);
        if (i >= 8 && h) { ep[i] = 0.f; en[i] = 0.f; }
        sp += ep[i]; sn += en[i];
    }
    sp = __builtin_amdgcn_rcpf(xhalf_sum(sp)); sn = __builtin_amdgcn_rcpf(xhalf_sum(sn));
#pragma unroll
    for (int i = 0; i < 4; ++i) { fp[i] = pk2(ep[2 * i] * sp, ep[2 * i + 1] * sp); fn[i] = pk2(en[2 * i] * sn, en[2 * i + 1] * sn); }
    ft = (u32x4){pk2(ep[8] * sp, ep[9] * sp), pk2(ep[10] * sp, ep[11] * sp), pk2(en[8] * sn, en[9] * sn), pk2(en[10] * sn, en[11] * sn)};
}

__device__ __forceinline__ void store_o(rsrc_t rs, const float (&o)[12], float sc, int tok, int kbase, int h) {
#pragma unroll
    for (int gq = 0; gq < 3; ++gq) {
        const int d = 8 * gq + 4 * h;
        if (d < 20) {
            const int k = kbase + ((kbase & 4) ? (d == 16 ? 0 : d + 4) : d);
            u32x2 ov = {pk2(o[4 * gq] * sc, o[4 * gq + 1] * sc), pk2(o[4 * gq + 2] * sc, o[4 * gq + 3] * sc)};
            ST8(ov, WS_AO + (((tok >> 4) * 40 + (k >> 5)) * 64 + ((k >> 3) & 3) * 16 + (tok & 15)) * 16 + (k & 7) * 2);
        }
    }
}

#define DSR128(dst, addr, off) asm volatile("ds_read_b128 %0, %1 offset:%2" : "=v"(dst) : "v"(addr), "i"(off))
#define DSR32(dst, addr, off) asm volatile("ds_read_b32 %0, %1 offset:%2" : "=v"(dst) : "v"(addr), "i"(off))
#define DSR128M2(d0, a0, o0, d1, a1, o1) asm volatile("s_mov_b64 exec, %6\n\tds_read_b128 %0, %2 offset:%3\n\tds_read_b128 %1, %4 offset:%5\n\ts_mov_b64 exec, -1" \
                                                      : "+v"(d0), "+v"(d1) : "v"(a0), "i"(o0), "v"(a1), "i"(o1), "s"(vmask))
#define LWAIT(n) do { asm volatile("s_waitcnt lgkmcnt(" #n ")" ::: "memory"); SBAR(); } while (0)

#define BAR() asm volatile("s_barrier" ::: "memory")
#define LDS_CNT_ADD(off) __hip_atomic_fetch_add((LDS3 unsigned*)(lds + (off)), 1u, __ATOMIC_RELAXED, __HIP_MEMORY_SCOPE_WORKGROUP)

__device__ __forceinline__ void lds_wait(LDS3 char* p, unsigned want) {
#pragma unroll 1
    for (unsigned spins = 0; spins < (1u << 20); ++spins) {
        if ((unsigned)__builtin_amdgcn_readfirstlane(__hip_atomic_load((LDS3 unsigned*)p, __ATOMIC_RELAXED, __HIP_MEMORY_SCOPE_WORKGROUP)) >= want) break;
        __builtin_amdgcn_s_sleep(1);
    }
    __builtin_amdgcn_fence(__ATOMIC_ACQUIRE, "wavefront");
}

#define POLL_ONE(cond, SLP, o0, o1, word) do { if (cond) { wait_flags<SLP>(ws, o0, o1, tag); \
        if (lane == 0) __hip_atomic_store((LDS3 unsigned*)(lds + (word)), 1u, __ATOMIC_RELAXED, __HIP_MEMORY_SCOPE_WORKGROUP); } else lds_wait(lds + (word), 1); } while (0)
constexpr int C_KV = 0, C_KVF = 4, C_OB = 8, C_PROG = 12, C_WA = 16, C_OA = 20, C_MRG = 32  , C_FIN = 64  , C_BP = 96  ;
constexpr int C_PROG2 = 24, C_BX = 28, C_P1 = 112, C_P2 = 116, C_P3 = 120, C_P4 = 124;
constexpr int G_W = 31744;
constexpr int G_FULL = G_W, G_FREE = G_W + 16, G_Q = G_W + 32, G_KV0 = G_W + 36, G_ST = G_W + 40;
constexpr int X_KVF = 1024, X_SLOT = 4096;
constexpr int X_MRG = 1024;
constexpr int L_WA = 24576;
constexpr int L_Q = 65536;
constexpr int X_FIN = 126976;

__global__ __launch_bounds__(768) void mega(Params P) {
    __shared__ __attribute__((aligned(16))) char smem[163840];
    LDS3 char* lds = (LDS3 char*)smem;
    char* ws = P.ws;
    const int tid = threadIdx.x, lane = tid & 63, r = lane & 31, h = lane >> 5;
    const int hw = __builtin_amdgcn_readfirstlane(tid >> 6);
    const bool helper = hw < 4;
    const int w = hw - 4, mtid = tid - 256;
    const int bid = blockIdx.x;
    const unsigned lds0 = (unsigned)(uintptr_t)smem;
    const rsrc_t rs = __builtin_amdgcn_make_buffer_rsrc(ws, 0, WS_END, 0x00020000);
    STAMP(0);
    u64 tag = 0;
#define GET_TAG() do { asm volatile("s_load_dwordx2 %0, %1, 0x0\n\ts_waitcnt lgkmcnt(0)" : "=s"(tag) : "s"(ws) : "memory"); tag += 1; } while (0)
    const int head = (bid & 7) * 4 + ((bid >> 3) & 3), qb = bid >> 5;
    const int nlo = (head * 20) >> 7, nhi = (head * 20 + 19) >> 7;
    const int mtO = (bid & 7) * 4 + ((bid >> 3) & 3), ntO = bid >> 5;
    const int lin = (bid & 7) * 30 + (bid >> 3), lh = lin >= 120, lrem = lin - 120 * lh;
    const int mt = lh * 8 + (lrem & 7), nt = lrem >> 3;
    const unsigned fG = WS_F_G + (mt * 15 + nt) * 8;
    const unsigned one = (r == 20) ? 0x3f803f80u : 0u;
    const f32x16 z16 = {};

    if (helper) {
        const unsigned fA0 = WS_F_G + ((lane & 15) * 15 + (lane < 32 ? 5 : 10) + ((lane & 16) ? nhi : nlo)) * 8;
        const unsigned fA1 = WS_F_G + ((qb * 2 + (lane & 1)) * 15 + ((lane & 2) ? nhi : nlo)) * 8;
        const char* kvb = ws + WS_KV + head * 163840;
        auto kv_issue = [&](int grp) {
#pragma unroll
            for (int t = 0; t < 8; ++t) { int pc = grp * 32 + hw + 4 * t; if (pc == 31) pc = 30; glds16s(lane * 16, kvb + pc * 1024, lds0 + pc * 1024); }
        };
        auto q_issue = [&]() {
            const char* qbase = ws + WS_Q + head * 81920 + qb * 10240;
#pragma unroll
            for (int t = 0; t < 3; ++t) { const int pc = hw + 4 * t < 10 ? hw + 4 * t : 9; glds16s(lane * 16, qbase + pc * 1024, lds0 + L_Q + pc * 1024); }
        };
        if (bid < 240) {
            const int rot = (nt * 3 + mt * 5 + (mt >> 1)) % 10;
            const unsigned xo = (mt * 128 + 8 * hw + (lane >> 3)) * 1280 + (((lane & 7) ^ ((4 * hw + (lane >> 4)) & 7)) << 4);
            const unsigned wo = (4 * hw + (lane >> 4)) * 3840 + nt * 256 + (((lane & 15) ^ (((lane >> 4) << 2) | hw)) << 4);
            const unsigned dX = lds0 + 32768 + hw * 1024;
            auto issue = [&](int s) {
                int kt = (s >= 10 ? s - 10 : s) + rot; if (kt >= 10) kt -= 10;
                const char* xb = (const char*)P.x + kt * 128;
                const char* wb = (const char*)(s >= 10 ? P.wb : P.wa) + (size_t)kt * (64 * 3840);
                const unsigned d = dX + (s & 3) * 32768;
#pragma unroll
                for (int i = 0; i < 4; ++i) { glds16(xo, xb + i * 40960, d + i * 4096); glds16(wo, wb + i * 61440, d + 16384 + i * 4096); }
            };
#pragma unroll 1
            for (int n = 0; n < 20; ++n) {
                if (n == 2) { if (hw == 0 && lane < 16) *(LDS3 unsigned*)(lds + G_W + lane * 4) = 0u; GET_TAG(); WAIT_BAR(8); }
                else if (n == 3) { WAIT_BAR(8); }
                else if (n >= 5) { WAIT_BAR(16); }
                if (n == 16 && tid == 0) set_flag(ws, fG, tag);
                if (n == 19 && hw == 0) {
                    glds4s(fA0, ws, lds0); glds4s(fA0 + 4, ws, lds0 + 256); glds4s(fA1, ws, lds0 + 512); glds4s(fA1 + 4, ws, lds0 + 768);
                }
                issue(n);
            }
            WAIT_BAR(16);
            asm volatile("s_waitcnt vmcnt(8)" ::: "memory");
            if (hw == 0) {
                LDS3 const unsigned* f = (LDS3 const unsigned*)lds + lane;
                const unsigned tl = (unsigned)tag, th = (unsigned)(tag >> 32);
                if (!__all(f[0] == tl && f[64] == th && f[128] == tl && f[192] == th)) wait_flags<8>(ws, fA0, fA1, tag);
            }
            WAIT_BAR(63);
            kv_issue(0); q_issue();
            WAIT_BAR(11);
            kv_issue(1);
            WAIT_BAR(63);
        } else {
            if (hw == 0 && lane < 16) *(LDS3 unsigned*)(lds + G_W + lane * 4) = 0u;
            GET_TAG();
            WAIT_BAR(63);
            if (hw == 0) {
#pragma unroll 1
                for (int t = 0; t < PRE_SLEEP; ++t) __builtin_amdgcn_s_sleep(127);
                wait_flags<16>(ws, fA0, fA1, tag);
                if (lane == 0) LDS_CNT_ADD(G_FREE);
            }
            lds_wait(lds + G_FREE, 1);
            kv_issue(0); q_issue(); kv_issue(1);
        }
        asm volatile("s_waitcnt vmcnt(6)" ::: "memory");
        if (lane == 0) LDS_CNT_ADD(G_Q);
        kv_issue(3); kv_issue(4);
        WAIT_BAR(63);
        if (hw == 3) glds16s(lane * 16, kvb + 31 * 1024, lds0 + 31 * 1024);
        kv_issue(2);
        if (hw == 0) *(LDS3 u32x4*)(lds + lane * 16) = (u32x4){0u, 0u, 0u, 0u};
        u32x4 phm; unsigned pht;
        {
            const u16* wp = P.wphi + head * 400 + (r < 20 ? r : 0);
            unsigned e[10];
#pragma unroll
            for (int j = 0; j < 8; ++j) e[j] = wp[(8 * h + j + 4 * (head & 1)) * 20];
            e[8] = wp[((head & 1 ? 0 : 16) + 2 * h) * 20]; e[9] = wp[((head & 1 ? 1 : 17) + 2 * h) * 20];
            phm = (u32x4){e[0] | (e[1] << 16), e[2] | (e[3] << 16), e[4] | (e[5] << 16), e[6] | (e[7] << 16)}; pht = e[8] | (e[9] << 16);
            if (r >= 20) { phm = (u32x4){0u, 0u, 0u, 0u}; pht = 0u; }
        }
        WAIT_BAR(0);
        if (tid == 0 && bid < 240) set_flag(ws, fG + 2048, tag);
        STAMP(3);
        if (hw < 3) *(LDS3 u32x4*)(lds + X_KVF + (hw * 64 + lane) * 16) = (u32x4){0u, 0u, 0u, 0u};
        const int tokS = qb * 256 + hw * 64, m7 = tokS >> 7, t4 = (tokS >> 5) & 3;
        {
            const unsigned pt3 = (lane >> 1) & 3;
            const unsigned tb = WS_F_G + 2048 + ((qb * 2 + ((lane >> 3) & 1)) * 15 + ((lane & 1) ? nhi : nlo) + (pt3 < 3 ? pt3 : 0) * 5) * 8;
            POLL_ONE(hw == 0, 8, tb, WS_F_SD + (lane & 15) * 8, C_P1);
        }
        const unsigned om = t4 * 1024 + lane * 16, ot = 4096 + (t4 * 32 + r) * 8 + 4 * h;
        u32x4 qf[2][3]; float gS[2];
        {
            const unsigned kb = WS_KV + (NH + head) * 163840 + (2 * (m7 & 7) + (m7 >> 3)) * 10240, qbb = WS_Q + (NH + head) * 81920 + m7 * 5120;
            const unsigned ov = 5120 + t4 * 1280 + h * 320 + (r < 20 ? r : 19) * 16;
            u32x4 e0[2], w0[2], w1[2], eq[2]; unsigned et[2], eqt[2];
#pragma unroll
            for (int i = 0; i < 2; ++i) {
                e0[i] = LD16(kb + om + i * 1024); et[i] = LD4(kb + ot + i * 256); w0[i] = LD16(kb + ov + i * 1280); w1[i] = LD16(kb + ov + i * 1280 + 640);
                eq[i] = LD16(qbb + om + i * 1024); eqt[i] = LD4(qbb + ot + i * 256); gS[i] = __uint_as_float(LD4(WS_G + (tokS + i * 32 + r) * 4));
            }
            u32x4 i0, i1;
#pragma unroll
            for (int j = 0; j < 4; ++j) {
                const int e = r - 8 * h - 2 * j;
                i0[j] = e == 0 ? 0x3f80u : (e == 1 ? 0x3f800000u : 0u);
                i1[j] = e == 16 ? 0x3f80u : (e == 17 ? 0x3f800000u : 0u);
            }
            f32x16 k0 = {}, k1 = {};
#pragma unroll
            for (int i = 0; i < 2; ++i) {
                if (r >= 20) { w0[i] = (u32x4){one, one, one, one}; w1[i] = w0[i]; }
                u32x4 fp, fn, ft;
                feature_map(phm, pht, e0[i], et[i], h, fp, fn, ft);
                f32x16 T0 = MFMA32_16(fp, i0, z16); T0 = MFMA32_16(fn, i1, T0);
                f32x16 T1 = MFMA32_16(ft, i0, z16);
                u32x4 b00, b01, b10, b11;
#pragma unroll
                for (int j = 0; j < 4; ++j) {
                    b00[j] = pk2(T0[2 * j], T0[2 * j + 1]); b01[j] = pk2(T0[8 + 2 * j], T0[9 + 2 * j]);
                    b10[j] = pk2(T1[2 * j], T1[2 * j + 1]); b11[j] = pk2(T1[8 + 2 * j], T1[9 + 2 * j]);
                }
                k0 = MFMA32_16(w0[i], b00, k0); k0 = MFMA32_16(w1[i], b01, k0);
                k1 = MFMA32_16(w0[i], b10, k1); k1 = MFMA32_16(w1[i], b11, k1);
            }
            LDS3 char* sp = lds + X_SLOT + hw * 4096;
#pragma unroll
            for (int gq = 0; gq < 3; ++gq) {
                *(LDS3 f32x4*)(sp + gq * 1024 + lane * 16) = (f32x4){k0[4 * gq], k0[4 * gq + 1], k0[4 * gq + 2], k0[4 * gq + 3]};
                if (r < 8) *(LDS3 f32x4*)(sp + 3072 + (gq * 16 + h * 8 + r) * 16) = (f32x4){k1[4 * gq], k1[4 * gq + 1], k1[4 * gq + 2], k1[4 * gq + 3]};
            }
            asm volatile("s_waitcnt lgkmcnt(0)" ::: "memory");
            unsigned arr = 0;
            if (lane == 0) arr = LDS_CNT_ADD(C_KV);
            if (__builtin_amdgcn_readfirstlane(arr) == 3) {
                __builtin_amdgcn_fence(__ATOMIC_ACQUIRE, "wavefront");
#pragma unroll
                for (int i = 0; i < 4; ++i) {
                    const int qd = lane + 64 * i;
                    LDS3 const char* s0 = lds + X_SLOT + qd * 16;
                    const f32x4 s = (*(LDS3 const f32x4*)s0 + *(LDS3 const f32x4*)(s0 + 4096)) + (*(LDS3 const f32x4*)(s0 + 8192) + *(LDS3 const f32x4*)(s0 + 12288));
                    if (qd < 240) ST16(__builtin_bit_cast(u32x4, s), WS_KVP + (head * 8 + qb) * 4096 + qd * 16);
                }
                DRAIN();
                if (lane == 0) set_flag(ws, WS_F_KV + (head * 8 + qb) * 8, tag);
            }
#pragma unroll
            for (int i = 0; i < 2; ++i) feature_map(phm, pht, eq[i], eqt[i], h, qf[i][0], qf[i][1], qf[i][2]);
        }
        STAMP(4);
        POLL_ONE(hw == 0, 8, WS_F_KV + (head * 8 + (lane & 7)) * 8, WS_F_KV + (head * 8 + (lane & 7)) * 8, C_P2);
        {
            const int qd = hw * 60 + (lane < 60 ? lane : 59);
            f32x4 pp[8];
#pragma unroll
            for (int p = 0; p < 8; ++p) pp[p] = __builtin_bit_cast(f32x4, LD16(WS_KVP + (head * 8 + p) * 4096 + qd * 16));
            const f32x4 s4 = ((pp[0] + pp[1]) + (pp[2] + pp[3])) + ((pp[4] + pp[5]) + (pp[6] + pp[7]));
            const int gq = qd < 192 ? qd >> 6 : (qd - 192) >> 4, hs = qd < 192 ? (qd >> 5) & 1 : (qd >> 3) & 1, sg = qd < 192 ? qd & 31 : 32 + (qd & 7);
            const unsigned a = pk2(s4[0], s4[1]), b = pk2(s4[2], s4[3]);
            LDS3 u16* kp = (LDS3 u16*)(lds + X_KVF + (8 * gq + 4 * hs) * 96 + sg * 2);
            kp[0] = (u16)a; kp[48] = (u16)(a >> 16); kp[96] = (u16)b; kp[144] = (u16)(b >> 16);
            asm volatile("s_waitcnt lgkmcnt(0)" ::: "memory");
            if (lane == 0) LDS_CNT_ADD(C_KVF);
            lds_wait(lds + C_KVF, 4);
        }
        {
            LDS3 const char* kp = lds + X_KVF + (r < 24 ? r : 23) * 96 + h * 16;
            const u32x4 a0 = *(LDS3 const u32x4*)kp, a1 = *(LDS3 const u32x4*)(kp + 32), a2 = *(LDS3 const u32x4*)(kp + 64);
#pragma unroll
            for (int i = 0; i < 2; ++i) {
                f32x16 d = MFMA32_16(a0, qf[i][0], z16); d = MFMA32_16(a1, qf[i][1], d); d = MFMA32_16(a2, qf[i][2], d);
                const float den = upper_half(d[8]) + 1e-6f;
                float o[12];
#pragma unroll
                for (int j = 0; j < 12; ++j) o[j] = d[j];
                store_o(rs, o, (1.f - gS[i]) / den, tokS + i * 32 + r, 640 + head * 20, h);
            }
            DRAIN();
            unsigned arr = 0;
            if (lane == 0) arr = LDS_CNT_ADD(C_OB);
            if (__builtin_amdgcn_readfirstlane(arr) == 3 && lane == 0) set_flag(ws, WS_F_OB + (qb * 32 + head) * 8, tag);
        }
        STAMP(5);
        f32x4 c[5];
        POLL_ONE(hw == 0, 8, WS_F_OB + ((mtO >> 2) * 32 + r) * 8, WS_F_OB + ((mtO >> 2) * 32 + r) * 8, C_P3);
        {
            f32x4 acc[4][5];
#pragma unroll
            for (int t = 0; t < 4; ++t)
#pragma unroll
                for (int n = 0; n < 5; ++n) acc[t][n] = (f32x4){0.f, 0.f, 0.f, 0.f};
            const unsigned ab = WS_AO + ((mtO * 4) * 40 + 20 + 5 * hw) * 1024 + lane * 16, bb = WS_WT + ((ntO * 5) * 40 + 20 + 5 * hw) * 1024 + lane * 16;
#pragma unroll
            for (int ks = 0; ks < 5; ++ks) {
                u32x4 af[4], bf[5];
#pragma unroll
                for (int t = 0; t < 4; ++t) af[t] = LD16(ab + ((t + hw) & 3) * 40960 + ks * 1024);
#pragma unroll
                for (int n = 0; n < 5; ++n) bf[n] = LD16(bb + n * 40960 + ks * 1024);
#pragma unroll
                for (int t = 0; t < 4; ++t)
#pragma unroll
                    for (int n = 0; n < 5; ++n) acc[t][n] = MFMA16_32(bf[n], af[t], acc[t][n]);
            }
            lds_wait(lds + C_PROG2, 8);
#pragma unroll
            for (int t = 1; t < 4; ++t)
#pragma unroll
                for (int n = 0; n < 5; ++n) *(LDS3 f32x4*)(lds + 24576 + (((t + hw) & 3) * 3 + t - 1) * 5120 + n * 1024 + lane * 16) = acc[t][n];
            asm volatile("s_waitcnt lgkmcnt(0)" ::: "memory");
            if (lane == 0) LDS_CNT_ADD(C_BX);
            lds_wait(lds + C_BX, 4);
#pragma unroll
            for (int n = 0; n < 5; ++n) {
                LDS3 const char* xp = lds + 24576 + hw * 15360 + n * 1024 + lane * 16;
                c[n] = (acc[0][n] + *(LDS3 const f32x4*)xp) + (*(LDS3 const f32x4*)(xp + 5120) + *(LDS3 const f32x4*)(xp + 10240));
            }
            asm volatile("s_waitcnt lgkmcnt(0)" ::: "memory");
            if (lane == 0) LDS_CNT_ADD(C_BX);
            lds_wait(lds + C_BX, 8);
        }
        STAMP(6);
        lds_wait(lds + C_PROG, 8);
        lds_wait(lds + C_OB, 4);
        {
            const char* wsrc = ws + WS_WT + (size_t)ntO * 5 * 40960;
#pragma unroll 1
            for (int n = 0; n < 5; ++n)
#pragma unroll
                for (int t = 0; t < 5; ++t) { const int ks = hw + 4 * t; glds16s(lane * 16 + (n * 40 + ks) * 1024, wsrc, lds0 + L_WA + (n * 20 + ks) * 1024); }
#pragma unroll
            for (int n = 0; n < 3; ++n) *(LDS3 f32x4*)(lds + X_FIN + hw * 3072 + n * 1024 + lane * 16) = c[n];
            DRAIN();
            asm volatile("s_waitcnt lgkmcnt(0)" ::: "memory");
            if (lane == 0) LDS_CNT_ADD(C_WA);
            lds_wait(lds + C_OA, 8);
#pragma unroll
            for (int n = 3; n < 5; ++n) *(LDS3 f32x4*)(lds + 143360 + hw * 2048 + (n - 3) * 1024 + lane * 16) = c[n];
            asm volatile("s_waitcnt lgkmcnt(0)" ::: "memory");
            if (lane == 0) __hip_atomic_store((LDS3 unsigned*)(lds + C_BP + hw * 4), 1u, __ATOMIC_RELAXED, __HIP_MEMORY_SCOPE_WORKGROUP);
        }
        STAMP(7);
    } else {
        const int qs = w & 3, kh = w >> 2;
        const int q0 = qb * 256 + qs * 64;
        GET_TAG();
        if (bid < 240) {
            const int part = nt / 5, c0 = (nt - part * 5) * 128;
            const int wm = w & 1, wn = w >> 1;
            constexpr int RB = 32768;
            const int xr = wm * 64 + r;
            const unsigned xa = RB + xr * 128 + ((h ^ ((xr >> 1) & 7)) << 4);
            unsigned wt[2];
#pragma unroll
            for (int t = 0; t < 2; ++t) {
                const int q = (lane & 15) >> 2, p = lane & 3, blk = (lane >> 4) & 1, row = 8 * h + 4 * t + q;
                wt[t] = RB + 16384 + row * 256 + (((4 * wn + 2 * blk + (p >> 1)) ^ ((q << 2) | (2 * h + t))) << 4) + 8 * (p & 1);
            }
            auto gbody = [&](auto trc) {
                constexpr bool TR = decltype(trc)::value;
                u32x4 xf[2][8], wf[2][4];
                auto rd = [&](auto bc, int s) {
                    constexpr int B = decltype(bc)::value;
                    LDS3 const char* sb = lds + (s & 3) * 32768;
#pragma unroll
                    for (int ks = 0; ks < 4; ++ks) {
                        xf[B][2 * ks] = *(LDS3 const u32x4*)(sb + (xa ^ (ks * 32)));
                        xf[B][2 * ks + 1] = *(LDS3 const u32x4*)(sb + ((xa + 4096) ^ (ks * 32)));
                        const u32x2 lo = trread(sb + wt[0] + ks * 4096), hi = trread(sb + wt[1] + ks * 4096);
                        wf[B][ks] = (u32x4){lo[0], lo[1], hi[0], hi[1]};
                    }
                };
                f32x16 acc0, acc1;
                auto step = [&](auto bc, int s) {
                    constexpr int B = decltype(bc)::value;
                    if (s == 12) DRAIN();
                    __builtin_amdgcn_s_waitcnt(0xC07F);
                    BAR(); SBAR();
                    rd(std::integral_constant<int, B ^ 1>{}, s + 1);
#pragma unroll
                    for (int ks = 0; ks < 4; ++ks) {
                        if constexpr (TR) { acc0 = MFMA32_16(wf[B][ks], xf[B][2 * ks], acc0); acc1 = MFMA32_16(wf[B][ks], xf[B][2 * ks + 1], acc1); }
                        else              { acc0 = MFMA32_16(xf[B][2 * ks], wf[B][ks], acc0); acc1 = MFMA32_16(xf[B][2 * ks + 1], wf[B][ks], acc1); }
                    }
#pragma unroll
                    for (int i = 0; i < 4; ++i) { __builtin_amdgcn_sched_group_barrier(0x8, 1, 0); __builtin_amdgcn_sched_group_barrier(0x2, 2, 0); __builtin_amdgcn_sched_group_barrier(0x100, 4, 0); }
                    __builtin_amdgcn_sched_group_barrier(0x8, 4, 0);
                    SBAR();
                };
                WAIT_BAR(63);
                rd(std::integral_constant<int, 0>{}, 0);
#pragma unroll 1
                for (int mat = 0; mat < 2; ++mat) {
                    acc0 = (f32x16){}; acc1 = (f32x16){};
#pragma unroll 1
                    for (int kk = 0; kk < 10; kk += 2) {
                        step(std::integral_constant<int, 0>{}, mat * 10 + kk);
                        step(std::integral_constant<int, 1>{}, mat * 10 + kk + 1);
                    }
                    const int cpos = 2 * (mt & 7) + (mt >> 3);
                    if constexpr (TR) {
                        const float sc = (part == 0 && mat == 0) ? QSCALE : 1.f;
                        const unsigned base = part == 0 ? WS_Q + mat * NH * 81920 + mt * 5120 : WS_KV + mat * NH * 163840 + cpos * 10240;
                        const unsigned hs = part == 0 ? 81920 : 163840;
#pragma unroll
                        for (int pr = 0; pr < 2; ++pr) {
                            const int G8 = ((c0 + 32 * wn) >> 3) + 2 * pr + h, pp = G8 / 5, gi = G8 - pp * 5;
                            const unsigned hb0 = base + (2 * pp + (gi > 2)) * hs;
#pragma unroll
                            for (int i = 0; i < 2; ++i) {
                                const f32x16& a = i ? acc1 : acc0;
                                const int tl = wm * 64 + i * 32 + r;
                                const auto s0 = __builtin_amdgcn_permlane32_swap(pk2(a[8 * pr] * sc, a[8 * pr + 1] * sc), pk2(a[8 * pr + 4] * sc, a[8 * pr + 5] * sc), false, false);
                                const auto s1 = __builtin_amdgcn_permlane32_swap(pk2(a[8 * pr + 2] * sc, a[8 * pr + 3] * sc), pk2(a[8 * pr + 6] * sc, a[8 * pr + 7] * sc), false, false);
                                if (gi != 2) { const u32x4 ov = {s0[0], s1[0], s0[1], s1[1]}; ST16(ov, hb0 + (tl >> 5) * 1024 + (gi > 2 ? gi - 3 : gi) * 512 + r * 16); }
                                else { const u32x2 o0 = {s0[0], s1[0]}, o1 = {s0[1], s1[1]}; ST8(o0, hb0 + 4096 + tl * 8); ST8(o1, hb0 + hs + 4096 + tl * 8); }
                            }
                        }
                    } else {
                        const int n = c0 + 32 * wn + r, hd = n / 20, d = n - hd * 20, rho = (hd & 1) ? (d < 4 ? d + 16 : d - 4) : d;
                        const unsigned base = WS_KV + (mat * NH + hd) * 163840 + cpos * 10240 + 5120 + h * 320 + rho * 16;
#pragma unroll
                        for (int i = 0; i < 2; ++i)
#pragma unroll
                            for (int s2 = 0; s2 < 2; ++s2) {
                                const f32x16& a = i ? acc1 : acc0;
                                u32x4 ov = {pk2(a[8 * s2], a[8 * s2 + 1]), pk2(a[8 * s2 + 2], a[8 * s2 + 3]), pk2(a[8 * s2 + 4], a[8 * s2 + 5]), pk2(a[8 * s2 + 6], a[8 * s2 + 7])};
                                ST16(ov, base + (wm * 4 + i * 2 + s2) * 640);
                            }
                    }
                    if (mat == 0) STAMP(1);
                }
            };
            if (part < 2) gbody(std::true_type{}); else gbody(std::false_type{});
        } else {
            const int sj = bid - 240;
            WAIT_BAR(63);
            {
                const u32x4 ga = *(const u32x4*)(P.gw + lane * 8), gb = *(const u32x4*)(P.gw + 512 + (lane & 15) * 8);
                u32x4 xa[16], xb[4];
                const u16* xr = P.x + (size_t)(sj * 128 + w * 16) * DM;
#pragma unroll
                for (int rr = 0; rr < 16; ++rr) xa[rr] = *(const u32x4*)(xr + rr * DM + lane * 8);
#pragma unroll
                for (int i = 0; i < 4; ++i) xb[i] = *(const u32x4*)(xr + (i * 4 + (lane >> 4)) * DM + 512 + (lane & 15) * 8);
                float res = 0.f;
#pragma unroll
                for (int rr = 0; rr < 16; ++rr) {
                    float s = 0.f, s2 = 0.f;
#pragma unroll
                    for (int j = 0; j < 4; ++j) { s += bf_lo(xa[rr][j]) * bf_lo(ga[j]) + bf_hi(xa[rr][j]) * bf_hi(ga[j]); s2 += bf_lo(xb[rr >> 2][j]) * bf_lo(gb[j]) + bf_hi(xb[rr >> 2][j]) * bf_hi(gb[j]); }
                    if ((lane >> 4) == (rr & 3)) s += s2;
#pragma unroll
                    for (int o = 32; o >= 1; o >>= 1) s += __shfl_xor(s, o);
                    if (lane == rr) res = s;
                }
                if (lane < 16) ST4(__float_as_uint(1.f / (1.f + __expf(-res))), WS_G + (sj * 128 + w * 16 + lane) * 4);
            }
#pragma unroll 1
            for (int it = 0; it < 2; ++it) {
                unsigned e[4][8];
#pragma unroll
                for (int q = 0; q < 4; ++q) {
                    int item = mtid + 512 * (it * 4 + q); if (item >= 3200) item = 3199;
                    const int u = item / 320, n2 = item - u * 320, k0 = (sj * 10 + u) * 8;
                    const u16* wsrc = (k0 < 640 ? P.woa + (size_t)k0 * DM : P.wob + (size_t)(k0 - 640) * DM) + n2 * 2;
#pragma unroll
                    for (int j = 0; j < 8; ++j) e[q][j] = *(const unsigned*)(wsrc + (size_t)j * DM);
                }
#pragma unroll
                for (int q = 0; q < 4; ++q) {
                    int item = mtid + 512 * (it * 4 + q); if (item >= 3200) item = 3199;
                    const int u = item / 320, n = (item - u * 320) * 2, k0 = (sj * 10 + u) * 8;
                    const unsigned* f = e[q];
                    u32x4 lo = {(f[0] & 0xffffu) | (f[1] << 16), (f[2] & 0xffffu) | (f[3] << 16), (f[4] & 0xffffu) | (f[5] << 16), (f[6] & 0xffffu) | (f[7] << 16)};
                    u32x4 hi = {(f[0] >> 16) | (f[1] & 0xffff0000u), (f[2] >> 16) | (f[3] & 0xffff0000u), (f[4] >> 16) | (f[5] & 0xffff0000u), (f[6] >> 16) | (f[7] & 0xffff0000u)};
                    const unsigned dp = WS_WT + (((n >> 4) * 40 + (k0 >> 5)) * 64 + ((k0 >> 3) & 3) * 16 + (n & 15)) * 16;
                    ST16(lo, dp); ST16(hi, dp + 16);
                }
            }
            DRAIN();
            unsigned arr = 0;
            if (lane == 0) arr = LDS_CNT_ADD(G_ST);
            if (__builtin_amdgcn_readfirstlane(arr) == 7 && lane == 0) set_flag(ws, WS_F_SD + sj * 8, tag);
        }
        STAMP(2);
        const unsigned hb = lds0 + kh * 10240;
        const unsigned laneK = hb + lane * 16, laneT = hb + 4096 + r * 8 + h * 4, laneV = hb + 5120 + h * 320 + (r < 20 ? r : 19) * 16;
        const u64 vmask = 0x000fffff000fffffull;
        u32x4 v0a = {one, one, one, one}, v0b = v0a, v1 = v0a;
        u32x4 qA, qB, ka; unsigned qtA, qtB, kt;
        {
            lds_wait(lds + G_Q, 4);
            LDS3 const char* qp = lds + L_Q + (qs >> 1) * 5120;
            const unsigned om = (qs & 1) * 2048 + lane * 16, ot = 4096 + ((qs & 1) * 64 + r) * 8 + 4 * h;
            qA = *(LDS3 const u32x4*)(qp + om); qB = *(LDS3 const u32x4*)(qp + om + 1024);
            qtA = *(LDS3 const unsigned*)(qp + ot); qtB = *(LDS3 const unsigned*)(qp + ot + 256);
            DSR128(ka, laneK, 0); DSR32(kt, laneT, 0); DSR128M2(v0a, laneV, 0, v1, laneV, 640);
            asm volatile("s_waitcnt lgkmcnt(0)\n\ts_barrier" : "+v"(ka), "+v"(kt), "+v"(v0a), "+v"(v1), "+v"(qA), "+v"(qB), "+v"(qtA), "+v"(qtB) :: "memory");
        }
        STAMP(5);
        float mA, mB; u32x2 qTA, qTB; f32x16 sA, sB;
        {
            const u32x2 t0 = {kt, 0x3f80u}, a0 = {qtA, 0u}, b0 = {qtB, 0u};
            const f32x16 s = MFMA32_16(ka, qA, z16);
            const f32x16 s0 = MFMA32_8(t0, a0, s);
            f32x16 s1 = MFMA32_16(ka, qB, z16); s1 = MFMA32_8(t0, b0, s1);
            float x0 = s0[0], x1 = s1[0];
#pragma unroll
            for (int i = 1; i < 16; ++i) { x0 = fmaxf(x0, s0[i]); x1 = fmaxf(x1, s1[i]); }
            x0 = -xhalf_max(x0); x1 = -xhalf_max(x1);
            const unsigned hh = pk2(x0, x1);
            const unsigned ll = pk2(x0 - bf_lo(hh), x1 - bf_hi(hh));
            mA = -(bf_lo(hh) + bf_lo(ll)); mB = -(bf_hi(hh) + bf_hi(ll));
            const unsigned sel = h ? ll : hh;
            qTA = (u32x2){qtA, sel & 0xffffu}; qTB = (u32x2){qtB, sel >> 16};
            sA = MFMA32_8(t0, qTA, s);
        }
        f32x16 oA = {}, oB = {};
        u32x4 pa, pb = {0u, 0u, 0u, 0u};
#define EX4(S, i, Pv, j) do { Pv[j] = pk2(ex2(S[i]), ex2(S[i + 1])); Pv[j + 1] = pk2(ex2(S[i + 2]), ex2(S[i + 3])); PIN(Pv); SBAR(); } while (0)
#define KT2 ((u32x2){kt, 0x3f80u})
#define TILE(V0C, V0N, aK1, oK1, aT1, oT1, oV1, aV0, oV0) do { \
            sB = MFMA32_16(ka, qB, z16); SBAR(); DSR128(ka, aK1, oK1); EX4(sA, 0, pa, 0); \
            oB = MFMA32_16(v1, pb, oB); SBAR(); DSR128M2(v1, aV, oV1, V0N, aV0, oV0); EX4(sA, 4, pa, 2); \
            sB = MFMA32_8(KT2, qTB, sB); SBAR(); DSR32(kt, aT1, oT1); EX4(sA, 8, pb, 0); \
            oA = MFMA32_16(V0C, pa, oA); SBAR(); EX4(sA, 12, pb, 2); \
            LWAIT(1); sA = MFMA32_16(ka, qA, z16); SBAR(); EX4(sB, 0, pa, 0); \
            oA = MFMA32_16(v1, pb, oA); SBAR(); EX4(sB, 4, pa, 2); \
            LWAIT(0); sA = MFMA32_8(KT2, qTA, sA); SBAR(); EX4(sB, 8, pb, 0); \
            oB = MFMA32_16(V0C, pa, oB); SBAR(); EX4(sB, 12, pb, 2); \
        } while (0)
        unsigned aK = laneK, aT = laneT, aV = laneV;
#pragma unroll 1
        for (int c = 0; c < 8; ++c) {
            if (c == 1) { WAIT_BAR(0); }
            else if (c == 5 || c == 7) { if (lane == 0) LDS_CNT_ADD(c == 5 ? C_PROG2 : C_PROG); asm volatile("s_waitcnt lgkmcnt(0)" ::: "memory"); }
            const unsigned nx = (c < 7 ? c + 1 : 7) * 20480;
            const unsigned nK = laneK + nx, nT = laneT + nx, nV = laneV + nx;
            SBAR();
            TILE(v0a, v0b, aK, 1024, aT, 256, 640,  aV, 1280);
            TILE(v0b, v0a, aK, 2048, aT, 512, 1920, aV, 2560);
            TILE(v0a, v0b, aK, 3072, aT, 768, 3200, aV, 3840);
            TILE(v0b, v0a, nK, 0,    nT, 0,   4480, nV, 0);
            aK = nK; aT = nT; aV = nV;
        }
        oB = MFMA32_16(v1, pb, oB);
#undef TILE
#undef EX4
        STAMP(6);
        {
            float chk = 0.f;
#pragma unroll
            for (int i = 0; i < 12; ++i) chk += fabsf(oA[i]) * 0.f + fabsf(oB[i]) * 0.f;
            bool bad = !(chk == 0.f);
            if (h) bad = bad || !(oA[8] >= 0x1p-60f && oA[8] <= 0x1p60f && oB[8] >= 0x1p-60f && oB[8] <= 0x1p60f);
            if (__builtin_expect(__any(bad), 0)) {
                mA = -INFINITY; mB = -INFINITY;
#pragma unroll
                for (int i = 0; i < 16; ++i) { oA[i] = 0.f; oB[i] = 0.f; }
                const u32x2 a0 = {qtA, 0u}, b0 = {qtB, 0u};
#pragma unroll 1
                for (int t = 0; t < 32; ++t) {
                    const unsigned kb = WS_KV + head * 163840 + (2 * (t >> 2) + kh) * 10240, t4 = t & 3;
                    const u32x4 k0 = LD16(kb + t4 * 1024 + lane * 16);
                    const u32x2 t0 = {LD4(kb + 4096 + (t4 * 32 + r) * 8 + 4 * h), 0u};
                    u32x4 w0 = LD16(kb + 5120 + t4 * 1280 + h * 320 + (r < 20 ? r : 19) * 16), w1 = LD16(kb + 5120 + t4 * 1280 + 640 + h * 320 + (r < 20 ? r : 19) * 16);
                    if (r >= 20) { w0 = (u32x4){one, one, one, one}; w1 = w0; }
                    f32x16 s0 = MFMA32_16(k0, qA, z16); s0 = MFMA32_8(t0, a0, s0);
                    f32x16 s1 = MFMA32_16(k0, qB, z16); s1 = MFMA32_8(t0, b0, s1);
                    float x0 = s0[0], x1 = s1[0];
#pragma unroll
                    for (int i = 1; i < 16; ++i) { x0 = fmaxf(x0, s0[i]); x1 = fmaxf(x1, s1[i]); }
                    x0 = fmaxf(xhalf_max(x0), mA); x1 = fmaxf(xhalf_max(x1), mB);
                    const float g0 = (mA == x0) ? 1.f : ex2(mA - x0), g1 = (mB == x1) ? 1.f : ex2(mB - x1);
                    mA = x0; mB = x1;
#pragma unroll
                    for (int i = 0; i < 16; ++i) { oA[i] *= g0; oB[i] *= g1; }
#pragma unroll
                    for (int i = 0; i < 4; ++i) { pa[i] = pk2(ex2(s0[2 * i] - x0), ex2(s0[2 * i + 1] - x0)); pb[i] = pk2(ex2(s0[8 + 2 * i] - x0), ex2(s0[9 + 2 * i] - x0)); }
                    oA = MFMA32_16(w0, pa, oA); oA = MFMA32_16(w1, pb, oA);
#pragma unroll
                    for (int i = 0; i < 4; ++i) { pa[i] = pk2(ex2(s1[2 * i] - x1), ex2(s1[2 * i + 1] - x1)); pb[i] = pk2(ex2(s1[8 + 2 * i] - x1), ex2(s1[9 + 2 * i] - x1)); }
                    oB = MFMA32_16(w0, pa, oB); oB = MFMA32_16(w1, pb, oB);
                }
            }
        }
        lds_wait(lds + C_OB, 4);
        lds_wait(lds + C_PROG, 8);
        const float gO = __uint_as_float(LD4(WS_G + (q0 + kh * 32 + r) * 4));
        {
            LDS3 char* xp = lds + X_MRG + w * 2816;
            const f32x16& s = kh ? oA : oB;
            *(LDS3 f32x4*)(xp + lane * 16) = (f32x4){s[0], s[1], s[2], s[3]};
            *(LDS3 f32x4*)(xp + 1024 + lane * 16) = (f32x4){s[4], s[5], s[6], s[7]};
            if (!h) *(LDS3 f32x4*)(xp + 2048 + r * 16) = (f32x4){s[8], s[9], s[10], s[11]};
            else *(LDS3 f32x2*)(xp + 2560 + r * 8) = (f32x2){s[8], kh ? mA : mB};
            asm volatile("s_waitcnt lgkmcnt(0)" ::: "memory");
            if (lane == 0) __hip_atomic_store((LDS3 unsigned*)(lds + C_MRG + w * 4), 1u, __ATOMIC_RELAXED, __HIP_MEMORY_SCOPE_WORKGROUP);
        }
        lds_wait(lds + C_MRG + (w ^ 4) * 4, 1);
        bool lastw;
        {
            LDS3 const char* xp = lds + X_MRG + (w ^ 4) * 2816;
            const f32x4 p0 = *(LDS3 const f32x4*)(xp + lane * 16), p1 = *(LDS3 const f32x4*)(xp + 1024 + lane * 16), p2 = *(LDS3 const f32x4*)(xp + 2048 + r * 16);
            const f32x2 lm = *(LDS3 const f32x2*)(xp + 2560 + r * 8);
            const f32x16& s = kh ? oB : oA;
            const float mo = kh ? mB : mA, mp = lm[1];
            const float mx = fmaxf(mo, mp);
            const float fo = (mo == mx) ? 1.f : ex2(mo - mx), fq = (mp == mx) ? 1.f : ex2(mp - mx);
            float o[12];
#pragma unroll
            for (int i = 0; i < 4; ++i) { o[i] = s[i] * fo + p0[i] * fq; o[4 + i] = s[4 + i] * fo + p1[i] * fq; o[8 + i] = s[8 + i] * fo + p2[i] * fq; }
            const float l = upper_half(s[8]) * fo + lm[0] * fq;
            store_o(rs, o, gO / l, q0 + kh * 32 + r, head * 20, h);
            DRAIN();
            unsigned arr = 0;
            if (lane == 0) arr = LDS_CNT_ADD(C_OA);
            lastw = __builtin_amdgcn_readfirstlane(arr) == 7;
            if (lastw && lane == 0) set_flag(ws, WS_F_OA + (qb * 32 + head) * 8, tag);
        }
        STAMP(7);
        {
            const int c16 = lane & 15, g = lane >> 4, tt = w & 3, k0 = kh * 10;
            POLL_ONE(lastw, 4, WS_F_OA + ((mtO >> 2) * 32 + r) * 8, WS_F_OA + ((mtO >> 2) * 32 + r) * 8, C_P4);
            STAMP(9);
            const unsigned ab = WS_AO + ((mtO * 4 + tt) * 40 + k0) * 1024 + lane * 16;
            u32x4 af[10];
#pragma unroll
            for (int ks = 0; ks < 10; ++ks) af[ks] = LD16(ab + ks * 1024);
            lds_wait(lds + C_WA, 4);
            lds_wait(lds + C_OA, 8);
            f32x4 c[5];
#pragma unroll
            for (int n = 0; n < 5; ++n) c[n] = (f32x4){0.f, 0.f, 0.f, 0.f};
#pragma unroll
            for (int ks = 0; ks < 10; ++ks)
#pragma unroll
                for (int n = 0; n < 5; ++n) c[n] = MFMA16_32(*(LDS3 const u32x4*)(lds + L_WA + (n * 20 + k0 + ks) * 1024 + lane * 16), af[ks], c[n]);
            LDS3 char* xp = lds + 1024 + tt * 5120 + lane * 16;
            if (w < 4) { *(LDS3 f32x4*)(xp + 3072) = c[3]; *(LDS3 f32x4*)(xp + 4096) = c[4]; }
            else { *(LDS3 f32x4*)(xp) = c[0]; *(LDS3 f32x4*)(xp + 1024) = c[1]; *(LDS3 f32x4*)(xp + 2048) = c[2]; }
            asm volatile("s_waitcnt lgkmcnt(0)" ::: "memory");
            if (lane == 0) __hip_atomic_store((LDS3 unsigned*)(lds + C_FIN + w * 4), 1u, __ATOMIC_RELAXED, __HIP_MEMORY_SCOPE_WORKGROUP);
            lds_wait(lds + C_FIN + (w ^ 4) * 4, 1);
            lds_wait(lds + C_BP + tt * 4, 1);
            u16* op = P.out + (size_t)(mtO * 64 + tt * 16 + c16) * DM + ntO * 80 + g * 4;
#pragma unroll
            for (int n = 0; n < 5; ++n) {
                if ((w < 4) == (n < 3)) {
                    const f32x4 bp = *(LDS3 const f32x4*)(n < 3 ? lds + X_FIN + tt * 3072 + n * 1024 + lane * 16 : lds + 143360 + tt * 2048 + (n - 3) * 1024 + lane * 16);
                    const f32x4 s = c[n] + *(LDS3 const f32x4*)(xp + n * 1024) + bp;
                    *(u32x2*)(op + n * 16) = (u32x2){pk2(s[0], s[1]), pk2(s[2], s[3])};
                }
            }
            if (bid == 0 && mtid == 0) set_flag(ws, WS_EPOCH, tag);
        }
        STAMP(10);
    }
}

extern "C" void kernel_launch(void* const* d_in, const int* in_sizes, int n_in, void* d_out, int out_size, void* d_ws, size_t ws_size,
                              hipStream_t stream) {
    Params P;
    P.x = (const u16*)d_in[0]; P.wa = (const u16*)d_in[1]; P.woa = (const u16*)d_in[2]; P.wb = (const u16*)d_in[4];
    P.wphi = (const u16*)d_in[5]; P.wob = (const u16*)d_in[6]; P.gw = (const u16*)d_in[7]; P.ws = (char*)d_ws; P.out = (u16*)d_out;
    mega<<<256, 768, 0, stream>>>(P);
}
```

```cpp
#include <hip/hip_runtime.h>
#include <hip/hip_bf16.h>
#include <stdint.h>
#include <type_traits>

typedef __attribute__((ext_vector_type(8))) short bf16x8;
typedef __attribute__((ext_vector_type(4))) short bf16x4;
typedef __attribute__((ext_vector_type(16))) float f32x16;
typedef __attribute__((ext_vector_type(4))) float f32x4;
typedef __attribute__((ext_vector_type(2))) float f32x2;
typedef __attribute__((ext_vector_type(4))) unsigned u32x4;
typedef __attribute__((ext_vector_type(2))) unsigned u32x2;
typedef __attribute__((ext_vector_type(2))) __bf16 bf2_t;
typedef unsigned short u16;
typedef unsigned long long u64;
#define LDS3 __attribute__((address_space(3)))
typedef __attribute__((address_space(1))) u64 gu64;
#define RLX_AGENT __ATOMIC_RELAXED, __HIP_MEMORY_SCOPE_AGENT

#define PRIO_MODE 3
#define PRE_SLEEP 2
constexpr int SEQ = 2048, DM = 640, NH = 32;
constexpr float LOG2E = 1.4426950408889634f;
constexpr float QSCALE = 0.22360679774997896f * LOG2E;

constexpr unsigned WS_EPOCH = 0;
constexpr unsigned WS_F_G   = 256;
constexpr unsigned WS_F_SD  = WS_F_G + 4096;
constexpr unsigned WS_F_KV  = WS_F_SD + 256;
constexpr unsigned WS_F_OB  = WS_F_KV + 2048;
constexpr unsigned WS_F_OA  = WS_F_OB + 2048;
constexpr unsigned WS_G     = 16384;
constexpr unsigned WS_Q     = 32768;
constexpr unsigned WS_KV    = WS_Q + 2 * NH * 81920;
constexpr unsigned WS_KVP   = WS_KV + 2 * NH * 163840;
constexpr unsigned WS_AO    = WS_KVP + NH * 8 * 4096;
constexpr unsigned WS_WT    = WS_AO + SEQ * 1280 * 2;
constexpr unsigned WS_END   = WS_WT + 1280 * 640 * 2;

#define STAMP(i)

__device__ __forceinline__ unsigned pk2(float a, float b) { f32x2 v = {a, b}; bf2_t r = __builtin_convertvector(v, bf2_t); return __builtin_bit_cast(unsigned, r); }
__device__ __forceinline__ float bf_lo(unsigned u) { return __uint_as_float(u << 16); }
__device__ __forceinline__ float bf_hi(unsigned u) { return __uint_as_float(u & 0xffff0000u); }
__device__ __forceinline__ float ex2(float x) { return __builtin_amdgcn_exp2f(x); }
#define MFMA32_16(a, b, c) __builtin_amdgcn_mfma_f32_32x32x16_bf16(__builtin_bit_cast(bf16x8, a), __builtin_bit_cast(bf16x8, b), c, 0, 0, 0)
#define MFMA32_8(a, b, c) __builtin_amdgcn_mfma_f32_32x32x8bf16_1k(__builtin_bit_cast(bf16x4, a), __builtin_bit_cast(bf16x4, b), c, 0, 0, 0)
#define MFMA16_32(a, b, c) __builtin_amdgcn_mfma_f32_16x16x32_bf16(__builtin_bit_cast(bf16x8, a), __builtin_bit_cast(bf16x8, b), c, 0, 0, 0)

__device__ __forceinline__ void glds16(unsigned voff, const void* sbase, unsigned lds_base) {
    unsigned sv;
    asm volatile("s_mov_b32 %0, m0\n\ts_mov_b32 m0, %3\n\ts_nop 0\n\tglobal_load_lds_dwordx4 %1, %2\n\ts_mov_b32 m0, %0"
                 : "=&s"(sv) : "v"(voff), "s"(sbase), "s"(__builtin_amdgcn_readfirstlane(lds_base)) : "memory");
}
__device__ __forceinline__ void glds16s(unsigned voff, const void* sbase, unsigned lds_base) {
    unsigned sv;
    asm volatile("s_mov_b32 %0, m0\n\ts_mov_b32 m0, %3\n\ts_nop 0\n\tglobal_load_lds_dwordx4 %1, %2 sc1\n\ts_mov_b32 m0, %0"
                 : "=&s"(sv) : "v"(voff), "s"(sbase), "s"(__builtin_amdgcn_readfirstlane(lds_base)) : "memory");
}
__device__ __forceinline__ void glds4s(unsigned voff, const void* sbase, unsigned lds_base) {
    unsigned sv;
    asm volatile("s_mov_b32 %0, m0\n\ts_mov_b32 m0, %3\n\ts_nop 0\n\tglobal_load_lds_dword %1, %2 sc1\n\ts_mov_b32 m0, %0"
                 : "=&s"(sv) : "v"(voff), "s"(sbase), "s"(__builtin_amdgcn_readfirstlane(lds_base)) : "memory");
}
typedef short v4i16_t __attribute__((ext_vector_type(4)));
__device__ __forceinline__ u32x2 trread(LDS3 const char* p) {
    return __builtin_bit_cast(u32x2, __builtin_amdgcn_ds_read_tr16_b64_v4i16((LDS3 v4i16_t*)p));
}
#define WAIT_BAR(N) asm volatile("s_waitcnt vmcnt(" #N ") lgkmcnt(0)\n\ts_barrier" ::: "memory")
#define DRAIN() asm volatile("s_waitcnt vmcnt(0)" ::: "memory")
#define SBAR() __builtin_amdgcn_sched_barrier(0)
#define PIN(x) asm volatile("" : "+v"(x))

__device__ __forceinline__ float xhalf_max(float x) {
    auto r = __builtin_amdgcn_permlane32_swap(__float_as_uint(x), __float_as_uint(x), false, false);
    return fmaxf(__uint_as_float(r[0]), __uint_as_float(r[1]));
}
__device__ __forceinline__ float xhalf_sum(float x) {
    auto r = __builtin_amdgcn_permlane32_swap(__float_as_uint(x), __float_as_uint(x), false, false);
    return __uint_as_float(r[0]) + __uint_as_float(r[1]);
}
__device__ __forceinline__ float upper_half(float x) {
    auto r = __builtin_amdgcn_permlane32_swap(__float_as_uint(x), __float_as_uint(x), false, false);
    return __uint_as_float(r[1]);
}

typedef __amdgpu_buffer_rsrc_t rsrc_t;
#define ST8(v, off)  __builtin_amdgcn_raw_buffer_store_b64(v, rs, (int)(off), 0, 16)
#define ST16(v, off) __builtin_amdgcn_raw_buffer_store_b128(v, rs, (int)(off), 0, 16)
#define ST4(v, off)  __builtin_amdgcn_raw_buffer_store_b32(v, rs, (int)(off), 0, 16)
#define LD16(off) __builtin_amdgcn_raw_buffer_load_b128(rs, (int)(off), 0, 16)
#define LD4(off)  __builtin_amdgcn_raw_buffer_load_b32(rs, (int)(off), 0, 16)

template <int SLP = 2>
__device__ __forceinline__ void wait_flags(const char* ws, unsigned off0, unsigned off1, u64 tag) {
    gu64* p0 = (gu64*)(ws + off0); gu64* p1 = (gu64*)(ws + off1);
#pragma unroll 1
    for (unsigned spins = 0; spins < (1u << 16); ++spins) {
        const u64 a = __hip_atomic_load(p0, RLX_AGENT), b = __hip_atomic_load(p1, RLX_AGENT);
        if (__all(a == tag && b == tag)) break;
        __builtin_amdgcn_s_sleep(SLP);
    }
    __builtin_amdgcn_fence(__ATOMIC_ACQUIRE, "wavefront");
}
__device__ __forceinline__ void set_flag(char* ws, unsigned off, u64 tag) { __hip_atomic_store((gu64*)(ws + off), tag, RLX_AGENT); }

struct Params { const u16* x; const u16* wa; const u16* wb; const u16* wphi; const u16* woa; const u16* wob; const u16* gw; char* ws; u16* out; };

__device__ __forceinline__ void feature_map(const u32x4& pm, unsigned pt, const u32x4& tm, unsigned tt, int h, u32x4& fp, u32x4& fn, u32x4& ft) {
    const f32x16 z16 = {};
    const u32x2 a8 = {pt, 0u}, b8 = {tt, 0u};
    f32x16 s = MFMA32_16(pm, tm, z16); s = MFMA32_8(a8, b8, s);
    float v[12];
#pragma unroll
    for (int i = 0; i < 12; i += 2) { const unsigned u = pk2(s[i], s[i + 1]); v[i] = bf_lo(u) * LOG2E; v[i + 1] = bf_hi(u) * LOG2E; }
    float mx = v[0], mn = v[0];
#pragma unroll
    for (int i = 1; i < 8; ++i) { mx = fmaxf(mx, v[i]); mn = fminf(mn, v[i]); }
    const float tx = fmaxf(fmaxf(v[8], v[9]), fmaxf(v[10], v[11])), tn = fminf(fminf(v[8], v[9]), fminf(v[10], v[11]));
    if (!h) { mx = fmaxf(mx, tx); mn = fminf(mn, tn); }
    mx = xhalf_max(mx); mn = -xhalf_max(-mn);
    float ep[12], en[12], sp = 0.f, sn = 0.f;
#pragma unroll
    for (int i = 0; i < 12; ++i) {
        ep[i] = ex2(v[i] - mx); en[i] = ex2(mn - v[i]);
        if (i >= 8 && h) { ep[i] = 0.f; en[i] = 0.f; }
        sp += ep[i]; sn += en[i];
    }
    sp = __builtin_amdgcn_rcpf(xhalf_sum(sp)); sn = __builtin_amdgcn_rcpf(xhalf_sum(sn));
#pragma unroll
    for (int i = 0; i < 4; ++i) { fp[i] = pk2(ep[2 * i] * sp, ep[2 * i + 1] * sp); fn[i] = pk2(en[2 * i] * sn, en[2 * i + 1] * sn); }
    ft = (u32x4){pk2(ep[8] * sp, ep[9] * sp), pk2(ep[10] * sp, ep[11] * sp), pk2(en[8] * sn, en[9] * sn), pk2(en[10] * sn, en[11] * sn)};
}

__device__ __forceinline__ void store_o(rsrc_t rs, const float (&o)[12], float sc, int tok, int kbase, int h) {
#pragma unroll
    for (int gq = 0; gq < 3; ++gq) {
        const int d = 8 * gq + 4 * h;
        if (d < 20) {
            const int k = kbase + ((kbase & 4) ? (d == 16 ? 0 : d + 4) : d);
            u32x2 ov = {pk2(o[4 * gq] * sc, o[4 * gq + 1] * sc), pk2(o[4 * gq + 2] * sc, o[4 * gq + 3] * sc)};
            ST8(ov, WS_AO + (((tok >> 4) * 40 + (k >> 5)) * 64 + ((k >> 3) & 3) * 16 + (tok & 15)) * 16 + (k & 7) * 2);
        }
    }
}

#define DSR128(dst, addr, off) asm volatile("ds_read_b128 %0, %1 offset:%2" : "=v"(dst) : "v"(addr), "i"(off))
#define DSR32(dst, addr, off) asm volatile("ds_read_b32 %0, %1 offset:%2" : "=v"(dst) : "v"(addr), "i"(off))
#define DSR128M2(d0, a0, o0, d1, a1, o1) asm volatile("s_mov_b64 exec, %6\n\tds_read_b128 %0, %2 offset:%3\n\tds_read_b128 %1, %4 offset:%5\n\ts_mov_b64 exec, -1" \
                                                      : "+v"(d0), "+v"(d1) : "v"(a0), "i"(o0), "v"(a1), "i"(o1), "s"(vmask))
#define LWAIT(n) do { asm volatile("s_waitcnt lgkmcnt(" #n ")" ::: "memory"); SBAR(); } while (0)

#define BAR() asm volatile("s_barrier" ::: "memory")
#define LDS_CNT_ADD(off) __hip_atomic_fetch_add((LDS3 unsigned*)(lds + (off)), 1u, __ATOMIC_RELAXED, __HIP_MEMORY_SCOPE_WORKGROUP)

template <int SLP = 1>
__device__ __forceinline__ void lds_wait(LDS3 char* p, unsigned want) {
#pragma unroll 1
    for (unsigned spins = 0; spins < (1u << 20); ++spins) {
        if ((unsigned)__builtin_amdgcn_readfirstlane(__hip_atomic_load((LDS3 unsigned*)p, __ATOMIC_RELAXED, __HIP_MEMORY_SCOPE_WORKGROUP)) >= want) break;
        __builtin_amdgcn_s_sleep(SLP);
    }
    __builtin_amdgcn_fence(__ATOMIC_ACQUIRE, "wavefront");
}

#define POLL_ONE(cond, SLP, o0, o1, word) do { if (cond) { wait_flags<SLP>(ws, o0, o1, tag); \
        if (lane == 0) __hip_atomic_store((LDS3 unsigned*)(lds + (word)), 1u, __ATOMIC_RELAXED, __HIP_MEMORY_SCOPE_WORKGROUP); } else lds_wait<SLP>(lds + (word), 1); } while (0)
constexpr int C_KV = 0, C_KVF = 4, C_OB = 8, C_PROG = 12, C_WA = 16, C_OA = 20, C_MRG = 32  , C_FIN = 64  , C_BP = 96  ;
constexpr int C_PROG2 = 24, C_BX = 28, C_P1 = 112, C_P2 = 116, C_P3 = 120, C_P4 = 124, C_PR = 128  ;
constexpr int G_W = 31744;
constexpr int G_FULL = G_W, G_FREE = G_W + 16, G_Q = G_W + 32, G_KV0 = G_W + 36, G_ST = G_W + 40;
constexpr int C_END = 160, X_G = 256;
constexpr int X_KVF = 1280, X_SLOT = 4096;
constexpr int X_MRG = 1280;
constexpr int L_WA = 24576;
constexpr int L_Q = 65536;
constexpr int X_FIN = 126976;

__global__ __launch_bounds__(768) void mega(Params P) {
    __shared__ __attribute__((aligned(16))) char smem[163840];
    LDS3 char* lds = (LDS3 char*)smem;
    char* ws = P.ws;
    const int tid = threadIdx.x, lane = tid & 63, r = lane & 31, h = lane >> 5;
    const int hw = __builtin_amdgcn_readfirstlane(tid >> 6);
    const bool helper = hw < 4;
    const int w = hw - 4, mtid = tid - 256;
    const int bid = blockIdx.x;
    const unsigned lds0 = (unsigned)(uintptr_t)smem;
    const rsrc_t rs = __builtin_amdgcn_make_buffer_rsrc(ws, 0, WS_END, 0x00020000);
    STAMP(0);
    u64 tag = 0;
#define GET_TAG() do { asm volatile("s_load_dwordx2 %0, %1, 0x0\n\ts_waitcnt lgkmcnt(0)" : "=s"(tag) : "s"(ws) : "memory"); tag += 1; } while (0)
    const int head = (bid & 7) * 4 + ((bid >> 3) & 3), qb = bid >> 5;
    const int nlo = (head * 20) >> 7, nhi = (head * 20 + 19) >> 7;
    const int mtO = (bid & 7) * 4 + ((bid >> 3) & 3), ntO = bid >> 5;
    const int lin = (bid & 7) * 30 + (bid >> 3), lh = lin >= 120, lrem = lin - 120 * lh;
    const int mt = lh * 8 + (lrem & 7), nt = lrem >> 3;
    const unsigned fG = WS_F_G + (mt * 15 + nt) * 8;
    const unsigned one = (r == 20) ? 0x3f803f80u : 0u;
    const f32x16 z16 = {};

    if (helper) {
        const unsigned fA0 = WS_F_G + ((lane & 15) * 15 + (lane < 32 ? 5 : 10) + ((lane & 16) ? nhi : nlo)) * 8;
        const unsigned fA1 = WS_F_G + ((qb * 2 + (lane & 1)) * 15 + ((lane & 2) ? nhi : nlo)) * 8;
        __builtin_amdgcn_s_setprio(3);
        const char* kvb = ws + WS_KV + head * 163840;
        auto kv_issue = [&](int grp) {
#pragma unroll
            for (int t = 0; t < 8; ++t) { int pc = grp * 32 + hw + 4 * t; if (pc == 31) pc = 30; glds16s(lane * 16, kvb + pc * 1024, lds0 + pc * 1024); }
        };
        auto q_issue = [&]() {
            const char* qbase = ws + WS_Q + head * 81920 + qb * 10240;
#pragma unroll
            for (int t = 0; t < 3; ++t) { const int pc = hw + 4 * t < 10 ? hw + 4 * t : 9; glds16s(lane * 16, qbase + pc * 1024, lds0 + L_Q + pc * 1024); }
        };
        if (bid < 240) {
            const int rot = (nt * 3 + mt * 5 + (mt >> 1)) % 10;
            const unsigned xo = (mt * 128 + 8 * hw + (lane >> 3)) * 1280 + (((lane & 7) ^ ((4 * hw + (lane >> 4)) & 7)) << 4);
            const unsigned wo = (4 * hw + (lane >> 4)) * 3840 + nt * 256 + (((lane & 15) ^ (((lane >> 4) << 2) | hw)) << 4);
            const unsigned dX = lds0 + 32768 + hw * 1024;
            auto issue = [&](int s) {
                int kt = (s >= 10 ? s - 10 : s) + rot; if (kt >= 10) kt -= 10;
                const char* xb = (const char*)P.x + kt * 128;
                const char* wb = (const char*)(s >= 10 ? P.wb : P.wa) + (size_t)kt * (64 * 3840);
                const unsigned d = dX + (s & 3) * 32768;
#pragma unroll
                for (int i = 0; i < 4; ++i) { glds16(xo, xb + i * 40960, d + i * 4096); glds16(wo, wb + i * 61440, d + 16384 + i * 4096); }
            };
#pragma unroll 1
            for (int n = 0; n < 20; ++n) {
                if (n == 2) { if (hw == 0 && lane < 16) *(LDS3 unsigned*)(lds + G_W + lane * 4) = 0u; GET_TAG(); WAIT_BAR(8); }
                else if (n == 3) { WAIT_BAR(8); }
                else if (n >= 5) { WAIT_BAR(16); }
                if (n == 16 && tid == 0) set_flag(ws, fG, tag);
                if (n == 19 && hw == 0) {
                    glds4s(fA0, ws, lds0); glds4s(fA0 + 4, ws, lds0 + 256); glds4s(fA1, ws, lds0 + 512); glds4s(fA1 + 4, ws, lds0 + 768);
                }
                issue(n);
            }
            WAIT_BAR(16);
            asm volatile("s_waitcnt vmcnt(8)" ::: "memory");
            if (hw == 0) {
                LDS3 const unsigned* f = (LDS3 const unsigned*)lds + lane;
                const unsigned tl = (unsigned)tag, th = (unsigned)(tag >> 32);
                if (!__all(f[0] == tl && f[64] == th && f[128] == tl && f[192] == th)) wait_flags<8>(ws, fA0, fA1, tag);
            }
            WAIT_BAR(63);
            kv_issue(0); q_issue();
            WAIT_BAR(11);
            kv_issue(1);
            WAIT_BAR(63);
        } else {
            if (hw == 0 && lane < 16) *(LDS3 unsigned*)(lds + G_W + lane * 4) = 0u;
            GET_TAG();
            WAIT_BAR(63);
            if (hw == 0) {
#pragma unroll 1
                for (int t = 0; t < PRE_SLEEP; ++t) __builtin_amdgcn_s_sleep(127);
                wait_flags<16>(ws, fA0, fA1, tag);
                if (lane == 0) LDS_CNT_ADD(G_FREE);
            }
            lds_wait<16>(lds + G_FREE, 1);
            kv_issue(0); q_issue(); kv_issue(1);
        }
        asm volatile("s_waitcnt vmcnt(6)" ::: "memory");
        if (lane == 0) LDS_CNT_ADD(G_Q);
        kv_issue(3); kv_issue(4);
        WAIT_BAR(63);
        if (hw == 3) glds16s(lane * 16, kvb + 31 * 1024, lds0 + 31 * 1024);
        kv_issue(2);
        if (hw == 0) *(LDS3 u32x4*)(lds + lane * 16) = (u32x4){0u, 0u, 0u, 0u};
        u32x4 phm; unsigned pht;
        {
            const u16* wp = P.wphi + head * 400 + (r < 20 ? r : 0);
            unsigned e[10];
#pragma unroll
            for (int j = 0; j < 8; ++j) e[j] = wp[(8 * h + j + 4 * (head & 1)) * 20];
            e[8] = wp[((head & 1 ? 0 : 16) + 2 * h) * 20]; e[9] = wp[((head & 1 ? 1 : 17) + 2 * h) * 20];
            phm = (u32x4){e[0] | (e[1] << 16), e[2] | (e[3] << 16), e[4] | (e[5] << 16), e[6] | (e[7] << 16)}; pht = e[8] | (e[9] << 16);
            if (r >= 20) { phm = (u32x4){0u, 0u, 0u, 0u}; pht = 0u; }
        }
        WAIT_BAR(0);
        if (tid == 0 && bid < 240) set_flag(ws, fG + 2048, tag);
        STAMP(3);
        if (hw * 64 + lane < 144) *(LDS3 u32x4*)(lds + X_KVF + (hw * 64 + lane) * 16) = (u32x4){0u, 0u, 0u, 0u};
        const int tokS = qb * 256 + hw * 64, m7 = tokS >> 7, t4 = (tokS >> 5) & 3;
        {
            const unsigned pt3 = (lane >> 1) & 3;
            const unsigned tb = WS_F_G + 2048 + ((qb * 2 + ((lane >> 3) & 1)) * 15 + ((lane & 1) ? nhi : nlo) + (pt3 < 3 ? pt3 : 0) * 5) * 8;
            POLL_ONE(hw == 0, 8, tb, WS_F_SD + (lane & 15) * 8, C_P1);
        }
        const unsigned om = t4 * 1024 + lane * 16, ot = 4096 + (t4 * 32 + r) * 8 + 4 * h;
        u32x4 qf[2][3]; float gS[2];
        {
            const unsigned kb = WS_KV + (NH + head) * 163840 + (2 * (m7 & 7) + (m7 >> 3)) * 10240, qbb = WS_Q + (NH + head) * 81920 + m7 * 5120;
            const unsigned ov = 5120 + t4 * 1280 + h * 320 + (r < 20 ? r : 19) * 16;
            u32x4 e0[2], w0[2], w1[2], eq[2]; unsigned et[2], eqt[2];
#pragma unroll
            for (int i = 0; i < 2; ++i) {
                e0[i] = LD16(kb + om + i * 1024); et[i] = LD4(kb + ot + i * 256); w0[i] = LD16(kb + ov + i * 1280); w1[i] = LD16(kb + ov + i * 1280 + 640);
                eq[i] = LD16(qbb + om + i * 1024); eqt[i] = LD4(qbb + ot + i * 256); gS[i] = __uint_as_float(LD4(WS_G + (tokS + i * 32 + r) * 4));
            }
            *(LDS3 float*)(lds + X_G + (hw * 64 + lane) * 4) = h ? gS[1] : gS[0];
            u32x4 i0, i1;
#pragma unroll
            for (int j = 0; j < 4; ++j) {
                const int e = r - 8 * h - 2 * j;
                i0[j] = e == 0 ? 0x3f80u : (e == 1 ? 0x3f800000u : 0u);
                i1[j] = e == 16 ? 0x3f80u : (e == 17 ? 0x3f800000u : 0u);
            }
            f32x16 k0 = {}, k1 = {};
#pragma unroll
            for (int i = 0; i < 2; ++i) {
                if (r >= 20) { w0[i] = (u32x4){one, one, one, one}; w1[i] = w0[i]; }
                u32x4 fp, fn, ft;
                feature_map(phm, pht, e0[i], et[i], h, fp, fn, ft);
                f32x16 T0 = MFMA32_16(fp, i0, z16); T0 = MFMA32_16(fn, i1, T0);
                f32x16 T1 = MFMA32_16(ft, i0, z16);
                u32x4 b00, b01, b10, b11;
#pragma unroll
                for (int j = 0; j < 4; ++j) {
                    b00[j] = pk2(T0[2 * j], T0[2 * j + 1]); b01[j] = pk2(T0[8 + 2 * j], T0[9 + 2 * j]);
                    b10[j] = pk2(T1[2 * j], T1[2 * j + 1]); b11[j] = pk2(T1[8 + 2 * j], T1[9 + 2 * j]);
                }
                k0 = MFMA32_16(w0[i], b00, k0); k0 = MFMA32_16(w1[i], b01, k0);
                k1 = MFMA32_16(w0[i], b10, k1); k1 = MFMA32_16(w1[i], b11, k1);
            }
            LDS3 char* sp = lds + X_SLOT + hw * 4096;
#pragma unroll
            for (int gq = 0; gq < 3; ++gq) {
                *(LDS3 f32x4*)(sp + gq * 1024 + lane * 16) = (f32x4){k0[4 * gq], k0[4 * gq + 1], k0[4 * gq + 2], k0[4 * gq + 3]};
                if (r < 8) *(LDS3 f32x4*)(sp + 3072 + (gq * 16 + h * 8 + r) * 16) = (f32x4){k1[4 * gq], k1[4 * gq + 1], k1[4 * gq + 2], k1[4 * gq + 3]};
            }
            asm volatile("s_waitcnt lgkmcnt(0)" ::: "memory");
            unsigned arr = 0;
            if (lane == 0) arr = LDS_CNT_ADD(C_KV);
            if (__builtin_amdgcn_readfirstlane(arr) == 3) {
                __builtin_amdgcn_fence(__ATOMIC_ACQUIRE, "wavefront");
#pragma unroll
                for (int i = 0; i < 4; ++i) {
                    const int qd = lane + 64 * i;
                    LDS3 const char* s0 = lds + X_SLOT + qd * 16;
                    const f32x4 s = (*(LDS3 const f32x4*)s0 + *(LDS3 const f32x4*)(s0 + 4096)) + (*(LDS3 const f32x4*)(s0 + 8192) + *(LDS3 const f32x4*)(s0 + 12288));
                    if (qd < 240) ST16(__builtin_bit_cast(u32x4, s), WS_KVP + (head * 8 + qb) * 4096 + qd * 16);
                }
                DRAIN();
                if (lane == 0) set_flag(ws, WS_F_KV + (head * 8 + qb) * 8, tag);
            }
#pragma unroll
            for (int i = 0; i < 2; ++i) feature_map(phm, pht, eq[i], eqt[i], h, qf[i][0], qf[i][1], qf[i][2]);
        }
        STAMP(4);
        POLL_ONE(hw == 0, 8, WS_F_KV + (head * 8 + (lane & 7)) * 8, WS_F_KV + (head * 8 + (lane & 7)) * 8, C_P2);
        {
            const int qd = hw * 60 + (lane < 60 ? lane : 59);
            f32x4 pp[8];
#pragma unroll
            for (int p = 0; p < 8; ++p) pp[p] = __builtin_bit_cast(f32x4, LD16(WS_KVP + (head * 8 + p) * 4096 + qd * 16));
            const f32x4 s4 = ((pp[0] + pp[1]) + (pp[2] + pp[3])) + ((pp[4] + pp[5]) + (pp[6] + pp[7]));
            const int gq = qd < 192 ? qd >> 6 : (qd - 192) >> 4, hs = qd < 192 ? (qd >> 5) & 1 : (qd >> 3) & 1, sg = qd < 192 ? qd & 31 : 32 + (qd & 7);
            const unsigned a = pk2(s4[0], s4[1]), b = pk2(s4[2], s4[3]);
            LDS3 u16* kp = (LDS3 u16*)(lds + X_KVF + (8 * gq + 4 * hs) * 96 + sg * 2);
            kp[0] = (u16)a; kp[48] = (u16)(a >> 16); kp[96] = (u16)b; kp[144] = (u16)(b >> 16);
            asm volatile("s_waitcnt lgkmcnt(0)" ::: "memory");
            if (lane == 0) LDS_CNT_ADD(C_KVF);
            lds_wait<8>(lds + C_KVF, 4);
        }
        {
            LDS3 const char* kp = lds + X_KVF + (r < 24 ? r : 23) * 96 + h * 16;
            const u32x4 a0 = *(LDS3 const u32x4*)kp, a1 = *(LDS3 const u32x4*)(kp + 32), a2 = *(LDS3 const u32x4*)(kp + 64);
#pragma unroll
            for (int i = 0; i < 2; ++i) {
                f32x16 d = MFMA32_16(a0, qf[i][0], z16); d = MFMA32_16(a1, qf[i][1], d); d = MFMA32_16(a2, qf[i][2], d);
                const float den = upper_half(d[8]) + 1e-6f;
                float o[12];
#pragma unroll
                for (int j = 0; j < 12; ++j) o[j] = d[j];
                store_o(rs, o, (1.f - gS[i]) / den, tokS + i * 32 + r, 640 + head * 20, h);
            }
            DRAIN();
            unsigned arr = 0;
            if (lane == 0) arr = LDS_CNT_ADD(C_OB);
            if (__builtin_amdgcn_readfirstlane(arr) == 3 && lane == 0) set_flag(ws, WS_F_OB + (qb * 32 + head) * 8, tag);
        }
        STAMP(5);
        f32x4 c[5];
#pragma unroll
        for (int n = 0; n < 5; ++n) c[n] = (f32x4){0.f, 0.f, 0.f, 0.f};
        {
            const unsigned ab = WS_AO + ((mtO * 4 + hw) * 40 + 20) * 1024 + lane * 16, bb = WS_WT + ((ntO * 5) * 40 + 20) * 1024 + lane * 16;
            u32x4 af[5], bf[5][5];
#pragma unroll
            for (int ks = 0; ks < 5; ++ks)
#pragma unroll
                for (int n = 0; n < 5; ++n) bf[ks][n] = LD16(bb + n * 40960 + ks * 1024);
            POLL_ONE(hw == 0, 8, WS_F_OB + ((mtO >> 2) * 32 + r) * 8, WS_F_OB + ((mtO >> 2) * 32 + r) * 8, C_P3);
#pragma unroll
            for (int ks = 0; ks < 5; ++ks) af[ks] = LD16(ab + ks * 1024);
#pragma unroll
            for (int ks = 0; ks < 20; ++ks) {
#pragma unroll
                for (int n = 0; n < 5; ++n) c[n] = MFMA16_32(bf[ks % 5][n], af[ks % 5], c[n]);
                SBAR();
                if (ks < 15) {
                    af[ks % 5] = LD16(ab + (ks + 5) * 1024);
#pragma unroll
                    for (int n = 0; n < 5; ++n) bf[ks % 5][n] = LD16(bb + n * 40960 + (ks + 5) * 1024);
                }
                SBAR();
            }
        }
        STAMP(6);
        lds_wait<8>(lds + C_PROG, 8);
#pragma unroll
        for (int n = 0; n < 3; ++n) *(LDS3 f32x4*)(lds + X_FIN + hw * 3072 + n * 1024 + lane * 16) = c[n];
        lds_wait<4>(lds + C_END, 8);
#pragma unroll
        for (int n = 3; n < 5; ++n) *(LDS3 f32x4*)(lds + 143360 + hw * 2048 + (n - 3) * 1024 + lane * 16) = c[n];
        asm volatile("s_waitcnt lgkmcnt(0)" ::: "memory");
        if (lane == 0) __hip_atomic_store((LDS3 unsigned*)(lds + C_BP + hw * 4), 1u, __ATOMIC_RELAXED, __HIP_MEMORY_SCOPE_WORKGROUP);
        STAMP(7);
    } else {
        const int qs = w & 3, kh = w >> 2;
        const int q0 = qb * 256 + qs * 64;
        GET_TAG();
        if (bid < 240) {
            const int part = nt / 5, c0 = (nt - part * 5) * 128;
            const int wm = w & 1, wn = w >> 1;
            constexpr int RB = 32768;
            const int xr = wm * 64 + r;
            const unsigned xa = RB + xr * 128 + ((h ^ ((xr >> 1) & 7)) << 4);
            unsigned wt[2];
#pragma unroll
            for (int t = 0; t < 2; ++t) {
                const int q = (lane & 15) >> 2, p = lane & 3, blk = (lane >> 4) & 1, row = 8 * h + 4 * t + q;
                wt[t] = RB + 16384 + row * 256 + (((4 * wn + 2 * blk + (p >> 1)) ^ ((q << 2) | (2 * h + t))) << 4) + 8 * (p & 1);
            }
            auto gbody = [&](auto trc) {
                constexpr bool TR = decltype(trc)::value;
                u32x4 xf[2][8], wf[2][4];
                auto rd = [&](auto bc, int s) {
                    constexpr int B = decltype(bc)::value;
                    LDS3 const char* sb = lds + (s & 3) * 32768;
#pragma unroll
                    for (int ks = 0; ks < 4; ++ks) {
                        xf[B][2 * ks] = *(LDS3 const u32x4*)(sb + (xa ^ (ks * 32)));
                        xf[B][2 * ks + 1] = *(LDS3 const u32x4*)(sb + ((xa + 4096) ^ (ks * 32)));
                        const u32x2 lo = trread(sb + wt[0] + ks * 4096), hi = trread(sb + wt[1] + ks * 4096);
                        wf[B][ks] = (u32x4){lo[0], lo[1], hi[0], hi[1]};
                    }
                };
                f32x16 acc0, acc1;
                auto step = [&](auto bc, int s) {
                    constexpr int B = decltype(bc)::value;
                    if (s == 12) DRAIN();
                    __builtin_amdgcn_s_waitcnt(0xC07F);
                    BAR(); SBAR();
                    rd(std::integral_constant<int, B ^ 1>{}, s + 1);
#pragma unroll
                    for (int ks = 0; ks < 4; ++ks) {
                        if constexpr (TR) { acc0 = MFMA32_16(wf[B][ks], xf[B][2 * ks], acc0); acc1 = MFMA32_16(wf[B][ks], xf[B][2 * ks + 1], acc1); }
                        else              { acc0 = MFMA32_16(xf[B][2 * ks], wf[B][ks], acc0); acc1 = MFMA32_16(xf[B][2 * ks + 1], wf[B][ks], acc1); }
                    }
#pragma unroll
                    for (int i = 0; i < 4; ++i) { __builtin_amdgcn_sched_group_barrier(0x8, 1, 0); __builtin_amdgcn_sched_group_barrier(0x2, 2, 0); __builtin_amdgcn_sched_group_barrier(0x100, 4, 0); }
                    __builtin_amdgcn_sched_group_barrier(0x8, 4, 0);
                    SBAR();
                };
                WAIT_BAR(63);
                rd(std::integral_constant<int, 0>{}, 0);
#pragma unroll 1
                for (int mat = 0; mat < 2; ++mat) {
                    acc0 = (f32x16){}; acc1 = (f32x16){};
#pragma unroll 1
                    for (int kk = 0; kk < 10; kk += 2) {
                        step(std::integral_constant<int, 0>{}, mat * 10 + kk);
                        step(std::integral_constant<int, 1>{}, mat * 10 + kk + 1);
                    }
                    const int cpos = 2 * (mt & 7) + (mt >> 3);
                    if constexpr (TR) {
                        const float sc = (part == 0 && mat == 0) ? QSCALE : 1.f;
                        const unsigned base = part == 0 ? WS_Q + mat * NH * 81920 + mt * 5120 : WS_KV + mat * NH * 163840 + cpos * 10240;
                        const unsigned hs = part == 0 ? 81920 : 163840;
#pragma unroll
                        for (int pr = 0; pr < 2; ++pr) {
                            const int G8 = ((c0 + 32 * wn) >> 3) + 2 * pr + h, pp = G8 / 5, gi = G8 - pp * 5;
                            const unsigned hb0 = base + (2 * pp + (gi > 2)) * hs;
#pragma unroll
                            for (int i = 0; i < 2; ++i) {
                                const f32x16& a = i ? acc1 : acc0;
                                const int tl = wm * 64 + i * 32 + r;
                                const auto s0 = __builtin_amdgcn_permlane32_swap(pk2(a[8 * pr] * sc, a[8 * pr + 1] * sc), pk2(a[8 * pr + 4] * sc, a[8 * pr + 5] * sc), false, false);
                                const auto s1 = __builtin_amdgcn_permlane32_swap(pk2(a[8 * pr + 2] * sc, a[8 * pr + 3] * sc), pk2(a[8 * pr + 6] * sc, a[8 * pr + 7] * sc), false, false);
                                if (gi != 2) { const u32x4 ov = {s0[0], s1[0], s0[1], s1[1]}; ST16(ov, hb0 + (tl >> 5) * 1024 + (gi > 2 ? gi - 3 : gi) * 512 + r * 16); }
                                else { const u32x2 o0 = {s0[0], s1[0]}, o1 = {s0[1], s1[1]}; ST8(o0, hb0 + 4096 + tl * 8); ST8(o1, hb0 + hs + 4096 + tl * 8); }
                            }
                        }
                    } else {
                        const int n = c0 + 32 * wn + r, hd = n / 20, d = n - hd * 20, rho = (hd & 1) ? (d < 4 ? d + 16 : d - 4) : d;
                        const unsigned base = WS_KV + (mat * NH + hd) * 163840 + cpos * 10240 + 5120 + h * 320 + rho * 16;
#pragma unroll
                        for (int i = 0; i < 2; ++i)
#pragma unroll
                            for (int s2 = 0; s2 < 2; ++s2) {
                                const f32x16& a = i ? acc1 : acc0;
                                u32x4 ov = {pk2(a[8 * s2], a[8 * s2 + 1]), pk2(a[8 * s2 + 2], a[8 * s2 + 3]), pk2(a[8 * s2 + 4], a[8 * s2 + 5]), pk2(a[8 * s2 + 6], a[8 * s2 + 7])};
                                ST16(ov, base + (wm * 4 + i * 2 + s2) * 640);
                            }
                    }
                    if (mat == 0) STAMP(1);
                }
            };
            if (part < 2) gbody(std::true_type{}); else gbody(std::false_type{});
        } else {
            const int sj = bid - 240;
            WAIT_BAR(63);
            {
                const u32x4 ga = *(const u32x4*)(P.gw + lane * 8), gb = *(const u32x4*)(P.gw + 512 + (lane & 15) * 8);
                u32x4 xa[16], xb[4];
                const u16* xr = P.x + (size_t)(sj * 128 + w * 16) * DM;
#pragma unroll
                for (int rr = 0; rr < 16; ++rr) xa[rr] = *(const u32x4*)(xr + rr * DM + lane * 8);
#pragma unroll
                for (int i = 0; i < 4; ++i) xb[i] = *(const u32x4*)(xr + (i * 4 + (lane >> 4)) * DM + 512 + (lane & 15) * 8);
                float res = 0.f;
#pragma unroll
                for (int rr = 0; rr < 16; ++rr) {
                    float s = 0.f, s2 = 0.f;
#pragma unroll
                    for (int j = 0; j < 4; ++j) { s += bf_lo(xa[rr][j]) * bf_lo(ga[j]) + bf_hi(xa[rr][j]) * bf_hi(ga[j]); s2 += bf_lo(xb[rr >> 2][j]) * bf_lo(gb[j]) + bf_hi(xb[rr >> 2][j]) * bf_hi(gb[j]); }
                    if ((lane >> 4) == (rr & 3)) s += s2;
#pragma unroll
                    for (int o = 32; o >= 1; o >>= 1) s += __shfl_xor(s, o);
                    if (lane == rr) res = s;
                }
                if (lane < 16) ST4(__float_as_uint(1.f / (1.f + __expf(-res))), WS_G + (sj * 128 + w * 16 + lane) * 4);
            }
#pragma unroll 1
            for (int it = 0; it < 2; ++it) {
                unsigned e[4][8];
#pragma unroll
                for (int q = 0; q < 4; ++q) {
                    int item = mtid + 512 * (it * 4 + q); if (item >= 3200) item = 3199;
                    const int u = item / 320, n2 = item - u * 320, k0 = (sj * 10 + u) * 8;
                    const u16* wsrc = (k0 < 640 ? P.woa + (size_t)k0 * DM : P.wob + (size_t)(k0 - 640) * DM) + n2 * 2;
#pragma unroll
                    for (int j = 0; j < 8; ++j) e[q][j] = *(const unsigned*)(wsrc + (size_t)j * DM);
                }
#pragma unroll
                for (int q = 0; q < 4; ++q) {
                    int item = mtid + 512 * (it * 4 + q); if (item >= 3200) item = 3199;
                    const int u = item / 320, n = (item - u * 320) * 2, k0 = (sj * 10 + u) * 8;
                    const unsigned* f = e[q];
                    u32x4 lo = {(f[0] & 0xffffu) | (f[1] << 16), (f[2] & 0xffffu) | (f[3] << 16), (f[4] & 0xffffu) | (f[5] << 16), (f[6] & 0xffffu) | (f[7] << 16)};
                    u32x4 hi = {(f[0] >> 16) | (f[1] & 0xffff0000u), (f[2] >> 16) | (f[3] & 0xffff0000u), (f[4] >> 16) | (f[5] & 0xffff0000u), (f[6] >> 16) | (f[7] & 0xffff0000u)};
                    const unsigned dp = WS_WT + (((n >> 4) * 40 + (k0 >> 5)) * 64 + ((k0 >> 3) & 3) * 16 + (n & 15)) * 16;
                    ST16(lo, dp); ST16(hi, dp + 16);
                }
            }
            DRAIN();
            unsigned arr = 0;
            if (lane == 0) arr = LDS_CNT_ADD(G_ST);
            if (__builtin_amdgcn_readfirstlane(arr) == 7 && lane == 0) set_flag(ws, WS_F_SD + sj * 8, tag);
        }
        STAMP(2);
        const unsigned hb = lds0 + kh * 10240;
        const unsigned laneK = hb + lane * 16, laneT = hb + 4096 + r * 8 + h * 4, laneV = hb + 5120 + h * 320 + (r < 20 ? r : 19) * 16;
        const u64 vmask = 0x000fffff000fffffull;
        u32x4 v0a = {one, one, one, one}, v0b = v0a, v1 = v0a;
        u32x4 qA, qB, ka; unsigned qtA, qtB, kt;
        {
            lds_wait(lds + G_Q, 4);
            LDS3 const char* qp = lds + L_Q + (qs >> 1) * 5120;
            const unsigned om = (qs & 1) * 2048 + lane * 16, ot = 4096 + ((qs & 1) * 64 + r) * 8 + 4 * h;
            qA = *(LDS3 const u32x4*)(qp + om); qB = *(LDS3 const u32x4*)(qp + om + 1024);
            qtA = *(LDS3 const unsigned*)(qp + ot); qtB = *(LDS3 const unsigned*)(qp + ot + 256);
            DSR128(ka, laneK, 0); DSR32(kt, laneT, 0); DSR128M2(v0a, laneV, 0, v1, laneV, 640);
            asm volatile("s_waitcnt lgkmcnt(0)\n\ts_barrier" : "+v"(ka), "+v"(kt), "+v"(v0a), "+v"(v1), "+v"(qA), "+v"(qB), "+v"(qtA), "+v"(qtB) :: "memory");
        }
        STAMP(5);
        float mA, mB; u32x2 qTA, qTB; f32x16 sA, sB;
        {
            const u32x2 t0 = {kt, 0x3f80u}, a0 = {qtA, 0u}, b0 = {qtB, 0u};
            const f32x16 s = MFMA32_16(ka, qA, z16);
            const f32x16 s0 = MFMA32_8(t0, a0, s);
            f32x16 s1 = MFMA32_16(ka, qB, z16); s1 = MFMA32_8(t0, b0, s1);
            float x0 = s0[0], x1 = s1[0];
#pragma unroll
            for (int i = 1; i < 16; ++i) { x0 = fmaxf(x0, s0[i]); x1 = fmaxf(x1, s1[i]); }
            x0 = -xhalf_max(x0); x1 = -xhalf_max(x1);
            const unsigned hh = pk2(x0, x1);
            const unsigned ll = pk2(x0 - bf_lo(hh), x1 - bf_hi(hh));
            mA = -(bf_lo(hh) + bf_lo(ll)); mB = -(bf_hi(hh) + bf_hi(ll));
            const unsigned sel = h ? ll : hh;
            qTA = (u32x2){qtA, sel & 0xffffu}; qTB = (u32x2){qtB, sel >> 16};
            sA = MFMA32_8(t0, qTA, s);
        }
        f32x16 oA = {}, oB = {};
        u32x4 pa, pb = {0u, 0u, 0u, 0u};
#define EX4(S, i, Pv, j) do { Pv[j] = pk2(ex2(S[i]), ex2(S[i + 1])); Pv[j + 1] = pk2(ex2(S[i + 2]), ex2(S[i + 3])); PIN(Pv); SBAR(); } while (0)
#define KT2 ((u32x2){kt, 0x3f80u})
#define TILE(V0C, V0N, aK1, oK1, aT1, oT1, oV1, aV0, oV0) do { \
            sB = MFMA32_16(ka, qB, z16); SBAR(); DSR128(ka, aK1, oK1); EX4(sA, 0, pa, 0); \
            oB = MFMA32_16(v1, pb, oB); SBAR(); DSR128M2(v1, aV, oV1, V0N, aV0, oV0); EX4(sA, 4, pa, 2); \
            sB = MFMA32_8(KT2, qTB, sB); SBAR(); DSR32(kt, aT1, oT1); EX4(sA, 8, pb, 0); \
            oA = MFMA32_16(V0C, pa, oA); SBAR(); EX4(sA, 12, pb, 2); \
            LWAIT(1); sA = MFMA32_16(ka, qA, z16); SBAR(); EX4(sB, 0, pa, 0); \
            oA = MFMA32_16(v1, pb, oA); SBAR(); EX4(sB, 4, pa, 2); \
            LWAIT(0); sA = MFMA32_8(KT2, qTA, sA); SBAR(); EX4(sB, 8, pb, 0); \
            oB = MFMA32_16(V0C, pa, oB); SBAR(); EX4(sB, 12, pb, 2); \
        } while (0)
        unsigned aK = laneK, aT = laneT, aV = laneV;
#pragma unroll 1
        for (int c = 0; c < 8; ++c) {
            if (c == 1) { WAIT_BAR(0); }
            else if (c == 7) { if (lane == 0) LDS_CNT_ADD(C_PROG); asm volatile("s_waitcnt lgkmcnt(0)" ::: "memory"); }
            if (c >= 1) {
                unsigned pp;
                asm volatile("ds_write_b32 %1, %2\n\tds_read_b32 %0, %3\n\ts_waitcnt lgkmcnt(0)" : "=&v"(pp) : "v"(lds0 + C_PR + w * 4), "v"(c), "v"(lds0 + C_PR + (w ^ 4) * 4) : "memory");
                const int pc = __builtin_amdgcn_readfirstlane(pp);
                if (pc > c || (pc == c && ((c ^ kh) & 1))) __builtin_amdgcn_s_setprio(1); else __builtin_amdgcn_s_setprio(0);
            } else if (kh) __builtin_amdgcn_s_setprio(1);
            const unsigned nx = (c < 7 ? c + 1 : 7) * 20480;
            const unsigned nK = laneK + nx, nT = laneT + nx, nV = laneV + nx;
            SBAR();
            TILE(v0a, v0b, aK, 1024, aT, 256, 640,  aV, 1280);
            TILE(v0b, v0a, aK, 2048, aT, 512, 1920, aV, 2560);
            TILE(v0a, v0b, aK, 3072, aT, 768, 3200, aV, 3840);
            TILE(v0b, v0a, nK, 0,    nT, 0,   4480, nV, 0);
            aK = nK; aT = nT; aV = nV;
        }
        oB = MFMA32_16(v1, pb, oB);
#undef TILE
#undef EX4
        STAMP(6);
        {
            float chk = 0.f;
#pragma unroll
            for (int i = 0; i < 12; ++i) chk += fabsf(oA[i]) * 0.f + fabsf(oB[i]) * 0.f;
            bool bad = !(chk == 0.f);
            if (h) bad = bad || !(oA[8] >= 0x1p-60f && oA[8] <= 0x1p60f && oB[8] >= 0x1p-60f && oB[8] <= 0x1p60f);
            if (__builtin_expect(__any(bad), 0)) {
                mA = -INFINITY; mB = -INFINITY;
#pragma unroll
                for (int i = 0; i < 16; ++i) { oA[i] = 0.f; oB[i] = 0.f; }
                const u32x2 a0 = {qtA, 0u}, b0 = {qtB, 0u};
#pragma unroll 1
                for (int t = 0; t < 32; ++t) {
                    const unsigned kb = WS_KV + head * 163840 + (2 * (t >> 2) + kh) * 10240, t4 = t & 3;
                    const u32x4 k0 = LD16(kb + t4 * 1024 + lane * 16);
                    const u32x2 t0 = {LD4(kb + 4096 + (t4 * 32 + r) * 8 + 4 * h), 0u};
                    u32x4 w0 = LD16(kb + 5120 + t4 * 1280 + h * 320 + (r < 20 ? r : 19) * 16), w1 = LD16(kb + 5120 + t4 * 1280 + 640 + h * 320 + (r < 20 ? r : 19) * 16);
                    if (r >= 20) { w0 = (u32x4){one, one, one, one}; w1 = w0; }
                    f32x16 s0 = MFMA32_16(k0, qA, z16); s0 = MFMA32_8(t0, a0, s0);
                    f32x16 s1 = MFMA32_16(k0, qB, z16); s1 = MFMA32_8(t0, b0, s1);
                    float x0 = s0[0], x1 = s1[0];
#pragma unroll
                    for (int i = 1; i < 16; ++i) { x0 = fmaxf(x0, s0[i]); x1 = fmaxf(x1, s1[i]); }
                    x0 = fmaxf(xhalf_max(x0), mA); x1 = fmaxf(xhalf_max(x1), mB);
                    const float g0 = (mA == x0) ? 1.f : ex2(mA - x0), g1 = (mB == x1) ? 1.f : ex2(mB - x1);
                    mA = x0; mB = x1;
#pragma unroll
                    for (int i = 0; i < 16; ++i) { oA[i] *= g0; oB[i] *= g1; }
#pragma unroll
                    for (int i = 0; i < 4; ++i) { pa[i] = pk2(ex2(s0[2 * i] - x0), ex2(s0[2 * i + 1] - x0)); pb[i] = pk2(ex2(s0[8 + 2 * i] - x0), ex2(s0[9 + 2 * i] - x0)); }
                    oA = MFMA32_16(w0, pa, oA); oA = MFMA32_16(w1, pb, oA);
#pragma unroll
                    for (int i = 0; i < 4; ++i) { pa[i] = pk2(ex2(s1[2 * i] - x1), ex2(s1[2 * i + 1] - x1)); pb[i] = pk2(ex2(s1[8 + 2 * i] - x1), ex2(s1[9 + 2 * i] - x1)); }
                    oB = MFMA32_16(w0, pa, oB); oB = MFMA32_16(w1, pb, oB);
                }
            }
        }
        unsigned fin = 0;
        if (lane == 0) fin = LDS_CNT_ADD(C_END);
        fin = __builtin_amdgcn_readfirstlane(fin);
        lds_wait<4>(lds + C_OB, 4);
        lds_wait<4>(lds + C_PROG, 8);
        const float gO = *(LDS3 const float*)(lds + X_G + (qs * 64 + kh * 32 + r) * 4);
        {
            LDS3 char* xp = lds + X_MRG + w * 2816;
            const f32x16& s = kh ? oA : oB;
            *(LDS3 f32x4*)(xp + lane * 16) = (f32x4){s[0], s[1], s[2], s[3]};
            *(LDS3 f32x4*)(xp + 1024 + lane * 16) = (f32x4){s[4], s[5], s[6], s[7]};
            if (!h) *(LDS3 f32x4*)(xp + 2048 + r * 16) = (f32x4){s[8], s[9], s[10], s[11]};
            else *(LDS3 f32x2*)(xp + 2560 + r * 8) = (f32x2){s[8], kh ? mA : mB};
            asm volatile("s_waitcnt lgkmcnt(0)" ::: "memory");
            if (lane == 0) __hip_atomic_store((LDS3 unsigned*)(lds + C_MRG + w * 4), 1u, __ATOMIC_RELAXED, __HIP_MEMORY_SCOPE_WORKGROUP);
        }
        if (fin < 4) {
            const char* wsrc = ws + WS_WT + (size_t)ntO * 5 * 40960;
#pragma unroll 1
            for (int n = 0; n < 5; ++n)
#pragma unroll
                for (int t = 0; t < 5; ++t) { const int ks = fin * 5 + t; glds16s(lane * 16, wsrc + (n * 40 + ks) * 1024, lds0 + L_WA + (n * 20 + ks) * 1024); }
        }
        lds_wait<2>(lds + C_MRG + (w ^ 4) * 4, 1);
        bool lastw;
        {
            LDS3 const char* xp = lds + X_MRG + (w ^ 4) * 2816;
            const f32x4 p0 = *(LDS3 const f32x4*)(xp + lane * 16), p1 = *(LDS3 const f32x4*)(xp + 1024 + lane * 16), p2 = *(LDS3 const f32x4*)(xp + 2048 + r * 16);
            const f32x2 lm = *(LDS3 const f32x2*)(xp + 2560 + r * 8);
            const f32x16& s = kh ? oB : oA;
            const float mo = kh ? mB : mA, mp = lm[1];
            const float mx = fmaxf(mo, mp);
            const float fo = (mo == mx) ? 1.f : ex2(mo - mx), fq = (mp == mx) ? 1.f : ex2(mp - mx);
            float o[12];
#pragma unroll
            for (int i = 0; i < 4; ++i) { o[i] = s[i] * fo + p0[i] * fq; o[4 + i] = s[4 + i] * fo + p1[i] * fq; o[8 + i] = s[8 + i] * fo + p2[i] * fq; }
            const float l = upper_half(s[8]) * fo + lm[0] * fq;
            store_o(rs, o, gO / l, q0 + kh * 32 + r, head * 20, h);
            DRAIN();
            unsigned arr = 0;
            if (lane == 0) arr = LDS_CNT_ADD(C_OA);
            lastw = __builtin_amdgcn_readfirstlane(arr) == 7;
            if (lastw && lane == 0) set_flag(ws, WS_F_OA + (qb * 32 + head) * 8, tag);
        }
        STAMP(7);
        {
            const int c16 = lane & 15, g = lane >> 4, tt = w & 3, k0 = kh * 10;
            POLL_ONE(lastw, 4, WS_F_OA + ((mtO >> 2) * 32 + r) * 8, WS_F_OA + ((mtO >> 2) * 32 + r) * 8, C_P4);
            STAMP(9);
            const unsigned ab = WS_AO + ((mtO * 4 + tt) * 40 + k0) * 1024 + lane * 16;
            u32x4 af[10];
#pragma unroll
            for (int ks = 0; ks < 10; ++ks) af[ks] = LD16(ab + ks * 1024);
            lds_wait(lds + C_OA, 8);
            f32x4 c[5];
#pragma unroll
            for (int n = 0; n < 5; ++n) c[n] = (f32x4){0.f, 0.f, 0.f, 0.f};
#pragma unroll
            for (int ks = 0; ks < 10; ++ks)
#pragma unroll
                for (int n = 0; n < 5; ++n) c[n] = MFMA16_32(*(LDS3 const u32x4*)(lds + L_WA + (n * 20 + k0 + ks) * 1024 + lane * 16), af[ks], c[n]);
            LDS3 char* xp = lds + X_MRG + tt * 5120 + lane * 16;
            if (w < 4) { *(LDS3 f32x4*)(xp + 3072) = c[3]; *(LDS3 f32x4*)(xp + 4096) = c[4]; }
            else { *(LDS3 f32x4*)(xp) = c[0]; *(LDS3 f32x4*)(xp + 1024) = c[1]; *(LDS3 f32x4*)(xp + 2048) = c[2]; }
            asm volatile("s_waitcnt lgkmcnt(0)" ::: "memory");
            if (lane == 0) __hip_atomic_store((LDS3 unsigned*)(lds + C_FIN + w * 4), 1u, __ATOMIC_RELAXED, __HIP_MEMORY_SCOPE_WORKGROUP);
            lds_wait(lds + C_FIN + (w ^ 4) * 4, 1);
            lds_wait(lds + C_BP + tt * 4, 1);
            u16* op = P.out + (size_t)(mtO * 64 + tt * 16 + c16) * DM + ntO * 80 + g * 4;
#pragma unroll
            for (int n = 0; n < 5; ++n) {
                if ((w < 4) == (n < 3)) {
                    const f32x4 bp = *(LDS3 const f32x4*)(n < 3 ? lds + X_FIN + tt * 3072 + n * 1024 + lane * 16 : lds + 143360 + tt * 2048 + (n - 3) * 1024 + lane * 16);
                    const f32x4 s = c[n] + *(LDS3 const f32x4*)(xp + n * 1024) + bp;
                    *(u32x2*)(op + n * 16) = (u32x2){pk2(s[0], s[1]), pk2(s[2], s[3])};
                }
            }
            if (bid == 0 && mtid == 0) set_flag(ws, WS_EPOCH, tag);
        }
        STAMP(10);
    }
}

extern "C" void kernel_launch(void* const* d_in, const int* in_sizes, int n_in, void* d_out, int out_size, void* d_ws, size_t ws_size,
                              hipStream_t stream) {
    Params P;
    P.x = (const u16*)d_in[0]; P.wa = (const u16*)d_in[1]; P.woa = (const u16*)d_in[2]; P.wb = (const u16*)d_in[4];
    P.wphi = (const u16*)d_in[5]; P.wob = (const u16*)d_in[6]; P.gw = (const u16*)d_in[7]; P.ws = (char*)d_ws; P.out = (u16*)d_out;
    mega<<<256, 768, 0, stream>>>(P);
}
```

```cpp
#include <hip/hip_runtime.h>
#include <hip/hip_bf16.h>
#include <stdint.h>
#include <type_traits>

typedef __attribute__((ext_vector_type(8))) short bf16x8;
typedef __attribute__((ext_vector_type(4))) short bf16x4;
typedef __attribute__((ext_vector_type(16))) float f32x16;
typedef __attribute__((ext_vector_type(4))) float f32x4;
typedef __attribute__((ext_vector_type(2))) float f32x2;
typedef __attribute__((ext_vector_type(4))) unsigned u32x4;
typedef __attribute__((ext_vector_type(2))) unsigned u32x2;
typedef __attribute__((ext_vector_type(2))) __bf16 bf2_t;
typedef unsigned short u16;
typedef unsigned long long u64;
#define LDS3 __attribute__((address_space(3)))
typedef __attribute__((address_space(1))) u64 gu64;
#define RLX_AGENT __ATOMIC_RELAXED, __HIP_MEMORY_SCOPE_AGENT

#define PRIO_MODE 3
#define WARM_MODE 1
#define PRE_SLEEP 2
constexpr int SEQ = 2048, DM = 640, NH = 32;
constexpr float LOG2E = 1.4426950408889634f;
constexpr float QSCALE = 0.22360679774997896f * LOG2E;

constexpr unsigned WS_EPOCH = 0;
constexpr unsigned WS_F_G   = 256;
constexpr unsigned WS_F_SD  = WS_F_G + 4096;
constexpr unsigned WS_F_KV  = WS_F_SD + 256;
constexpr unsigned WS_F_OB  = WS_F_KV + 2048;
constexpr unsigned WS_F_OA  = WS_F_OB + 2048;
constexpr unsigned WS_G     = 16384;
constexpr unsigned WS_Q     = 32768;
constexpr unsigned WS_KV    = WS_Q + 2 * NH * 81920;
constexpr unsigned WS_KVP   = WS_KV + 2 * NH * 163840;
constexpr unsigned WS_AO    = WS_KVP + NH * 8 * 4096;
constexpr unsigned WS_WT    = WS_AO + SEQ * 1280 * 2;
constexpr unsigned WS_END   = WS_WT + 1280 * 640 * 2;

#define STAMP(i)

__device__ __forceinline__ unsigned pk2(float a, float b) { f32x2 v = {a, b}; bf2_t r = __builtin_convertvector(v, bf2_t); return __builtin_bit_cast(unsigned, r); }
__device__ __forceinline__ float bf_lo(unsigned u) { return __uint_as_float(u << 16); }
__device__ __forceinline__ float bf_hi(unsigned u) { return __uint_as_float(u & 0xffff0000u); }
__device__ __forceinline__ float ex2(float x) { return __builtin_amdgcn_exp2f(x); }
#define MFMA32_16(a, b, c) __builtin_amdgcn_mfma_f32_32x32x16_bf16(__builtin_bit_cast(bf16x8, a), __builtin_bit_cast(bf16x8, b), c, 0, 0, 0)
#define MFMA32_8(a, b, c) __builtin_amdgcn_mfma_f32_32x32x8bf16_1k(__builtin_bit_cast(bf16x4, a), __builtin_bit_cast(bf16x4, b), c, 0, 0, 0)
#define MFMA16_32(a, b, c) __builtin_amdgcn_mfma_f32_16x16x32_bf16(__builtin_bit_cast(bf16x8, a), __builtin_bit_cast(bf16x8, b), c, 0, 0, 0)

__device__ __forceinline__ void glds16(unsigned voff, const void* sbase, unsigned lds_base) {
    unsigned sv;
    asm volatile("s_mov_b32 %0, m0\n\ts_mov_b32 m0, %3\n\ts_nop 0\n\tglobal_load_lds_dwordx4 %1, %2\n\ts_mov_b32 m0, %0"
                 : "=&s"(sv) : "v"(voff), "s"(sbase), "s"(__builtin_amdgcn_readfirstlane(lds_base)) : "memory");
}
__device__ __forceinline__ void glds16s(unsigned voff, const void* sbase, unsigned lds_base) {
    unsigned sv;
    asm volatile("s_mov_b32 %0, m0\n\ts_mov_b32 m0, %3\n\ts_nop 0\n\tglobal_load_lds_dwordx4 %1, %2 sc1\n\ts_mov_b32 m0, %0"
                 : "=&s"(sv) : "v"(voff), "s"(sbase), "s"(__builtin_amdgcn_readfirstlane(lds_base)) : "memory");
}
__device__ __forceinline__ void glds4s(unsigned voff, const void* sbase, unsigned lds_base) {
    unsigned sv;
    asm volatile("s_mov_b32 %0, m0\n\ts_mov_b32 m0, %3\n\ts_nop 0\n\tglobal_load_lds_dword %1, %2 sc1\n\ts_mov_b32 m0, %0"
                 : "=&s"(sv) : "v"(voff), "s"(sbase), "s"(__builtin_amdgcn_readfirstlane(lds_base)) : "memory");
}
typedef short v4i16_t __attribute__((ext_vector_type(4)));
__device__ __forceinline__ u32x2 trread(LDS3 const char* p) {
    return __builtin_bit_cast(u32x2, __builtin_amdgcn_ds_read_tr16_b64_v4i16((LDS3 v4i16_t*)p));
}
#define WAIT_BAR(N) asm volatile("s_waitcnt vmcnt(" #N ") lgkmcnt(0)\n\ts_barrier" ::: "memory")
#define DRAIN() asm volatile("s_waitcnt vmcnt(0)" ::: "memory")
#define SBAR() __builtin_amdgcn_sched_barrier(0)
#define PIN(x) asm volatile("" : "+v"(x))

__device__ __forceinline__ float xhalf_max(float x) {
    auto r = __builtin_amdgcn_permlane32_swap(__float_as_uint(x), __float_as_uint(x), false, false);
    return fmaxf(__uint_as_float(r[0]), __uint_as_float(r[1]));
}
__device__ __forceinline__ float xhalf_sum(float x) {
    auto r = __builtin_amdgcn_permlane32_swap(__float_as_uint(x), __float_as_uint(x), false, false);
    return __uint_as_float(r[0]) + __uint_as_float(r[1]);
}
__device__ __forceinline__ float upper_half(float x) {
    auto r = __builtin_amdgcn_permlane32_swap(__float_as_uint(x), __float_as_uint(x), false, false);
    return __uint_as_float(r[1]);
}

typedef __amdgpu_buffer_rsrc_t rsrc_t;
#define ST8(v, off)  __builtin_amdgcn_raw_buffer_store_b64(v, rs, (int)(off), 0, 16)
#define ST16(v, off) __builtin_amdgcn_raw_buffer_store_b128(v, rs, (int)(off), 0, 16)
#define ST4(v, off)  __builtin_amdgcn_raw_buffer_store_b32(v, rs, (int)(off), 0, 16)
#define LD16(off) __builtin_amdgcn_raw_buffer_load_b128(rs, (int)(off), 0, 16)
#define LD4(off)  __builtin_amdgcn_raw_buffer_load_b32(rs, (int)(off), 0, 16)

template <int SLP = 2>
__device__ __forceinline__ void wait_flags(const char* ws, unsigned off0, unsigned off1, u64 tag) {
    gu64* p0 = (gu64*)(ws + off0); gu64* p1 = (gu64*)(ws + off1);
#pragma unroll 1
    for (unsigned spins = 0; spins < (1u << 16); ++spins) {
        const u64 a = __hip_atomic_load(p0, RLX_AGENT), b = __hip_atomic_load(p1, RLX_AGENT);
        if (__all(a == tag && b == tag)) break;
        __builtin_amdgcn_s_sleep(SLP);
    }
    __builtin_amdgcn_fence(__ATOMIC_ACQUIRE, "wavefront");
}
__device__ __forceinline__ void set_flag(char* ws, unsigned off, u64 tag) { __hip_atomic_store((gu64*)(ws + off), tag, RLX_AGENT); }

struct Params { const u16* x; const u16* wa; const u16* wb; const u16* wphi; const u16* woa; const u16* wob; const u16* gw; char* ws; u16* out; };

__device__ __forceinline__ void feature_map(const u32x4& pm, unsigned pt, const u32x4& tm, unsigned tt, int h, u32x4& fp, u32x4& fn, u32x4& ft) {
    const f32x16 z16 = {};
    const u32x2 a8 = {pt, 0u}, b8 = {tt, 0u};
    f32x16 s = MFMA32_16(pm, tm, z16); s = MFMA32_8(a8, b8, s);
    float v[12];
#pragma unroll
    for (int i = 0; i < 12; i += 2) { const unsigned u = pk2(s[i], s[i + 1]); v[i] = bf_lo(u) * LOG2E; v[i + 1] = bf_hi(u) * LOG2E; }
    float mx = v[0], mn = v[0];
#pragma unroll
    for (int i = 1; i < 8; ++i) { mx = fmaxf(mx, v[i]); mn = fminf(mn, v[i]); }
    const float tx = fmaxf(fmaxf(v[8], v[9]), fmaxf(v[10], v[11])), tn = fminf(fminf(v[8], v[9]), fminf(v[10], v[11]));
    if (!h) { mx = fmaxf(mx, tx); mn = fminf(mn, tn); }
    mx = xhalf_max(mx); mn = -xhalf_max(-mn);
    float ep[12], en[12], sp = 0.f, sn = 0.f;
#pragma unroll
    for (int i = 0; i < 12; ++i) {
        ep[i] = ex2(v[i] - mx); en[i] = ex2(mn - v[i]);
        if (i >= 8 && h) { ep[i] = 0.f; en[i] = 0.f; }
        sp += ep[i]; sn += en[i];
    }
    sp = __builtin_amdgcn_rcpf(xhalf_sum(sp)); sn = __builtin_amdgcn_rcpf(xhalf_sum(sn));
#pragma unroll
    for (int i = 0; i < 4; ++i) { fp[i] = pk2(ep[2 * i] * sp, ep[2 * i + 1] * sp); fn[i] = pk2(en[2 * i] * sn, en[2 * i + 1] * sn); }
    ft = (u32x4){pk2(ep[8] * sp, ep[9] * sp), pk2(ep[10] * sp, ep[11] * sp), pk2(en[8] * sn, en[9] * sn), pk2(en[10] * sn, en[11] * sn)};
}

__device__ __forceinline__ void store_o(rsrc_t rs, const float (&o)[12], float sc, int tok, int kbase, int h) {
#pragma unroll
    for (int gq = 0; gq < 3; ++gq) {
        const int d = 8 * gq + 4 * h;
        if (d < 20) {
            const int k = kbase + ((kbase & 4) ? (d == 16 ? 0 : d + 4) : d);
            u32x2 ov = {pk2(o[4 * gq] * sc, o[4 * gq + 1] * sc), pk2(o[4 * gq + 2] * sc, o[4 * gq + 3] * sc)};
            ST8(ov, WS_AO + (((tok >> 4) * 40 + (k >> 5)) * 64 + ((k >> 3) & 3) * 16 + (tok & 15)) * 16 + (k & 7) * 2);
        }
    }
}

#define DSR128(dst, addr, off) asm volatile("ds_read_b128 %0, %1 offset:%2" : "=v"(dst) : "v"(addr), "i"(off))
#define DSR32(dst, addr, off) asm volatile("ds_read_b32 %0, %1 offset:%2" : "=v"(dst) : "v"(addr), "i"(off))
#define DSR128M2(d0, a0, o0, d1, a1, o1) asm volatile("s_mov_b64 exec, %6\n\tds_read_b128 %0, %2 offset:%3\n\tds_read_b128 %1, %4 offset:%5\n\ts_mov_b64 exec, -1" \
                                                      : "+v"(d0), "+v"(d1) : "v"(a0), "i"(o0), "v"(a1), "i"(o1), "s"(vmask))
#define LWAIT(n) do { asm volatile("s_waitcnt lgkmcnt(" #n ")" ::: "memory"); SBAR(); } while (0)

#define BAR() asm volatile("s_barrier" ::: "memory")
#define LDS_CNT_ADD(off) __hip_atomic_fetch_add((LDS3 unsigned*)(lds + (off)), 1u, __ATOMIC_RELAXED, __HIP_MEMORY_SCOPE_WORKGROUP)

template <int SLP = 1>
__device__ __forceinline__ void lds_wait(LDS3 char* p, unsigned want) {
#pragma unroll 1
    for (unsigned spins = 0; spins < (1u << 20); ++spins) {
        if ((unsigned)__builtin_amdgcn_readfirstlane(__hip_atomic_load((LDS3 unsigned*)p, __ATOMIC_RELAXED, __HIP_MEMORY_SCOPE_WORKGROUP)) >= want) break;
        __builtin_amdgcn_s_sleep(SLP);
    }
    __builtin_amdgcn_fence(__ATOMIC_ACQUIRE, "wavefront");
}

#define POLL_ONE(cond, SLP, o0, o1, word) do { if (cond) { wait_flags<SLP>(ws, o0, o1, tag); \
        if (lane == 0) __hip_atomic_store((LDS3 unsigned*)(lds + (word)), 1u, __ATOMIC_RELAXED, __HIP_MEMORY_SCOPE_WORKGROUP); } else lds_wait<SLP>(lds + (word), 1); } while (0)
constexpr int C_KV = 0, C_KVF = 4, C_OB = 8, C_PROG = 12, C_WA = 16, C_OA = 20, C_MRG = 32  , C_FIN = 64  , C_BP = 96  ;
constexpr int C_PROG2 = 24, C_BX = 28, C_P1 = 112, C_P2 = 116, C_P3 = 120, C_P4 = 124, C_PR = 128  ;
constexpr int G_W = 31744;
constexpr int G_FULL = G_W, G_FREE = G_W + 16, G_Q = G_W + 32, G_KV0 = G_W + 36, G_ST = G_W + 40;
constexpr int C_END = 160, X_G = 256;
constexpr int X_KVF = 1280, X_SLOT = 4096;
constexpr int X_MRG = 1280;
constexpr int L_WA = 24576;
constexpr int L_Q = 65536;
constexpr int X_FIN = 126976;

__global__ __launch_bounds__(768) void mega(Params P) {
    __shared__ __attribute__((aligned(16))) char smem[163840];
    LDS3 char* lds = (LDS3 char*)smem;
    char* ws = P.ws;
    const int tid = threadIdx.x, lane = tid & 63, r = lane & 31, h = lane >> 5;
    const int hw = __builtin_amdgcn_readfirstlane(tid >> 6);
    const bool helper = hw < 4;
    const int w = hw - 4, mtid = tid - 256;
    const int bid = blockIdx.x;
    const unsigned lds0 = (unsigned)(uintptr_t)smem;
    const rsrc_t rs = __builtin_amdgcn_make_buffer_rsrc(ws, 0, WS_END, 0x00020000);
    STAMP(0);
    u64 tag = 0;
#define GET_TAG() do { asm volatile("s_load_dwordx2 %0, %1, 0x0\n\ts_waitcnt lgkmcnt(0)" : "=s"(tag) : "s"(ws) : "memory"); tag += 1; } while (0)
    const int head = (bid & 7) * 4 + ((bid >> 3) & 3), qb = bid >> 5;
    const int nlo = (head * 20) >> 7, nhi = (head * 20 + 19) >> 7;
    const int mtO = (bid & 7) * 4 + ((bid >> 3) & 3), ntO = bid >> 5;
    const int lin = (bid & 7) * 30 + (bid >> 3), lh = lin >= 120, lrem = lin - 120 * lh;
    const int mt = lh * 8 + (lrem & 7), nt = lrem >> 3;
    const unsigned fG = WS_F_G + (mt * 15 + nt) * 8;
    const unsigned one = (r == 20) ? 0x3f803f80u : 0u;
    const f32x16 z16 = {};

    if (helper) {
        const unsigned fA0 = WS_F_G + ((lane & 15) * 15 + (lane < 32 ? 5 : 10) + ((lane & 16) ? nhi : nlo)) * 8;
        const unsigned fA1 = WS_F_G + ((qb * 2 + (lane & 1)) * 15 + ((lane & 2) ? nhi : nlo)) * 8;
        __builtin_amdgcn_s_setprio(3);
        const char* kvb = ws + WS_KV + head * 163840;
        auto kv_issue = [&](int grp) {
#pragma unroll
            for (int t = 0; t < 8; ++t) { int pc = grp * 32 + hw + 4 * t; if (pc == 31) pc = 30; glds16s(lane * 16, kvb + pc * 1024, lds0 + pc * 1024); }
        };
        auto q_issue = [&]() {
            const char* qbase = ws + WS_Q + head * 81920 + qb * 10240;
#pragma unroll
            for (int t = 0; t < 3; ++t) { const int pc = hw + 4 * t < 10 ? hw + 4 * t : 9; glds16s(lane * 16, qbase + pc * 1024, lds0 + L_Q + pc * 1024); }
        };
        if (bid < 240) {
            const int rot = (nt * 3 + mt * 5 + (mt >> 1)) % 10;
            const unsigned xo = (mt * 128 + 8 * hw + (lane >> 3)) * 1280 + (((lane & 7) ^ ((4 * hw + (lane >> 4)) & 7)) << 4);
            const unsigned wo = (4 * hw + (lane >> 4)) * 3840 + nt * 256 + (((lane & 15) ^ (((lane >> 4) << 2) | hw)) << 4);
            const unsigned dX = lds0 + 32768 + hw * 1024;
            auto issue = [&](int s) {
                int kt = (s >= 10 ? s - 10 : s) + rot; if (kt >= 10) kt -= 10;
                const char* xb = (const char*)P.x + kt * 128;
                const char* wb = (const char*)(s >= 10 ? P.wb : P.wa) + (size_t)kt * (64 * 3840);
                const unsigned d = dX + (s & 3) * 32768;
#pragma unroll
                for (int i = 0; i < 4; ++i) { glds16(xo, xb + i * 40960, d + i * 4096); glds16(wo, wb + i * 61440, d + 16384 + i * 4096); }
            };
#pragma unroll 1
            for (int n = 0; n < 20; ++n) {
                if (n == 2) { if (hw == 0 && lane < 16) *(LDS3 unsigned*)(lds + G_W + lane * 4) = 0u; GET_TAG(); WAIT_BAR(8); }
                else if (n == 3) { WAIT_BAR(8); }
                else if (n >= 5) { WAIT_BAR(16); }
                if (n == 16 && tid == 0) set_flag(ws, fG, tag);
                if (n == 19 && hw == 0) {
                    glds4s(fA0, ws, lds0); glds4s(fA0 + 4, ws, lds0 + 256); glds4s(fA1, ws, lds0 + 512); glds4s(fA1 + 4, ws, lds0 + 768);
                }
                issue(n);
            }
            WAIT_BAR(16);
            asm volatile("s_waitcnt vmcnt(8)" ::: "memory");
            if (hw == 0) {
                LDS3 const unsigned* f = (LDS3 const unsigned*)lds + lane;
                const unsigned tl = (unsigned)tag, th = (unsigned)(tag >> 32);
                if (!__all(f[0] == tl && f[64] == th && f[128] == tl && f[192] == th)) wait_flags<8>(ws, fA0, fA1, tag);
            }
            WAIT_BAR(63);
            kv_issue(0); q_issue();
            WAIT_BAR(11);
            kv_issue(1);
            WAIT_BAR(63);
        } else {
            if (hw == 0 && lane < 16) *(LDS3 unsigned*)(lds + G_W + lane * 4) = 0u;
            GET_TAG();
            WAIT_BAR(63);
            if (hw == 0) {
#pragma unroll 1
                for (int t = 0; t < PRE_SLEEP; ++t) __builtin_amdgcn_s_sleep(127);
                wait_flags<16>(ws, fA0, fA1, tag);
                if (lane == 0) LDS_CNT_ADD(G_FREE);
            }
            lds_wait<16>(lds + G_FREE, 1);
            kv_issue(0); q_issue(); kv_issue(1);
        }
        asm volatile("s_waitcnt vmcnt(6)" ::: "memory");
        if (lane == 0) LDS_CNT_ADD(G_Q);
        kv_issue(3); kv_issue(4);
        WAIT_BAR(63);
        if (hw == 3) glds16s(lane * 16, kvb + 31 * 1024, lds0 + 31 * 1024);
        kv_issue(2);
        if (hw == 0) *(LDS3 u32x4*)(lds + lane * 16) = (u32x4){0u, 0u, 0u, 0u};
        u32x4 phm; unsigned pht;
        {
            const u16* wp = P.wphi + head * 400 + (r < 20 ? r : 0);
            unsigned e[10];
#pragma unroll
            for (int j = 0; j < 8; ++j) e[j] = wp[(8 * h + j + 4 * (head & 1)) * 20];
            e[8] = wp[((head & 1 ? 0 : 16) + 2 * h) * 20]; e[9] = wp[((head & 1 ? 1 : 17) + 2 * h) * 20];
            phm = (u32x4){e[0] | (e[1] << 16), e[2] | (e[3] << 16), e[4] | (e[5] << 16), e[6] | (e[7] << 16)}; pht = e[8] | (e[9] << 16);
            if (r >= 20) { phm = (u32x4){0u, 0u, 0u, 0u}; pht = 0u; }
        }
        WAIT_BAR(0);
        if (tid == 0 && bid < 240) set_flag(ws, fG + 2048, tag);
        STAMP(3);
        if (hw * 64 + lane < 144) *(LDS3 u32x4*)(lds + X_KVF + (hw * 64 + lane) * 16) = (u32x4){0u, 0u, 0u, 0u};
        const int tokS = qb * 256 + hw * 64, m7 = tokS >> 7, t4 = (tokS >> 5) & 3;
        {
            const unsigned pt3 = (lane >> 1) & 3;
            const unsigned tb = WS_F_G + 2048 + ((qb * 2 + ((lane >> 3) & 1)) * 15 + ((lane & 1) ? nhi : nlo) + (pt3 < 3 ? pt3 : 0) * 5) * 8;
            POLL_ONE(hw == 0, 8, tb, WS_F_SD + (lane & 15) * 8, C_P1);
        }
        const unsigned om = t4 * 1024 + lane * 16, ot = 4096 + (t4 * 32 + r) * 8 + 4 * h;
        u32x4 qf[2][3]; float gS[2];
        {
            const unsigned kb = WS_KV + (NH + head) * 163840 + (2 * (m7 & 7) + (m7 >> 3)) * 10240, qbb = WS_Q + (NH + head) * 81920 + m7 * 5120;
            const unsigned ov = 5120 + t4 * 1280 + h * 320 + (r < 20 ? r : 19) * 16;
            u32x4 e0[2], w0[2], w1[2], eq[2]; unsigned et[2], eqt[2];
#pragma unroll
            for (int i = 0; i < 2; ++i) {
                e0[i] = LD16(kb + om + i * 1024); et[i] = LD4(kb + ot + i * 256); w0[i] = LD16(kb + ov + i * 1280); w1[i] = LD16(kb + ov + i * 1280 + 640);
                eq[i] = LD16(qbb + om + i * 1024); eqt[i] = LD4(qbb + ot + i * 256); gS[i] = __uint_as_float(LD4(WS_G + (tokS + i * 32 + r) * 4));
            }
            *(LDS3 float*)(lds + X_G + (hw * 64 + lane) * 4) = h ? gS[1] : gS[0];
            u32x4 i0, i1;
#pragma unroll
            for (int j = 0; j < 4; ++j) {
                const int e = r - 8 * h - 2 * j;
                i0[j] = e == 0 ? 0x3f80u : (e == 1 ? 0x3f800000u : 0u);
                i1[j] = e == 16 ? 0x3f80u : (e == 17 ? 0x3f800000u : 0u);
            }
            f32x16 k0 = {}, k1 = {};
#pragma unroll
            for (int i = 0; i < 2; ++i) {
                if (r >= 20) { w0[i] = (u32x4){one, one, one, one}; w1[i] = w0[i]; }
                u32x4 fp, fn, ft;
                feature_map(phm, pht, e0[i], et[i], h, fp, fn, ft);
                f32x16 T0 = MFMA32_16(fp, i0, z16); T0 = MFMA32_16(fn, i1, T0);
                f32x16 T1 = MFMA32_16(ft, i0, z16);
                u32x4 b00, b01, b10, b11;
#pragma unroll
                for (int j = 0; j < 4; ++j) {
                    b00[j] = pk2(T0[2 * j], T0[2 * j + 1]); b01[j] = pk2(T0[8 + 2 * j], T0[9 + 2 * j]);
                    b10[j] = pk2(T1[2 * j], T1[2 * j + 1]); b11[j] = pk2(T1[8 + 2 * j], T1[9 + 2 * j]);
                }
                k0 = MFMA32_16(w0[i], b00, k0); k0 = MFMA32_16(w1[i], b01, k0);
                k1 = MFMA32_16(w0[i], b10, k1); k1 = MFMA32_16(w1[i], b11, k1);
            }
            LDS3 char* sp = lds + X_SLOT + hw * 4096;
#pragma unroll
            for (int gq = 0; gq < 3; ++gq) {
                *(LDS3 f32x4*)(sp + gq * 1024 + lane * 16) = (f32x4){k0[4 * gq], k0[4 * gq + 1], k0[4 * gq + 2], k0[4 * gq + 3]};
                if (r < 8) *(LDS3 f32x4*)(sp + 3072 + (gq * 16 + h * 8 + r) * 16) = (f32x4){k1[4 * gq], k1[4 * gq + 1], k1[4 * gq + 2], k1[4 * gq + 3]};
            }
            asm volatile("s_waitcnt lgkmcnt(0)" ::: "memory");
            unsigned arr = 0;
            if (lane == 0) arr = LDS_CNT_ADD(C_KV);
            if (__builtin_amdgcn_readfirstlane(arr) == 3) {
                __builtin_amdgcn_fence(__ATOMIC_ACQUIRE, "wavefront");
#pragma unroll
                for (int i = 0; i < 4; ++i) {
                    const int qd = lane + 64 * i;
                    LDS3 const char* s0 = lds + X_SLOT + qd * 16;
                    const f32x4 s = (*(LDS3 const f32x4*)s0 + *(LDS3 const f32x4*)(s0 + 4096)) + (*(LDS3 const f32x4*)(s0 + 8192) + *(LDS3 const f32x4*)(s0 + 12288));
                    if (qd < 240) ST16(__builtin_bit_cast(u32x4, s), WS_KVP + (head * 8 + qb) * 4096 + qd * 16);
                }
                DRAIN();
                if (lane == 0) set_flag(ws, WS_F_KV + (head * 8 + qb) * 8, tag);
            }
#pragma unroll
            for (int i = 0; i < 2; ++i) feature_map(phm, pht, eq[i], eqt[i], h, qf[i][0], qf[i][1], qf[i][2]);
        }
        STAMP(4);
        POLL_ONE(hw == 0, 8, WS_F_KV + (head * 8 + (lane & 7)) * 8, WS_F_KV + (head * 8 + (lane & 7)) * 8, C_P2);
        {
            const int qd = hw * 60 + (lane < 60 ? lane : 59);
            f32x4 pp[8];
#pragma unroll
            for (int p = 0; p < 8; ++p) pp[p] = __builtin_bit_cast(f32x4, LD16(WS_KVP + (head * 8 + p) * 4096 + qd * 16));
            const f32x4 s4 = ((pp[0] + pp[1]) + (pp[2] + pp[3])) + ((pp[4] + pp[5]) + (pp[6] + pp[7]));
            const int gq = qd < 192 ? qd >> 6 : (qd - 192) >> 4, hs = qd < 192 ? (qd >> 5) & 1 : (qd >> 3) & 1, sg = qd < 192 ? qd & 31 : 32 + (qd & 7);
            const unsigned a = pk2(s4[0], s4[1]), b = pk2(s4[2], s4[3]);
            LDS3 u16* kp = (LDS3 u16*)(lds + X_KVF + (8 * gq + 4 * hs) * 96 + sg * 2);
            kp[0] = (u16)a; kp[48] = (u16)(a >> 16); kp[96] = (u16)b; kp[144] = (u16)(b >> 16);
            asm volatile("s_waitcnt lgkmcnt(0)" ::: "memory");
            if (lane == 0) LDS_CNT_ADD(C_KVF);
            lds_wait<8>(lds + C_KVF, 4);
        }
        {
            LDS3 const char* kp = lds + X_KVF + (r < 24 ? r : 23) * 96 + h * 16;
            const u32x4 a0 = *(LDS3 const u32x4*)kp, a1 = *(LDS3 const u32x4*)(kp + 32), a2 = *(LDS3 const u32x4*)(kp + 64);
#pragma unroll
            for (int i = 0; i < 2; ++i) {
                f32x16 d = MFMA32_16(a0, qf[i][0], z16); d = MFMA32_16(a1, qf[i][1], d); d = MFMA32_16(a2, qf[i][2], d);
                const float den = upper_half(d[8]) + 1e-6f;
                float o[12];
#pragma unroll
                for (int j = 0; j < 12; ++j) o[j] = d[j];
                store_o(rs, o, (1.f - gS[i]) / den, tokS + i * 32 + r, 640 + head * 20, h);
            }
            DRAIN();
            unsigned arr = 0;
            if (lane == 0) arr = LDS_CNT_ADD(C_OB);
            if (__builtin_amdgcn_readfirstlane(arr) == 3 && lane == 0) set_flag(ws, WS_F_OB + (qb * 32 + head) * 8, tag);
        }
        STAMP(5);
        f32x4 c[5];
#pragma unroll
        for (int n = 0; n < 5; ++n) c[n] = (f32x4){0.f, 0.f, 0.f, 0.f};
        {
            const unsigned ab = WS_AO + ((mtO * 4 + hw) * 40 + 20) * 1024 + lane * 16, bb = WS_WT + ((ntO * 5) * 40 + 20) * 1024 + lane * 16;
            u32x4 af[5], bf[5][5];
#pragma unroll
            for (int ks = 0; ks < 5; ++ks)
#pragma unroll
                for (int n = 0; n < 5; ++n) bf[ks][n] = LD16(bb + n * 40960 + ks * 1024);
            POLL_ONE(hw == 0, 8, WS_F_OB + ((mtO >> 2) * 32 + r) * 8, WS_F_OB + ((mtO >> 2) * 32 + r) * 8, C_P3);
#pragma unroll
            for (int ks = 0; ks < 5; ++ks) af[ks] = LD16(ab + ks * 1024);
#pragma unroll
            for (int ks = 0; ks < 20; ++ks) {
#pragma unroll
                for (int n = 0; n < 5; ++n) c[n] = MFMA16_32(bf[ks % 5][n], af[ks % 5], c[n]);
                SBAR();
                if (ks < 15) {
                    af[ks % 5] = LD16(ab + (ks + 5) * 1024);
#pragma unroll
                    for (int n = 0; n < 5; ++n) bf[ks % 5][n] = LD16(bb + n * 40960 + (ks + 5) * 1024);
                }
                SBAR();
            }
        }
        STAMP(6);
        lds_wait<8>(lds + C_PROG, 8);
#pragma unroll
        for (int n = 0; n < 3; ++n) *(LDS3 f32x4*)(lds + X_FIN + hw * 3072 + n * 1024 + lane * 16) = c[n];
        lds_wait<4>(lds + C_END, 8);
#pragma unroll
        for (int n = 3; n < 5; ++n) *(LDS3 f32x4*)(lds + 143360 + hw * 2048 + (n - 3) * 1024 + lane * 16) = c[n];
        asm volatile("s_waitcnt lgkmcnt(0)" ::: "memory");
        if (lane == 0) __hip_atomic_store((LDS3 unsigned*)(lds + C_BP + hw * 4), 1u, __ATOMIC_RELAXED, __HIP_MEMORY_SCOPE_WORKGROUP);
        STAMP(7);
        asm volatile("mega_code_end2:");
    } else {
        const int qs = w & 3, kh = w >> 2;
        const int q0 = qb * 256 + qs * 64;
        {
            unsigned sink;
            if (w == 0 && (WARM_MODE & 1)) {
                const char* pc0; int len, len2;
                asm volatile("s_getpc_b64 %0\nmega_code_mark:\n\ts_mov_b32 %1, mega_code_end-mega_code_mark\n\ts_mov_b32 %2, mega_code_end2-mega_code_mark" : "=s"(pc0), "=s"(len), "=s"(len2));
                len = len > len2 ? len : len2;
                const int o = ((bid >> 3) * 64 + lane) * 64;
                if (o + 4 <= len) asm volatile("global_load_dword %0, %1, %2" : "=v"(sink) : "v"(o), "s"(pc0) : "memory");
            } else if (w >= 1 && w <= 6 && (WARM_MODE & 2)) {
                const unsigned o = ((w - 1) * 64 + lane) * 65536 + 65532;
                if (o < WS_END) asm volatile("global_load_dword %0, %1, %2 sc1" : "=v"(sink) : "v"(o), "s"(ws) : "memory");
            } else if (w == 7 && (WARM_MODE & 2)) {
                if (lane < 40) asm volatile("global_load_dword %0, %1, %2 sc1" : "=v"(sink) : "v"(lane * 65536), "s"(P.out) : "memory");
            }
            asm volatile("s_waitcnt vmcnt(0)" ::: "memory");
        }
        GET_TAG();
        if (bid < 240) {
            const int part = nt / 5, c0 = (nt - part * 5) * 128;
            const int wm = w & 1, wn = w >> 1;
            constexpr int RB = 32768;
            const int xr = wm * 64 + r;
            const unsigned xa = RB + xr * 128 + ((h ^ ((xr >> 1) & 7)) << 4);
            unsigned wt[2];
#pragma unroll
            for (int t = 0; t < 2; ++t) {
                const int q = (lane & 15) >> 2, p = lane & 3, blk = (lane >> 4) & 1, row = 8 * h + 4 * t + q;
                wt[t] = RB + 16384 + row * 256 + (((4 * wn + 2 * blk + (p >> 1)) ^ ((q << 2) | (2 * h + t))) << 4) + 8 * (p & 1);
            }
            auto gbody = [&](auto trc) {
                constexpr bool TR = decltype(trc)::value;
                u32x4 xf[2][8], wf[2][4];
                auto rd = [&](auto bc, int s) {
                    constexpr int B = decltype(bc)::value;
                    LDS3 const char* sb = lds + (s & 3) * 32768;
#pragma unroll
                    for (int ks = 0; ks < 4; ++ks) {
                        xf[B][2 * ks] = *(LDS3 const u32x4*)(sb + (xa ^ (ks * 32)));
                        xf[B][2 * ks + 1] = *(LDS3 const u32x4*)(sb + ((xa + 4096) ^ (ks * 32)));
                        const u32x2 lo = trread(sb + wt[0] + ks * 4096), hi = trread(sb + wt[1] + ks * 4096);
                        wf[B][ks] = (u32x4){lo[0], lo[1], hi[0], hi[1]};
                    }
                };
                f32x16 acc0, acc1;
                auto step = [&](auto bc, int s) {
                    constexpr int B = decltype(bc)::value;
                    if (s == 12) DRAIN();
                    __builtin_amdgcn_s_waitcnt(0xC07F);
                    BAR(); SBAR();
                    rd(std::integral_constant<int, B ^ 1>{}, s + 1);
#pragma unroll
                    for (int ks = 0; ks < 4; ++ks) {
                        if constexpr (TR) { acc0 = MFMA32_16(wf[B][ks], xf[B][2 * ks], acc0); acc1 = MFMA32_16(wf[B][ks], xf[B][2 * ks + 1], acc1); }
                        else              { acc0 = MFMA32_16(xf[B][2 * ks], wf[B][ks], acc0); acc1 = MFMA32_16(xf[B][2 * ks + 1], wf[B][ks], acc1); }
                    }
#pragma unroll
                    for (int i = 0; i < 4; ++i) { __builtin_amdgcn_sched_group_barrier(0x8, 1, 0); __builtin_amdgcn_sched_group_barrier(0x2, 2, 0); __builtin_amdgcn_sched_group_barrier(0x100, 4, 0); }
                    __builtin_amdgcn_sched_group_barrier(0x8, 4, 0);
                    SBAR();
                };
                WAIT_BAR(63);
                rd(std::integral_constant<int, 0>{}, 0);
#pragma unroll 1
                for (int mat = 0; mat < 2; ++mat) {
                    acc0 = (f32x16){}; acc1 = (f32x16){};
#pragma unroll 1
                    for (int kk = 0; kk < 10; kk += 2) {
                        step(std::integral_constant<int, 0>{}, mat * 10 + kk);
                        step(std::integral_constant<int, 1>{}, mat * 10 + kk + 1);
                    }
                    const int cpos = 2 * (mt & 7) + (mt >> 3);
                    if constexpr (TR) {
                        const float sc = (part == 0 && mat == 0) ? QSCALE : 1.f;
                        const unsigned base = part == 0 ? WS_Q + mat * NH * 81920 + mt * 5120 : WS_KV + mat * NH * 163840 + cpos * 10240;
                        const unsigned hs = part == 0 ? 81920 : 163840;
#pragma unroll
                        for (int pr = 0; pr < 2; ++pr) {
                            const int G8 = ((c0 + 32 * wn) >> 3) + 2 * pr + h, pp = G8 / 5, gi = G8 - pp * 5;
                            const unsigned hb0 = base + (2 * pp + (gi > 2)) * hs;
#pragma unroll
                            for (int i = 0; i < 2; ++i) {
                                const f32x16& a = i ? acc1 : acc0;
                                const int tl = wm * 64 + i * 32 + r;
                                const auto s0 = __builtin_amdgcn_permlane32_swap(pk2(a[8 * pr] * sc, a[8 * pr + 1] * sc), pk2(a[8 * pr + 4] * sc, a[8 * pr + 5] * sc), false, false);
                                const auto s1 = __builtin_amdgcn_permlane32_swap(pk2(a[8 * pr + 2] * sc, a[8 * pr + 3] * sc), pk2(a[8 * pr + 6] * sc, a[8 * pr + 7] * sc), false, false);
                                if (gi != 2) { const u32x4 ov = {s0[0], s1[0], s0[1], s1[1]}; ST16(ov, hb0 + (tl >> 5) * 1024 + (gi > 2 ? gi - 3 : gi) * 512 + r * 16); }
                                else { const u32x2 o0 = {s0[0], s1[0]}, o1 = {s0[1], s1[1]}; ST8(o0, hb0 + 4096 + tl * 8); ST8(o1, hb0 + hs + 4096 + tl * 8); }
                            }
                        }
                    } else {
                        const int n = c0 + 32 * wn + r, hd = n / 20, d = n - hd * 20, rho = (hd & 1) ? (d < 4 ? d + 16 : d - 4) : d;
                        const unsigned base = WS_KV + (mat * NH + hd) * 163840 + cpos * 10240 + 5120 + h * 320 + rho * 16;
#pragma unroll
                        for (int i = 0; i < 2; ++i)
#pragma unroll
                            for (int s2 = 0; s2 < 2; ++s2) {
                                const f32x16& a = i ? acc1 : acc0;
                                u32x4 ov = {pk2(a[8 * s2], a[8 * s2 + 1]), pk2(a[8 * s2 + 2], a[8 * s2 + 3]), pk2(a[8 * s2 + 4], a[8 * s2 + 5]), pk2(a[8 * s2 + 6], a[8 * s2 + 7])};
                                ST16(ov, base + (wm * 4 + i * 2 + s2) * 640);
                            }
                    }
                    if (mat == 0) STAMP(1);
                }
            };
            if (part < 2) gbody(std::true_type{}); else gbody(std::false_type{});
        } else {
            const int sj = bid - 240;
            WAIT_BAR(63);
            {
                const u32x4 ga = *(const u32x4*)(P.gw + lane * 8), gb = *(const u32x4*)(P.gw + 512 + (lane & 15) * 8);
                u32x4 xa[16], xb[4];
                const u16* xr = P.x + (size_t)(sj * 128 + w * 16) * DM;
#pragma unroll
                for (int rr = 0; rr < 16; ++rr) xa[rr] = *(const u32x4*)(xr + rr * DM + lane * 8);
#pragma unroll
                for (int i = 0; i < 4; ++i) xb[i] = *(const u32x4*)(xr + (i * 4 + (lane >> 4)) * DM + 512 + (lane & 15) * 8);
                float res = 0.f;
#pragma unroll
                for (int rr = 0; rr < 16; ++rr) {
                    float s = 0.f, s2 = 0.f;
#pragma unroll
                    for (int j = 0; j < 4; ++j) { s += bf_lo(xa[rr][j]) * bf_lo(ga[j]) + bf_hi(xa[rr][j]) * bf_hi(ga[j]); s2 += bf_lo(xb[rr >> 2][j]) * bf_lo(gb[j]) + bf_hi(xb[rr >> 2][j]) * bf_hi(gb[j]); }
                    if ((lane >> 4) == (rr & 3)) s += s2;
#pragma unroll
                    for (int o = 32; o >= 1; o >>= 1) s += __shfl_xor(s, o);
                    if (lane == rr) res = s;
                }
                if (lane < 16) ST4(__float_as_uint(1.f / (1.f + __expf(-res))), WS_G + (sj * 128 + w * 16 + lane) * 4);
            }
#pragma unroll 1
            for (int it = 0; it < 2; ++it) {
                unsigned e[4][8];
#pragma unroll
                for (int q = 0; q < 4; ++q) {
                    int item = mtid + 512 * (it * 4 + q); if (item >= 3200) item = 3199;
                    const int u = item / 320, n2 = item - u * 320, k0 = (sj * 10 + u) * 8;
                    const u16* wsrc = (k0 < 640 ? P.woa + (size_t)k0 * DM : P.wob + (size_t)(k0 - 640) * DM) + n2 * 2;
#pragma unroll
                    for (int j = 0; j < 8; ++j) e[q][j] = *(const unsigned*)(wsrc + (size_t)j * DM);
                }
#pragma unroll
                for (int q = 0; q < 4; ++q) {
                    int item = mtid + 512 * (it * 4 + q); if (item >= 3200) item = 3199;
                    const int u = item / 320, n = (item - u * 320) * 2, k0 = (sj * 10 + u) * 8;
                    const unsigned* f = e[q];
                    u32x4 lo = {(f[0] & 0xffffu) | (f[1] << 16), (f[2] & 0xffffu) | (f[3] << 16), (f[4] & 0xffffu) | (f[5] << 16), (f[6] & 0xffffu) | (f[7] << 16)};
                    u32x4 hi = {(f[0] >> 16) | (f[1] & 0xffff0000u), (f[2] >> 16) | (f[3] & 0xffff0000u), (f[4] >> 16) | (f[5] & 0xffff0000u), (f[6] >> 16) | (f[7] & 0xffff0000u)};
                    const unsigned dp = WS_WT + (((n >> 4) * 40 + (k0 >> 5)) * 64 + ((k0 >> 3) & 3) * 16 + (n & 15)) * 16;
                    ST16(lo, dp); ST16(hi, dp + 16);
                }
            }
            DRAIN();
            unsigned arr = 0;
            if (lane == 0) arr = LDS_CNT_ADD(G_ST);
            if (__builtin_amdgcn_readfirstlane(arr) == 7 && lane == 0) set_flag(ws, WS_F_SD + sj * 8, tag);
        }
        STAMP(2);
        const unsigned hb = lds0 + kh * 10240;
        const unsigned laneK = hb + lane * 16, laneT = hb + 4096 + r * 8 + h * 4, laneV = hb + 5120 + h * 320 + (r < 20 ? r : 19) * 16;
        const u64 vmask = 0x000fffff000fffffull;
        u32x4 v0a = {one, one, one, one}, v0b = v0a, v1 = v0a;
        u32x4 qA, qB, ka; unsigned qtA, qtB, kt;
        {
            lds_wait(lds + G_Q, 4);
            LDS3 const char* qp = lds + L_Q + (qs >> 1) * 5120;
            const unsigned om = (qs & 1) * 2048 + lane * 16, ot = 4096 + ((qs & 1) * 64 + r) * 8 + 4 * h;
            qA = *(LDS3 const u32x4*)(qp + om); qB = *(LDS3 const u32x4*)(qp + om + 1024);
            qtA = *(LDS3 const unsigned*)(qp + ot); qtB = *(LDS3 const unsigned*)(qp + ot + 256);
            DSR128(ka, laneK, 0); DSR32(kt, laneT, 0); DSR128M2(v0a, laneV, 0, v1, laneV, 640);
            asm volatile("s_waitcnt lgkmcnt(0)\n\ts_barrier" : "+v"(ka), "+v"(kt), "+v"(v0a), "+v"(v1), "+v"(qA), "+v"(qB), "+v"(qtA), "+v"(qtB) :: "memory");
        }
        STAMP(5);
        float mA, mB; u32x2 qTA, qTB; f32x16 sA, sB;
        {
            const u32x2 t0 = {kt, 0x3f80u}, a0 = {qtA, 0u}, b0 = {qtB, 0u};
            const f32x16 s = MFMA32_16(ka, qA, z16);
            const f32x16 s0 = MFMA32_8(t0, a0, s);
            f32x16 s1 = MFMA32_16(ka, qB, z16); s1 = MFMA32_8(t0, b0, s1);
            float x0 = s0[0], x1 = s1[0];
#pragma unroll
            for (int i = 1; i < 16; ++i) { x0 = fmaxf(x0, s0[i]); x1 = fmaxf(x1, s1[i]); }
            x0 = -xhalf_max(x0); x1 = -xhalf_max(x1);
            const unsigned hh = pk2(x0, x1);
            const unsigned ll = pk2(x0 - bf_lo(hh), x1 - bf_hi(hh));
            mA = -(bf_lo(hh) + bf_lo(ll)); mB = -(bf_hi(hh) + bf_hi(ll));
            const unsigned sel = h ? ll : hh;
            qTA = (u32x2){qtA, sel & 0xffffu}; qTB = (u32x2){qtB, sel >> 16};
            sA = MFMA32_8(t0, qTA, s);
        }
        f32x16 oA = {}, oB = {};
        u32x4 pa, pb = {0u, 0u, 0u, 0u};
#define EX4(S, i, Pv, j) do { Pv[j] = pk2(ex2(S[i]), ex2(S[i + 1])); Pv[j + 1] = pk2(ex2(S[i + 2]), ex2(S[i + 3])); PIN(Pv); SBAR(); } while (0)
#define KT2 ((u32x2){kt, 0x3f80u})
#define TILE(V0C, V0N, aK1, oK1, aT1, oT1, oV1, aV0, oV0) do { \
            sB = MFMA32_16(ka, qB, z16); SBAR(); DSR128(ka, aK1, oK1); EX4(sA, 0, pa, 0); \
            oB = MFMA32_16(v1, pb, oB); SBAR(); DSR128M2(v1, aV, oV1, V0N, aV0, oV0); EX4(sA, 4, pa, 2); \
            sB = MFMA32_8(KT2, qTB, sB); SBAR(); DSR32(kt, aT1, oT1); EX4(sA, 8, pb, 0); \
            oA = MFMA32_16(V0C, pa, oA); SBAR(); EX4(sA, 12, pb, 2); \
            LWAIT(1); sA = MFMA32_16(ka, qA, z16); SBAR(); EX4(sB, 0, pa, 0); \
            oA = MFMA32_16(v1, pb, oA); SBAR(); EX4(sB, 4, pa, 2); \
            LWAIT(0); sA = MFMA32_8(KT2, qTA, sA); SBAR(); EX4(sB, 8, pb, 0); \
            oB = MFMA32_16(V0C, pa, oB); SBAR(); EX4(sB, 12, pb, 2); \
        } while (0)
        unsigned aK = laneK, aT = laneT, aV = laneV;
#pragma unroll 1
        for (int c = 0; c < 8; ++c) {
            if (c == 1) { WAIT_BAR(0); }
            else if (c == 7) { if (lane == 0) LDS_CNT_ADD(C_PROG); asm volatile("s_waitcnt lgkmcnt(0)" ::: "memory"); }
            if (c >= 1) {
                unsigned pp;
                asm volatile("ds_write_b32 %1, %2\n\tds_read_b32 %0, %3\n\ts_waitcnt lgkmcnt(0)" : "=&v"(pp) : "v"(lds0 + C_PR + w * 4), "v"(c), "v"(lds0 + C_PR + (w ^ 4) * 4) : "memory");
                const int pc = __builtin_amdgcn_readfirstlane(pp);
                if (pc > c || (pc == c && ((c ^ kh) & 1))) __builtin_amdgcn_s_setprio(1); else __builtin_amdgcn_s_setprio(0);
            } else if (kh) __builtin_amdgcn_s_setprio(1);
            const unsigned nx = (c < 7 ? c + 1 : 7) * 20480;
            const unsigned nK = laneK + nx, nT = laneT + nx, nV = laneV + nx;
            SBAR();
            TILE(v0a, v0b, aK, 1024, aT, 256, 640,  aV, 1280);
            TILE(v0b, v0a, aK, 2048, aT, 512, 1920, aV, 2560);
            TILE(v0a, v0b, aK, 3072, aT, 768, 3200, aV, 3840);
            TILE(v0b, v0a, nK, 0,    nT, 0,   4480, nV, 0);
            aK = nK; aT = nT; aV = nV;
        }
        oB = MFMA32_16(v1, pb, oB);
#undef TILE
#undef EX4
        STAMP(6);
        {
            float chk = 0.f;
#pragma unroll
            for (int i = 0; i < 12; ++i) chk += fabsf(oA[i]) * 0.f + fabsf(oB[i]) * 0.f;
            bool bad = !(chk == 0.f);
            if (h) bad = bad || !(oA[8] >= 0x1p-60f && oA[8] <= 0x1p60f && oB[8] >= 0x1p-60f && oB[8] <= 0x1p60f);
            if (__builtin_expect(__any(bad), 0)) {
                mA = -INFINITY; mB = -INFINITY;
#pragma unroll
                for (int i = 0; i < 16; ++i) { oA[i] = 0.f; oB[i] = 0.f; }
                const u32x2 a0 = {qtA, 0u}, b0 = {qtB, 0u};
#pragma unroll 1
                for (int t = 0; t < 32; ++t) {
                    const unsigned kb = WS_KV + head * 163840 + (2 * (t >> 2) + kh) * 10240, t4 = t & 3;
                    const u32x4 k0 = LD16(kb + t4 * 1024 + lane * 16);
                    const u32x2 t0 = {LD4(kb + 4096 + (t4 * 32 + r) * 8 + 4 * h), 0u};
                    u32x4 w0 = LD16(kb + 5120 + t4 * 1280 + h * 320 + (r < 20 ? r : 19) * 16), w1 = LD16(kb + 5120 + t4 * 1280 + 640 + h * 320 + (r < 20 ? r : 19) * 16);
                    if (r >= 20) { w0 = (u32x4){one, one, one, one}; w1 = w0; }
                    f32x16 s0 = MFMA32_16(k0, qA, z16); s0 = MFMA32_8(t0, a0, s0);
                    f32x16 s1 = MFMA32_16(k0, qB, z16); s1 = MFMA32_8(t0, b0, s1);
                    float x0 = s0[0], x1 = s1[0];
#pragma unroll
                    for (int i = 1; i < 16; ++i) { x0 = fmaxf(x0, s0[i]); x1 = fmaxf(x1, s1[i]); }
                    x0 = fmaxf(xhalf_max(x0), mA); x1 = fmaxf(xhalf_max(x1), mB);
                    const float g0 = (mA == x0) ? 1.f : ex2(mA - x0), g1 = (mB == x1) ? 1.f : ex2(mB - x1);
                    mA = x0; mB = x1;
#pragma unroll
                    for (int i = 0; i < 16; ++i) { oA[i] *= g0; oB[i] *= g1; }
#pragma unroll
                    for (int i = 0; i < 4; ++i) { pa[i] = pk2(ex2(s0[2 * i] - x0), ex2(s0[2 * i + 1] - x0)); pb[i] = pk2(ex2(s0[8 + 2 * i] - x0), ex2(s0[9 + 2 * i] - x0)); }
                    oA = MFMA32_16(w0, pa, oA); oA = MFMA32_16(w1, pb, oA);
#pragma unroll
                    for (int i = 0; i < 4; ++i) { pa[i] = pk2(ex2(s1[2 * i] - x1), ex2(s1[2 * i + 1] - x1)); pb[i] = pk2(ex2(s1[8 + 2 * i] - x1), ex2(s1[9 + 2 * i] - x1)); }
                    oB = MFMA32_16(w0, pa, oB); oB = MFMA32_16(w1, pb, oB);
                }
            }
        }
        unsigned fin = 0;
        if (lane == 0) fin = LDS_CNT_ADD(C_END);
        fin = __builtin_amdgcn_readfirstlane(fin);
        lds_wait<4>(lds + C_OB, 4);
        lds_wait<4>(lds + C_PROG, 8);
        const float gO = *(LDS3 const float*)(lds + X_G + (qs * 64 + kh * 32 + r) * 4);
        {
            LDS3 char* xp = lds + X_MRG + w * 2816;
            const f32x16& s = kh ? oA : oB;
            *(LDS3 f32x4*)(xp + lane * 16) = (f32x4){s[0], s[1], s[2], s[3]};
            *(LDS3 f32x4*)(xp + 1024 + lane * 16) = (f32x4){s[4], s[5], s[6], s[7]};
            if (!h) *(LDS3 f32x4*)(xp + 2048 + r * 16) = (f32x4){s[8], s[9], s[10], s[11]};
            else *(LDS3 f32x2*)(xp + 2560 + r * 8) = (f32x2){s[8], kh ? mA : mB};
            asm volatile("s_waitcnt lgkmcnt(0)" ::: "memory");
            if (lane == 0) __hip_atomic_store((LDS3 unsigned*)(lds + C_MRG + w * 4), 1u, __ATOMIC_RELAXED, __HIP_MEMORY_SCOPE_WORKGROUP);
        }
        if (fin < 4) {
            const char* wsrc = ws + WS_WT + (size_t)ntO * 5 * 40960;
#pragma unroll 1
            for (int n = 0; n < 5; ++n)
#pragma unroll
                for (int t = 0; t < 5; ++t) { const int ks = fin * 5 + t; glds16s(lane * 16, wsrc + (n * 40 + ks) * 1024, lds0 + L_WA + (n * 20 + ks) * 1024); }
        }
        lds_wait<2>(lds + C_MRG + (w ^ 4) * 4, 1);
        bool lastw;
        {
            LDS3 const char* xp = lds + X_MRG + (w ^ 4) * 2816;
            const f32x4 p0 = *(LDS3 const f32x4*)(xp + lane * 16), p1 = *(LDS3 const f32x4*)(xp + 1024 + lane * 16), p2 = *(LDS3 const f32x4*)(xp + 2048 + r * 16);
            const f32x2 lm = *(LDS3 const f32x2*)(xp + 2560 + r * 8);
            const f32x16& s = kh ? oB : oA;
            const float mo = kh ? mB : mA, mp = lm[1];
            const float mx = fmaxf(mo, mp);
            const float fo = (mo == mx) ? 1.f : ex2(mo - mx), fq = (mp == mx) ? 1.f : ex2(mp - mx);
            float o[12];
#pragma unroll
            for (int i = 0; i < 4; ++i) { o[i] = s[i] * fo + p0[i] * fq; o[4 + i] = s[4 + i] * fo + p1[i] * fq; o[8 + i] = s[8 + i] * fo + p2[i] * fq; }
            const float l = upper_half(s[8]) * fo + lm[0] * fq;
            store_o(rs, o, gO / l, q0 + kh * 32 + r, head * 20, h);
            DRAIN();
            unsigned arr = 0;
            if (lane == 0) arr = LDS_CNT_ADD(C_OA);
            lastw = __builtin_amdgcn_readfirstlane(arr) == 7;
            if (lastw && lane == 0) set_flag(ws, WS_F_OA + (qb * 32 + head) * 8, tag);
        }
        STAMP(7);
        {
            const int c16 = lane & 15, g = lane >> 4, tt = w & 3, k0 = kh * 10;
            POLL_ONE(lastw, 4, WS_F_OA + ((mtO >> 2) * 32 + r) * 8, WS_F_OA + ((mtO >> 2) * 32 + r) * 8, C_P4);
            STAMP(9);
            const unsigned ab = WS_AO + ((mtO * 4 + tt) * 40 + k0) * 1024 + lane * 16;
            u32x4 af[10];
#pragma unroll
            for (int ks = 0; ks < 10; ++ks) af[ks] = LD16(ab + ks * 1024);
            lds_wait(lds + C_OA, 8);
            f32x4 c[5];
#pragma unroll
            for (int n = 0; n < 5; ++n) c[n] = (f32x4){0.f, 0.f, 0.f, 0.f};
#pragma unroll
            for (int ks = 0; ks < 10; ++ks)
#pragma unroll
                for (int n = 0; n < 5; ++n) c[n] = MFMA16_32(*(LDS3 const u32x4*)(lds + L_WA + (n * 20 + k0 + ks) * 1024 + lane * 16), af[ks], c[n]);
            LDS3 char* xp = lds + X_MRG + tt * 5120 + lane * 16;
            if (w < 4) { *(LDS3 f32x4*)(xp + 3072) = c[3]; *(LDS3 f32x4*)(xp + 4096) = c[4]; }
            else { *(LDS3 f32x4*)(xp) = c[0]; *(LDS3 f32x4*)(xp + 1024) = c[1]; *(LDS3 f32x4*)(xp + 2048) = c[2]; }
            asm volatile("s_waitcnt lgkmcnt(0)" ::: "memory");
            if (lane == 0) __hip_atomic_store((LDS3 unsigned*)(lds + C_FIN + w * 4), 1u, __ATOMIC_RELAXED, __HIP_MEMORY_SCOPE_WORKGROUP);
            lds_wait(lds + C_FIN + (w ^ 4) * 4, 1);
            lds_wait(lds + C_BP + tt * 4, 1);
            u16* op = P.out + (size_t)(mtO * 64 + tt * 16 + c16) * DM + ntO * 80 + g * 4;
#pragma unroll
            for (int n = 0; n < 5; ++n) {
                if ((w < 4) == (n < 3)) {
                    const f32x4 bp = *(LDS3 const f32x4*)(n < 3 ? lds + X_FIN + tt * 3072 + n * 1024 + lane * 16 : lds + 143360 + tt * 2048 + (n - 3) * 1024 + lane * 16);
                    const f32x4 s = c[n] + *(LDS3 const f32x4*)(xp + n * 1024) + bp;
                    *(u32x2*)(op + n * 16) = (u32x2){pk2(s[0], s[1]), pk2(s[2], s[3])};
                }
            }
            if (bid == 0 && mtid == 0) set_flag(ws, WS_EPOCH, tag);
        }
        STAMP(10);
        asm volatile("mega_code_end:");
    }
}

extern "C" void kernel_launch(void* const* d_in, const int* in_sizes, int n_in, void* d_out, int out_size, void* d_ws, size_t ws_size,
                              hipStream_t stream) {
    Params P;
    P.x = (const u16*)d_in[0]; P.wa = (const u16*)d_in[1]; P.woa = (const u16*)d_in[2]; P.wb = (const u16*)d_in[4];
    P.wphi = (const u16*)d_in[5]; P.wob = (const u16*)d_in[6]; P.gw = (const u16*)d_in[7]; P.ws = (char*)d_ws; P.out = (u16*)d_out;
    mega<<<256, 768, 0, stream>>>(P);
}
```

```cpp
#include <hip/hip_runtime.h>
#include <hip/hip_bf16.h>
#include <stdint.h>
#include <type_traits>

typedef __attribute__((ext_vector_type(8))) short bf16x8;
typedef __attribute__((ext_vector_type(4))) short bf16x4;
typedef __attribute__((ext_vector_type(16))) float f32x16;
typedef __attribute__((ext_vector_type(4))) float f32x4;
typedef __attribute__((ext_vector_type(2))) float f32x2;
typedef __attribute__((ext_vector_type(4))) unsigned u32x4;
typedef __attribute__((ext_vector_type(2))) unsigned u32x2;
typedef __attribute__((ext_vector_type(2))) __bf16 bf2_t;
typedef unsigned short u16;
typedef unsigned long long u64;
#define LDS3 __attribute__((address_space(3)))
typedef __attribute__((address_space(1))) u64 gu64;
#define RLX_AGENT __ATOMIC_RELAXED, __HIP_MEMORY_SCOPE_AGENT

#define PRIO_MODE 3
#define WARM_MODE 7
#define PRE_SLEEP 2
constexpr int SEQ = 2048, DM = 640, NH = 32;
constexpr float LOG2E = 1.4426950408889634f;
constexpr float QSCALE = 0.22360679774997896f * LOG2E;

constexpr unsigned WS_EPOCH = 0;
constexpr unsigned WS_F_G   = 256;
constexpr unsigned WS_F_SD  = WS_F_G + 4096;
constexpr unsigned WS_F_KV  = WS_F_SD + 256;
constexpr unsigned WS_F_OB  = WS_F_KV + 2048;
constexpr unsigned WS_F_OA  = WS_F_OB + 2048;
constexpr unsigned WS_G     = 16384;
constexpr unsigned WS_Q     = 32768;
constexpr unsigned WS_KV    = WS_Q + 2 * NH * 81920;
constexpr unsigned WS_KVP   = WS_KV + 2 * NH * 163840;
constexpr unsigned WS_AO    = WS_KVP + NH * 8 * 4096;
constexpr unsigned WS_WT    = WS_AO + SEQ * 1280 * 2;
constexpr unsigned WS_END   = WS_WT + 1280 * 640 * 2;

#define STAMP(i)

__device__ __forceinline__ unsigned pk2(float a, float b) { f32x2 v = {a, b}; bf2_t r = __builtin_convertvector(v, bf2_t); return __builtin_bit_cast(unsigned, r); }
__device__ __forceinline__ float bf_lo(unsigned u) { return __uint_as_float(u << 16); }
__device__ __forceinline__ float bf_hi(unsigned u) { return __uint_as_float(u & 0xffff0000u); }
__device__ __forceinline__ float ex2(float x) { return __builtin_amdgcn_exp2f(x); }
#define MFMA32_16(a, b, c) __builtin_amdgcn_mfma_f32_32x32x16_bf16(__builtin_bit_cast(bf16x8, a), __builtin_bit_cast(bf16x8, b), c, 0, 0, 0)
#define MFMA32_8(a, b, c) __builtin_amdgcn_mfma_f32_32x32x8bf16_1k(__builtin_bit_cast(bf16x4, a), __builtin_bit_cast(bf16x4, b), c, 0, 0, 0)
#define MFMA16_32(a, b, c) __builtin_amdgcn_mfma_f32_16x16x32_bf16(__builtin_bit_cast(bf16x8, a), __builtin_bit_cast(bf16x8, b), c, 0, 0, 0)

__device__ __forceinline__ void glds16(unsigned voff, const void* sbase, unsigned lds_base) {
    unsigned sv;
    asm volatile("s_mov_b32 %0, m0\n\ts_mov_b32 m0, %3\n\ts_nop 0\n\tglobal_load_lds_dwordx4 %1, %2\n\ts_mov_b32 m0, %0"
                 : "=&s"(sv) : "v"(voff), "s"(sbase), "s"(__builtin_amdgcn_readfirstlane(lds_base)) : "memory");
}
__device__ __forceinline__ void glds16s(unsigned voff, const void* sbase, unsigned lds_base) {
    unsigned sv;
    asm volatile("s_mov_b32 %0, m0\n\ts_mov_b32 m0, %3\n\ts_nop 0\n\tglobal_load_lds_dwordx4 %1, %2 sc1\n\ts_mov_b32 m0, %0"
                 : "=&s"(sv) : "v"(voff), "s"(sbase), "s"(__builtin_amdgcn_readfirstlane(lds_base)) : "memory");
}
__device__ __forceinline__ void glds4s(unsigned voff, const void* sbase, unsigned lds_base) {
    unsigned sv;
    asm volatile("s_mov_b32 %0, m0\n\ts_mov_b32 m0, %3\n\ts_nop 0\n\tglobal_load_lds_dword %1, %2 sc1\n\ts_mov_b32 m0, %0"
                 : "=&s"(sv) : "v"(voff), "s"(sbase), "s"(__builtin_amdgcn_readfirstlane(lds_base)) : "memory");
}
typedef short v4i16_t __attribute__((ext_vector_type(4)));
__device__ __forceinline__ u32x2 trread(LDS3 const char* p) {
    return __builtin_bit_cast(u32x2, __builtin_amdgcn_ds_read_tr16_b64_v4i16((LDS3 v4i16_t*)p));
}
#define WAIT_BAR(N) asm volatile("s_waitcnt vmcnt(" #N ") lgkmcnt(0)\n\ts_barrier" ::: "memory")
#define DRAIN() asm volatile("s_waitcnt vmcnt(0)" ::: "memory")
#define SBAR() __builtin_amdgcn_sched_barrier(0)
#define PIN(x) asm volatile("" : "+v"(x))

__device__ __forceinline__ float xhalf_max(float x) {
    auto r = __builtin_amdgcn_permlane32_swap(__float_as_uint(x), __float_as_uint(x), false, false);
    return fmaxf(__uint_as_float(r[0]), __uint_as_float(r[1]));
}
__device__ __forceinline__ float xhalf_sum(float x) {
    auto r = __builtin_amdgcn_permlane32_swap(__float_as_uint(x), __float_as_uint(x), false, false);
    return __uint_as_float(r[0]) + __uint_as_float(r[1]);
}
__device__ __forceinline__ float upper_half(float x) {
    auto r = __builtin_amdgcn_permlane32_swap(__float_as_uint(x), __float_as_uint(x), false, false);
    return __uint_as_float(r[1]);
}

typedef __amdgpu_buffer_rsrc_t rsrc_t;
#define ST8(v, off)  __builtin_amdgcn_raw_buffer_store_b64(v, rs, (int)(off), 0, 16)
#define ST16(v, off) __builtin_amdgcn_raw_buffer_store_b128(v, rs, (int)(off), 0, 16)
#define ST4(v, off)  __builtin_amdgcn_raw_buffer_store_b32(v, rs, (int)(off), 0, 16)
#define LD16(off) __builtin_amdgcn_raw_buffer_load_b128(rs, (int)(off), 0, 16)
#define LD4(off)  __builtin_amdgcn_raw_buffer_load_b32(rs, (int)(off), 0, 16)

template <int SLP = 2>
__device__ __forceinline__ void wait_flags(const char* ws, unsigned off0, unsigned off1, u64 tag) {
    gu64* p0 = (gu64*)(ws + off0); gu64* p1 = (gu64*)(ws + off1);
#pragma unroll 1
    for (unsigned spins = 0; spins < (1u << 16); ++spins) {
        const u64 a = __hip_atomic_load(p0, RLX_AGENT), b = __hip_atomic_load(p1, RLX_AGENT);
        if (__all(a == tag && b == tag)) break;
        __builtin_amdgcn_s_sleep(SLP);
    }
    __builtin_amdgcn_fence(__ATOMIC_ACQUIRE, "wavefront");
}
__device__ __forceinline__ void set_flag(char* ws, unsigned off, u64 tag) { __hip_atomic_store((gu64*)(ws + off), tag, RLX_AGENT); }

struct Params { const u16* x; const u16* wa; const u16* wb; const u16* wphi; const u16* woa; const u16* wob; const u16* gw; char* ws; u16* out; };

__device__ __forceinline__ void feature_map(const u32x4& pm, unsigned pt, const u32x4& tm, unsigned tt, int h, u32x4& fp, u32x4& fn, u32x4& ft) {
    const f32x16 z16 = {};
    const u32x2 a8 = {pt, 0u}, b8 = {tt, 0u};
    f32x16 s = MFMA32_16(pm, tm, z16); s = MFMA32_8(a8, b8, s);
    float v[12];
#pragma unroll
    for (int i = 0; i < 12; i += 2) { const unsigned u = pk2(s[i], s[i + 1]); v[i] = bf_lo(u) * LOG2E; v[i + 1] = bf_hi(u) * LOG2E; }
    float mx = v[0], mn = v[0];
#pragma unroll
    for (int i = 1; i < 8; ++i) { mx = fmaxf(mx, v[i]); mn = fminf(mn, v[i]); }
    const float tx = fmaxf(fmaxf(v[8], v[9]), fmaxf(v[10], v[11])), tn = fminf(fminf(v[8], v[9]), fminf(v[10], v[11]));
    if (!h) { mx = fmaxf(mx, tx); mn = fminf(mn, tn); }
    mx = xhalf_max(mx); mn = -xhalf_max(-mn);
    float ep[12], en[12], sp = 0.f, sn = 0.f;
#pragma unroll
    for (int i = 0; i < 12; ++i) {
        ep[i] = ex2(v[i] - mx); en[i] = ex2(mn - v[i]);
        if (i >= 8 && h) { ep[i] = 0.f; en[i] = 0.f; }
        sp += ep[i]; sn += en[i];
    }
    sp = __builtin_amdgcn_rcpf(xhalf_sum(sp)); sn = __builtin_amdgcn_rcpf(xhalf_sum(sn));
#pragma unroll
    for (int i = 0; i < 4; ++i) { fp[i] = pk2(ep[2 * i] * sp, ep[2 * i + 1] * sp); fn[i] = pk2(en[2 * i] * sn, en[2 * i + 1] * sn); }
    ft = (u32x4){pk2(ep[8] * sp, ep[9] * sp), pk2(ep[10] * sp, ep[11] * sp), pk2(en[8] * sn, en[9] * sn), pk2(en[10] * sn, en[11] * sn)};
}

__device__ __forceinline__ void store_o(rsrc_t rs, const float (&o)[12], float sc, int tok, int kbase, int h) {
#pragma unroll
    for (int gq = 0; gq < 3; ++gq) {
        const int d = 8 * gq + 4 * h;
        if (d < 20) {
            const int k = kbase + ((kbase & 4) ? (d == 16 ? 0 : d + 4) : d);
            u32x2 ov = {pk2(o[4 * gq] * sc, o[4 * gq + 1] * sc), pk2(o[4 * gq + 2] * sc, o[4 * gq + 3] * sc)};
            ST8(ov, WS_AO + (((tok >> 4) * 40 + (k >> 5)) * 64 + ((k >> 3) & 3) * 16 + (tok & 15)) * 16 + (k & 7) * 2);
        }
    }
}

#define DSR128(dst, addr, off) asm volatile("ds_read_b128 %0, %1 offset:%2" : "=v"(dst) : "v"(addr), "i"(off))
#define DSR32(dst, addr, off) asm volatile("ds_read_b32 %0, %1 offset:%2" : "=v"(dst) : "v"(addr), "i"(off))
#define DSR128M2(d0, a0, o0, d1, a1, o1) asm volatile("s_mov_b64 exec, %6\n\tds_read_b128 %0, %2 offset:%3\n\tds_read_b128 %1, %4 offset:%5\n\ts_mov_b64 exec, -1" \
                                                      : "+v"(d0), "+v"(d1) : "v"(a0), "i"(o0), "v"(a1), "i"(o1), "s"(vmask))
#define LWAIT(n) do { asm volatile("s_waitcnt lgkmcnt(" #n ")" ::: "memory"); SBAR(); } while (0)

#define BAR() asm volatile("s_barrier" ::: "memory")
#define LDS_CNT_ADD(off) __hip_atomic_fetch_add((LDS3 unsigned*)(lds + (off)), 1u, __ATOMIC_RELAXED, __HIP_MEMORY_SCOPE_WORKGROUP)

template <int SLP = 1>
__device__ __forceinline__ void lds_wait(LDS3 char* p, unsigned want) {
#pragma unroll 1
    for (unsigned spins = 0; spins < (1u << 20); ++spins) {
        if ((unsigned)__builtin_amdgcn_readfirstlane(__hip_atomic_load((LDS3 unsigned*)p, __ATOMIC_RELAXED, __HIP_MEMORY_SCOPE_WORKGROUP)) >= want) break;
        __builtin_amdgcn_s_sleep(SLP);
    }
    __builtin_amdgcn_fence(__ATOMIC_ACQUIRE, "wavefront");
}

#define POLL_ONE(cond, SLP, o0, o1, word) do { if (cond) { wait_flags<SLP>(ws, o0, o1, tag); \
        if (lane == 0) __hip_atomic_store((LDS3 unsigned*)(lds + (word)), 1u, __ATOMIC_RELAXED, __HIP_MEMORY_SCOPE_WORKGROUP); } else lds_wait<SLP>(lds + (word), 1); } while (0)
constexpr int C_KV = 0, C_KVF = 4, C_OB = 8, C_PROG = 12, C_WA = 16, C_OA = 20, C_MRG = 32  , C_FIN = 64  , C_BP = 96  ;
constexpr int C_PROG2 = 24, C_BX = 28, C_P1 = 112, C_P2 = 116, C_P3 = 120, C_P4 = 124, C_PR = 128  ;
constexpr int G_W = 31744;
constexpr int G_FULL = G_W, G_FREE = G_W + 16, G_Q = G_W + 32, G_KV0 = G_W + 36, G_ST = G_W + 40;
constexpr int C_END = 160, X_G = 256;
constexpr int X_KVF = 1280, X_SLOT = 4096;
constexpr int X_MRG = 1280;
constexpr int L_WA = 24576;
constexpr int L_Q = 65536;
constexpr int X_FIN = 126976;

__global__ __launch_bounds__(768) void mega(Params P) {
    __shared__ __attribute__((aligned(16))) char smem[163840];
    LDS3 char* lds = (LDS3 char*)smem;
    char* ws = P.ws;
    const int tid = threadIdx.x, lane = tid & 63, r = lane & 31, h = lane >> 5;
    const int hw = __builtin_amdgcn_readfirstlane(tid >> 6);
    const bool helper = hw < 4;
    const int w = hw - 4, mtid = tid - 256;
    const int bid = blockIdx.x;
    const unsigned lds0 = (unsigned)(uintptr_t)smem;
    const rsrc_t rs = __builtin_amdgcn_make_buffer_rsrc(ws, 0, WS_END, 0x00020000);
    STAMP(0);
    u64 tag = 0;
#define GET_TAG() do { asm volatile("s_load_dwordx2 %0, %1, 0x0\n\ts_waitcnt lgkmcnt(0)" : "=s"(tag) : "s"(ws) : "memory"); tag += 1; } while (0)
    const int head = (bid & 7) * 4 + ((bid >> 3) & 3), qb = bid >> 5;
    const int nlo = (head * 20) >> 7, nhi = (head * 20 + 19) >> 7;
    const int mtO = (bid & 7) * 4 + ((bid >> 3) & 3), ntO = bid >> 5;
    const int lin = (bid & 7) * 30 + (bid >> 3), lh = lin >= 120, lrem = lin - 120 * lh;
    const int mt = lh * 8 + (lrem & 7), nt = lrem >> 3;
    const unsigned fG = WS_F_G + (mt * 15 + nt) * 8;
    const unsigned one = (r == 20) ? 0x3f803f80u : 0u;
    const f32x16 z16 = {};

    if (helper) {
        const unsigned fA0 = WS_F_G + ((lane & 15) * 15 + (lane < 32 ? 5 : 10) + ((lane & 16) ? nhi : nlo)) * 8;
        const unsigned fA1 = WS_F_G + ((qb * 2 + (lane & 1)) * 15 + ((lane & 2) ? nhi : nlo)) * 8;
        __builtin_amdgcn_s_setprio(3);
        const char* kvb = ws + WS_KV + head * 163840;
        auto kv_issue = [&](int grp) {
#pragma unroll
            for (int t = 0; t < 8; ++t) { int pc = grp * 32 + hw + 4 * t; if (pc == 31) pc = 30; glds16s(lane * 16, kvb + pc * 1024, lds0 + pc * 1024); }
        };
        auto q_issue = [&]() {
            const char* qbase = ws + WS_Q + head * 81920 + qb * 10240;
#pragma unroll
            for (int t = 0; t < 3; ++t) { const int pc = hw + 4 * t < 10 ? hw + 4 * t : 9; glds16s(lane * 16, qbase + pc * 1024, lds0 + L_Q + pc * 1024); }
        };
        if (bid < 240) {
            const int rot = (nt * 3 + mt * 5 + (mt >> 1)) % 10;
            const unsigned xo = (mt * 128 + 8 * hw + (lane >> 3)) * 1280 + (((lane & 7) ^ ((4 * hw + (lane >> 4)) & 7)) << 4);
            const unsigned wo = (4 * hw + (lane >> 4)) * 3840 + nt * 256 + (((lane & 15) ^ (((lane >> 4) << 2) | hw)) << 4);
            const unsigned dX = lds0 + 32768 + hw * 1024;
            auto issue = [&](int s) {
                int kt = (s >= 10 ? s - 10 : s) + rot; if (kt >= 10) kt -= 10;
                const char* xb = (const char*)P.x + kt * 128;
                const char* wb = (const char*)(s >= 10 ? P.wb : P.wa) + (size_t)kt * (64 * 3840);
                const unsigned d = dX + (s & 3) * 32768;
#pragma unroll
                for (int i = 0; i < 4; ++i) { glds16(xo, xb + i * 40960, d + i * 4096); glds16(wo, wb + i * 61440, d + 16384 + i * 4096); }
            };
#pragma unroll 1
            for (int n = 0; n < 20; ++n) {
                if (n == 2) { if (hw == 0 && lane < 16) *(LDS3 unsigned*)(lds + G_W + lane * 4) = 0u; GET_TAG(); WAIT_BAR(8); }
                else if (n == 3) { WAIT_BAR(8); }
                else if (n >= 5) { WAIT_BAR(16); }
                if (n == 16 && tid == 0) set_flag(ws, fG, tag);
                if (n == 19 && hw == 0) {
                    glds4s(fA0, ws, lds0); glds4s(fA0 + 4, ws, lds0 + 256); glds4s(fA1, ws, lds0 + 512); glds4s(fA1 + 4, ws, lds0 + 768);
                }
                if (n >= 5 && n < 10) {
                    const int pc = (mt & 7) * 20 + (n - 5) * 4 + hw;
                    glds16((lane >> 4) * 3840 + nt * 256 + (lane & 15) * 16, (const char*)P.wb + (size_t)pc * 15360, lds0 + 1024 + hw * 1024);
                }
                issue(n);
            }
            WAIT_BAR(16);
            asm volatile("s_waitcnt vmcnt(8)" ::: "memory");
            if (hw == 0) {
                LDS3 const unsigned* f = (LDS3 const unsigned*)lds + lane;
                const unsigned tl = (unsigned)tag, th = (unsigned)(tag >> 32);
                if (!__all(f[0] == tl && f[64] == th && f[128] == tl && f[192] == th)) wait_flags<8>(ws, fA0, fA1, tag);
            }
            WAIT_BAR(63);
            kv_issue(0); q_issue();
            WAIT_BAR(11);
            kv_issue(1);
            WAIT_BAR(63);
        } else {
            if (hw == 0 && lane < 16) *(LDS3 unsigned*)(lds + G_W + lane * 4) = 0u;
            GET_TAG();
            WAIT_BAR(63);
            if (hw == 0) {
#pragma unroll 1
                for (int t = 0; t < PRE_SLEEP; ++t) __builtin_amdgcn_s_sleep(127);
                wait_flags<16>(ws, fA0, fA1, tag);
                if (lane == 0) LDS_CNT_ADD(G_FREE);
            }
            lds_wait<16>(lds + G_FREE, 1);
            kv_issue(0); q_issue(); kv_issue(1);
        }
        asm volatile("s_waitcnt vmcnt(6)" ::: "memory");
        if (lane == 0) LDS_CNT_ADD(G_Q);
        kv_issue(3); kv_issue(4);
        WAIT_BAR(63);
        if (hw == 3) glds16s(lane * 16, kvb + 31 * 1024, lds0 + 31 * 1024);
        kv_issue(2);
        if (hw == 0) *(LDS3 u32x4*)(lds + lane * 16) = (u32x4){0u, 0u, 0u, 0u};
        u32x4 phm; unsigned pht;
        {
            const u16* wp = P.wphi + head * 400 + (r < 20 ? r : 0);
            unsigned e[10];
#pragma unroll
            for (int j = 0; j < 8; ++j) e[j] = wp[(8 * h + j + 4 * (head & 1)) * 20];
            e[8] = wp[((head & 1 ? 0 : 16) + 2 * h) * 20]; e[9] = wp[((head & 1 ? 1 : 17) + 2 * h) * 20];
            phm = (u32x4){e[0] | (e[1] << 16), e[2] | (e[3] << 16), e[4] | (e[5] << 16), e[6] | (e[7] << 16)}; pht = e[8] | (e[9] << 16);
            if (r >= 20) { phm = (u32x4){0u, 0u, 0u, 0u}; pht = 0u; }
        }
        WAIT_BAR(0);
        if (tid == 0 && bid < 240) set_flag(ws, fG + 2048, tag);
        STAMP(3);
        if (hw * 64 + lane < 144) *(LDS3 u32x4*)(lds + X_KVF + (hw * 64 + lane) * 16) = (u32x4){0u, 0u, 0u, 0u};
        const int tokS = qb * 256 + hw * 64, m7 = tokS >> 7, t4 = (tokS >> 5) & 3;
        {
            const unsigned pt3 = (lane >> 1) & 3;
            const unsigned tb = WS_F_G + 2048 + ((qb * 2 + ((lane >> 3) & 1)) * 15 + ((lane & 1) ? nhi : nlo) + (pt3 < 3 ? pt3 : 0) * 5) * 8;
            POLL_ONE(hw == 0, 8, tb, WS_F_SD + (lane & 15) * 8, C_P1);
        }
        const unsigned om = t4 * 1024 + lane * 16, ot = 4096 + (t4 * 32 + r) * 8 + 4 * h;
        u32x4 qf[2][3]; float gS[2];
        {
            const unsigned kb = WS_KV + (NH + head) * 163840 + (2 * (m7 & 7) + (m7 >> 3)) * 10240, qbb = WS_Q + (NH + head) * 81920 + m7 * 5120;
            const unsigned ov = 5120 + t4 * 1280 + h * 320 + (r < 20 ? r : 19) * 16;
            u32x4 e0[2], w0[2], w1[2], eq[2]; unsigned et[2], eqt[2];
#pragma unroll
            for (int i = 0; i < 2; ++i) {
                e0[i] = LD16(kb + om + i * 1024); et[i] = LD4(kb + ot + i * 256); w0[i] = LD16(kb + ov + i * 1280); w1[i] = LD16(kb + ov + i * 1280 + 640);
                eq[i] = LD16(qbb + om + i * 1024); eqt[i] = LD4(qbb + ot + i * 256); gS[i] = __uint_as_float(LD4(WS_G + (tokS + i * 32 + r) * 4));
            }
            *(LDS3 float*)(lds + X_G + (hw * 64 + lane) * 4) = h ? gS[1] : gS[0];
            u32x4 i0, i1;
#pragma unroll
            for (int j = 0; j < 4; ++j) {
                const int e = r - 8 * h - 2 * j;
                i0[j] = e == 0 ? 0x3f80u : (e == 1 ? 0x3f800000u : 0u);
                i1[j] = e == 16 ? 0x3f80u : (e == 17 ? 0x3f800000u : 0u);
            }
            f32x16 k0 = {}, k1 = {};
#pragma unroll
            for (int i = 0; i < 2; ++i) {
                if (r >= 20) { w0[i] = (u32x4){one, one, one, one}; w1[i] = w0[i]; }
                u32x4 fp, fn, ft;
                feature_map(phm, pht, e0[i], et[i], h, fp, fn, ft);
                f32x16 T0 = MFMA32_16(fp, i0, z16); T0 = MFMA32_16(fn, i1, T0);
                f32x16 T1 = MFMA32_16(ft, i0, z16);
                u32x4 b00, b01, b10, b11;
#pragma unroll
                for (int j = 0; j < 4; ++j) {
                    b00[j] = pk2(T0[2 * j], T0[2 * j + 1]); b01[j] = pk2(T0[8 + 2 * j], T0[9 + 2 * j]);
                    b10[j] = pk2(T1[2 * j], T1[2 * j + 1]); b11[j] = pk2(T1[8 + 2 * j], T1[9 + 2 * j]);
                }
                k0 = MFMA32_16(w0[i], b00, k0); k0 = MFMA32_16(w1[i], b01, k0);
                k1 = MFMA32_16(w0[i], b10, k1); k1 = MFMA32_16(w1[i], b11, k1);
            }
            LDS3 char* sp = lds + X_SLOT + hw * 4096;
#pragma unroll
            for (int gq = 0; gq < 3; ++gq) {
                *(LDS3 f32x4*)(sp + gq * 1024 + lane * 16) = (f32x4){k0[4 * gq], k0[4 * gq + 1], k0[4 * gq + 2], k0[4 * gq + 3]};
                if (r < 8) *(LDS3 f32x4*)(sp + 3072 + (gq * 16 + h * 8 + r) * 16) = (f32x4){k1[4 * gq], k1[4 * gq + 1], k1[4 * gq + 2], k1[4 * gq + 3]};
            }
            asm volatile("s_waitcnt lgkmcnt(0)" ::: "memory");
            unsigned arr = 0;
            if (lane == 0) arr = LDS_CNT_ADD(C_KV);
            if (__builtin_amdgcn_readfirstlane(arr) == 3) {
                __builtin_amdgcn_fence(__ATOMIC_ACQUIRE, "wavefront");
#pragma unroll
                for (int i = 0; i < 4; ++i) {
                    const int qd = lane + 64 * i;
                    LDS3 const char* s0 = lds + X_SLOT + qd * 16;
                    const f32x4 s = (*(LDS3 const f32x4*)s0 + *(LDS3 const f32x4*)(s0 + 4096)) + (*(LDS3 const f32x4*)(s0 + 8192) + *(LDS3 const f32x4*)(s0 + 12288));
                    if (qd < 240) ST16(__builtin_bit_cast(u32x4, s), WS_KVP + (head * 8 + qb) * 4096 + qd * 16);
                }
                DRAIN();
                if (lane == 0) set_flag(ws, WS_F_KV + (head * 8 + qb) * 8, tag);
            }
#pragma unroll
            for (int i = 0; i < 2; ++i) feature_map(phm, pht, eq[i], eqt[i], h, qf[i][0], qf[i][1], qf[i][2]);
        }
        STAMP(4);
        POLL_ONE(hw == 0, 8, WS_F_KV + (head * 8 + (lane & 7)) * 8, WS_F_KV + (head * 8 + (lane & 7)) * 8, C_P2);
        {
            const int qd = hw * 60 + (lane < 60 ? lane : 59);
            f32x4 pp[8];
#pragma unroll
            for (int p = 0; p < 8; ++p) pp[p] = __builtin_bit_cast(f32x4, LD16(WS_KVP + (head * 8 + p) * 4096 + qd * 16));
            const f32x4 s4 = ((pp[0] + pp[1]) + (pp[2] + pp[3])) + ((pp[4] + pp[5]) + (pp[6] + pp[7]));
            const int gq = qd < 192 ? qd >> 6 : (qd - 192) >> 4, hs = qd < 192 ? (qd >> 5) & 1 : (qd >> 3) & 1, sg = qd < 192 ? qd & 31 : 32 + (qd & 7);
            const unsigned a = pk2(s4[0], s4[1]), b = pk2(s4[2], s4[3]);
            LDS3 u16* kp = (LDS3 u16*)(lds + X_KVF + (8 * gq + 4 * hs) * 96 + sg * 2);
            kp[0] = (u16)a; kp[48] = (u16)(a >> 16); kp[96] = (u16)b; kp[144] = (u16)(b >> 16);
            asm volatile("s_waitcnt lgkmcnt(0)" ::: "memory");
            if (lane == 0) LDS_CNT_ADD(C_KVF);
            lds_wait<8>(lds + C_KVF, 4);
        }
        {
            LDS3 const char* kp = lds + X_KVF + (r < 24 ? r : 23) * 96 + h * 16;
            const u32x4 a0 = *(LDS3 const u32x4*)kp, a1 = *(LDS3 const u32x4*)(kp + 32), a2 = *(LDS3 const u32x4*)(kp + 64);
#pragma unroll
            for (int i = 0; i < 2; ++i) {
                f32x16 d = MFMA32_16(a0, qf[i][0], z16); d = MFMA32_16(a1, qf[i][1], d); d = MFMA32_16(a2, qf[i][2], d);
                const float den = upper_half(d[8]) + 1e-6f;
                float o[12];
#pragma unroll
                for (int j = 0; j < 12; ++j) o[j] = d[j];
                store_o(rs, o, (1.f - gS[i]) / den, tokS + i * 32 + r, 640 + head * 20, h);
            }
            DRAIN();
            unsigned arr = 0;
            if (lane == 0) arr = LDS_CNT_ADD(C_OB);
            if (__builtin_amdgcn_readfirstlane(arr) == 3 && lane == 0) set_flag(ws, WS_F_OB + (qb * 32 + head) * 8, tag);
        }
        STAMP(5);
        f32x4 c[5];
#pragma unroll
        for (int n = 0; n < 5; ++n) c[n] = (f32x4){0.f, 0.f, 0.f, 0.f};
        {
            const unsigned ab = WS_AO + ((mtO * 4 + hw) * 40 + 20) * 1024 + lane * 16, bb = WS_WT + ((ntO * 5) * 40 + 20) * 1024 + lane * 16;
            u32x4 af[5], bf[5][5];
#pragma unroll
            for (int ks = 0; ks < 5; ++ks)
#pragma unroll
                for (int n = 0; n < 5; ++n) bf[ks][n] = LD16(bb + n * 40960 + ks * 1024);
            POLL_ONE(hw == 0, 8, WS_F_OB + ((mtO >> 2) * 32 + r) * 8, WS_F_OB + ((mtO >> 2) * 32 + r) * 8, C_P3);
#pragma unroll
            for (int ks = 0; ks < 5; ++ks) af[ks] = LD16(ab + ks * 1024);
#pragma unroll
            for (int ks = 0; ks < 20; ++ks) {
#pragma unroll
                for (int n = 0; n < 5; ++n) c[n] = MFMA16_32(bf[ks % 5][n], af[ks % 5], c[n]);
                SBAR();
                if (ks < 15) {
                    af[ks % 5] = LD16(ab + (ks + 5) * 1024);
#pragma unroll
                    for (int n = 0; n < 5; ++n) bf[ks % 5][n] = LD16(bb + n * 40960 + (ks + 5) * 1024);
                }
                SBAR();
            }
        }
        STAMP(6);
        lds_wait<8>(lds + C_PROG, 8);
#pragma unroll
        for (int n = 0; n < 3; ++n) *(LDS3 f32x4*)(lds + X_FIN + hw * 3072 + n * 1024 + lane * 16) = c[n];
        lds_wait<4>(lds + C_END, 8);
#pragma unroll
        for (int n = 3; n < 5; ++n) *(LDS3 f32x4*)(lds + 143360 + hw * 2048 + (n - 3) * 1024 + lane * 16) = c[n];
        asm volatile("s_waitcnt lgkmcnt(0)" ::: "memory");
        if (lane == 0) __hip_atomic_store((LDS3 unsigned*)(lds + C_BP + hw * 4), 1u, __ATOMIC_RELAXED, __HIP_MEMORY_SCOPE_WORKGROUP);
        STAMP(7);
        asm volatile("mega_code_end2:");
    } else {
        const int qs = w & 3, kh = w >> 2;
        const int q0 = qb * 256 + qs * 64;
        {
            unsigned sink;
            if (w == 0 && (WARM_MODE & 1)) {
                const char* pc0; int len, len2;
                asm volatile("s_getpc_b64 %0\nmega_code_mark:\n\ts_mov_b32 %1, mega_code_end-mega_code_mark\n\ts_mov_b32 %2, mega_code_end2-mega_code_mark" : "=s"(pc0), "=s"(len), "=s"(len2));
                len = len > len2 ? len : len2;
                const int o = ((bid >> 3) * 64 + lane) * 64;
                if (o + 4 <= len) asm volatile("global_load_dword %0, %1, %2" : "=v"(sink) : "v"(o), "s"(pc0) : "memory");
            } else if (w >= 1 && w <= 6 && (WARM_MODE & 2)) {
                const unsigned o = ((w - 1) * 64 + lane) * 65536 + 65532;
                if (o < WS_END) asm volatile("global_load_dword %0, %1, %2 sc1" : "=v"(sink) : "v"(o), "s"(ws) : "memory");
            } else if (w == 7 && (WARM_MODE & 2)) {
                if (lane < 40) asm volatile("global_load_dword %0, %1, %2 sc1" : "=v"(sink) : "v"(lane * 65536), "s"(P.out) : "memory");
            }
            asm volatile("s_waitcnt vmcnt(0)" ::: "memory");
        }
        GET_TAG();
        if (bid < 240) {
            const int part = nt / 5, c0 = (nt - part * 5) * 128;
            const int wm = w & 1, wn = w >> 1;
            constexpr int RB = 32768;
            const int xr = wm * 64 + r;
            const unsigned xa = RB + xr * 128 + ((h ^ ((xr >> 1) & 7)) << 4);
            unsigned wt[2];
#pragma unroll
            for (int t = 0; t < 2; ++t) {
                const int q = (lane & 15) >> 2, p = lane & 3, blk = (lane >> 4) & 1, row = 8 * h + 4 * t + q;
                wt[t] = RB + 16384 + row * 256 + (((4 * wn + 2 * blk + (p >> 1)) ^ ((q << 2) | (2 * h + t))) << 4) + 8 * (p & 1);
            }
            auto gbody = [&](auto trc) {
                constexpr bool TR = decltype(trc)::value;
                u32x4 xf[2][8], wf[2][4];
                auto rd = [&](auto bc, int s) {
                    constexpr int B = decltype(bc)::value;
                    LDS3 const char* sb = lds + (s & 3) * 32768;
#pragma unroll
                    for (int ks = 0; ks < 4; ++ks) {
                        xf[B][2 * ks] = *(LDS3 const u32x4*)(sb + (xa ^ (ks * 32)));
                        xf[B][2 * ks + 1] = *(LDS3 const u32x4*)(sb + ((xa + 4096) ^ (ks * 32)));
                        const u32x2 lo = trread(sb + wt[0] + ks * 4096), hi = trread(sb + wt[1] + ks * 4096);
                        wf[B][ks] = (u32x4){lo[0], lo[1], hi[0], hi[1]};
                    }
                };
                f32x16 acc0, acc1;
                auto step = [&](auto bc, int s) {
                    constexpr int B = decltype(bc)::value;
                    if (s == 12) DRAIN();
                    __builtin_amdgcn_s_waitcnt(0xC07F);
                    BAR(); SBAR();
                    rd(std::integral_constant<int, B ^ 1>{}, s + 1);
#pragma unroll
                    for (int ks = 0; ks < 4; ++ks) {
                        if constexpr (TR) { acc0 = MFMA32_16(wf[B][ks], xf[B][2 * ks], acc0); acc1 = MFMA32_16(wf[B][ks], xf[B][2 * ks + 1], acc1); }
                        else              { acc0 = MFMA32_16(xf[B][2 * ks], wf[B][ks], acc0); acc1 = MFMA32_16(xf[B][2 * ks + 1], wf[B][ks], acc1); }
                    }
#pragma unroll
                    for (int i = 0; i < 4; ++i) { __builtin_amdgcn_sched_group_barrier(0x8, 1, 0); __builtin_amdgcn_sched_group_barrier(0x2, 2, 0); __builtin_amdgcn_sched_group_barrier(0x100, 4, 0); }
                    __builtin_amdgcn_sched_group_barrier(0x8, 4, 0);
                    SBAR();
                };
                WAIT_BAR(63);
                rd(std::integral_constant<int, 0>{}, 0);
#pragma unroll 1
                for (int mat = 0; mat < 2; ++mat) {
                    acc0 = (f32x16){}; acc1 = (f32x16){};
#pragma unroll 1
                    for (int kk = 0; kk < 10; kk += 2) {
                        step(std::integral_constant<int, 0>{}, mat * 10 + kk);
                        step(std::integral_constant<int, 1>{}, mat * 10 + kk + 1);
                    }
                    const int cpos = 2 * (mt & 7) + (mt >> 3);
                    if constexpr (TR) {
                        const float sc = (part == 0 && mat == 0) ? QSCALE : 1.f;
                        const unsigned base = part == 0 ? WS_Q + mat * NH * 81920 + mt * 5120 : WS_KV + mat * NH * 163840 + cpos * 10240;
                        const unsigned hs = part == 0 ? 81920 : 163840;
#pragma unroll
                        for (int pr = 0; pr < 2; ++pr) {
                            const int G8 = ((c0 + 32 * wn) >> 3) + 2 * pr + h, pp = G8 / 5, gi = G8 - pp * 5;
                            const unsigned hb0 = base + (2 * pp + (gi > 2)) * hs;
#pragma unroll
                            for (int i = 0; i < 2; ++i) {
                                const f32x16& a = i ? acc1 : acc0;
                                const int tl = wm * 64 + i * 32 + r;
                                const auto s0 = __builtin_amdgcn_permlane32_swap(pk2(a[8 * pr] * sc, a[8 * pr + 1] * sc), pk2(a[8 * pr + 4] * sc, a[8 * pr + 5] * sc), false, false);
                                const auto s1 = __builtin_amdgcn_permlane32_swap(pk2(a[8 * pr + 2] * sc, a[8 * pr + 3] * sc), pk2(a[8 * pr + 6] * sc, a[8 * pr + 7] * sc), false, false);
                                if (gi != 2) { const u32x4 ov = {s0[0], s1[0], s0[1], s1[1]}; ST16(ov, hb0 + (tl >> 5) * 1024 + (gi > 2 ? gi - 3 : gi) * 512 + r * 16); }
                                else { const u32x2 o0 = {s0[0], s1[0]}, o1 = {s0[1], s1[1]}; ST8(o0, hb0 + 4096 + tl * 8); ST8(o1, hb0 + hs + 4096 + tl * 8); }
                            }
                        }
                    } else {
                        const int n = c0 + 32 * wn + r, hd = n / 20, d = n - hd * 20, rho = (hd & 1) ? (d < 4 ? d + 16 : d - 4) : d;
                        const unsigned base = WS_KV + (mat * NH + hd) * 163840 + cpos * 10240 + 5120 + h * 320 + rho * 16;
#pragma unroll
                        for (int i = 0; i < 2; ++i)
#pragma unroll
                            for (int s2 = 0; s2 < 2; ++s2) {
                                const f32x16& a = i ? acc1 : acc0;
                                u32x4 ov = {pk2(a[8 * s2], a[8 * s2 + 1]), pk2(a[8 * s2 + 2], a[8 * s2 + 3]), pk2(a[8 * s2 + 4], a[8 * s2 + 5]), pk2(a[8 * s2 + 6], a[8 * s2 + 7])};
                                ST16(ov, base + (wm * 4 + i * 2 + s2) * 640);
                            }
                    }
                    if (mat == 0) STAMP(1);
                }
            };
            if (part < 2) gbody(std::true_type{}); else gbody(std::false_type{});
        } else {
            const int sj = bid - 240;
            WAIT_BAR(63);
            {
                const u32x4 ga = *(const u32x4*)(P.gw + lane * 8), gb = *(const u32x4*)(P.gw + 512 + (lane & 15) * 8);
                u32x4 xa[16], xb[4];
                const u16* xr = P.x + (size_t)(sj * 128 + w * 16) * DM;
#pragma unroll
                for (int rr = 0; rr < 16; ++rr) xa[rr] = *(const u32x4*)(xr + rr * DM + lane * 8);
#pragma unroll
                for (int i = 0; i < 4; ++i) xb[i] = *(const u32x4*)(xr + (i * 4 + (lane >> 4)) * DM + 512 + (lane & 15) * 8);
                float res = 0.f;
#pragma unroll
                for (int rr = 0; rr < 16; ++rr) {
                    float s = 0.f, s2 = 0.f;
#pragma unroll
                    for (int j = 0; j < 4; ++j) { s += bf_lo(xa[rr][j]) * bf_lo(ga[j]) + bf_hi(xa[rr][j]) * bf_hi(ga[j]); s2 += bf_lo(xb[rr >> 2][j]) * bf_lo(gb[j]) + bf_hi(xb[rr >> 2][j]) * bf_hi(gb[j]); }
                    if ((lane >> 4) == (rr & 3)) s += s2;
#pragma unroll
                    for (int o = 32; o >= 1; o >>= 1) s += __shfl_xor(s, o);
                    if (lane == rr) res = s;
                }
                if (lane < 16) ST4(__float_as_uint(1.f / (1.f + __expf(-res))), WS_G + (sj * 128 + w * 16 + lane) * 4);
            }
#pragma unroll 1
            for (int it = 0; it < 2; ++it) {
                unsigned e[4][8];
#pragma unroll
                for (int q = 0; q < 4; ++q) {
                    int item = mtid + 512 * (it * 4 + q); if (item >= 3200) item = 3199;
                    const int u = item / 320, n2 = item - u * 320, k0 = (sj * 10 + u) * 8;
                    const u16* wsrc = (k0 < 640 ? P.woa + (size_t)k0 * DM : P.wob + (size_t)(k0 - 640) * DM) + n2 * 2;
#pragma unroll
                    for (int j = 0; j < 8; ++j) e[q][j] = *(const unsigned*)(wsrc + (size_t)j * DM);
                }
#pragma unroll
                for (int q = 0; q < 4; ++q) {
                    int item = mtid + 512 * (it * 4 + q); if (item >= 3200) item = 3199;
                    const int u = item / 320, n = (item - u * 320) * 2, k0 = (sj * 10 + u) * 8;
                    const unsigned* f = e[q];
                    u32x4 lo = {(f[0] & 0xffffu) | (f[1] << 16), (f[2] & 0xffffu) | (f[3] << 16), (f[4] & 0xffffu) | (f[5] << 16), (f[6] & 0xffffu) | (f[7] << 16)};
                    u32x4 hi = {(f[0] >> 16) | (f[1] & 0xffff0000u), (f[2] >> 16) | (f[3] & 0xffff0000u), (f[4] >> 16) | (f[5] & 0xffff0000u), (f[6] >> 16) | (f[7] & 0xffff0000u)};
                    const unsigned dp = WS_WT + (((n >> 4) * 40 + (k0 >> 5)) * 64 + ((k0 >> 3) & 3) * 16 + (n & 15)) * 16;
                    ST16(lo, dp); ST16(hi, dp + 16);
                }
            }
            DRAIN();
            unsigned arr = 0;
            if (lane == 0) arr = LDS_CNT_ADD(G_ST);
            if (__builtin_amdgcn_readfirstlane(arr) == 7 && lane == 0) set_flag(ws, WS_F_SD + sj * 8, tag);
        }
        STAMP(2);
        const unsigned hb = lds0 + kh * 10240;
        const unsigned laneK = hb + lane * 16, laneT = hb + 4096 + r * 8 + h * 4, laneV = hb + 5120 + h * 320 + (r < 20 ? r : 19) * 16;
        const u64 vmask = 0x000fffff000fffffull;
        u32x4 v0a = {one, one, one, one}, v0b = v0a, v1 = v0a;
        u32x4 qA, qB, ka; unsigned qtA, qtB, kt;
        {
            lds_wait(lds + G_Q, 4);
            LDS3 const char* qp = lds + L_Q + (qs >> 1) * 5120;
            const unsigned om = (qs & 1) * 2048 + lane * 16, ot = 4096 + ((qs & 1) * 64 + r) * 8 + 4 * h;
            qA = *(LDS3 const u32x4*)(qp + om); qB = *(LDS3 const u32x4*)(qp + om + 1024);
            qtA = *(LDS3 const unsigned*)(qp + ot); qtB = *(LDS3 const unsigned*)(qp + ot + 256);
            DSR128(ka, laneK, 0); DSR32(kt, laneT, 0); DSR128M2(v0a, laneV, 0, v1, laneV, 640);
            asm volatile("s_waitcnt lgkmcnt(0)\n\ts_barrier" : "+v"(ka), "+v"(kt), "+v"(v0a), "+v"(v1), "+v"(qA), "+v"(qB), "+v"(qtA), "+v"(qtB) :: "memory");
        }
        STAMP(5);
        float mA, mB; u32x2 qTA, qTB; f32x16 sA, sB;
        {
            const u32x2 t0 = {kt, 0x3f80u}, a0 = {qtA, 0u}, b0 = {qtB, 0u};
            const f32x16 s = MFMA32_16(ka, qA, z16);
            const f32x16 s0 = MFMA32_8(t0, a0, s);
            f32x16 s1 = MFMA32_16(ka, qB, z16); s1 = MFMA32_8(t0, b0, s1);
            float x0 = s0[0], x1 = s1[0];
#pragma unroll
            for (int i = 1; i < 16; ++i) { x0 = fmaxf(x0, s0[i]); x1 = fmaxf(x1, s1[i]); }
            x0 = -xhalf_max(x0); x1 = -xhalf_max(x1);
            const unsigned hh = pk2(x0, x1);
            const unsigned ll = pk2(x0 - bf_lo(hh), x1 - bf_hi(hh));
            mA = -(bf_lo(hh) + bf_lo(ll)); mB = -(bf_hi(hh) + bf_hi(ll));
            const unsigned sel = h ? ll : hh;
            qTA = (u32x2){qtA, sel & 0xffffu}; qTB = (u32x2){qtB, sel >> 16};
            sA = MFMA32_8(t0, qTA, s);
        }
        f32x16 oA = {}, oB = {};
        u32x4 pa, pb = {0u, 0u, 0u, 0u};
#define EX4(S, i, Pv, j) do { Pv[j] = pk2(ex2(S[i]), ex2(S[i + 1])); Pv[j + 1] = pk2(ex2(S[i + 2]), ex2(S[i + 3])); PIN(Pv); SBAR(); } while (0)
#define KT2 ((u32x2){kt, 0x3f80u})
#define TILE(V0C, V0N, aK1, oK1, aT1, oT1, oV1, aV0, oV0) do { \
            sB = MFMA32_16(ka, qB, z16); SBAR(); DSR128(ka, aK1, oK1); EX4(sA, 0, pa, 0); \
            oB = MFMA32_16(v1, pb, oB); SBAR(); DSR128M2(v1, aV, oV1, V0N, aV0, oV0); EX4(sA, 4, pa, 2); \
            sB = MFMA32_8(KT2, qTB, sB); SBAR(); DSR32(kt, aT1, oT1); EX4(sA, 8, pb, 0); \
            oA = MFMA32_16(V0C, pa, oA); SBAR(); EX4(sA, 12, pb, 2); \
            LWAIT(1); sA = MFMA32_16(ka, qA, z16); SBAR(); EX4(sB, 0, pa, 0); \
            oA = MFMA32_16(v1, pb, oA); SBAR(); EX4(sB, 4, pa, 2); \
            LWAIT(0); sA = MFMA32_8(KT2, qTA, sA); SBAR(); EX4(sB, 8, pb, 0); \
            oB = MFMA32_16(V0C, pa, oB); SBAR(); EX4(sB, 12, pb, 2); \
        } while (0)
        unsigned aK = laneK, aT = laneT, aV = laneV;
#pragma unroll 1
        for (int c = 0; c < 8; ++c) {
            if (c == 1) { WAIT_BAR(0); }
            else if (c == 7) { if (lane == 0) LDS_CNT_ADD(C_PROG); asm volatile("s_waitcnt lgkmcnt(0)" ::: "memory"); }
            if (c >= 1) {
                unsigned pp;
                asm volatile("ds_write_b32 %1, %2\n\tds_read_b32 %0, %3\n\ts_waitcnt lgkmcnt(0)" : "=&v"(pp) : "v"(lds0 + C_PR + w * 4), "v"(c), "v"(lds0 + C_PR + (w ^ 4) * 4) : "memory");
                const int pc = __builtin_amdgcn_readfirstlane(pp);
                if (pc > c || (pc == c && ((c ^ kh) & 1))) __builtin_amdgcn_s_setprio(1); else __builtin_amdgcn_s_setprio(0);
            } else if (kh) __builtin_amdgcn_s_setprio(1);
            const unsigned nx = (c < 7 ? c + 1 : 7) * 20480;
            const unsigned nK = laneK + nx, nT = laneT + nx, nV = laneV + nx;
            SBAR();
            TILE(v0a, v0b, aK, 1024, aT, 256, 640,  aV, 1280);
            TILE(v0b, v0a, aK, 2048, aT, 512, 1920, aV, 2560);
            TILE(v0a, v0b, aK, 3072, aT, 768, 3200, aV, 3840);
            TILE(v0b, v0a, nK, 0,    nT, 0,   4480, nV, 0);
            aK = nK; aT = nT; aV = nV;
        }
        oB = MFMA32_16(v1, pb, oB);
#undef TILE
#undef EX4
        STAMP(6);
        {
            float chk = 0.f;
#pragma unroll
            for (int i = 0; i < 12; ++i) chk += fabsf(oA[i]) * 0.f + fabsf(oB[i]) * 0.f;
            bool bad = !(chk == 0.f);
            if (h) bad = bad || !(oA[8] >= 0x1p-60f && oA[8] <= 0x1p60f && oB[8] >= 0x1p-60f && oB[8] <= 0x1p60f);
            if (__builtin_expect(__any(bad), 0)) {
                mA = -INFINITY; mB = -INFINITY;
#pragma unroll
                for (int i = 0; i < 16; ++i) { oA[i] = 0.f; oB[i] = 0.f; }
                const u32x2 a0 = {qtA, 0u}, b0 = {qtB, 0u};
#pragma unroll 1
                for (int t = 0; t < 32; ++t) {
                    const unsigned kb = WS_KV + head * 163840 + (2 * (t >> 2) + kh) * 10240, t4 = t & 3;
                    const u32x4 k0 = LD16(kb + t4 * 1024 + lane * 16);
                    const u32x2 t0 = {LD4(kb + 4096 + (t4 * 32 + r) * 8 + 4 * h), 0u};
                    u32x4 w0 = LD16(kb + 5120 + t4 * 1280 + h * 320 + (r < 20 ? r : 19) * 16), w1 = LD16(kb + 5120 + t4 * 1280 + 640 + h * 320 + (r < 20 ? r : 19) * 16);
                    if (r >= 20) { w0 = (u32x4){one, one, one, one}; w1 = w0; }
                    f32x16 s0 = MFMA32_16(k0, qA, z16); s0 = MFMA32_8(t0, a0, s0);
                    f32x16 s1 = MFMA32_16(k0, qB, z16); s1 = MFMA32_8(t0, b0, s1);
                    float x0 = s0[0], x1 = s1[0];
#pragma unroll
                    for (int i = 1; i < 16; ++i) { x0 = fmaxf(x0, s0[i]); x1 = fmaxf(x1, s1[i]); }
                    x0 = fmaxf(xhalf_max(x0), mA); x1 = fmaxf(xhalf_max(x1), mB);
                    const float g0 = (mA == x0) ? 1.f : ex2(mA - x0), g1 = (mB == x1) ? 1.f : ex2(mB - x1);
                    mA = x0; mB = x1;
#pragma unroll
                    for (int i = 0; i < 16; ++i) { oA[i] *= g0; oB[i] *= g1; }
#pragma unroll
                    for (int i = 0; i < 4; ++i) { pa[i] = pk2(ex2(s0[2 * i] - x0), ex2(s0[2 * i + 1] - x0)); pb[i] = pk2(ex2(s0[8 + 2 * i] - x0), ex2(s0[9 + 2 * i] - x0)); }
                    oA = MFMA32_16(w0, pa, oA); oA = MFMA32_16(w1, pb, oA);
#pragma unroll
                    for (int i = 0; i < 4; ++i) { pa[i] = pk2(ex2(s1[2 * i] - x1), ex2(s1[2 * i + 1] - x1)); pb[i] = pk2(ex2(s1[8 + 2 * i] - x1), ex2(s1[9 + 2 * i] - x1)); }
                    oB = MFMA32_16(w0, pa, oB); oB = MFMA32_16(w1, pb, oB);
                }
            }
        }
        unsigned fin = 0;
        if (lane == 0) fin = LDS_CNT_ADD(C_END);
        fin = __builtin_amdgcn_readfirstlane(fin);
        lds_wait<4>(lds + C_OB, 4);
        lds_wait<4>(lds + C_PROG, 8);
        const float gO = *(LDS3 const float*)(lds + X_G + (qs * 64 + kh * 32 + r) * 4);
        {
            LDS3 char* xp = lds + X_MRG + w * 2816;
            const f32x16& s = kh ? oA : oB;
            *(LDS3 f32x4*)(xp + lane * 16) = (f32x4){s[0], s[1], s[2], s[3]};
            *(LDS3 f32x4*)(xp + 1024 + lane * 16) = (f32x4){s[4], s[5], s[6], s[7]};
            if (!h) *(LDS3 f32x4*)(xp + 2048 + r * 16) = (f32x4){s[8], s[9], s[10], s[11]};
            else *(LDS3 f32x2*)(xp + 2560 + r * 8) = (f32x2){s[8], kh ? mA : mB};
            asm volatile("s_waitcnt lgkmcnt(0)" ::: "memory");
            if (lane == 0) __hip_atomic_store((LDS3 unsigned*)(lds + C_MRG + w * 4), 1u, __ATOMIC_RELAXED, __HIP_MEMORY_SCOPE_WORKGROUP);
        }
        if (fin < 4) {
            const char* wsrc = ws + WS_WT + (size_t)ntO * 5 * 40960;
#pragma unroll 1
            for (int n = 0; n < 5; ++n)
#pragma unroll
                for (int t = 0; t < 5; ++t) { const int ks = fin * 5 + t; glds16s(lane * 16, wsrc + (n * 40 + ks) * 1024, lds0 + L_WA + (n * 20 + ks) * 1024); }
        }
        lds_wait<2>(lds + C_MRG + (w ^ 4) * 4, 1);
        bool lastw;
        {
            LDS3 const char* xp = lds + X_MRG + (w ^ 4) * 2816;
            const f32x4 p0 = *(LDS3 const f32x4*)(xp + lane * 16), p1 = *(LDS3 const f32x4*)(xp + 1024 + lane * 16), p2 = *(LDS3 const f32x4*)(xp + 2048 + r * 16);
            const f32x2 lm = *(LDS3 const f32x2*)(xp + 2560 + r * 8);
            const f32x16& s = kh ? oB : oA;
            const float mo = kh ? mB : mA, mp = lm[1];
            const float mx = fmaxf(mo, mp);
            const float fo = (mo == mx) ? 1.f : ex2(mo - mx), fq = (mp == mx) ? 1.f : ex2(mp - mx);
            float o[12];
#pragma unroll
            for (int i = 0; i < 4; ++i) { o[i] = s[i] * fo + p0[i] * fq; o[4 + i] = s[4 + i] * fo + p1[i] * fq; o[8 + i] = s[8 + i] * fo + p2[i] * fq; }
            const float l = upper_half(s[8]) * fo + lm[0] * fq;
            store_o(rs, o, gO / l, q0 + kh * 32 + r, head * 20, h);
            DRAIN();
            unsigned arr = 0;
            if (lane == 0) arr = LDS_CNT_ADD(C_OA);
            lastw = __builtin_amdgcn_readfirstlane(arr) == 7;
            if (lastw && lane == 0) set_flag(ws, WS_F_OA + (qb * 32 + head) * 8, tag);
        }
        STAMP(7);
        {
            const int c16 = lane & 15, g = lane >> 4, tt = w & 3, k0 = kh * 10;
            POLL_ONE(lastw, 4, WS_F_OA + ((mtO >> 2) * 32 + r) * 8, WS_F_OA + ((mtO >> 2) * 32 + r) * 8, C_P4);
            STAMP(9);
            const unsigned ab = WS_AO + ((mtO * 4 + tt) * 40 + k0) * 1024 + lane * 16;
            u32x4 af[10];
#pragma unroll
            for (int ks = 0; ks < 10; ++ks) af[ks] = LD16(ab + ks * 1024);
            lds_wait(lds + C_OA, 8);
            f32x4 c[5];
#pragma unroll
            for (int n = 0; n < 5; ++n) c[n] = (f32x4){0.f, 0.f, 0.f, 0.f};
#pragma unroll
            for (int ks = 0; ks < 10; ++ks)
#pragma unroll
                for (int n = 0; n < 5; ++n) c[n] = MFMA16_32(*(LDS3 const u32x4*)(lds + L_WA + (n * 20 + k0 + ks) * 1024 + lane * 16), af[ks], c[n]);
            LDS3 char* xp = lds + X_MRG + tt * 5120 + lane * 16;
            if (w < 4) { *(LDS3 f32x4*)(xp + 3072) = c[3]; *(LDS3 f32x4*)(xp + 4096) = c[4]; }
            else { *(LDS3 f32x4*)(xp) = c[0]; *(LDS3 f32x4*)(xp + 1024) = c[1]; *(LDS3 f32x4*)(xp + 2048) = c[2]; }
            asm volatile("s_waitcnt lgkmcnt(0)" ::: "memory");
            if (lane == 0) __hip_atomic_store((LDS3 unsigned*)(lds + C_FIN + w * 4), 1u, __ATOMIC_RELAXED, __HIP_MEMORY_SCOPE_WORKGROUP);
            lds_wait(lds + C_FIN + (w ^ 4) * 4, 1);
            lds_wait(lds + C_BP + tt * 4, 1);
            u16* op = P.out + (size_t)(mtO * 64 + tt * 16 + c16) * DM + ntO * 80 + g * 4;
#pragma unroll
            for (int n = 0; n < 5; ++n) {
                if ((w < 4) == (n < 3)) {
                    const f32x4 bp = *(LDS3 const f32x4*)(n < 3 ? lds + X_FIN + tt * 3072 + n * 1024 + lane * 16 : lds + 143360 + tt * 2048 + (n - 3) * 1024 + lane * 16);
                    const f32x4 s = c[n] + *(LDS3 const f32x4*)(xp + n * 1024) + bp;
                    *(u32x2*)(op + n * 16) = (u32x2){pk2(s[0], s[1]), pk2(s[2], s[3])};
                }
            }
            if (bid == 0 && mtid == 0) set_flag(ws, WS_EPOCH, tag);
        }
        STAMP(10);
        asm volatile("mega_code_end:");
    }
}

extern "C" void kernel_launch(void* const* d_in, const int* in_sizes, int n_in, void* d_out, int out_size, void* d_ws, size_t ws_size,
                              hipStream_t stream) {
    Params P;
    P.x = (const u16*)d_in[0]; P.wa = (const u16*)d_in[1]; P.woa = (const u16*)d_in[2]; P.wb = (const u16*)d_in[4];
    P.wphi = (const u16*)d_in[5]; P.wob = (const u16*)d_in[6]; P.gw = (const u16*)d_in[7]; P.ws = (char*)d_ws; P.out = (u16*)d_out;
    mega<<<256, 768, 0, stream>>>(P);
}
```

```cpp
#include <hip/hip_runtime.h>
#include <hip/hip_bf16.h>
#include <stdint.h>
#include <type_traits>

typedef __attribute__((ext_vector_type(8))) short bf16x8;
typedef __attribute__((ext_vector_type(4))) short bf16x4;
typedef __attribute__((ext_vector_type(16))) float f32x16;
typedef __attribute__((ext_vector_type(4))) float f32x4;
typedef __attribute__((ext_vector_type(2))) float f32x2;
typedef __attribute__((ext_vector_type(4))) unsigned u32x4;
typedef __attribute__((ext_vector_type(2))) unsigned u32x2;
typedef __attribute__((ext_vector_type(2))) __bf16 bf2_t;
typedef unsigned short u16;
typedef unsigned long long u64;
#define LDS3 __attribute__((address_space(3)))
typedef __attribute__((address_space(1))) u64 gu64;
#define RLX_AGENT __ATOMIC_RELAXED, __HIP_MEMORY_SCOPE_AGENT

#define PRIO_MODE 3
#define WARM_MODE 1
#define PRE_SLEEP 2
constexpr int SEQ = 2048, DM = 640, NH = 32;
constexpr float LOG2E = 1.4426950408889634f;
constexpr float QSCALE = 0.22360679774997896f * LOG2E;

constexpr unsigned WS_EPOCH = 0;
constexpr unsigned WS_F_G   = 256;
constexpr unsigned WS_F_SD  = WS_F_G + 4096;
constexpr unsigned WS_F_KV  = WS_F_SD + 256;
constexpr unsigned WS_F_OB  = WS_F_KV + 2048;
constexpr unsigned WS_F_OA  = WS_F_OB + 2048;
constexpr unsigned WS_G     = 16384;
constexpr unsigned WS_Q     = 32768;
constexpr unsigned WS_KV    = WS_Q + 2 * NH * 81920;
constexpr unsigned WS_KVP   = WS_KV + 2 * NH * 163840;
constexpr unsigned WS_AO    = WS_KVP + NH * 8 * 4096;
constexpr unsigned WS_WT    = WS_AO + SEQ * 1280 * 2;
constexpr unsigned WS_END   = WS_WT + 1280 * 640 * 2;

#define STAMP(i)

__device__ __forceinline__ unsigned pk2(float a, float b) { f32x2 v = {a, b}; bf2_t r = __builtin_convertvector(v, bf2_t); return __builtin_bit_cast(unsigned, r); }
__device__ __forceinline__ float bf_lo(unsigned u) { return __uint_as_float(u << 16); }
__device__ __forceinline__ float bf_hi(unsigned u) { return __uint_as_float(u & 0xffff0000u); }
__device__ __forceinline__ float ex2(float x) { return __builtin_amdgcn_exp2f(x); }
#define MFMA32_16(a, b, c) __builtin_amdgcn_mfma_f32_32x32x16_bf16(__builtin_bit_cast(bf16x8, a), __builtin_bit_cast(bf16x8, b), c, 0, 0, 0)
#define MFMA32_8(a, b, c) __builtin_amdgcn_mfma_f32_32x32x8bf16_1k(__builtin_bit_cast(bf16x4, a), __builtin_bit_cast(bf16x4, b), c, 0, 0, 0)
#define MFMA16_32(a, b, c) __builtin_amdgcn_mfma_f32_16x16x32_bf16(__builtin_bit_cast(bf16x8, a), __builtin_bit_cast(bf16x8, b), c, 0, 0, 0)

__device__ __forceinline__ void glds16(unsigned voff, const void* sbase, unsigned lds_base) {
    unsigned sv;
    asm volatile("s_mov_b32 %0, m0\n\ts_mov_b32 m0, %3\n\ts_nop 0\n\tglobal_load_lds_dwordx4 %1, %2\n\ts_mov_b32 m0, %0"
                 : "=&s"(sv) : "v"(voff), "s"(sbase), "s"(__builtin_amdgcn_readfirstlane(lds_base)) : "memory");
}
__device__ __forceinline__ void glds16s(unsigned voff, const void* sbase, unsigned lds_base) {
    unsigned sv;
    asm volatile("s_mov_b32 %0, m0\n\ts_mov_b32 m0, %3\n\ts_nop 0\n\tglobal_load_lds_dwordx4 %1, %2 sc1\n\ts_mov_b32 m0, %0"
                 : "=&s"(sv) : "v"(voff), "s"(sbase), "s"(__builtin_amdgcn_readfirstlane(lds_base)) : "memory");
}
__device__ __forceinline__ void glds4s(unsigned voff, const void* sbase, unsigned lds_base) {
    unsigned sv;
    asm volatile("s_mov_b32 %0, m0\n\ts_mov_b32 m0, %3\n\ts_nop 0\n\tglobal_load_lds_dword %1, %2 sc1\n\ts_mov_b32 m0, %0"
                 : "=&s"(sv) : "v"(voff), "s"(sbase), "s"(__builtin_amdgcn_readfirstlane(lds_base)) : "memory");
}
typedef short v4i16_t __attribute__((ext_vector_type(4)));
__device__ __forceinline__ u32x2 trread(LDS3 const char* p) {
    return __builtin_bit_cast(u32x2, __builtin_amdgcn_ds_read_tr16_b64_v4i16((LDS3 v4i16_t*)p));
}
#define WAIT_BAR(N) asm volatile("s_waitcnt vmcnt(" #N ") lgkmcnt(0)\n\ts_barrier" ::: "memory")
#define DRAIN() asm volatile("s_waitcnt vmcnt(0)" ::: "memory")
#define SBAR() __builtin_amdgcn_sched_barrier(0)
#define PIN(x) asm volatile("" : "+v"(x))

__device__ __forceinline__ float xhalf_max(float x) {
    auto r = __builtin_amdgcn_permlane32_swap(__float_as_uint(x), __float_as_uint(x), false, false);
    return fmaxf(__uint_as_float(r[0]), __uint_as_float(r[1]));
}
__device__ __forceinline__ float xhalf_sum(float x) {
    auto r = __builtin_amdgcn_permlane32_swap(__float_as_uint(x), __float_as_uint(x), false, false);
    return __uint_as_float(r[0]) + __uint_as_float(r[1]);
}
__device__ __forceinline__ float upper_half(float x) {
    auto r = __builtin_amdgcn_permlane32_swap(__float_as_uint(x), __float_as_uint(x), false, false);
    return __uint_as_float(r[1]);
}

typedef __amdgpu_buffer_rsrc_t rsrc_t;
#define ST8(v, off)  __builtin_amdgcn_raw_buffer_store_b64(v, rs, (int)(off), 0, 16)
#define ST16(v, off) __builtin_amdgcn_raw_buffer_store_b128(v, rs, (int)(off), 0, 16)
#define ST4(v, off)  __builtin_amdgcn_raw_buffer_store_b32(v, rs, (int)(off), 0, 16)
#define LD16(off) __builtin_amdgcn_raw_buffer_load_b128(rs, (int)(off), 0, 16)
#define LD4(off)  __builtin_amdgcn_raw_buffer_load_b32(rs, (int)(off), 0, 16)

template <int SLP = 2>
__device__ __forceinline__ void wait_flags(const char* ws, unsigned off0, unsigned off1, u64 tag) {
    gu64* p0 = (gu64*)(ws + off0); gu64* p1 = (gu64*)(ws + off1);
#pragma unroll 1
    for (unsigned spins = 0; spins < (1u << 16); ++spins) {
        const u64 a = __hip_atomic_load(p0, RLX_AGENT), b = __hip_atomic_load(p1, RLX_AGENT);
        if (__all(a == tag && b == tag)) break;
        __builtin_amdgcn_s_sleep(SLP);
    }
    __builtin_amdgcn_fence(__ATOMIC_ACQUIRE, "wavefront");
}
__device__ __forceinline__ void set_flag(char* ws, unsigned off, u64 tag) { __hip_atomic_store((gu64*)(ws + off), tag, RLX_AGENT); }

struct Params { const u16* x; const u16* wa; const u16* wb; const u16* wphi; const u16* woa; const u16* wob; const u16* gw; char* ws; u16* out; };

__device__ __forceinline__ void feature_map(const u32x4& pm, unsigned pt, const u32x4& tm, unsigned tt, int h, u32x4& fp, u32x4& fn, u32x4& ft) {
    const f32x16 z16 = {};
    const u32x2 a8 = {pt, 0u}, b8 = {tt, 0u};
    f32x16 s = MFMA32_16(pm, tm, z16); s = MFMA32_8(a8, b8, s);
    float v[12];
#pragma unroll
    for (int i = 0; i < 12; i += 2) { const unsigned u = pk2(s[i], s[i + 1]); v[i] = bf_lo(u) * LOG2E; v[i + 1] = bf_hi(u) * LOG2E; }
    float mx = v[0], mn = v[0];
#pragma unroll
    for (int i = 1; i < 8; ++i) { mx = fmaxf(mx, v[i]); mn = fminf(mn, v[i]); }
    const float tx = fmaxf(fmaxf(v[8], v[9]), fmaxf(v[10], v[11])), tn = fminf(fminf(v[8], v[9]), fminf(v[10], v[11]));
    if (!h) { mx = fmaxf(mx, tx); mn = fminf(mn, tn); }
    mx = xhalf_max(mx); mn = -xhalf_max(-mn);
    float ep[12], en[12], sp = 0.f, sn = 0.f;
#pragma unroll
    for (int i = 0; i < 12; ++i) {
        ep[i] = ex2(v[i] - mx); en[i] = ex2(mn - v[i]);
        if (i >= 8 && h) { ep[i] = 0.f; en[i] = 0.f; }
        sp += ep[i]; sn += en[i];
    }
    sp = __builtin_amdgcn_rcpf(xhalf_sum(sp)); sn = __builtin_amdgcn_rcpf(xhalf_sum(sn));
#pragma unroll
    for (int i = 0; i < 4; ++i) { fp[i] = pk2(ep[2 * i] * sp, ep[2 * i + 1] * sp); fn[i] = pk2(en[2 * i] * sn, en[2 * i + 1] * sn); }
    ft = (u32x4){pk2(ep[8] * sp, ep[9] * sp), pk2(ep[10] * sp, ep[11] * sp), pk2(en[8] * sn, en[9] * sn), pk2(en[10] * sn, en[11] * sn)};
}

__device__ __forceinline__ void store_o(rsrc_t rs, const float (&o)[12], float sc, int tok, int kbase, int h) {
#pragma unroll
    for (int gq = 0; gq < 3; ++gq) {
        const int d = 8 * gq + 4 * h;
        if (d < 20) {
            const int k = kbase + ((kbase & 4) ? (d == 16 ? 0 : d + 4) : d);
            u32x2 ov = {pk2(o[4 * gq] * sc, o[4 * gq + 1] * sc), pk2(o[4 * gq + 2] * sc, o[4 * gq + 3] * sc)};
            ST8(ov, WS_AO + (((tok >> 4) * 40 + (k >> 5)) * 64 + ((k >> 3) & 3) * 16 + (tok & 15)) * 16 + (k & 7) * 2);
        }
    }
}

#define DSR128(dst, addr, off) asm volatile("ds_read_b128 %0, %1 offset:%2" : "=v"(dst) : "v"(addr), "i"(off))
#define DSR32(dst, addr, off) asm volatile("ds_read_b32 %0, %1 offset:%2" : "=v"(dst) : "v"(addr), "i"(off))
#define DSR128M2(d0, a0, o0, d1, a1, o1) asm volatile("s_mov_b64 exec, %6\n\tds_read_b128 %0, %2 offset:%3\n\tds_read_b128 %1, %4 offset:%5\n\ts_mov_b64 exec, -1" \
                                                      : "+v"(d0), "+v"(d1) : "v"(a0), "i"(o0), "v"(a1), "i"(o1), "s"(vmask))
#define LWAIT(n) do { asm volatile("s_waitcnt lgkmcnt(" #n ")" ::: "memory"); SBAR(); } while (0)

#define BAR() asm volatile("s_barrier" ::: "memory")
#define LDS_CNT_ADD(off) __hip_atomic_fetch_add((LDS3 unsigned*)(lds + (off)), 1u, __ATOMIC_RELAXED, __HIP_MEMORY_SCOPE_WORKGROUP)

template <int SLP = 1>
__device__ __forceinline__ void lds_wait(LDS3 char* p, unsigned want) {
#pragma unroll 1
    for (unsigned spins = 0; spins < (1u << 20); ++spins) {
        if ((unsigned)__builtin_amdgcn_readfirstlane(__hip_atomic_load((LDS3 unsigned*)p, __ATOMIC_RELAXED, __HIP_MEMORY_SCOPE_WORKGROUP)) >= want) break;
        __builtin_amdgcn_s_sleep(SLP);
    }
    __builtin_amdgcn_fence(__ATOMIC_ACQUIRE, "wavefront");
}

#define POLL_ONE(cond, SLP, o0, o1, word) do { if (cond) { wait_flags<SLP>(ws, o0, o1, tag); \
        if (lane == 0) __hip_atomic_store((LDS3 unsigned*)(lds + (word)), 1u, __ATOMIC_RELAXED, __HIP_MEMORY_SCOPE_WORKGROUP); } else lds_wait<SLP>(lds + (word), 1); } while (0)
constexpr int C_KV = 0, C_KVF = 4, C_OB = 8, C_PROG = 12, C_WA = 16, C_OA = 20, C_MRG = 32  , C_FIN = 64  , C_BP = 96  ;
constexpr int C_PROG2 = 24, C_BX = 28, C_P1 = 112, C_P2 = 116, C_P3 = 120, C_P4 = 124, C_PR = 128  ;
constexpr int G_W = 31744;
constexpr int G_FULL = G_W, G_FREE = G_W + 16, G_Q = G_W + 32, G_KV0 = G_W + 36, G_ST = G_W + 40;
constexpr int C_END = 160, X_G = 256;
constexpr int X_KVF = 1280, X_SLOT = 4096;
constexpr int X_MRG = 1280;
constexpr int L_WA = 24576;
constexpr int L_Q = 65536;
constexpr int X_FIN = 126976;

__global__ __launch_bounds__(768) void mega(Params P) {
    __shared__ __attribute__((aligned(16))) char smem[163840];
    LDS3 char* lds = (LDS3 char*)smem;
    char* ws = P.ws;
    const int tid = threadIdx.x, lane = tid & 63, r = lane & 31, h = lane >> 5;
    const int hw = __builtin_amdgcn_readfirstlane(tid >> 6);
    const bool helper = hw < 4;
    const int w = hw - 4, mtid = tid - 256;
    const int bid = blockIdx.x;
    const unsigned lds0 = (unsigned)(uintptr_t)smem;
    const rsrc_t rs = __builtin_amdgcn_make_buffer_rsrc(ws, 0, WS_END, 0x00020000);
    STAMP(0);
    u64 tag = 0;
#define GET_TAG() do { asm volatile("s_load_dwordx2 %0, %1, 0x0\n\ts_waitcnt lgkmcnt(0)" : "=s"(tag) : "s"(ws) : "memory"); tag += 1; } while (0)
    const int head = (bid & 7) * 4 + ((bid >> 3) & 3), qb = bid >> 5;
    const int nlo = (head * 20) >> 7, nhi = (head * 20 + 19) >> 7;
    const int mtO = (bid & 7) * 4 + ((bid >> 3) & 3), ntO = bid >> 5;
    const int lin = (bid & 7) * 30 + (bid >> 3), lh = lin >= 120, lrem = lin - 120 * lh;
    const int mt = lh * 8 + (lrem & 7), nt = lrem >> 3;
    const unsigned fG = WS_F_G + (mt * 15 + nt) * 8;
    const unsigned one = (r == 20) ? 0x3f803f80u : 0u;
    const f32x16 z16 = {};

    if (helper) {
        const unsigned fA0 = WS_F_G + ((lane & 15) * 15 + (lane < 32 ? 5 : 10) + ((lane & 16) ? nhi : nlo)) * 8;
        const unsigned fA1 = WS_F_G + ((qb * 2 + (lane & 1)) * 15 + ((lane & 2) ? nhi : nlo)) * 8;
        __builtin_amdgcn_s_setprio(3);
        const char* kvb = ws + WS_KV + head * 163840;
        auto kv_issue = [&](int grp) {
#pragma unroll
            for (int t = 0; t < 8; ++t) { int pc = grp * 32 + hw + 4 * t; if (pc == 31) pc = 30; glds16s(lane * 16, kvb + pc * 1024, lds0 + pc * 1024); }
        };
        auto q_issue = [&]() {
            const char* qbase = ws + WS_Q + head * 81920 + qb * 10240;
#pragma unroll
            for (int t = 0; t < 3; ++t) { const int pc = hw + 4 * t < 10 ? hw + 4 * t : 9; glds16s(lane * 16, qbase + pc * 1024, lds0 + L_Q + pc * 1024); }
        };
        if (bid < 240) {
            const int rot = (nt * 3 + mt * 5 + (mt >> 1)) % 10;
            const unsigned xo = (mt * 128 + 8 * hw + (lane >> 3)) * 1280 + (((lane & 7) ^ ((4 * hw + (lane >> 4)) & 7)) << 4);
            const unsigned wo = (4 * hw + (lane >> 4)) * 3840 + nt * 256 + (((lane & 15) ^ (((lane >> 4) << 2) | hw)) << 4);
            const unsigned dX = lds0 + 32768 + hw * 1024;
            auto issue = [&](int s) {
                int kt = (s >= 10 ? s - 10 : s) + rot; if (kt >= 10) kt -= 10;
                const char* xb = (const char*)P.x + kt * 128;
                const char* wb = (const char*)(s >= 10 ? P.wb : P.wa) + (size_t)kt * (64 * 3840);
                const unsigned d = dX + (s & 3) * 32768;
#pragma unroll
                for (int i = 0; i < 4; ++i) { glds16(xo, xb + i * 40960, d + i * 4096); glds16(wo, wb + i * 61440, d + 16384 + i * 4096); }
            };
#pragma unroll 1
            for (int n = 0; n < 20; ++n) {
                if (n == 2) { if (hw == 0 && lane < 16) *(LDS3 unsigned*)(lds + G_W + lane * 4) = 0u; GET_TAG(); WAIT_BAR(8); }
                else if (n == 3) { WAIT_BAR(8); }
                else if (n >= 5) { WAIT_BAR(16); }
                if (n == 16 && tid == 0) set_flag(ws, fG, tag);
                if (n == 19 && hw == 0) {
                    glds4s(fA0, ws, lds0); glds4s(fA0 + 4, ws, lds0 + 256); glds4s(fA1, ws, lds0 + 512); glds4s(fA1 + 4, ws, lds0 + 768);
                }
                issue(n);
            }
            WAIT_BAR(16);
            asm volatile("s_waitcnt vmcnt(8)" ::: "memory");
            if (hw == 0) {
                LDS3 const unsigned* f = (LDS3 const unsigned*)lds + lane;
                const unsigned tl = (unsigned)tag, th = (unsigned)(tag >> 32);
                if (!__all(f[0] == tl && f[64] == th && f[128] == tl && f[192] == th)) wait_flags<8>(ws, fA0, fA1, tag);
            }
            WAIT_BAR(63);
            kv_issue(0); q_issue();
            WAIT_BAR(11);
            kv_issue(1);
            WAIT_BAR(63);
        } else {
            if (hw == 0 && lane < 16) *(LDS3 unsigned*)(lds + G_W + lane * 4) = 0u;
            GET_TAG();
            WAIT_BAR(63);
            if (hw == 0) {
#pragma unroll 1
                for (int t = 0; t < PRE_SLEEP; ++t) __builtin_amdgcn_s_sleep(127);
                wait_flags<16>(ws, fA0, fA1, tag);
                if (lane == 0) LDS_CNT_ADD(G_FREE);
            }
            lds_wait<16>(lds + G_FREE, 1);
            kv_issue(0); q_issue(); kv_issue(1);
        }
        asm volatile("s_waitcnt vmcnt(6)" ::: "memory");
        if (lane == 0) LDS_CNT_ADD(G_Q);
        kv_issue(3); kv_issue(4);
        WAIT_BAR(63);
        if (hw == 3) glds16s(lane * 16, kvb + 31 * 1024, lds0 + 31 * 1024);
        kv_issue(2);
        if (hw == 0) *(LDS3 u32x4*)(lds + lane * 16) = (u32x4){0u, 0u, 0u, 0u};
        u32x4 phm; unsigned pht;
        {
            const u16* wp = P.wphi + head * 400 + (r < 20 ? r : 0);
            unsigned e[10];
#pragma unroll
            for (int j = 0; j < 8; ++j) e[j] = wp[(8 * h + j + 4 * (head & 1)) * 20];
            e[8] = wp[((head & 1 ? 0 : 16) + 2 * h) * 20]; e[9] = wp[((head & 1 ? 1 : 17) + 2 * h) * 20];
            phm = (u32x4){e[0] | (e[1] << 16), e[2] | (e[3] << 16), e[4] | (e[5] << 16), e[6] | (e[7] << 16)}; pht = e[8] | (e[9] << 16);
            if (r >= 20) { phm = (u32x4){0u, 0u, 0u, 0u}; pht = 0u; }
        }
        WAIT_BAR(0);
        if (tid == 0 && bid < 240) set_flag(ws, fG + 2048, tag);
        STAMP(3);
        if (hw * 64 + lane < 144) *(LDS3 u32x4*)(lds + X_KVF + (hw * 64 + lane) * 16) = (u32x4){0u, 0u, 0u, 0u};
        if (bid < 160 && tid < 160) {
            const int k0 = bid * 8, n = tid * 4;
            const u16* wsrc = (k0 < 640 ? P.woa + (size_t)k0 * DM : P.wob + (size_t)(k0 - 640) * DM) + n;
            u32x2 e[8];
#pragma unroll
            for (int j = 0; j < 8; ++j) e[j] = *(const u32x2*)(wsrc + (size_t)j * DM);
            const unsigned dp = WS_WT + (((n >> 4) * 40 + (k0 >> 5)) * 64 + ((k0 >> 3) & 3) * 16 + (n & 15)) * 16;
#pragma unroll
            for (int q = 0; q < 2; ++q) {
                const u32x4 lo = {(e[0][q] & 0xffffu) | (e[1][q] << 16), (e[2][q] & 0xffffu) | (e[3][q] << 16), (e[4][q] & 0xffffu) | (e[5][q] << 16), (e[6][q] & 0xffffu) | (e[7][q] << 16)};
                const u32x4 hi = {(e[0][q] >> 16) | (e[1][q] & 0xffff0000u), (e[2][q] >> 16) | (e[3][q] & 0xffff0000u), (e[4][q] >> 16) | (e[5][q] & 0xffff0000u), (e[6][q] >> 16) | (e[7][q] & 0xffff0000u)};
                ST16(lo, dp + q * 32); ST16(hi, dp + q * 32 + 16);
            }
        }
        const int tokS = qb * 256 + hw * 64, m7 = tokS >> 7, t4 = (tokS >> 5) & 3;
        {
            const unsigned pt3 = (lane >> 1) & 3;
            const unsigned tb = WS_F_G + 2048 + ((qb * 2 + ((lane >> 3) & 1)) * 15 + ((lane & 1) ? nhi : nlo) + (pt3 < 3 ? pt3 : 0) * 5) * 8;
            POLL_ONE(hw == 0, 8, tb, WS_F_SD + (qb * 2 + (lane & 1)) * 8, C_P1);
        }
        const unsigned om = t4 * 1024 + lane * 16, ot = 4096 + (t4 * 32 + r) * 8 + 4 * h;
        u32x4 qf[2][3]; float gS[2];
        {
            const unsigned kb = WS_KV + (NH + head) * 163840 + (2 * (m7 & 7) + (m7 >> 3)) * 10240, qbb = WS_Q + (NH + head) * 81920 + m7 * 5120;
            const unsigned ov = 5120 + t4 * 1280 + h * 320 + (r < 20 ? r : 19) * 16;
            u32x4 e0[2], w0[2], w1[2], eq[2]; unsigned et[2], eqt[2];
#pragma unroll
            for (int i = 0; i < 2; ++i) {
                e0[i] = LD16(kb + om + i * 1024); et[i] = LD4(kb + ot + i * 256); w0[i] = LD16(kb + ov + i * 1280); w1[i] = LD16(kb + ov + i * 1280 + 640);
                eq[i] = LD16(qbb + om + i * 1024); eqt[i] = LD4(qbb + ot + i * 256); gS[i] = __uint_as_float(LD4(WS_G + (tokS + i * 32 + r) * 4));
            }
            *(LDS3 float*)(lds + X_G + (hw * 64 + lane) * 4) = h ? gS[1] : gS[0];
            u32x4 i0, i1;
#pragma unroll
            for (int j = 0; j < 4; ++j) {
                const int e = r - 8 * h - 2 * j;
                i0[j] = e == 0 ? 0x3f80u : (e == 1 ? 0x3f800000u : 0u);
                i1[j] = e == 16 ? 0x3f80u : (e == 17 ? 0x3f800000u : 0u);
            }
            f32x16 k0 = {}, k1 = {};
#pragma unroll
            for (int i = 0; i < 2; ++i) {
                if (r >= 20) { w0[i] = (u32x4){one, one, one, one}; w1[i] = w0[i]; }
                u32x4 fp, fn, ft;
                feature_map(phm, pht, e0[i], et[i], h, fp, fn, ft);
                f32x16 T0 = MFMA32_16(fp, i0, z16); T0 = MFMA32_16(fn, i1, T0);
                f32x16 T1 = MFMA32_16(ft, i0, z16);
                u32x4 b00, b01, b10, b11;
#pragma unroll
                for (int j = 0; j < 4; ++j) {
                    b00[j] = pk2(T0[2 * j], T0[2 * j + 1]); b01[j] = pk2(T0[8 + 2 * j], T0[9 + 2 * j]);
                    b10[j] = pk2(T1[2 * j], T1[2 * j + 1]); b11[j] = pk2(T1[8 + 2 * j], T1[9 + 2 * j]);
                }
                k0 = MFMA32_16(w0[i], b00, k0); k0 = MFMA32_16(w1[i], b01, k0);
                k1 = MFMA32_16(w0[i], b10, k1); k1 = MFMA32_16(w1[i], b11, k1);
            }
            LDS3 char* sp = lds + X_SLOT + hw * 4096;
#pragma unroll
            for (int gq = 0; gq < 3; ++gq) {
                *(LDS3 f32x4*)(sp + gq * 1024 + lane * 16) = (f32x4){k0[4 * gq], k0[4 * gq + 1], k0[4 * gq + 2], k0[4 * gq + 3]};
                if (r < 8) *(LDS3 f32x4*)(sp + 3072 + (gq * 16 + h * 8 + r) * 16) = (f32x4){k1[4 * gq], k1[4 * gq + 1], k1[4 * gq + 2], k1[4 * gq + 3]};
            }
            asm volatile("s_waitcnt vmcnt(0) lgkmcnt(0)" ::: "memory");
            unsigned arr = 0;
            if (lane == 0) arr = LDS_CNT_ADD(C_KV);
            if (__builtin_amdgcn_readfirstlane(arr) == 3) {
                __builtin_amdgcn_fence(__ATOMIC_ACQUIRE, "wavefront");
#pragma unroll
                for (int i = 0; i < 4; ++i) {
                    const int qd = lane + 64 * i;
                    LDS3 const char* s0 = lds + X_SLOT + qd * 16;
                    const f32x4 s = (*(LDS3 const f32x4*)s0 + *(LDS3 const f32x4*)(s0 + 4096)) + (*(LDS3 const f32x4*)(s0 + 8192) + *(LDS3 const f32x4*)(s0 + 12288));
                    if (qd < 240) ST16(__builtin_bit_cast(u32x4, s), WS_KVP + (head * 8 + qb) * 4096 + qd * 16);
                }
                DRAIN();
                if (lane == 0) set_flag(ws, WS_F_KV + (head * 8 + qb) * 8, tag);
            }
#pragma unroll
            for (int i = 0; i < 2; ++i) feature_map(phm, pht, eq[i], eqt[i], h, qf[i][0], qf[i][1], qf[i][2]);
        }
        STAMP(4);
        POLL_ONE(hw == 0, 8, WS_F_KV + (head * 8 + (lane & 7)) * 8, WS_F_KV + (head * 8 + (lane & 7)) * 8, C_P2);
        {
            const int qd = hw * 60 + (lane < 60 ? lane : 59);
            f32x4 pp[8];
#pragma unroll
            for (int p = 0; p < 8; ++p) pp[p] = __builtin_bit_cast(f32x4, LD16(WS_KVP + (head * 8 + p) * 4096 + qd * 16));
            const f32x4 s4 = ((pp[0] + pp[1]) + (pp[2] + pp[3])) + ((pp[4] + pp[5]) + (pp[6] + pp[7]));
            const int gq = qd < 192 ? qd >> 6 : (qd - 192) >> 4, hs = qd < 192 ? (qd >> 5) & 1 : (qd >> 3) & 1, sg = qd < 192 ? qd & 31 : 32 + (qd & 7);
            const unsigned a = pk2(s4[0], s4[1]), b = pk2(s4[2], s4[3]);
            LDS3 u16* kp = (LDS3 u16*)(lds + X_KVF + (8 * gq + 4 * hs) * 96 + sg * 2);
            kp[0] = (u16)a; kp[48] = (u16)(a >> 16); kp[96] = (u16)b; kp[144] = (u16)(b >> 16);
            asm volatile("s_waitcnt lgkmcnt(0)" ::: "memory");
            if (lane == 0) LDS_CNT_ADD(C_KVF);
            lds_wait<8>(lds + C_KVF, 4);
        }
        {
            LDS3 const char* kp = lds + X_KVF + (r < 24 ? r : 23) * 96 + h * 16;
            const u32x4 a0 = *(LDS3 const u32x4*)kp, a1 = *(LDS3 const u32x4*)(kp + 32), a2 = *(LDS3 const u32x4*)(kp + 64);
#pragma unroll
            for (int i = 0; i < 2; ++i) {
                f32x16 d = MFMA32_16(a0, qf[i][0], z16); d = MFMA32_16(a1, qf[i][1], d); d = MFMA32_16(a2, qf[i][2], d);
                const float den = upper_half(d[8]) + 1e-6f;
                float o[12];
#pragma unroll
                for (int j = 0; j < 12; ++j) o[j] = d[j];
                store_o(rs, o, (1.f - gS[i]) / den, tokS + i * 32 + r, 640 + head * 20, h);
            }
            DRAIN();
            unsigned arr = 0;
            if (lane == 0) arr = LDS_CNT_ADD(C_OB);
            if (__builtin_amdgcn_readfirstlane(arr) == 3 && lane == 0) set_flag(ws, WS_F_OB + (qb * 32 + head) * 8, tag);
        }
        STAMP(5);
        f32x4 c[5];
#pragma unroll
        for (int n = 0; n < 5; ++n) c[n] = (f32x4){0.f, 0.f, 0.f, 0.f};
        {
            const unsigned ab = WS_AO + ((mtO * 4 + hw) * 40 + 20) * 1024 + lane * 16, bb = WS_WT + ((ntO * 5) * 40 + 20) * 1024 + lane * 16;
            u32x4 af[5], bf[5][5];
            POLL_ONE(hw == 0, 8, WS_F_OB + ((mtO >> 2) * 32 + r) * 8, WS_F_OB + ((mtO >> 2) * 32 + r) * 8, C_P3);
#pragma unroll
            for (int ks = 0; ks < 5; ++ks) {
                af[ks] = LD16(ab + ks * 1024);
#pragma unroll
                for (int n = 0; n < 5; ++n) bf[ks][n] = LD16(bb + n * 40960 + ks * 1024);
            }
#pragma unroll
            for (int ks = 0; ks < 20; ++ks) {
#pragma unroll
                for (int n = 0; n < 5; ++n) c[n] = MFMA16_32(bf[ks % 5][n], af[ks % 5], c[n]);
                SBAR();
                if (ks < 15) {
                    af[ks % 5] = LD16(ab + (ks + 5) * 1024);
#pragma unroll
                    for (int n = 0; n < 5; ++n) bf[ks % 5][n] = LD16(bb + n * 40960 + (ks + 5) * 1024);
                }
                SBAR();
            }
        }
        STAMP(6);
        lds_wait<8>(lds + C_PROG, 8);
#pragma unroll
        for (int n = 0; n < 3; ++n) *(LDS3 f32x4*)(lds + X_FIN + hw * 3072 + n * 1024 + lane * 16) = c[n];
        lds_wait<4>(lds + C_END, 8);
#pragma unroll
        for (int n = 3; n < 5; ++n) *(LDS3 f32x4*)(lds + 143360 + hw * 2048 + (n - 3) * 1024 + lane * 16) = c[n];
        asm volatile("s_waitcnt lgkmcnt(0)" ::: "memory");
        if (lane == 0) __hip_atomic_store((LDS3 unsigned*)(lds + C_BP + hw * 4), 1u, __ATOMIC_RELAXED, __HIP_MEMORY_SCOPE_WORKGROUP);
        STAMP(7);
        asm volatile("mega_code_end2:");
    } else {
        const int qs = w & 3, kh = w >> 2;
        const int q0 = qb * 256 + qs * 64;
        {
            unsigned sink;
            if (w == 0 && (WARM_MODE & 1)) {
                const char* pc0; int len, len2;
                asm volatile("s_getpc_b64 %0\nmega_code_mark:\n\ts_mov_b32 %1, mega_code_end-mega_code_mark\n\ts_mov_b32 %2, mega_code_end2-mega_code_mark" : "=s"(pc0), "=s"(len), "=s"(len2));
                len = len > len2 ? len : len2;
                const int o = ((bid >> 3) * 64 + lane) * 64;
                if (o + 4 <= len) asm volatile("global_load_dword %0, %1, %2" : "=v"(sink) : "v"(o), "s"(pc0) : "memory");
            } else if (w >= 1 && w <= 6 && (WARM_MODE & 2)) {
                const unsigned o = ((w - 1) * 64 + lane) * 65536 + 65532;
                if (o < WS_END) asm volatile("global_load_dword %0, %1, %2 sc1" : "=v"(sink) : "v"(o), "s"(ws) : "memory");
            } else if (w == 7 && (WARM_MODE & 2)) {
                if (lane < 40) asm volatile("global_load_dword %0, %1, %2 sc1" : "=v"(sink) : "v"(lane * 65536), "s"(P.out) : "memory");
            }
            asm volatile("s_waitcnt vmcnt(0)" ::: "memory");
        }
        GET_TAG();
        if (bid < 240) {
            const int part = nt / 5, c0 = (nt - part * 5) * 128;
            const int wm = w & 1, wn = w >> 1;
            constexpr int RB = 32768;
            const int xr = wm * 64 + r;
            const unsigned xa = RB + xr * 128 + ((h ^ ((xr >> 1) & 7)) << 4);
            unsigned wt[2];
#pragma unroll
            for (int t = 0; t < 2; ++t) {
                const int q = (lane & 15) >> 2, p = lane & 3, blk = (lane >> 4) & 1, row = 8 * h + 4 * t + q;
                wt[t] = RB + 16384 + row * 256 + (((4 * wn + 2 * blk + (p >> 1)) ^ ((q << 2) | (2 * h + t))) << 4) + 8 * (p & 1);
            }
            auto gbody = [&](auto trc) {
                constexpr bool TR = decltype(trc)::value;
                u32x4 xf[2][8], wf[2][4];
                auto rd = [&](auto bc, int s) {
                    constexpr int B = decltype(bc)::value;
                    LDS3 const char* sb = lds + (s & 3) * 32768;
#pragma unroll
                    for (int ks = 0; ks < 4; ++ks) {
                        xf[B][2 * ks] = *(LDS3 const u32x4*)(sb + (xa ^ (ks * 32)));
                        xf[B][2 * ks + 1] = *(LDS3 const u32x4*)(sb + ((xa + 4096) ^ (ks * 32)));
                        const u32x2 lo = trread(sb + wt[0] + ks * 4096), hi = trread(sb + wt[1] + ks * 4096);
                        wf[B][ks] = (u32x4){lo[0], lo[1], hi[0], hi[1]};
                    }
                };
                f32x16 acc0, acc1;
                auto step = [&](auto bc, int s) {
                    constexpr int B = decltype(bc)::value;
                    if (s == 12) DRAIN();
                    __builtin_amdgcn_s_waitcnt(0xC07F);
                    BAR(); SBAR();
                    rd(std::integral_constant<int, B ^ 1>{}, s + 1);
#pragma unroll
                    for (int ks = 0; ks < 4; ++ks) {
                        if constexpr (TR) { acc0 = MFMA32_16(wf[B][ks], xf[B][2 * ks], acc0); acc1 = MFMA32_16(wf[B][ks], xf[B][2 * ks + 1], acc1); }
                        else              { acc0 = MFMA32_16(xf[B][2 * ks], wf[B][ks], acc0); acc1 = MFMA32_16(xf[B][2 * ks + 1], wf[B][ks], acc1); }
                    }
#pragma unroll
                    for (int i = 0; i < 4; ++i) { __builtin_amdgcn_sched_group_barrier(0x8, 1, 0); __builtin_amdgcn_sched_group_barrier(0x2, 2, 0); __builtin_amdgcn_sched_group_barrier(0x100, 4, 0); }
                    __builtin_amdgcn_sched_group_barrier(0x8, 4, 0);
                    SBAR();
                };
                WAIT_BAR(63);
                rd(std::integral_constant<int, 0>{}, 0);
#pragma unroll 1
                for (int mat = 0; mat < 2; ++mat) {
                    acc0 = (f32x16){}; acc1 = (f32x16){};
#pragma unroll 1
                    for (int kk = 0; kk < 10; kk += 2) {
                        step(std::integral_constant<int, 0>{}, mat * 10 + kk);
                        step(std::integral_constant<int, 1>{}, mat * 10 + kk + 1);
                    }
                    const int cpos = 2 * (mt & 7) + (mt >> 3);
                    if constexpr (TR) {
                        const float sc = (part == 0 && mat == 0) ? QSCALE : 1.f;
                        const unsigned base = part == 0 ? WS_Q + mat * NH * 81920 + mt * 5120 : WS_KV + mat * NH * 163840 + cpos * 10240;
                        const unsigned hs = part == 0 ? 81920 : 163840;
#pragma unroll
                        for (int pr = 0; pr < 2; ++pr) {
                            const int G8 = ((c0 + 32 * wn) >> 3) + 2 * pr + h, pp = G8 / 5, gi = G8 - pp * 5;
                            const unsigned hb0 = base + (2 * pp + (gi > 2)) * hs;
#pragma unroll
                            for (int i = 0; i < 2; ++i) {
                                const f32x16& a = i ? acc1 : acc0;
                                const int tl = wm * 64 + i * 32 + r;
                                const auto s0 = __builtin_amdgcn_permlane32_swap(pk2(a[8 * pr] * sc, a[8 * pr + 1] * sc), pk2(a[8 * pr + 4] * sc, a[8 * pr + 5] * sc), false, false);
                                const auto s1 = __builtin_amdgcn_permlane32_swap(pk2(a[8 * pr + 2] * sc, a[8 * pr + 3] * sc), pk2(a[8 * pr + 6] * sc, a[8 * pr + 7] * sc), false, false);
                                if (gi != 2) { const u32x4 ov = {s0[0], s1[0], s0[1], s1[1]}; ST16(ov, hb0 + (tl >> 5) * 1024 + (gi > 2 ? gi - 3 : gi) * 512 + r * 16); }
                                else { const u32x2 o0 = {s0[0], s1[0]}, o1 = {s0[1], s1[1]}; ST8(o0, hb0 + 4096 + tl * 8); ST8(o1, hb0 + hs + 4096 + tl * 8); }
                            }
                        }
                    } else {
                        const int n = c0 + 32 * wn + r, hd = n / 20, d = n - hd * 20, rho = (hd & 1) ? (d < 4 ? d + 16 : d - 4) : d;
                        const unsigned base = WS_KV + (mat * NH + hd) * 163840 + cpos * 10240 + 5120 + h * 320 + rho * 16;
#pragma unroll
                        for (int i = 0; i < 2; ++i)
#pragma unroll
                            for (int s2 = 0; s2 < 2; ++s2) {
                                const f32x16& a = i ? acc1 : acc0;
                                u32x4 ov = {pk2(a[8 * s2], a[8 * s2 + 1]), pk2(a[8 * s2 + 2], a[8 * s2 + 3]), pk2(a[8 * s2 + 4], a[8 * s2 + 5]), pk2(a[8 * s2 + 6], a[8 * s2 + 7])};
                                ST16(ov, base + (wm * 4 + i * 2 + s2) * 640);
                            }
                    }
                    if (mat == 0) STAMP(1);
                }
            };
            if (part < 2) gbody(std::true_type{}); else gbody(std::false_type{});
        } else {
            const int sj = bid - 240;
            WAIT_BAR(63);
            {
                const u32x4 ga = *(const u32x4*)(P.gw + lane * 8), gb = *(const u32x4*)(P.gw + 512 + (lane & 15) * 8);
                u32x4 xa[16], xb[4];
                const u16* xr = P.x + (size_t)(sj * 128 + w * 16) * DM;
#pragma unroll
                for (int rr = 0; rr < 16; ++rr) xa[rr] = *(const u32x4*)(xr + rr * DM + lane * 8);
#pragma unroll
                for (int i = 0; i < 4; ++i) xb[i] = *(const u32x4*)(xr + (i * 4 + (lane >> 4)) * DM + 512 + (lane & 15) * 8);
                float res = 0.f;
#pragma unroll
                for (int rr = 0; rr < 16; ++rr) {
                    float s = 0.f, s2 = 0.f;
#pragma unroll
                    for (int j = 0; j < 4; ++j) { s += bf_lo(xa[rr][j]) * bf_lo(ga[j]) + bf_hi(xa[rr][j]) * bf_hi(ga[j]); s2 += bf_lo(xb[rr >> 2][j]) * bf_lo(gb[j]) + bf_hi(xb[rr >> 2][j]) * bf_hi(gb[j]); }
                    if ((lane >> 4) == (rr & 3)) s += s2;
#pragma unroll
                    for (int o = 32; o >= 1; o >>= 1) s += __shfl_xor(s, o);
                    if (lane == rr) res = s;
                }
                if (lane < 16) ST4(__float_as_uint(1.f / (1.f + __expf(-res))), WS_G + (sj * 128 + w * 16 + lane) * 4);
            }
            DRAIN();
            unsigned arr = 0;
            if (lane == 0) arr = LDS_CNT_ADD(G_ST);
            if (__builtin_amdgcn_readfirstlane(arr) == 7 && lane == 0) set_flag(ws, WS_F_SD + sj * 8, tag);
        }
        STAMP(2);
        const unsigned hb = lds0 + kh * 10240;
        const unsigned laneK = hb + lane * 16, laneT = hb + 4096 + r * 8 + h * 4, laneV = hb + 5120 + h * 320 + (r < 20 ? r : 19) * 16;
        const u64 vmask = 0x000fffff000fffffull;
        u32x4 v0a = {one, one, one, one}, v0b = v0a, v1 = v0a;
        u32x4 qA, qB, ka; unsigned qtA, qtB, kt;
        {
            lds_wait(lds + G_Q, 4);
            LDS3 const char* qp = lds + L_Q + (qs >> 1) * 5120;
            const unsigned om = (qs & 1) * 2048 + lane * 16, ot = 4096 + ((qs & 1) * 64 + r) * 8 + 4 * h;
            qA = *(LDS3 const u32x4*)(qp + om); qB = *(LDS3 const u32x4*)(qp + om + 1024);
            qtA = *(LDS3 const unsigned*)(qp + ot); qtB = *(LDS3 const unsigned*)(qp + ot + 256);
            DSR128(ka, laneK, 0); DSR32(kt, laneT, 0); DSR128M2(v0a, laneV, 0, v1, laneV, 640);
            asm volatile("s_waitcnt lgkmcnt(0)\n\ts_barrier" : "+v"(ka), "+v"(kt), "+v"(v0a), "+v"(v1), "+v"(qA), "+v"(qB), "+v"(qtA), "+v"(qtB) :: "memory");
        }
        STAMP(5);
        float mA, mB; u32x2 qTA, qTB; f32x16 sA, sB;
        {
            const u32x2 t0 = {kt, 0x3f80u}, a0 = {qtA, 0u}, b0 = {qtB, 0u};
            const f32x16 s = MFMA32_16(ka, qA, z16);
            const f32x16 s0 = MFMA32_8(t0, a0, s);
            f32x16 s1 = MFMA32_16(ka, qB, z16); s1 = MFMA32_8(t0, b0, s1);
            float x0 = s0[0], x1 = s1[0];
#pragma unroll
            for (int i = 1; i < 16; ++i) { x0 = fmaxf(x0, s0[i]); x1 = fmaxf(x1, s1[i]); }
            x0 = -xhalf_max(x0); x1 = -xhalf_max(x1);
            const unsigned hh = pk2(x0, x1);
            const unsigned ll = pk2(x0 - bf_lo(hh), x1 - bf_hi(hh));
            mA = -(bf_lo(hh) + bf_lo(ll)); mB = -(bf_hi(hh) + bf_hi(ll));
            const unsigned sel = h ? ll : hh;
            qTA = (u32x2){qtA, sel & 0xffffu}; qTB = (u32x2){qtB, sel >> 16};
            sA = MFMA32_8(t0, qTA, s);
        }
        f32x16 oA = {}, oB = {};
        u32x4 pa, pb = {0u, 0u, 0u, 0u};
#define EX4(S, i, Pv, j) do { Pv[j] = pk2(ex2(S[i]), ex2(S[i + 1])); Pv[j + 1] = pk2(ex2(S[i + 2]), ex2(S[i + 3])); PIN(Pv); SBAR(); } while (0)
#define KT2 ((u32x2){kt, 0x3f80u})
#define TILE(V0C, V0N, aK1, oK1, aT1, oT1, oV1, aV0, oV0) do { \
            sB = MFMA32_16(ka, qB, z16); SBAR(); DSR128(ka, aK1, oK1); EX4(sA, 0, pa, 0); \
            oB = MFMA32_16(v1, pb, oB); SBAR(); DSR128M2(v1, aV, oV1, V0N, aV0, oV0); EX4(sA, 4, pa, 2); \
            sB = MFMA32_8(KT2, qTB, sB); SBAR(); DSR32(kt, aT1, oT1); EX4(sA, 8, pb, 0); \
            oA = MFMA32_16(V0C, pa, oA); SBAR(); EX4(sA, 12, pb, 2); \
            LWAIT(1); sA = MFMA32_16(ka, qA, z16); SBAR(); EX4(sB, 0, pa, 0); \
            oA = MFMA32_16(v1, pb, oA); SBAR(); EX4(sB, 4, pa, 2); \
            LWAIT(0); sA = MFMA32_8(KT2, qTA, sA); SBAR(); EX4(sB, 8, pb, 0); \
            oB = MFMA32_16(V0C, pa, oB); SBAR(); EX4(sB, 12, pb, 2); \
        } while (0)
        unsigned aK = laneK, aT = laneT, aV = laneV;
#pragma unroll 1
        for (int c = 0; c < 8; ++c) {
            if (c == 1) { WAIT_BAR(0); }
            else if (c == 7) { if (lane == 0) LDS_CNT_ADD(C_PROG); asm volatile("s_waitcnt lgkmcnt(0)" ::: "memory"); }
            if (c >= 1) {
                unsigned pp;
                asm volatile("ds_write_b32 %1, %2\n\tds_read_b32 %0, %3\n\ts_waitcnt lgkmcnt(0)" : "=&v"(pp) : "v"(lds0 + C_PR + w * 4), "v"(c), "v"(lds0 + C_PR + (w ^ 4) * 4) : "memory");
                const int pc = __builtin_amdgcn_readfirstlane(pp);
                if (pc > c || (pc == c && ((c ^ kh) & 1))) __builtin_amdgcn_s_setprio(1); else __builtin_amdgcn_s_setprio(0);
            } else if (kh) __builtin_amdgcn_s_setprio(1);
            const unsigned nx = (c < 7 ? c + 1 : 7) * 20480;
            const unsigned nK = laneK + nx, nT = laneT + nx, nV = laneV + nx;
            SBAR();
            TILE(v0a, v0b, aK, 1024, aT, 256, 640,  aV, 1280);
            TILE(v0b, v0a, aK, 2048, aT, 512, 1920, aV, 2560);
            TILE(v0a, v0b, aK, 3072, aT, 768, 3200, aV, 3840);
            TILE(v0b, v0a, nK, 0,    nT, 0,   4480, nV, 0);
            aK = nK; aT = nT; aV = nV;
        }
        oB = MFMA32_16(v1, pb, oB);
#undef TILE
#undef EX4
        STAMP(6);
        {
            float chk = 0.f;
#pragma unroll
            for (int i = 0; i < 12; ++i) chk += fabsf(oA[i]) * 0.f + fabsf(oB[i]) * 0.f;
            bool bad = !(chk == 0.f);
            if (h) bad = bad || !(oA[8] >= 0x1p-60f && oA[8] <= 0x1p60f && oB[8] >= 0x1p-60f && oB[8] <= 0x1p60f);
            if (__builtin_expect(__any(bad), 0)) {
                mA = -INFINITY; mB = -INFINITY;
#pragma unroll
                for (int i = 0; i < 16; ++i) { oA[i] = 0.f; oB[i] = 0.f; }
                const u32x2 a0 = {qtA, 0u}, b0 = {qtB, 0u};
#pragma unroll 1
                for (int t = 0; t < 32; ++t) {
                    const unsigned kb = WS_KV + head * 163840 + (2 * (t >> 2) + kh) * 10240, t4 = t & 3;
                    const u32x4 k0 = LD16(kb + t4 * 1024 + lane * 16);
                    const u32x2 t0 = {LD4(kb + 4096 + (t4 * 32 + r) * 8 + 4 * h), 0u};
                    u32x4 w0 = LD16(kb + 5120 + t4 * 1280 + h * 320 + (r < 20 ? r : 19) * 16), w1 = LD16(kb + 5120 + t4 * 1280 + 640 + h * 320 + (r < 20 ? r : 19) * 16);
                    if (r >= 20) { w0 = (u32x4){one, one, one, one}; w1 = w0; }
                    f32x16 s0 = MFMA32_16(k0, qA, z16); s0 = MFMA32_8(t0, a0, s0);
                    f32x16 s1 = MFMA32_16(k0, qB, z16); s1 = MFMA32_8(t0, b0, s1);
                    float x0 = s0[0], x1 = s1[0];
#pragma unroll
                    for (int i = 1; i < 16; ++i) { x0 = fmaxf(x0, s0[i]); x1 = fmaxf(x1, s1[i]); }
                    x0 = fmaxf(xhalf_max(x0), mA); x1 = fmaxf(xhalf_max(x1), mB);
                    const float g0 = (mA == x0) ? 1.f : ex2(mA - x0), g1 = (mB == x1) ? 1.f : ex2(mB - x1);
                    mA = x0; mB = x1;
#pragma unroll
                    for (int i = 0; i < 16; ++i) { oA[i] *= g0; oB[i] *= g1; }
#pragma unroll
                    for (int i = 0; i < 4; ++i) { pa[i] = pk2(ex2(s0[2 * i] - x0), ex2(s0[2 * i + 1] - x0)); pb[i] = pk2(ex2(s0[8 + 2 * i] - x0), ex2(s0[9 + 2 * i] - x0)); }
                    oA = MFMA32_16(w0, pa, oA); oA = MFMA32_16(w1, pb, oA);
#pragma unroll
                    for (int i = 0; i < 4; ++i) { pa[i] = pk2(ex2(s1[2 * i] - x1), ex2(s1[2 * i + 1] - x1)); pb[i] = pk2(ex2(s1[8 + 2 * i] - x1), ex2(s1[9 + 2 * i] - x1)); }
                    oB = MFMA32_16(w0, pa, oB); oB = MFMA32_16(w1, pb, oB);
                }
            }
        }
        unsigned fin = 0;
        if (lane == 0) fin = LDS_CNT_ADD(C_END);
        fin = __builtin_amdgcn_readfirstlane(fin);
        lds_wait<4>(lds + C_OB, 4);
        lds_wait<4>(lds + C_PROG, 8);
        const float gO = *(LDS3 const float*)(lds + X_G + (qs * 64 + kh * 32 + r) * 4);
        {
            LDS3 char* xp = lds + X_MRG + w * 2816;
            const f32x16& s = kh ? oA : oB;
            *(LDS3 f32x4*)(xp + lane * 16) = (f32x4){s[0], s[1], s[2], s[3]};
            *(LDS3 f32x4*)(xp + 1024 + lane * 16) = (f32x4){s[4], s[5], s[6], s[7]};
            if (!h) *(LDS3 f32x4*)(xp + 2048 + r * 16) = (f32x4){s[8], s[9], s[10], s[11]};
            else *(LDS3 f32x2*)(xp + 2560 + r * 8) = (f32x2){s[8], kh ? mA : mB};
            asm volatile("s_waitcnt lgkmcnt(0)" ::: "memory");
            if (lane == 0) __hip_atomic_store((LDS3 unsigned*)(lds + C_MRG + w * 4), 1u, __ATOMIC_RELAXED, __HIP_MEMORY_SCOPE_WORKGROUP);
        }
        if (fin < 4) {
            lds_wait<4>(lds + C_P3, 1);
            const char* wsrc = ws + WS_WT + (size_t)ntO * 5 * 40960;
#pragma unroll 1
            for (int n = 0; n < 5; ++n)
#pragma unroll
                for (int t = 0; t < 5; ++t) { const int ks = fin * 5 + t; glds16s(lane * 16, wsrc + (n * 40 + ks) * 1024, lds0 + L_WA + (n * 20 + ks) * 1024); }
        }
        lds_wait<2>(lds + C_MRG + (w ^ 4) * 4, 1);
        bool lastw;
        {
            LDS3 const char* xp = lds + X_MRG + (w ^ 4) * 2816;
            const f32x4 p0 = *(LDS3 const f32x4*)(xp + lane * 16), p1 = *(LDS3 const f32x4*)(xp + 1024 + lane * 16), p2 = *(LDS3 const f32x4*)(xp + 2048 + r * 16);
            const f32x2 lm = *(LDS3 const f32x2*)(xp + 2560 + r * 8);
            const f32x16& s = kh ? oB : oA;
            const float mo = kh ? mB : mA, mp = lm[1];
            const float mx = fmaxf(mo, mp);
            const float fo = (mo == mx) ? 1.f : ex2(mo - mx), fq = (mp == mx) ? 1.f : ex2(mp - mx);
            float o[12];
#pragma unroll
            for (int i = 0; i < 4; ++i) { o[i] = s[i] * fo + p0[i] * fq; o[4 + i] = s[4 + i] * fo + p1[i] * fq; o[8 + i] = s[8 + i] * fo + p2[i] * fq; }
            const float l = upper_half(s[8]) * fo + lm[0] * fq;
            store_o(rs, o, gO / l, q0 + kh * 32 + r, head * 20, h);
            DRAIN();
            unsigned arr = 0;
            if (lane == 0) arr = LDS_CNT_ADD(C_OA);
            lastw = __builtin_amdgcn_readfirstlane(arr) == 7;
            if (lastw && lane == 0) set_flag(ws, WS_F_OA + (qb * 32 + head) * 8, tag);
        }
        STAMP(7);
        {
            const int c16 = lane & 15, g = lane >> 4, tt = w & 3, k0 = kh * 10;
            POLL_ONE(lastw, 4, WS_F_OA + ((mtO >> 2) * 32 + r) * 8, WS_F_OA + ((mtO >> 2) * 32 + r) * 8, C_P4);
            STAMP(9);
            const unsigned ab = WS_AO + ((mtO * 4 + tt) * 40 + k0) * 1024 + lane * 16;
            u32x4 af[10];
#pragma unroll
            for (int ks = 0; ks < 10; ++ks) af[ks] = LD16(ab + ks * 1024);
            lds_wait(lds + C_OA, 8);
            f32x4 c[5];
#pragma unroll
            for (int n = 0; n < 5; ++n) c[n] = (f32x4){0.f, 0.f, 0.f, 0.f};
#pragma unroll
            for (int ks = 0; ks < 10; ++ks)
#pragma unroll
                for (int n = 0; n < 5; ++n) c[n] = MFMA16_32(*(LDS3 const u32x4*)(lds + L_WA + (n * 20 + k0 + ks) * 1024 + lane * 16), af[ks], c[n]);
            LDS3 char* xp = lds + X_MRG + tt * 5120 + lane * 16;
            if (w < 4) { *(LDS3 f32x4*)(xp + 3072) = c[3]; *(LDS3 f32x4*)(xp + 4096) = c[4]; }
            else { *(LDS3 f32x4*)(xp) = c[0]; *(LDS3 f32x4*)(xp + 1024) = c[1]; *(LDS3 f32x4*)(xp + 2048) = c[2]; }
            asm volatile("s_waitcnt lgkmcnt(0)" ::: "memory");
            if (lane == 0) __hip_atomic_store((LDS3 unsigned*)(lds + C_FIN + w * 4), 1u, __ATOMIC_RELAXED, __HIP_MEMORY_SCOPE_WORKGROUP);
            lds_wait(lds + C_FIN + (w ^ 4) * 4, 1);
            lds_wait(lds + C_BP + tt * 4, 1);
            u16* op = P.out + (size_t)(mtO * 64 + tt * 16 + c16) * DM + ntO * 80 + g * 4;
#pragma unroll
            for (int n = 0; n < 5; ++n) {
                if ((w < 4) == (n < 3)) {
                    const f32x4 bp = *(LDS3 const f32x4*)(n < 3 ? lds + X_FIN + tt * 3072 + n * 1024 + lane * 16 : lds + 143360 + tt * 2048 + (n - 3) * 1024 + lane * 16);
                    const f32x4 s = c[n] + *(LDS3 const f32x4*)(xp + n * 1024) + bp;
                    *(u32x2*)(op + n * 16) = (u32x2){pk2(s[0], s[1]), pk2(s[2], s[3])};
                }
            }
            if (bid == 0 && mtid == 0) set_flag(ws, WS_EPOCH, tag);
        }
        STAMP(10);
        asm volatile("mega_code_end:");
    }
}

extern "C" void kernel_launch(void* const* d_in, const int* in_sizes, int n_in, void* d_out, int out_size, void* d_ws, size_t ws_size,
                              hipStream_t stream) {
    Params P;
    P.x = (const u16*)d_in[0]; P.wa = (const u16*)d_in[1]; P.woa = (const u16*)d_in[2]; P.wb = (const u16*)d_in[4];
    P.wphi = (const u16*)d_in[5]; P.wob = (const u16*)d_in[6]; P.gw = (const u16*)d_in[7]; P.ws = (char*)d_ws; P.out = (u16*)d_out;
    mega<<<256, 768, 0, stream>>>(P);
}
```

```cpp
#include <hip/hip_runtime.h>
#include <hip/hip_bf16.h>
#include <stdint.h>
#include <type_traits>

typedef __attribute__((ext_vector_type(8))) short bf16x8;
typedef __attribute__((ext_vector_type(4))) short bf16x4;
typedef __attribute__((ext_vector_type(16))) float f32x16;
typedef __attribute__((ext_vector_type(4))) float f32x4;
typedef __attribute__((ext_vector_type(2))) float f32x2;
typedef __attribute__((ext_vector_type(4))) unsigned u32x4;
typedef __attribute__((ext_vector_type(2))) unsigned u32x2;
typedef __attribute__((ext_vector_type(2))) __bf16 bf2_t;
typedef unsigned short u16;
typedef unsigned long long u64;
#define LDS3 __attribute__((address_space(3)))
typedef __attribute__((address_space(1))) u64 gu64;
#define RLX_AGENT __ATOMIC_RELAXED, __HIP_MEMORY_SCOPE_AGENT

#define PRIO_MODE 3
#define WARM_MODE 1
#define PRE_SLEEP 2
constexpr int SEQ = 2048, DM = 640, NH = 32;
constexpr float LOG2E = 1.4426950408889634f;
constexpr float QSCALE = 0.22360679774997896f * LOG2E;

constexpr unsigned WS_EPOCH = 0;
constexpr unsigned WS_F_G   = 256;
constexpr unsigned WS_F_SD  = WS_F_G + 4096;
constexpr unsigned WS_F_KV  = WS_F_SD + 256;
constexpr unsigned WS_F_OB  = WS_F_KV + 2048;
constexpr unsigned WS_F_OA  = WS_F_OB + 2048;
constexpr unsigned WS_G     = 16384;
constexpr unsigned WS_Q     = 32768;
constexpr unsigned WS_KV    = WS_Q + 2 * NH * 81920;
constexpr unsigned WS_KVP   = WS_KV + 2 * NH * 163840;
constexpr unsigned WS_AO    = WS_KVP + NH * 8 * 4096;
constexpr unsigned WS_WT    = WS_AO + SEQ * 1280 * 2;
constexpr unsigned WS_END   = WS_WT + 1280 * 640 * 2;

#define STAMP(i)

__device__ __forceinline__ unsigned pk2(float a, float b) { f32x2 v = {a, b}; bf2_t r = __builtin_convertvector(v, bf2_t); return __builtin_bit_cast(unsigned, r); }
__device__ __forceinline__ float bf_lo(unsigned u) { return __uint_as_float(u << 16); }
__device__ __forceinline__ float bf_hi(unsigned u) { return __uint_as_float(u & 0xffff0000u); }
__device__ __forceinline__ float ex2(float x) { return __builtin_amdgcn_exp2f(x); }
#define MFMA32_16(a, b, c) __builtin_amdgcn_mfma_f32_32x32x16_bf16(__builtin_bit_cast(bf16x8, a), __builtin_bit_cast(bf16x8, b), c, 0, 0, 0)
#define MFMA32_8(a, b, c) __builtin_amdgcn_mfma_f32_32x32x8bf16_1k(__builtin_bit_cast(bf16x4, a), __builtin_bit_cast(bf16x4, b), c, 0, 0, 0)
#define MFMA16_32(a, b, c) __builtin_amdgcn_mfma_f32_16x16x32_bf16(__builtin_bit_cast(bf16x8, a), __builtin_bit_cast(bf16x8, b), c, 0, 0, 0)

__device__ __forceinline__ void glds16(unsigned voff, const void* sbase, unsigned lds_base) {
    unsigned sv;
    asm volatile("s_mov_b32 %0, m0\n\ts_mov_b32 m0, %3\n\ts_nop 0\n\tglobal_load_lds_dwordx4 %1, %2\n\ts_mov_b32 m0, %0"
                 : "=&s"(sv) : "v"(voff), "s"(sbase), "s"(__builtin_amdgcn_readfirstlane(lds_base)) : "memory");
}
__device__ __forceinline__ void glds16s(unsigned voff, const void* sbase, unsigned lds_base) {
    unsigned sv;
    asm volatile("s_mov_b32 %0, m0\n\ts_mov_b32 m0, %3\n\ts_nop 0\n\tglobal_load_lds_dwordx4 %1, %2 sc1\n\ts_mov_b32 m0, %0"
                 : "=&s"(sv) : "v"(voff), "s"(sbase), "s"(__builtin_amdgcn_readfirstlane(lds_base)) : "memory");
}
__device__ __forceinline__ void glds4s(unsigned voff, const void* sbase, unsigned lds_base) {
    unsigned sv;
    asm volatile("s_mov_b32 %0, m0\n\ts_mov_b32 m0, %3\n\ts_nop 0\n\tglobal_load_lds_dword %1, %2 sc1\n\ts_mov_b32 m0, %0"
                 : "=&s"(sv) : "v"(voff), "s"(sbase), "s"(__builtin_amdgcn_readfirstlane(lds_base)) : "memory");
}
typedef short v4i16_t __attribute__((ext_vector_type(4)));
__device__ __forceinline__ u32x2 trread(LDS3 const char* p) {
    return __builtin_bit_cast(u32x2, __builtin_amdgcn_ds_read_tr16_b64_v4i16((LDS3 v4i16_t*)p));
}
#define WAIT_BAR(N) asm volatile("s_waitcnt vmcnt(" #N ") lgkmcnt(0)\n\ts_barrier" ::: "memory")
#define DRAIN() asm volatile("s_waitcnt vmcnt(0)" ::: "memory")
#define SBAR() __builtin_amdgcn_sched_barrier(0)
#define PIN(x) asm volatile("" : "+v"(x))

__device__ __forceinline__ float xhalf_max(float x) {
    auto r = __builtin_amdgcn_permlane32_swap(__float_as_uint(x), __float_as_uint(x), false, false);
    return fmaxf(__uint_as_float(r[0]), __uint_as_float(r[1]));
}
__device__ __forceinline__ float xhalf_sum(float x) {
    auto r = __builtin_amdgcn_permlane32_swap(__float_as_uint(x), __float_as_uint(x), false, false);
    return __uint_as_float(r[0]) + __uint_as_float(r[1]);
}
__device__ __forceinline__ float upper_half(float x) {
    auto r = __builtin_amdgcn_permlane32_swap(__float_as_uint(x), __float_as_uint(x), false, false);
    return __uint_as_float(r[1]);
}

typedef __amdgpu_buffer_rsrc_t rsrc_t;
#define ST8(v, off)  __builtin_amdgcn_raw_buffer_store_b64(v, rs, (int)(off), 0, 16)
#define ST16(v, off) __builtin_amdgcn_raw_buffer_store_b128(v, rs, (int)(off), 0, 16)
#define ST4(v, off)  __builtin_amdgcn_raw_buffer_store_b32(v, rs, (int)(off), 0, 16)
#define LD16(off) __builtin_amdgcn_raw_buffer_load_b128(rs, (int)(off), 0, 16)
#define LD4(off)  __builtin_amdgcn_raw_buffer_load_b32(rs, (int)(off), 0, 16)

template <int SLP = 2>
__device__ __forceinline__ void wait_flags(const char* ws, unsigned off0, unsigned off1, u64 tag) {
    gu64* p0 = (gu64*)(ws + off0); gu64* p1 = (gu64*)(ws + off1);
#pragma unroll 1
    for (unsigned spins = 0; spins < (1u << 16); ++spins) {
        const u64 a = __hip_atomic_load(p0, RLX_AGENT), b = __hip_atomic_load(p1, RLX_AGENT);
        if (__all(a == tag && b == tag)) break;
        __builtin_amdgcn_s_sleep(SLP);
    }
    __builtin_amdgcn_fence(__ATOMIC_ACQUIRE, "wavefront");
}
__device__ __forceinline__ void set_flag(char* ws, unsigned off, u64 tag) { __hip_atomic_store((gu64*)(ws + off), tag, RLX_AGENT); }

struct Params { const u16* x; const u16* wa; const u16* wb; const u16* wphi; const u16* woa; const u16* wob; const u16* gw; char* ws; u16* out; };

__device__ __forceinline__ void feature_map(const u32x4& pm, unsigned pt, const u32x4& tm, unsigned tt, int h, u32x4& fp, u32x4& fn, u32x4& ft) {
    const f32x16 z16 = {};
    const u32x2 a8 = {pt, 0u}, b8 = {tt, 0u};
    f32x16 s = MFMA32_16(pm, tm, z16); s = MFMA32_8(a8, b8, s);
    float v[12];
#pragma unroll
    for (int i = 0; i < 12; i += 2) { const unsigned u = pk2(s[i], s[i + 1]); v[i] = bf_lo(u) * LOG2E; v[i + 1] = bf_hi(u) * LOG2E; }
    float mx = v[0], mn = v[0];
#pragma unroll
    for (int i = 1; i < 8; ++i) { mx = fmaxf(mx, v[i]); mn = fminf(mn, v[i]); }
    const float tx = fmaxf(fmaxf(v[8], v[9]), fmaxf(v[10], v[11])), tn = fminf(fminf(v[8], v[9]), fminf(v[10], v[11]));
    if (!h) { mx = fmaxf(mx, tx); mn = fminf(mn, tn); }
    mx = xhalf_max(mx); mn = -xhalf_max(-mn);
    float ep[12], en[12], sp = 0.f, sn = 0.f;
#pragma unroll
    for (int i = 0; i < 12; ++i) {
        ep[i] = ex2(v[i] - mx); en[i] = ex2(mn - v[i]);
        if (i >= 8 && h) { ep[i] = 0.f; en[i] = 0.f; }
        sp += ep[i]; sn += en[i];
    }
    sp = __builtin_amdgcn_rcpf(xhalf_sum(sp)); sn = __builtin_amdgcn_rcpf(xhalf_sum(sn));
#pragma unroll
    for (int i = 0; i < 4; ++i) { fp[i] = pk2(ep[2 * i] * sp, ep[2 * i + 1] * sp); fn[i] = pk2(en[2 * i] * sn, en[2 * i + 1] * sn); }
    ft = (u32x4){pk2(ep[8] * sp, ep[9] * sp), pk2(ep[10] * sp, ep[11] * sp), pk2(en[8] * sn, en[9] * sn), pk2(en[10] * sn, en[11] * sn)};
}

__device__ __forceinline__ void store_o(rsrc_t rs, const float (&o)[12], float sc, int tok, int kbase, int h) {
#pragma unroll
    for (int gq = 0; gq < 3; ++gq) {
        const int d = 8 * gq + 4 * h;
        if (d < 20) {
            const int k = kbase + ((kbase & 4) ? (d == 16 ? 0 : d + 4) : d);
            u32x2 ov = {pk2(o[4 * gq] * sc, o[4 * gq + 1] * sc), pk2(o[4 * gq + 2] * sc, o[4 * gq + 3] * sc)};
            ST8(ov, WS_AO + (((tok >> 4) * 40 + (k >> 5)) * 64 + ((k >> 3) & 3) * 16 + (tok & 15)) * 16 + (k & 7) * 2);
        }
    }
}

#define DSR128(dst, addr, off) asm volatile("ds_read_b128 %0, %1 offset:%2" : "=v"(dst) : "v"(addr), "i"(off))
#define DSR32(dst, addr, off) asm volatile("ds_read_b32 %0, %1 offset:%2" : "=v"(dst) : "v"(addr), "i"(off))
#define DSR128M2(d0, a0, o0, d1, a1, o1) asm volatile("s_mov_b64 exec, %6\n\tds_read_b128 %0, %2 offset:%3\n\tds_read_b128 %1, %4 offset:%5\n\ts_mov_b64 exec, -1" \
                                                      : "+v"(d0), "+v"(d1) : "v"(a0), "i"(o0), "v"(a1), "i"(o1), "s"(vmask))
#define LWAIT(n) do { asm volatile("s_waitcnt lgkmcnt(" #n ")" ::: "memory"); SBAR(); } while (0)

#define BAR() asm volatile("s_barrier" ::: "memory")
#define LDS_CNT_ADD(off) __hip_atomic_fetch_add((LDS3 unsigned*)(lds + (off)), 1u, __ATOMIC_RELAXED, __HIP_MEMORY_SCOPE_WORKGROUP)

template <int SLP = 1>
__device__ __forceinline__ void lds_wait(LDS3 char* p, unsigned want) {
#pragma unroll 1
    for (unsigned spins = 0; spins < (1u << 20); ++spins) {
        if ((unsigned)__builtin_amdgcn_readfirstlane(__hip_atomic_load((LDS3 unsigned*)p, __ATOMIC_RELAXED, __HIP_MEMORY_SCOPE_WORKGROUP)) >= want) break;
        __builtin_amdgcn_s_sleep(SLP);
    }
    __builtin_amdgcn_fence(__ATOMIC_ACQUIRE, "wavefront");
}

template <int SLP>
__device__ __forceinline__ void prog_wait(LDS3 char* p, int lane, unsigned want) {
#pragma unroll 1
    for (unsigned spins = 0; spins < (1u << 20); ++spins) {
        if (__all(__hip_atomic_load((LDS3 unsigned*)p + (lane & 7), __ATOMIC_RELAXED, __HIP_MEMORY_SCOPE_WORKGROUP) >= want)) break;
        __builtin_amdgcn_s_sleep(SLP);
    }
    __builtin_amdgcn_fence(__ATOMIC_ACQUIRE, "wavefront");
}
#define POLL_ONE(cond, SLP, o0, o1, word) do { if (cond) { wait_flags<SLP>(ws, o0, o1, tag); \
        if (lane == 0) __hip_atomic_store((LDS3 unsigned*)(lds + (word)), 1u, __ATOMIC_RELAXED, __HIP_MEMORY_SCOPE_WORKGROUP); } else lds_wait<SLP>(lds + (word), 1); } while (0)
constexpr int C_KV = 0, C_KVF = 4, C_OB = 8, C_PROG = 12, C_WA = 16, C_OA = 20, C_MRG = 32  , C_FIN = 64  , C_BP = 96  ;
constexpr int C_PROG2 = 24, C_BX = 28, C_P1 = 112, C_P2 = 116, C_P3 = 120, C_P4 = 124, C_PR = 128  ;
constexpr int G_W = 31744;
constexpr int G_FULL = G_W, G_FREE = G_W + 16, G_Q = G_W + 32, G_KV0 = G_W + 36, G_ST = G_W + 40;
constexpr int C_KVL = 160, X_G = 256;
constexpr int X_KVF = 1280, X_SLOT = 4096;
constexpr int X_MRG = 1280;
constexpr int L_WA = 24576;
constexpr int L_Q = 65536;
constexpr int X_FIN = 126976;

__global__ __launch_bounds__(768) void mega(Params P) {
    __shared__ __attribute__((aligned(16))) char smem[163840];
    LDS3 char* lds = (LDS3 char*)smem;
    char* ws = P.ws;
    const int tid = threadIdx.x, lane = tid & 63, r = lane & 31, h = lane >> 5;
    const int hw = __builtin_amdgcn_readfirstlane(tid >> 6);
    const bool helper = hw < 4;
    const int w = hw - 4, mtid = tid - 256;
    const int bid = blockIdx.x;
    const unsigned lds0 = (unsigned)(uintptr_t)smem;
    const rsrc_t rs = __builtin_amdgcn_make_buffer_rsrc(ws, 0, WS_END, 0x00020000);
    STAMP(0);
    u64 tag = 0;
#define GET_TAG() do { asm volatile("s_load_dwordx2 %0, %1, 0x0\n\ts_waitcnt lgkmcnt(0)" : "=s"(tag) : "s"(ws) : "memory"); tag += 1; } while (0)
    const int head = (bid & 7) * 4 + ((bid >> 3) & 3), qb = bid >> 5;
    const int nlo = (head * 20) >> 7, nhi = (head * 20 + 19) >> 7;
    const int mtO = (bid & 7) * 4 + ((bid >> 3) & 3), ntO = bid >> 5;
    const int lin = (bid & 7) * 30 + (bid >> 3), lh = lin >= 120, lrem = lin - 120 * lh;
    const int mt = lh * 8 + (lrem & 7), nt = lrem >> 3;
    const unsigned fG = WS_F_G + (mt * 15 + nt) * 8;
    const unsigned one = (r == 20) ? 0x3f803f80u : 0u;
    const f32x16 z16 = {};

    if (helper) {
        const unsigned fA0 = WS_F_G + ((lane & 15) * 15 + (lane < 32 ? 5 : 10) + ((lane & 16) ? nhi : nlo)) * 8;
        const unsigned fA1 = WS_F_G + ((qb * 2 + (lane & 1)) * 15 + ((lane & 2) ? nhi : nlo)) * 8;
        __builtin_amdgcn_s_setprio(3);
        const char* kvb = ws + WS_KV + head * 163840;
        auto kv_issue = [&](int grp) {
#pragma unroll
            for (int t = 0; t < 8; ++t) { int pc = grp * 32 + hw + 4 * t; if (pc == 31) pc = 30; glds16s(lane * 16, kvb + pc * 1024, lds0 + pc * 1024); }
        };
        auto q_issue = [&]() {
            const char* qbase = ws + WS_Q + head * 81920 + qb * 10240;
#pragma unroll
            for (int t = 0; t < 3; ++t) { const int pc = hw + 4 * t < 10 ? hw + 4 * t : 9; glds16s(lane * 16, qbase + pc * 1024, lds0 + L_Q + pc * 1024); }
        };
        if (bid < 240) {
            const int rot = (nt * 3 + mt * 5 + (mt >> 1)) % 10;
            const unsigned xo = (mt * 128 + 8 * hw + (lane >> 3)) * 1280 + (((lane & 7) ^ ((4 * hw + (lane >> 4)) & 7)) << 4);
            const unsigned wo = (4 * hw + (lane >> 4)) * 3840 + nt * 256 + (((lane & 15) ^ (((lane >> 4) << 2) | hw)) << 4);
            const unsigned dX = lds0 + 32768 + hw * 1024;
            auto issue = [&](int s) {
                int kt = (s >= 10 ? s - 10 : s) + rot; if (kt >= 10) kt -= 10;
                const char* xb = (const char*)P.x + kt * 128;
                const char* wb = (const char*)(s >= 10 ? P.wb : P.wa) + (size_t)kt * (64 * 3840);
                const unsigned d = dX + (s & 3) * 32768;
#pragma unroll
                for (int i = 0; i < 4; ++i) { glds16(xo, xb + i * 40960, d + i * 4096); glds16(wo, wb + i * 61440, d + 16384 + i * 4096); }
            };
#pragma unroll 1
            for (int n = 0; n < 20; ++n) {
                if (n == 2) { if (hw == 0 && lane < 16) *(LDS3 unsigned*)(lds + G_W + lane * 4) = 0u; GET_TAG(); WAIT_BAR(8); }
                else if (n == 3) { WAIT_BAR(8); }
                else if (n >= 5) { WAIT_BAR(16); }
                if (n == 16 && tid == 0) set_flag(ws, fG, tag);
                if (n == 19 && hw == 0) {
                    glds4s(fA0, ws, lds0); glds4s(fA0 + 4, ws, lds0 + 256); glds4s(fA1, ws, lds0 + 512); glds4s(fA1 + 4, ws, lds0 + 768);
                }
                issue(n);
            }
            WAIT_BAR(16);
            asm volatile("s_waitcnt vmcnt(8)" ::: "memory");
            if (hw == 0) {
                LDS3 const unsigned* f = (LDS3 const unsigned*)lds + lane;
                const unsigned tl = (unsigned)tag, th = (unsigned)(tag >> 32);
                if (!__all(f[0] == tl && f[64] == th && f[128] == tl && f[192] == th)) wait_flags<8>(ws, fA0, fA1, tag);
            }
            WAIT_BAR(63);
            kv_issue(0); q_issue();
            WAIT_BAR(11);
            kv_issue(1);
            WAIT_BAR(63);
        } else {
            if (hw == 0 && lane < 16) *(LDS3 unsigned*)(lds + G_W + lane * 4) = 0u;
            GET_TAG();
            WAIT_BAR(63);
            if (hw == 0) {
#pragma unroll 1
                for (int t = 0; t < PRE_SLEEP; ++t) __builtin_amdgcn_s_sleep(127);
                wait_flags<16>(ws, fA0, fA1, tag);
                if (lane == 0) LDS_CNT_ADD(G_FREE);
            }
            lds_wait<16>(lds + G_FREE, 1);
            kv_issue(0); q_issue(); kv_issue(1);
        }
        asm volatile("s_waitcnt vmcnt(6)" ::: "memory");
        if (lane == 0) LDS_CNT_ADD(G_Q);
        kv_issue(3); kv_issue(4);
        WAIT_BAR(63);
        if (hw == 0) *(LDS3 u32x4*)(lds + lane * 16) = (u32x4){0u, 0u, 0u, 0u};
        if (hw == 3) glds16s(lane * 16, kvb + 31 * 1024, lds0 + 31 * 1024);
        kv_issue(2);
        WAIT_BAR(63);
        if (tid == 0 && bid < 240) set_flag(ws, fG + 2048, tag);
        STAMP(3);
        u32x4 phm; unsigned pht;
        {
            const u16* wp = P.wphi + head * 400 + (r < 20 ? r : 0);
            unsigned e[10];
#pragma unroll
            for (int j = 0; j < 8; ++j) e[j] = wp[(8 * h + j + 4 * (head & 1)) * 20];
            e[8] = wp[((head & 1 ? 0 : 16) + 2 * h) * 20]; e[9] = wp[((head & 1 ? 1 : 17) + 2 * h) * 20];
            const bool cp = bid < 160 && tid < 160;
            const int k0 = cp ? bid * 8 : 0, n = cp ? tid * 4 : 0;
            const u16* wsrc = (k0 < 640 ? P.woa + (size_t)k0 * DM : P.wob + (size_t)(k0 - 640) * DM) + n;
            u32x2 f[8];
#pragma unroll
            for (int j = 0; j < 8; ++j) f[j] = *(const u32x2*)(wsrc + (size_t)j * DM);
            DRAIN();
            if (lane == 0) LDS_CNT_ADD(C_KVL);
            phm = (u32x4){e[0] | (e[1] << 16), e[2] | (e[3] << 16), e[4] | (e[5] << 16), e[6] | (e[7] << 16)}; pht = e[8] | (e[9] << 16);
            if (r >= 20) { phm = (u32x4){0u, 0u, 0u, 0u}; pht = 0u; }
            const unsigned dp = WS_WT + (((n >> 4) * 40 + (k0 >> 5)) * 64 + ((k0 >> 3) & 3) * 16 + (n & 15)) * 16;
            if (cp) {
#pragma unroll
                for (int q = 0; q < 2; ++q) {
                    const u32x4 lo = {(f[0][q] & 0xffffu) | (f[1][q] << 16), (f[2][q] & 0xffffu) | (f[3][q] << 16), (f[4][q] & 0xffffu) | (f[5][q] << 16), (f[6][q] & 0xffffu) | (f[7][q] << 16)};
                    const u32x4 hi = {(f[0][q] >> 16) | (f[1][q] & 0xffff0000u), (f[2][q] >> 16) | (f[3][q] & 0xffff0000u), (f[4][q] >> 16) | (f[5][q] & 0xffff0000u), (f[6][q] >> 16) | (f[7][q] & 0xffff0000u)};
                    ST16(lo, dp + q * 32); ST16(hi, dp + q * 32 + 16);
                }
            }
        }
        const int tokS = qb * 256 + hw * 64, m7 = tokS >> 7, t4 = (tokS >> 5) & 3;
        {
            const unsigned pt3 = (lane >> 1) & 3;
            const unsigned tb = WS_F_G + 2048 + ((qb * 2 + ((lane >> 3) & 1)) * 15 + ((lane & 1) ? nhi : nlo) + (pt3 < 3 ? pt3 : 0) * 5) * 8;
            POLL_ONE(hw == 0, 8, tb, WS_F_SD + (qb * 2 + (lane & 1)) * 8, C_P1);
        }
        const unsigned om = t4 * 1024 + lane * 16, ot = 4096 + (t4 * 32 + r) * 8 + 4 * h;
        u32x4 qf[2][3]; float gS[2];
        {
            const unsigned kb = WS_KV + (NH + head) * 163840 + (2 * (m7 & 7) + (m7 >> 3)) * 10240, qbb = WS_Q + (NH + head) * 81920 + m7 * 5120;
            const unsigned ov = 5120 + t4 * 1280 + h * 320 + (r < 20 ? r : 19) * 16;
            u32x4 e0[2], w0[2], w1[2], eq[2]; unsigned et[2], eqt[2];
#pragma unroll
            for (int i = 0; i < 2; ++i) {
                e0[i] = LD16(kb + om + i * 1024); et[i] = LD4(kb + ot + i * 256); w0[i] = LD16(kb + ov + i * 1280); w1[i] = LD16(kb + ov + i * 1280 + 640);
                eq[i] = LD16(qbb + om + i * 1024); eqt[i] = LD4(qbb + ot + i * 256); gS[i] = __uint_as_float(LD4(WS_G + (tokS + i * 32 + r) * 4));
            }
            u32x4 i0, i1;
#pragma unroll
            for (int j = 0; j < 4; ++j) {
                const int e = r - 8 * h - 2 * j;
                i0[j] = e == 0 ? 0x3f80u : (e == 1 ? 0x3f800000u : 0u);
                i1[j] = e == 16 ? 0x3f80u : (e == 17 ? 0x3f800000u : 0u);
            }
            f32x16 k0 = {}, k1 = {};
#pragma unroll
            for (int i = 0; i < 2; ++i) {
                if (r >= 20) { w0[i] = (u32x4){one, one, one, one}; w1[i] = w0[i]; }
                u32x4 fp, fn, ft;
                feature_map(phm, pht, e0[i], et[i], h, fp, fn, ft);
                f32x16 T0 = MFMA32_16(fp, i0, z16); T0 = MFMA32_16(fn, i1, T0);
                f32x16 T1 = MFMA32_16(ft, i0, z16);
                u32x4 b00, b01, b10, b11;
#pragma unroll
                for (int j = 0; j < 4; ++j) {
                    b00[j] = pk2(T0[2 * j], T0[2 * j + 1]); b01[j] = pk2(T0[8 + 2 * j], T0[9 + 2 * j]);
                    b10[j] = pk2(T1[2 * j], T1[2 * j + 1]); b11[j] = pk2(T1[8 + 2 * j], T1[9 + 2 * j]);
                }
                k0 = MFMA32_16(w0[i], b00, k0); k0 = MFMA32_16(w1[i], b01, k0);
                k1 = MFMA32_16(w0[i], b10, k1); k1 = MFMA32_16(w1[i], b11, k1);
            }
            prog_wait<8>(lds + C_PR, lane, 1);
            *(LDS3 float*)(lds + X_G + (hw * 64 + lane) * 4) = h ? gS[1] : gS[0];
            if (hw * 64 + lane < 144) *(LDS3 u32x4*)(lds + X_KVF + (hw * 64 + lane) * 16) = (u32x4){0u, 0u, 0u, 0u};
            LDS3 char* sp = lds + X_SLOT + hw * 4096;
#pragma unroll
            for (int gq = 0; gq < 3; ++gq) {
                *(LDS3 f32x4*)(sp + gq * 1024 + lane * 16) = (f32x4){k0[4 * gq], k0[4 * gq + 1], k0[4 * gq + 2], k0[4 * gq + 3]};
                if (r < 8) *(LDS3 f32x4*)(sp + 3072 + (gq * 16 + h * 8 + r) * 16) = (f32x4){k1[4 * gq], k1[4 * gq + 1], k1[4 * gq + 2], k1[4 * gq + 3]};
            }
            asm volatile("s_waitcnt vmcnt(0) lgkmcnt(0)" ::: "memory");
            unsigned arr = 0;
            if (lane == 0) arr = LDS_CNT_ADD(C_KV);
            if (__builtin_amdgcn_readfirstlane(arr) == 3) {
                __builtin_amdgcn_fence(__ATOMIC_ACQUIRE, "wavefront");
#pragma unroll
                for (int i = 0; i < 4; ++i) {
                    const int qd = lane + 64 * i;
                    LDS3 const char* s0 = lds + X_SLOT + qd * 16;
                    const f32x4 s = (*(LDS3 const f32x4*)s0 + *(LDS3 const f32x4*)(s0 + 4096)) + (*(LDS3 const f32x4*)(s0 + 8192) + *(LDS3 const f32x4*)(s0 + 12288));
                    if (qd < 240) ST16(__builtin_bit_cast(u32x4, s), WS_KVP + (head * 8 + qb) * 4096 + qd * 16);
                }
                DRAIN();
                if (lane == 0) set_flag(ws, WS_F_KV + (head * 8 + qb) * 8, tag);
            }
#pragma unroll
            for (int i = 0; i < 2; ++i) feature_map(phm, pht, eq[i], eqt[i], h, qf[i][0], qf[i][1], qf[i][2]);
        }
        STAMP(4);
        POLL_ONE(hw == 0, 8, WS_F_KV + (head * 8 + (lane & 7)) * 8, WS_F_KV + (head * 8 + (lane & 7)) * 8, C_P2);
        {
            const int qd = hw * 60 + (lane < 60 ? lane : 59);
            f32x4 pp[8];
#pragma unroll
            for (int p = 0; p < 8; ++p) pp[p] = __builtin_bit_cast(f32x4, LD16(WS_KVP + (head * 8 + p) * 4096 + qd * 16));
            const f32x4 s4 = ((pp[0] + pp[1]) + (pp[2] + pp[3])) + ((pp[4] + pp[5]) + (pp[6] + pp[7]));
            const int gq = qd < 192 ? qd >> 6 : (qd - 192) >> 4, hs = qd < 192 ? (qd >> 5) & 1 : (qd >> 3) & 1, sg = qd < 192 ? qd & 31 : 32 + (qd & 7);
            const unsigned a = pk2(s4[0], s4[1]), b = pk2(s4[2], s4[3]);
            LDS3 u16* kp = (LDS3 u16*)(lds + X_KVF + (8 * gq + 4 * hs) * 96 + sg * 2);
            kp[0] = (u16)a; kp[48] = (u16)(a >> 16); kp[96] = (u16)b; kp[144] = (u16)(b >> 16);
            asm volatile("s_waitcnt lgkmcnt(0)" ::: "memory");
            if (lane == 0) LDS_CNT_ADD(C_KVF);
            lds_wait<8>(lds + C_KVF, 4);
        }
        {
            LDS3 const char* kp = lds + X_KVF + (r < 24 ? r : 23) * 96 + h * 16;
            const u32x4 a0 = *(LDS3 const u32x4*)kp, a1 = *(LDS3 const u32x4*)(kp + 32), a2 = *(LDS3 const u32x4*)(kp + 64);
#pragma unroll
            for (int i = 0; i < 2; ++i) {
                f32x16 d = MFMA32_16(a0, qf[i][0], z16); d = MFMA32_16(a1, qf[i][1], d); d = MFMA32_16(a2, qf[i][2], d);
                const float den = upper_half(d[8]) + 1e-6f;
                float o[12];
#pragma unroll
                for (int j = 0; j < 12; ++j) o[j] = d[j];
                store_o(rs, o, (1.f - gS[i]) / den, tokS + i * 32 + r, 640 + head * 20, h);
            }
            DRAIN();
            unsigned arr = 0;
            if (lane == 0) arr = LDS_CNT_ADD(C_OB);
            if (__builtin_amdgcn_readfirstlane(arr) == 3 && lane == 0) set_flag(ws, WS_F_OB + (qb * 32 + head) * 8, tag);
        }
        STAMP(5);
        f32x4 c[5];
#pragma unroll
        for (int n = 0; n < 5; ++n) c[n] = (f32x4){0.f, 0.f, 0.f, 0.f};
        const char* wsrc = ws + WS_WT + (size_t)ntO * 5 * 40960;
        {
            POLL_ONE(hw == 0, 8, WS_F_OB + ((mtO >> 2) * 32 + r) * 8, WS_F_OB + ((mtO >> 2) * 32 + r) * 8, C_P3);
            const unsigned ab = WS_AO + ((mtO * 4 + hw) * 40 + 20) * 1024 + lane * 16;
            u32x4 af[20];
#pragma unroll
            for (int ks = 0; ks < 20; ++ks) af[ks] = LD16(ab + ks * 1024);
#pragma unroll 1
            for (int t = 0; t < 5; ++t) {
                if (t == 0) prog_wait<8>(lds + C_PR, lane, 4); else if (t == 3) prog_wait<8>(lds + C_PR, lane, 6);
                const int ks = hw + 4 * t;
#pragma unroll
                for (int n = 0; n < 5; ++n) glds16s(lane * 16, wsrc + (n * 40 + 20 + ks) * 1024, lds0 + 20480 + (ks * 5 + n) * 1024);
            }
            DRAIN();
            if (lane == 0) LDS_CNT_ADD(C_BX);
            lds_wait<4>(lds + C_BX, 4);
#pragma unroll
            for (int ks = 0; ks < 20; ++ks)
#pragma unroll
                for (int n = 0; n < 5; ++n) c[n] = MFMA16_32(*(LDS3 const u32x4*)(lds + 20480 + (ks * 5 + n) * 1024 + lane * 16), af[ks], c[n]);
            asm volatile("s_waitcnt lgkmcnt(0)" ::: "memory");
            if (lane == 0) LDS_CNT_ADD(C_BX);
            lds_wait<4>(lds + C_BX, 8);
        }
        STAMP(6);
        prog_wait<4>(lds + C_PR, lane, 7);
#pragma unroll 1
        for (int n = 0; n < 5; ++n)
#pragma unroll
            for (int t = 0; t < 5; ++t) { const int ks = hw + 4 * t; glds16s(lane * 16, wsrc + (n * 40 + ks) * 1024, lds0 + L_WA + (n * 20 + ks) * 1024); }
#pragma unroll
        for (int n = 0; n < 3; ++n) *(LDS3 f32x4*)(lds + X_FIN + hw * 3072 + n * 1024 + lane * 16) = c[n];
        DRAIN();
        if (lane == 0) LDS_CNT_ADD(C_WA);
        prog_wait<4>(lds + C_PR, lane, 8);
#pragma unroll
        for (int n = 3; n < 5; ++n) *(LDS3 f32x4*)(lds + 143360 + hw * 2048 + (n - 3) * 1024 + lane * 16) = c[n];
        asm volatile("s_waitcnt lgkmcnt(0)" ::: "memory");
        if (lane == 0) __hip_atomic_store((LDS3 unsigned*)(lds + C_BP + hw * 4), 1u, __ATOMIC_RELAXED, __HIP_MEMORY_SCOPE_WORKGROUP);
        STAMP(7);
        asm volatile("mega_code_end2:");
    } else {
        const int qs = w & 3, kh = w >> 2;
        const int q0 = qb * 256 + qs * 64;
        {
            unsigned sink;
            if (w == 0 && (WARM_MODE & 1)) {
                const char* pc0; int len, len2;
                asm volatile("s_getpc_b64 %0\nmega_code_mark:\n\ts_mov_b32 %1, mega_code_end-mega_code_mark\n\ts_mov_b32 %2, mega_code_end2-mega_code_mark" : "=s"(pc0), "=s"(len), "=s"(len2));
                len = len > len2 ? len : len2;
                const int o = ((bid >> 3) * 64 + lane) * 64;
                if (o + 4 <= len) asm volatile("global_load_dword %0, %1, %2" : "=v"(sink) : "v"(o), "s"(pc0) : "memory");
            } else if (w >= 1 && w <= 6 && (WARM_MODE & 2)) {
                const unsigned o = ((w - 1) * 64 + lane) * 65536 + 65532;
                if (o < WS_END) asm volatile("global_load_dword %0, %1, %2 sc1" : "=v"(sink) : "v"(o), "s"(ws) : "memory");
            } else if (w == 7 && (WARM_MODE & 2)) {
                if (lane < 40) asm volatile("global_load_dword %0, %1, %2 sc1" : "=v"(sink) : "v"(lane * 65536), "s"(P.out) : "memory");
            }
            asm volatile("s_waitcnt vmcnt(0)" ::: "memory");
        }
        GET_TAG();
        if (bid < 240) {
            const int part = nt / 5, c0 = (nt - part * 5) * 128;
            const int wm = w & 1, wn = w >> 1;
            constexpr int RB = 32768;
            const int xr = wm * 64 + r;
            const unsigned xa = RB + xr * 128 + ((h ^ ((xr >> 1) & 7)) << 4);
            unsigned wt[2];
#pragma unroll
            for (int t = 0; t < 2; ++t) {
                const int q = (lane & 15) >> 2, p = lane & 3, blk = (lane >> 4) & 1, row = 8 * h + 4 * t + q;
                wt[t] = RB + 16384 + row * 256 + (((4 * wn + 2 * blk + (p >> 1)) ^ ((q << 2) | (2 * h + t))) << 4) + 8 * (p & 1);
            }
            auto gbody = [&](auto trc) {
                constexpr bool TR = decltype(trc)::value;
                u32x4 xf[2][8], wf[2][4];
                auto rd = [&](auto bc, int s) {
                    constexpr int B = decltype(bc)::value;
                    LDS3 const char* sb = lds + (s & 3) * 32768;
#pragma unroll
                    for (int ks = 0; ks < 4; ++ks) {
                        xf[B][2 * ks] = *(LDS3 const u32x4*)(sb + (xa ^ (ks * 32)));
                        xf[B][2 * ks + 1] = *(LDS3 const u32x4*)(sb + ((xa + 4096) ^ (ks * 32)));
                        const u32x2 lo = trread(sb + wt[0] + ks * 4096), hi = trread(sb + wt[1] + ks * 4096);
                        wf[B][ks] = (u32x4){lo[0], lo[1], hi[0], hi[1]};
                    }
                };
                f32x16 acc0, acc1;
                auto step = [&](auto bc, int s) {
                    constexpr int B = decltype(bc)::value;
                    if (s == 12) DRAIN();
                    __builtin_amdgcn_s_waitcnt(0xC07F);
                    BAR(); SBAR();
                    rd(std::integral_constant<int, B ^ 1>{}, s + 1);
#pragma unroll
                    for (int ks = 0; ks < 4; ++ks) {
                        if constexpr (TR) { acc0 = MFMA32_16(wf[B][ks], xf[B][2 * ks], acc0); acc1 = MFMA32_16(wf[B][ks], xf[B][2 * ks + 1], acc1); }
                        else              { acc0 = MFMA32_16(xf[B][2 * ks], wf[B][ks], acc0); acc1 = MFMA32_16(xf[B][2 * ks + 1], wf[B][ks], acc1); }
                    }
#pragma unroll
                    for (int i = 0; i < 4; ++i) { __builtin_amdgcn_sched_group_barrier(0x8, 1, 0); __builtin_amdgcn_sched_group_barrier(0x2, 2, 0); __builtin_amdgcn_sched_group_barrier(0x100, 4, 0); }
                    __builtin_amdgcn_sched_group_barrier(0x8, 4, 0);
                    SBAR();
                };
                WAIT_BAR(63);
                rd(std::integral_constant<int, 0>{}, 0);
#pragma unroll 1
                for (int mat = 0; mat < 2; ++mat) {
                    acc0 = (f32x16){}; acc1 = (f32x16){};
#pragma unroll 1
                    for (int kk = 0; kk < 10; kk += 2) {
                        step(std::integral_constant<int, 0>{}, mat * 10 + kk);
                        step(std::integral_constant<int, 1>{}, mat * 10 + kk + 1);
                    }
                    const int cpos = 2 * (mt & 7) + (mt >> 3);
                    if constexpr (TR) {
                        const float sc = (part == 0 && mat == 0) ? QSCALE : 1.f;
                        const unsigned base = part == 0 ? WS_Q + mat * NH * 81920 + mt * 5120 : WS_KV + mat * NH * 163840 + cpos * 10240;
                        const unsigned hs = part == 0 ? 81920 : 163840;
#pragma unroll
                        for (int pr = 0; pr < 2; ++pr) {
                            const int G8 = ((c0 + 32 * wn) >> 3) + 2 * pr + h, pp = G8 / 5, gi = G8 - pp * 5;
                            const unsigned hb0 = base + (2 * pp + (gi > 2)) * hs;
#pragma unroll
                            for (int i = 0; i < 2; ++i) {
                                const f32x16& a = i ? acc1 : acc0;
                                const int tl = wm * 64 + i * 32 + r;
                                const auto s0 = __builtin_amdgcn_permlane32_swap(pk2(a[8 * pr] * sc, a[8 * pr + 1] * sc), pk2(a[8 * pr + 4] * sc, a[8 * pr + 5] * sc), false, false);
                                const auto s1 = __builtin_amdgcn_permlane32_swap(pk2(a[8 * pr + 2] * sc, a[8 * pr + 3] * sc), pk2(a[8 * pr + 6] * sc, a[8 * pr + 7] * sc), false, false);
                                if (gi != 2) { const u32x4 ov = {s0[0], s1[0], s0[1], s1[1]}; ST16(ov, hb0 + (tl >> 5) * 1024 + (gi > 2 ? gi - 3 : gi) * 512 + r * 16); }
                                else { const u32x2 o0 = {s0[0], s1[0]}, o1 = {s0[1], s1[1]}; ST8(o0, hb0 + 4096 + tl * 8); ST8(o1, hb0 + hs + 4096 + tl * 8); }
                            }
                        }
                    } else {
                        const int n = c0 + 32 * wn + r, hd = n / 20, d = n - hd * 20, rho = (hd & 1) ? (d < 4 ? d + 16 : d - 4) : d;
                        const unsigned base = WS_KV + (mat * NH + hd) * 163840 + cpos * 10240 + 5120 + h * 320 + rho * 16;
#pragma unroll
                        for (int i = 0; i < 2; ++i)
#pragma unroll
                            for (int s2 = 0; s2 < 2; ++s2) {
                                const f32x16& a = i ? acc1 : acc0;
                                u32x4 ov = {pk2(a[8 * s2], a[8 * s2 + 1]), pk2(a[8 * s2 + 2], a[8 * s2 + 3]), pk2(a[8 * s2 + 4], a[8 * s2 + 5]), pk2(a[8 * s2 + 6], a[8 * s2 + 7])};
                                ST16(ov, base + (wm * 4 + i * 2 + s2) * 640);
                            }
                    }
                    if (mat == 0) STAMP(1);
                }
            };
            if (part < 2) gbody(std::true_type{}); else gbody(std::false_type{});
        } else {
            const int sj = bid - 240;
            WAIT_BAR(63);
            {
                const u32x4 ga = *(const u32x4*)(P.gw + lane * 8), gb = *(const u32x4*)(P.gw + 512 + (lane & 15) * 8);
                u32x4 xa[16], xb[4];
                const u16* xr = P.x + (size_t)(sj * 128 + w * 16) * DM;
#pragma unroll
                for (int rr = 0; rr < 16; ++rr) xa[rr] = *(const u32x4*)(xr + rr * DM + lane * 8);
#pragma unroll
                for (int i = 0; i < 4; ++i) xb[i] = *(const u32x4*)(xr + (i * 4 + (lane >> 4)) * DM + 512 + (lane & 15) * 8);
                float res = 0.f;
#pragma unroll
                for (int rr = 0; rr < 16; ++rr) {
                    float s = 0.f, s2 = 0.f;
#pragma unroll
                    for (int j = 0; j < 4; ++j) { s += bf_lo(xa[rr][j]) * bf_lo(ga[j]) + bf_hi(xa[rr][j]) * bf_hi(ga[j]); s2 += bf_lo(xb[rr >> 2][j]) * bf_lo(gb[j]) + bf_hi(xb[rr >> 2][j]) * bf_hi(gb[j]); }
                    if ((lane >> 4) == (rr & 3)) s += s2;
#pragma unroll
                    for (int o = 32; o >= 1; o >>= 1) s += __shfl_xor(s, o);
                    if (lane == rr) res = s;
                }
                if (lane < 16) ST4(__float_as_uint(1.f / (1.f + __expf(-res))), WS_G + (sj * 128 + w * 16 + lane) * 4);
            }
            DRAIN();
            unsigned arr = 0;
            if (lane == 0) arr = LDS_CNT_ADD(G_ST);
            if (__builtin_amdgcn_readfirstlane(arr) == 7 && lane == 0) set_flag(ws, WS_F_SD + sj * 8, tag);
        }
        STAMP(2);
        const unsigned hb = lds0 + kh * 10240;
        const unsigned laneK = hb + lane * 16, laneT = hb + 4096 + r * 8 + h * 4, laneV = hb + 5120 + h * 320 + (r < 20 ? r : 19) * 16;
        const u64 vmask = 0x000fffff000fffffull;
        u32x4 v0a = {one, one, one, one}, v0b = v0a, v1 = v0a;
        u32x4 qA, qB, ka; unsigned qtA, qtB, kt;
        {
            lds_wait(lds + G_Q, 4);
            LDS3 const char* qp = lds + L_Q + (qs >> 1) * 5120;
            const unsigned om = (qs & 1) * 2048 + lane * 16, ot = 4096 + ((qs & 1) * 64 + r) * 8 + 4 * h;
            qA = *(LDS3 const u32x4*)(qp + om); qB = *(LDS3 const u32x4*)(qp + om + 1024);
            qtA = *(LDS3 const unsigned*)(qp + ot); qtB = *(LDS3 const unsigned*)(qp + ot + 256);
            DSR128(ka, laneK, 0); DSR32(kt, laneT, 0); DSR128M2(v0a, laneV, 0, v1, laneV, 640);
            asm volatile("s_waitcnt lgkmcnt(0)\n\ts_barrier" : "+v"(ka), "+v"(kt), "+v"(v0a), "+v"(v1), "+v"(qA), "+v"(qB), "+v"(qtA), "+v"(qtB) :: "memory");
        }
        STAMP(5);
        float mA, mB; u32x2 qTA, qTB; f32x16 sA, sB;
        {
            const u32x2 t0 = {kt, 0x3f80u}, a0 = {qtA, 0u}, b0 = {qtB, 0u};
            const f32x16 s = MFMA32_16(ka, qA, z16);
            const f32x16 s0 = MFMA32_8(t0, a0, s);
            f32x16 s1 = MFMA32_16(ka, qB, z16); s1 = MFMA32_8(t0, b0, s1);
            float x0 = s0[0], x1 = s1[0];
#pragma unroll
            for (int i = 1; i < 16; ++i) { x0 = fmaxf(x0, s0[i]); x1 = fmaxf(x1, s1[i]); }
            x0 = -xhalf_max(x0); x1 = -xhalf_max(x1);
            const unsigned hh = pk2(x0, x1);
            const unsigned ll = pk2(x0 - bf_lo(hh), x1 - bf_hi(hh));
            mA = -(bf_lo(hh) + bf_lo(ll)); mB = -(bf_hi(hh) + bf_hi(ll));
            const unsigned sel = h ? ll : hh;
            qTA = (u32x2){qtA, sel & 0xffffu}; qTB = (u32x2){qtB, sel >> 16};
            sA = MFMA32_8(t0, qTA, s);
        }
        f32x16 oA = {}, oB = {};
        u32x4 pa, pb = {0u, 0u, 0u, 0u};
#define EX4(S, i, Pv, j) do { Pv[j] = pk2(ex2(S[i]), ex2(S[i + 1])); Pv[j + 1] = pk2(ex2(S[i + 2]), ex2(S[i + 3])); PIN(Pv); SBAR(); } while (0)
#define KT2 ((u32x2){kt, 0x3f80u})
#define TILE(V0C, V0N, aK1, oK1, aT1, oT1, oV1, aV0, oV0) do { \
            sB = MFMA32_16(ka, qB, z16); SBAR(); DSR128(ka, aK1, oK1); EX4(sA, 0, pa, 0); \
            oB = MFMA32_16(v1, pb, oB); SBAR(); DSR128M2(v1, aV, oV1, V0N, aV0, oV0); EX4(sA, 4, pa, 2); \
            sB = MFMA32_8(KT2, qTB, sB); SBAR(); DSR32(kt, aT1, oT1); EX4(sA, 8, pb, 0); \
            oA = MFMA32_16(V0C, pa, oA); SBAR(); EX4(sA, 12, pb, 2); \
            LWAIT(1); sA = MFMA32_16(ka, qA, z16); SBAR(); EX4(sB, 0, pa, 0); \
            oA = MFMA32_16(v1, pb, oA); SBAR(); EX4(sB, 4, pa, 2); \
            LWAIT(0); sA = MFMA32_8(KT2, qTA, sA); SBAR(); EX4(sB, 8, pb, 0); \
            oB = MFMA32_16(V0C, pa, oB); SBAR(); EX4(sB, 12, pb, 2); \
        } while (0)
        unsigned aK = laneK, aT = laneT, aV = laneV;
#pragma unroll 1
        for (int c = 0; c < 8; ++c) {
            if (c == 1) lds_wait(lds + C_KVL, 4);
            if (c >= 1) {
                unsigned pp;
                asm volatile("ds_write_b32 %1, %2\n\tds_read_b32 %0, %3\n\ts_waitcnt lgkmcnt(0)" : "=&v"(pp) : "v"(lds0 + C_PR + w * 4), "v"(c), "v"(lds0 + C_PR + (w ^ 4) * 4) : "memory");
                const int pc = __builtin_amdgcn_readfirstlane(pp);
                if (pc > c || (pc == c && ((c ^ kh) & 1))) __builtin_amdgcn_s_setprio(1); else __builtin_amdgcn_s_setprio(0);
            } else if (kh) __builtin_amdgcn_s_setprio(1);
            const unsigned nx = (c < 7 ? c + 1 : 7) * 20480;
            const unsigned nK = laneK + nx, nT = laneT + nx, nV = laneV + nx;
            SBAR();
            TILE(v0a, v0b, aK, 1024, aT, 256, 640,  aV, 1280);
            TILE(v0b, v0a, aK, 2048, aT, 512, 1920, aV, 2560);
            if (c == 0) { WAIT_BAR(0); SBAR(); }
            TILE(v0a, v0b, aK, 3072, aT, 768, 3200, aV, 3840);
            TILE(v0b, v0a, nK, 0,    nT, 0,   4480, nV, 0);
            aK = nK; aT = nT; aV = nV;
        }
        oB = MFMA32_16(v1, pb, oB);
#undef TILE
#undef EX4
        if (lane == 0) __hip_atomic_store((LDS3 unsigned*)(lds + C_PR + w * 4), 8u, __ATOMIC_RELAXED, __HIP_MEMORY_SCOPE_WORKGROUP);
        STAMP(6);
        {
            float chk = 0.f;
#pragma unroll
            for (int i = 0; i < 12; ++i) chk += fabsf(oA[i]) * 0.f + fabsf(oB[i]) * 0.f;
            bool bad = !(chk == 0.f);
            if (h) bad = bad || !(oA[8] >= 0x1p-60f && oA[8] <= 0x1p60f && oB[8] >= 0x1p-60f && oB[8] <= 0x1p60f);
            if (__builtin_expect(__any(bad), 0)) {
                mA = -INFINITY; mB = -INFINITY;
#pragma unroll
                for (int i = 0; i < 16; ++i) { oA[i] = 0.f; oB[i] = 0.f; }
                const u32x2 a0 = {qtA, 0u}, b0 = {qtB, 0u};
#pragma unroll 1
                for (int t = 0; t < 32; ++t) {
                    const unsigned kb = WS_KV + head * 163840 + (2 * (t >> 2) + kh) * 10240, t4 = t & 3;
                    const u32x4 k0 = LD16(kb + t4 * 1024 + lane * 16);
                    const u32x2 t0 = {LD4(kb + 4096 + (t4 * 32 + r) * 8 + 4 * h), 0u};
                    u32x4 w0 = LD16(kb + 5120 + t4 * 1280 + h * 320 + (r < 20 ? r : 19) * 16), w1 = LD16(kb + 5120 + t4 * 1280 + 640 + h * 320 + (r < 20 ? r : 19) * 16);
                    if (r >= 20) { w0 = (u32x4){one, one, one, one}; w1 = w0; }
                    f32x16 s0 = MFMA32_16(k0, qA, z16); s0 = MFMA32_8(t0, a0, s0);
                    f32x16 s1 = MFMA32_16(k0, qB, z16); s1 = MFMA32_8(t0, b0, s1);
                    float x0 = s0[0], x1 = s1[0];
#pragma unroll
                    for (int i = 1; i < 16; ++i) { x0 = fmaxf(x0, s0[i]); x1 = fmaxf(x1, s1[i]); }
                    x0 = fmaxf(xhalf_max(x0), mA); x1 = fmaxf(xhalf_max(x1), mB);
                    const float g0 = (mA == x0) ? 1.f : ex2(mA - x0), g1 = (mB == x1) ? 1.f : ex2(mB - x1);
                    mA = x0; mB = x1;
#pragma unroll
                    for (int i = 0; i < 16; ++i) { oA[i] *= g0; oB[i] *= g1; }
#pragma unroll
                    for (int i = 0; i < 4; ++i) { pa[i] = pk2(ex2(s0[2 * i] - x0), ex2(s0[2 * i + 1] - x0)); pb[i] = pk2(ex2(s0[8 + 2 * i] - x0), ex2(s0[9 + 2 * i] - x0)); }
                    oA = MFMA32_16(w0, pa, oA); oA = MFMA32_16(w1, pb, oA);
#pragma unroll
                    for (int i = 0; i < 4; ++i) { pa[i] = pk2(ex2(s1[2 * i] - x1), ex2(s1[2 * i + 1] - x1)); pb[i] = pk2(ex2(s1[8 + 2 * i] - x1), ex2(s1[9 + 2 * i] - x1)); }
                    oB = MFMA32_16(w0, pa, oB); oB = MFMA32_16(w1, pb, oB);
                }
            }
        }
        lds_wait<4>(lds + C_BX, 8);
        const float gO = *(LDS3 const float*)(lds + X_G + (qs * 64 + kh * 32 + r) * 4);
        {
            LDS3 char* xp = lds + X_MRG + w * 2816;
            const f32x16& s = kh ? oA : oB;
            *(LDS3 f32x4*)(xp + lane * 16) = (f32x4){s[0], s[1], s[2], s[3]};
            *(LDS3 f32x4*)(xp + 1024 + lane * 16) = (f32x4){s[4], s[5], s[6], s[7]};
            if (!h) *(LDS3 f32x4*)(xp + 2048 + r * 16) = (f32x4){s[8], s[9], s[10], s[11]};
            else *(LDS3 f32x2*)(xp + 2560 + r * 8) = (f32x2){s[8], kh ? mA : mB};
            asm volatile("s_waitcnt lgkmcnt(0)" ::: "memory");
            if (lane == 0) __hip_atomic_store((LDS3 unsigned*)(lds + C_MRG + w * 4), 1u, __ATOMIC_RELAXED, __HIP_MEMORY_SCOPE_WORKGROUP);
        }
        lds_wait<2>(lds + C_MRG + (w ^ 4) * 4, 1);
        bool lastw;
        {
            LDS3 const char* xp = lds + X_MRG + (w ^ 4) * 2816;
            const f32x4 p0 = *(LDS3 const f32x4*)(xp + lane * 16), p1 = *(LDS3 const f32x4*)(xp + 1024 + lane * 16), p2 = *(LDS3 const f32x4*)(xp + 2048 + r * 16);
            const f32x2 lm = *(LDS3 const f32x2*)(xp + 2560 + r * 8);
            const f32x16& s = kh ? oB : oA;
            const float mo = kh ? mB : mA, mp = lm[1];
            const float mx = fmaxf(mo, mp);
            const float fo = (mo == mx) ? 1.f : ex2(mo - mx), fq = (mp == mx) ? 1.f : ex2(mp - mx);
            float o[12];
#pragma unroll
            for (int i = 0; i < 4; ++i) { o[i] = s[i] * fo + p0[i] * fq; o[4 + i] = s[4 + i] * fo + p1[i] * fq; o[8 + i] = s[8 + i] * fo + p2[i] * fq; }
            const float l = upper_half(s[8]) * fo + lm[0] * fq;
            store_o(rs, o, gO / l, q0 + kh * 32 + r, head * 20, h);
            DRAIN();
            unsigned arr = 0;
            if (lane == 0) arr = LDS_CNT_ADD(C_OA);
            lastw = __builtin_amdgcn_readfirstlane(arr) == 7;
            if (lastw && lane == 0) set_flag(ws, WS_F_OA + (qb * 32 + head) * 8, tag);
        }
        STAMP(7);
        {
            const int c16 = lane & 15, g = lane >> 4, tt = w & 3, k0 = kh * 10;
            POLL_ONE(lastw, 4, WS_F_OA + ((mtO >> 2) * 32 + r) * 8, WS_F_OA + ((mtO >> 2) * 32 + r) * 8, C_P4);
            STAMP(9);
            const unsigned ab = WS_AO + ((mtO * 4 + tt) * 40 + k0) * 1024 + lane * 16;
            u32x4 af[10];
#pragma unroll
            for (int ks = 0; ks < 10; ++ks) af[ks] = LD16(ab + ks * 1024);
            lds_wait(lds + C_WA, 4);
            lds_wait(lds + C_OA, 8);
            f32x4 c[5];
#pragma unroll
            for (int n = 0; n < 5; ++n) c[n] = (f32x4){0.f, 0.f, 0.f, 0.f};
#pragma unroll
            for (int ks = 0; ks < 10; ++ks)
#pragma unroll
                for (int n = 0; n < 5; ++n) c[n] = MFMA16_32(*(LDS3 const u32x4*)(lds + L_WA + (n * 20 + k0 + ks) * 1024 + lane * 16), af[ks], c[n]);
            LDS3 char* xp = lds + X_MRG + tt * 5120 + lane * 16;
            if (w < 4) { *(LDS3 f32x4*)(xp + 3072) = c[3]; *(LDS3 f32x4*)(xp + 4096) = c[4]; }
            else { *(LDS3 f32x4*)(xp) = c[0]; *(LDS3 f32x4*)(xp + 1024) = c[1]; *(LDS3 f32x4*)(xp + 2048) = c[2]; }
            asm volatile("s_waitcnt lgkmcnt(0)" ::: "memory");
            if (lane == 0) __hip_atomic_store((LDS3 unsigned*)(lds + C_FIN + w * 4), 1u, __ATOMIC_RELAXED, __HIP_MEMORY_SCOPE_WORKGROUP);
            lds_wait(lds + C_FIN + (w ^ 4) * 4, 1);
            lds_wait(lds + C_BP + tt * 4, 1);
            u16* op = P.out + (size_t)(mtO * 64 + tt * 16 + c16) * DM + ntO * 80 + g * 4;
#pragma unroll
            for (int n = 0; n < 5; ++n) {
                if ((w < 4) == (n < 3)) {
                    const f32x4 bp = *(LDS3 const f32x4*)(n < 3 ? lds + X_FIN + tt * 3072 + n * 1024 + lane * 16 : lds + 143360 + tt * 2048 + (n - 3) * 1024 + lane * 16);
                    const f32x4 s = c[n] + *(LDS3 const f32x4*)(xp + n * 1024) + bp;
                    *(u32x2*)(op + n * 16) = (u32x2){pk2(s[0], s[1]), pk2(s[2], s[3])};
                }
            }
            if (bid == 0 && mtid == 0) set_flag(ws, WS_EPOCH, tag);
        }
        STAMP(10);
        asm volatile("mega_code_end:");
    }
}

extern "C" void kernel_launch(void* const* d_in, const int* in_sizes, int n_in, void* d_out, int out_size, void* d_ws, size_t ws_size,
                              hipStream_t stream) {
    Params P;
    P.x = (const u16*)d_in[0]; P.wa = (const u16*)d_in[1]; P.woa = (const u16*)d_in[2]; P.wb = (const u16*)d_in[4];
    P.wphi = (const u16*)d_in[5]; P.wob = (const u16*)d_in[6]; P.gw = (const u16*)d_in[7]; P.ws = (char*)d_ws; P.out = (u16*)d_out;
    mega<<<256, 768, 0, stream>>>(P);
}
```

```cpp
#include <hip/hip_runtime.h>
#include <hip/hip_bf16.h>
#include <stdint.h>
#include <type_traits>

typedef __attribute__((ext_vector_type(8))) short bf16x8;
typedef __attribute__((ext_vector_type(4))) short bf16x4;
typedef __attribute__((ext_vector_type(16))) float f32x16;
typedef __attribute__((ext_vector_type(4))) float f32x4;
typedef __attribute__((ext_vector_type(2))) float f32x2;
typedef __attribute__((ext_vector_type(4))) unsigned u32x4;
typedef __attribute__((ext_vector_type(2))) unsigned u32x2;
typedef __attribute__((ext_vector_type(2))) __bf16 bf2_t;
typedef unsigned short u16;
typedef unsigned long long u64;
#define LDS3 __attribute__((address_space(3)))
typedef __attribute__((address_space(1))) u64 gu64;
#define RLX_AGENT __ATOMIC_RELAXED, __HIP_MEMORY_SCOPE_AGENT

#define PRIO_MODE 3
#define WARM_MODE 1
#define PRE_SLEEP 2
constexpr int SEQ = 2048, DM = 640, NH = 32;
constexpr float LOG2E = 1.4426950408889634f;
constexpr float QSCALE = 0.22360679774997896f * LOG2E;

constexpr unsigned WS_EPOCH = 0;
constexpr unsigned WS_F_G   = 256;
constexpr unsigned WS_F_SD  = WS_F_G + 4096;
constexpr unsigned WS_F_KV  = WS_F_SD + 256;
constexpr unsigned WS_F_OB  = WS_F_KV + 2048;
constexpr unsigned WS_F_OA  = WS_F_OB + 2048;
constexpr unsigned WS_G     = 16384;
constexpr unsigned WS_Q     = 32768;
constexpr unsigned WS_KV    = WS_Q + 2 * NH * 81920;
constexpr unsigned WS_KVP   = WS_KV + 2 * NH * 163840;
constexpr unsigned WS_AO    = WS_KVP + NH * 8 * 4096;
constexpr unsigned WS_WT    = WS_AO + SEQ * 1280 * 2;
constexpr unsigned WS_END   = WS_WT + 1280 * 640 * 2;

#define STAMP(i)

__device__ __forceinline__ unsigned pk2(float a, float b) { f32x2 v = {a, b}; bf2_t r = __builtin_convertvector(v, bf2_t); return __builtin_bit_cast(unsigned, r); }
__device__ __forceinline__ float bf_lo(unsigned u) { return __uint_as_float(u << 16); }
__device__ __forceinline__ float bf_hi(unsigned u) { return __uint_as_float(u & 0xffff0000u); }
__device__ __forceinline__ float ex2(float x) { return __builtin_amdgcn_exp2f(x); }
#define MFMA32_16(a, b, c) __builtin_amdgcn_mfma_f32_32x32x16_bf16(__builtin_bit_cast(bf16x8, a), __builtin_bit_cast(bf16x8, b), c, 0, 0, 0)
#define MFMA32_8(a, b, c) __builtin_amdgcn_mfma_f32_32x32x8bf16_1k(__builtin_bit_cast(bf16x4, a), __builtin_bit_cast(bf16x4, b), c, 0, 0, 0)
#define MFMA16_32(a, b, c) __builtin_amdgcn_mfma_f32_16x16x32_bf16(__builtin_bit_cast(bf16x8, a), __builtin_bit_cast(bf16x8, b), c, 0, 0, 0)

__device__ __forceinline__ void glds16(unsigned voff, const void* sbase, unsigned lds_base) {
    unsigned sv;
    asm volatile("s_mov_b32 %0, m0\n\ts_mov_b32 m0, %3\n\ts_nop 0\n\tglobal_load_lds_dwordx4 %1, %2\n\ts_mov_b32 m0, %0"
                 : "=&s"(sv) : "v"(voff), "s"(sbase), "s"(__builtin_amdgcn_readfirstlane(lds_base)) : "memory");
}
__device__ __forceinline__ void glds16s(unsigned voff, const void* sbase, unsigned lds_base) {
    unsigned sv;
    asm volatile("s_mov_b32 %0, m0\n\ts_mov_b32 m0, %3\n\ts_nop 0\n\tglobal_load_lds_dwordx4 %1, %2 sc1\n\ts_mov_b32 m0, %0"
                 : "=&s"(sv) : "v"(voff), "s"(sbase), "s"(__builtin_amdgcn_readfirstlane(lds_base)) : "memory");
}
__device__ __forceinline__ void glds4s(unsigned voff, const void* sbase, unsigned lds_base) {
    unsigned sv;
    asm volatile("s_mov_b32 %0, m0\n\ts_mov_b32 m0, %3\n\ts_nop 0\n\tglobal_load_lds_dword %1, %2 sc1\n\ts_mov_b32 m0, %0"
                 : "=&s"(sv) : "v"(voff), "s"(sbase), "s"(__builtin_amdgcn_readfirstlane(lds_base)) : "memory");
}
typedef short v4i16_t __attribute__((ext_vector_type(4)));
__device__ __forceinline__ u32x2 trread(LDS3 const char* p) {
    return __builtin_bit_cast(u32x2, __builtin_amdgcn_ds_read_tr16_b64_v4i16((LDS3 v4i16_t*)p));
}
#define WAIT_BAR(N) asm volatile("s_waitcnt vmcnt(" #N ") lgkmcnt(0)\n\ts_barrier" ::: "memory")
#define DRAIN() asm volatile("s_waitcnt vmcnt(0)" ::: "memory")
#define SBAR() __builtin_amdgcn_sched_barrier(0)
#define PIN(x) asm volatile("" : "+v"(x))

__device__ __forceinline__ float xhalf_max(float x) {
    auto r = __builtin_amdgcn_permlane32_swap(__float_as_uint(x), __float_as_uint(x), false, false);
    return fmaxf(__uint_as_float(r[0]), __uint_as_float(r[1]));
}
__device__ __forceinline__ float xhalf_sum(float x) {
    auto r = __builtin_amdgcn_permlane32_swap(__float_as_uint(x), __float_as_uint(x), false, false);
    return __uint_as_float(r[0]) + __uint_as_float(r[1]);
}
__device__ __forceinline__ float upper_half(float x) {
    auto r = __builtin_amdgcn_permlane32_swap(__float_as_uint(x), __float_as_uint(x), false, false);
    return __uint_as_float(r[1]);
}

typedef __amdgpu_buffer_rsrc_t rsrc_t;
#define ST8(v, off)  __builtin_amdgcn_raw_buffer_store_b64(v, rs, (int)(off), 0, 16)
#define ST16(v, off) __builtin_amdgcn_raw_buffer_store_b128(v, rs, (int)(off), 0, 16)
#define ST4(v, off)  __builtin_amdgcn_raw_buffer_store_b32(v, rs, (int)(off), 0, 16)
#define LD16(off) __builtin_amdgcn_raw_buffer_load_b128(rs, (int)(off), 0, 16)
#define LD4(off)  __builtin_amdgcn_raw_buffer_load_b32(rs, (int)(off), 0, 16)

template <int SLP = 2>
__device__ __forceinline__ void wait_flags(const char* ws, unsigned off0, unsigned off1, u64 tag) {
    gu64* p0 = (gu64*)(ws + off0); gu64* p1 = (gu64*)(ws + off1);
#pragma unroll 1
    for (unsigned spins = 0; spins < (1u << 16); ++spins) {
        const u64 a = __hip_atomic_load(p0, RLX_AGENT), b = __hip_atomic_load(p1, RLX_AGENT);
        if (__all(a == tag && b == tag)) break;
        __builtin_amdgcn_s_sleep(SLP);
    }
    __builtin_amdgcn_fence(__ATOMIC_ACQUIRE, "wavefront");
}
__device__ __forceinline__ void set_flag(char* ws, unsigned off, u64 tag) { __hip_atomic_store((gu64*)(ws + off), tag, RLX_AGENT); }

struct Params { const u16* x; const u16* wa; const u16* wb; const u16* wphi; const u16* woa; const u16* wob; const u16* gw; char* ws; u16* out; };

__device__ __forceinline__ void feature_map(const u32x4& pm, unsigned pt, const u32x4& tm, unsigned tt, int h, u32x4& fp, u32x4& fn, u32x4& ft) {
    const f32x16 z16 = {};
    const u32x2 a8 = {pt, 0u}, b8 = {tt, 0u};
    f32x16 s = MFMA32_16(pm, tm, z16); s = MFMA32_8(a8, b8, s);
    float v[12];
#pragma unroll
    for (int i = 0; i < 12; i += 2) { const unsigned u = pk2(s[i], s[i + 1]); v[i] = bf_lo(u) * LOG2E; v[i + 1] = bf_hi(u) * LOG2E; }
    float mx = v[0], mn = v[0];
#pragma unroll
    for (int i = 1; i < 8; ++i) { mx = fmaxf(mx, v[i]); mn = fminf(mn, v[i]); }
    const float tx = fmaxf(fmaxf(v[8], v[9]), fmaxf(v[10], v[11])), tn = fminf(fminf(v[8], v[9]), fminf(v[10], v[11]));
    if (!h) { mx = fmaxf(mx, tx); mn = fminf(mn, tn); }
    mx = xhalf_max(mx); mn = -xhalf_max(-mn);
    float ep[12], en[12], sp = 0.f, sn = 0.f;
#pragma unroll
    for (int i = 0; i < 12; ++i) {
        ep[i] = ex2(v[i] - mx); en[i] = ex2(mn - v[i]);
        if (i >= 8 && h) { ep[i] = 0.f; en[i] = 0.f; }
        sp += ep[i]; sn += en[i];
    }
    sp = __builtin_amdgcn_rcpf(xhalf_sum(sp)); sn = __builtin_amdgcn_rcpf(xhalf_sum(sn));
#pragma unroll
    for (int i = 0; i < 4; ++i) { fp[i] = pk2(ep[2 * i] * sp, ep[2 * i + 1] * sp); fn[i] = pk2(en[2 * i] * sn, en[2 * i + 1] * sn); }
    ft = (u32x4){pk2(ep[8] * sp, ep[9] * sp), pk2(ep[10] * sp, ep[11] * sp), pk2(en[8] * sn, en[9] * sn), pk2(en[10] * sn, en[11] * sn)};
}

__device__ __forceinline__ void store_o(rsrc_t rs, const float (&o)[12], float sc, int tok, int kbase, int h) {
#pragma unroll
    for (int gq = 0; gq < 3; ++gq) {
        const int d = 8 * gq + 4 * h;
        if (d < 20) {
            const int k = kbase + ((kbase & 4) ? (d == 16 ? 0 : d + 4) : d);
            u32x2 ov = {pk2(o[4 * gq] * sc, o[4 * gq + 1] * sc), pk2(o[4 * gq + 2] * sc, o[4 * gq + 3] * sc)};
            ST8(ov, WS_AO + (((tok >> 4) * 40 + (k >> 5)) * 64 + ((k >> 3) & 3) * 16 + (tok & 15)) * 16 + (k & 7) * 2);
        }
    }
}

#define DSR128(dst, addr, off) asm volatile("ds_read_b128 %0, %1 offset:%2" : "=v"(dst) : "v"(addr), "i"(off))
#define DSR32(dst, addr, off) asm volatile("ds_read_b32 %0, %1 offset:%2" : "=v"(dst) : "v"(addr), "i"(off))
#define DSR128M2(d0, a0, o0, d1, a1, o1) asm volatile("s_mov_b64 exec, %6\n\tds_read_b128 %0, %2 offset:%3\n\tds_read_b128 %1, %4 offset:%5\n\ts_mov_b64 exec, -1" \
                                                      : "+v"(d0), "+v"(d1) : "v"(a0), "i"(o0), "v"(a1), "i"(o1), "s"(vmask))
#define LWAIT(n) do { asm volatile("s_waitcnt lgkmcnt(" #n ")" ::: "memory"); SBAR(); } while (0)

#define BAR() asm volatile("s_barrier" ::: "memory")
#define LDS_CNT_ADD(off) __hip_atomic_fetch_add((LDS3 unsigned*)(lds + (off)), 1u, __ATOMIC_RELAXED, __HIP_MEMORY_SCOPE_WORKGROUP)

template <int SLP = 1>
__device__ __forceinline__ void lds_wait(LDS3 char* p, unsigned want) {
#pragma unroll 1
    for (unsigned spins = 0; spins < (1u << 20); ++spins) {
        if ((unsigned)__builtin_amdgcn_readfirstlane(__hip_atomic_load((LDS3 unsigned*)p, __ATOMIC_RELAXED, __HIP_MEMORY_SCOPE_WORKGROUP)) >= want) break;
        __builtin_amdgcn_s_sleep(SLP);
    }
    __builtin_amdgcn_fence(__ATOMIC_ACQUIRE, "wavefront");
}

template <int SLP>
__device__ __forceinline__ void prog_wait(LDS3 char* p, int lane, unsigned want) {
#pragma unroll 1
    for (unsigned spins = 0; spins < (1u << 20); ++spins) {
        if (__all(__hip_atomic_load((LDS3 unsigned*)p + (lane & 7), __ATOMIC_RELAXED, __HIP_MEMORY_SCOPE_WORKGROUP) >= want)) break;
        __builtin_amdgcn_s_sleep(SLP);
    }
    __builtin_amdgcn_fence(__ATOMIC_ACQUIRE, "wavefront");
}
#define POLL_ONE(cond, SLP, o0, o1, word) do { if (cond) { wait_flags<SLP>(ws, o0, o1, tag); \
        if (lane == 0) __hip_atomic_store((LDS3 unsigned*)(lds + (word)), 1u, __ATOMIC_RELAXED, __HIP_MEMORY_SCOPE_WORKGROUP); } else lds_wait<SLP>(lds + (word), 1); } while (0)
constexpr int C_KV = 0, C_KVF = 4, C_OB = 8, C_PROG = 12, C_WA = 16, C_OA = 20, C_MRG = 32  , C_FIN = 64  , C_BP = 96  ;
constexpr int C_PROG2 = 24, C_BX = 28, C_P1 = 112, C_P2 = 116, C_P3 = 120, C_P4 = 124, C_PR = 128  ;
constexpr int G_W = 31744;
constexpr int G_FULL = G_W, G_FREE = G_W + 16, G_Q = G_W + 32, G_KV0 = G_W + 36, G_ST = G_W + 40;
constexpr int C_KVL = 160, C_P5 = 164, X_G = 256;
constexpr int X_KVF = 1280, X_SLOT = 4096;
constexpr int X_MRG = 1280;
constexpr int L_WA = 24576;
constexpr int L_Q = 65536;
constexpr int X_FIN = 126976;

__global__ __launch_bounds__(768) void mega(Params P) {
    __shared__ __attribute__((aligned(16))) char smem[163840];
    LDS3 char* lds = (LDS3 char*)smem;
    char* ws = P.ws;
    const int tid = threadIdx.x, lane = tid & 63, r = lane & 31, h = lane >> 5;
    const int hw = __builtin_amdgcn_readfirstlane(tid >> 6);
    const bool helper = hw < 4;
    const int w = hw - 4, mtid = tid - 256;
    const int bid = blockIdx.x;
    const unsigned lds0 = (unsigned)(uintptr_t)smem;
    const rsrc_t rs = __builtin_amdgcn_make_buffer_rsrc(ws, 0, WS_END, 0x00020000);
    STAMP(0);
    u64 tag = 0;
#define GET_TAG() do { asm volatile("s_load_dwordx2 %0, %1, 0x0\n\ts_waitcnt lgkmcnt(0)" : "=s"(tag) : "s"(ws) : "memory"); tag += 1; } while (0)
    const int head = (bid & 7) * 4 + ((bid >> 3) & 3), qb = bid >> 5;
    const int nlo = (head * 20) >> 7, nhi = (head * 20 + 19) >> 7;
    const int mtO = (bid & 7) * 4 + ((bid >> 3) & 3), ntO = bid >> 5;
    const int lin = (bid & 7) * 30 + (bid >> 3), lh = lin >= 120, lrem = lin - 120 * lh;
    const int mt = lh * 8 + (lrem & 7), nt = lrem >> 3;
    const unsigned fG = WS_F_G + (mt * 15 + nt) * 8;
    const unsigned one = (r == 20) ? 0x3f803f80u : 0u;
    const f32x16 z16 = {};

    if (helper) {
        const unsigned fA0 = WS_F_G + ((lane & 15) * 15 + (lane < 32 ? 5 : 10) + ((lane & 16) ? nhi : nlo)) * 8;
        const unsigned fA1 = WS_F_G + ((qb * 2 + (lane & 1)) * 15 + ((lane & 2) ? nhi : nlo)) * 8;
        __builtin_amdgcn_s_setprio(3);
        const char* kvb = ws + WS_KV + head * 163840;
        auto kv_issue = [&](int grp) {
#pragma unroll
            for (int t = 0; t < 8; ++t) { int pc = grp * 32 + hw + 4 * t; if (pc == 31) pc = 30; glds16s(lane * 16, kvb + pc * 1024, lds0 + pc * 1024); }
        };
        auto q_issue = [&]() {
            const char* qbase = ws + WS_Q + head * 81920 + qb * 10240;
#pragma unroll
            for (int t = 0; t < 3; ++t) { const int pc = hw + 4 * t < 10 ? hw + 4 * t : 9; glds16s(lane * 16, qbase + pc * 1024, lds0 + L_Q + pc * 1024); }
        };
        if (bid < 240) {
            const int rot = (nt * 3 + mt * 5 + (mt >> 1)) % 10;
            const unsigned xo = (mt * 128 + 8 * hw + (lane >> 3)) * 1280 + (((lane & 7) ^ ((4 * hw + (lane >> 4)) & 7)) << 4);
            const unsigned wo = (4 * hw + (lane >> 4)) * 3840 + nt * 256 + (((lane & 15) ^ (((lane >> 4) << 2) | hw)) << 4);
            const unsigned dX = lds0 + 32768 + hw * 1024;
            auto issue = [&](int s) {
                int kt = (s >= 10 ? s - 10 : s) + rot; if (kt >= 10) kt -= 10;
                const char* xb = (const char*)P.x + kt * 128;
                const char* wb = (const char*)(s >= 10 ? P.wb : P.wa) + (size_t)kt * (64 * 3840);
                const unsigned d = dX + (s & 3) * 32768;
#pragma unroll
                for (int i = 0; i < 4; ++i) { glds16(xo, xb + i * 40960, d + i * 4096); glds16(wo, wb + i * 61440, d + 16384 + i * 4096); }
            };
#pragma unroll 1
            for (int n = 0; n < 20; ++n) {
                if (n == 2) { if (hw == 0 && lane < 16) *(LDS3 unsigned*)(lds + G_W + lane * 4) = 0u; GET_TAG(); WAIT_BAR(8); }
                else if (n == 3) { WAIT_BAR(8); }
                else if (n >= 5) { WAIT_BAR(16); }
                if (n == 16 && tid == 0) set_flag(ws, fG, tag);
                if (n == 19 && hw == 0) {
                    glds4s(fA0, ws, lds0); glds4s(fA0 + 4, ws, lds0 + 256); glds4s(fA1, ws, lds0 + 512); glds4s(fA1 + 4, ws, lds0 + 768);
                }
                issue(n);
            }
            WAIT_BAR(16);
            asm volatile("s_waitcnt vmcnt(8)" ::: "memory");
            if (hw == 0) {
                LDS3 const unsigned* f = (LDS3 const unsigned*)lds + lane;
                const unsigned tl = (unsigned)tag, th = (unsigned)(tag >> 32);
                if (!__all(f[0] == tl && f[64] == th && f[128] == tl && f[192] == th)) wait_flags<8>(ws, fA0, fA1, tag);
            }
            WAIT_BAR(63);
            kv_issue(0); q_issue();
            WAIT_BAR(11);
            kv_issue(1);
            WAIT_BAR(63);
        } else {
            if (hw == 0 && lane < 16) *(LDS3 unsigned*)(lds + G_W + lane * 4) = 0u;
            GET_TAG();
            WAIT_BAR(63);
            if (hw == 0) {
#pragma unroll 1
                for (int t = 0; t < PRE_SLEEP; ++t) __builtin_amdgcn_s_sleep(127);
                wait_flags<16>(ws, fA0, fA1, tag);
                if (lane == 0) LDS_CNT_ADD(G_FREE);
            }
            lds_wait<16>(lds + G_FREE, 1);
            kv_issue(0); q_issue(); kv_issue(1);
        }
        asm volatile("s_waitcnt vmcnt(6)" ::: "memory");
        if (lane == 0) LDS_CNT_ADD(G_Q);
        kv_issue(3); kv_issue(4);
        WAIT_BAR(63);
        if (hw == 0) *(LDS3 u32x4*)(lds + lane * 16) = (u32x4){0u, 0u, 0u, 0u};
        if (hw == 3) glds16s(lane * 16, kvb + 31 * 1024, lds0 + 31 * 1024);
        kv_issue(2);
        WAIT_BAR(63);
        if (tid == 0 && bid < 240) set_flag(ws, fG + 2048, tag);
        STAMP(3);
        u32x4 phm; unsigned pht;
        {
            const u16* wp = P.wphi + head * 400 + (r < 20 ? r : 0);
            unsigned e[10];
#pragma unroll
            for (int j = 0; j < 8; ++j) e[j] = wp[(8 * h + j + 4 * (head & 1)) * 20];
            e[8] = wp[((head & 1 ? 0 : 16) + 2 * h) * 20]; e[9] = wp[((head & 1 ? 1 : 17) + 2 * h) * 20];
            const bool cp = bid < 160 && tid < 160;
            const int k0 = cp ? bid * 8 : 0, n = cp ? tid * 4 : 0;
            const u16* wsrc = (k0 < 640 ? P.woa + (size_t)k0 * DM : P.wob + (size_t)(k0 - 640) * DM) + n;
            u32x2 f[8];
#pragma unroll
            for (int j = 0; j < 8; ++j) f[j] = *(const u32x2*)(wsrc + (size_t)j * DM);
            DRAIN();
            if (lane == 0) LDS_CNT_ADD(C_KVL);
            phm = (u32x4){e[0] | (e[1] << 16), e[2] | (e[3] << 16), e[4] | (e[5] << 16), e[6] | (e[7] << 16)}; pht = e[8] | (e[9] << 16);
            if (r >= 20) { phm = (u32x4){0u, 0u, 0u, 0u}; pht = 0u; }
            const unsigned dp = WS_WT + (((n >> 4) * 40 + (k0 >> 5)) * 64 + ((k0 >> 3) & 3) * 16 + (n & 15)) * 16;
            if (cp) {
#pragma unroll
                for (int q = 0; q < 2; ++q) {
                    const u32x4 lo = {(f[0][q] & 0xffffu) | (f[1][q] << 16), (f[2][q] & 0xffffu) | (f[3][q] << 16), (f[4][q] & 0xffffu) | (f[5][q] << 16), (f[6][q] & 0xffffu) | (f[7][q] << 16)};
                    const u32x4 hi = {(f[0][q] >> 16) | (f[1][q] & 0xffff0000u), (f[2][q] >> 16) | (f[3][q] & 0xffff0000u), (f[4][q] >> 16) | (f[5][q] & 0xffff0000u), (f[6][q] >> 16) | (f[7][q] & 0xffff0000u)};
                    ST16(lo, dp + q * 32); ST16(hi, dp + q * 32 + 16);
                }
            }
        }
        const int tokS = qb * 256 + hw * 64, m7 = tokS >> 7, t4 = (tokS >> 5) & 3;
        {
            const unsigned pt3 = (lane >> 1) & 3;
            const unsigned tb = WS_F_G + 2048 + ((qb * 2 + ((lane >> 3) & 1)) * 15 + ((lane & 1) ? nhi : nlo) + (pt3 < 3 ? pt3 : 0) * 5) * 8;
            POLL_ONE(hw == 0, 4, tb, WS_F_SD + (qb * 2 + (lane & 1)) * 8, C_P1);
        }
        const unsigned om = t4 * 1024 + lane * 16, ot = 4096 + (t4 * 32 + r) * 8 + 4 * h;
        u32x4 qf[2][3]; float gS[2];
        {
            const unsigned kb = WS_KV + (NH + head) * 163840 + (2 * (m7 & 7) + (m7 >> 3)) * 10240, qbb = WS_Q + (NH + head) * 81920 + m7 * 5120;
            const unsigned ov = 5120 + t4 * 1280 + h * 320 + (r < 20 ? r : 19) * 16;
            u32x4 e0[2], w0[2], w1[2], eq[2]; unsigned et[2], eqt[2];
#pragma unroll
            for (int i = 0; i < 2; ++i) {
                e0[i] = LD16(kb + om + i * 1024); et[i] = LD4(kb + ot + i * 256); w0[i] = LD16(kb + ov + i * 1280); w1[i] = LD16(kb + ov + i * 1280 + 640);
                eq[i] = LD16(qbb + om + i * 1024); eqt[i] = LD4(qbb + ot + i * 256); gS[i] = __uint_as_float(LD4(WS_G + (tokS + i * 32 + r) * 4));
            }
            u32x4 i0, i1;
#pragma unroll
            for (int j = 0; j < 4; ++j) {
                const int e = r - 8 * h - 2 * j;
                i0[j] = e == 0 ? 0x3f80u : (e == 1 ? 0x3f800000u : 0u);
                i1[j] = e == 16 ? 0x3f80u : (e == 17 ? 0x3f800000u : 0u);
            }
            f32x16 k0 = {}, k1 = {};
#pragma unroll
            for (int i = 0; i < 2; ++i) {
                if (r >= 20) { w0[i] = (u32x4){one, one, one, one}; w1[i] = w0[i]; }
                u32x4 fp, fn, ft;
                feature_map(phm, pht, e0[i], et[i], h, fp, fn, ft);
                f32x16 T0 = MFMA32_16(fp, i0, z16); T0 = MFMA32_16(fn, i1, T0);
                f32x16 T1 = MFMA32_16(ft, i0, z16);
                u32x4 b00, b01, b10, b11;
#pragma unroll
                for (int j = 0; j < 4; ++j) {
                    b00[j] = pk2(T0[2 * j], T0[2 * j + 1]); b01[j] = pk2(T0[8 + 2 * j], T0[9 + 2 * j]);
                    b10[j] = pk2(T1[2 * j], T1[2 * j + 1]); b11[j] = pk2(T1[8 + 2 * j], T1[9 + 2 * j]);
                }
                k0 = MFMA32_16(w0[i], b00, k0); k0 = MFMA32_16(w1[i], b01, k0);
                k1 = MFMA32_16(w0[i], b10, k1); k1 = MFMA32_16(w1[i], b11, k1);
            }
            prog_wait<8>(lds + C_PR, lane, 1);
            *(LDS3 float*)(lds + X_G + (hw * 64 + lane) * 4) = h ? gS[1] : gS[0];
            if (hw * 64 + lane < 144) *(LDS3 u32x4*)(lds + X_KVF + (hw * 64 + lane) * 16) = (u32x4){0u, 0u, 0u, 0u};
            LDS3 char* sp = lds + X_SLOT + hw * 4096;
#pragma unroll
            for (int gq = 0; gq < 3; ++gq) {
                *(LDS3 f32x4*)(sp + gq * 1024 + lane * 16) = (f32x4){k0[4 * gq], k0[4 * gq + 1], k0[4 * gq + 2], k0[4 * gq + 3]};
                if (r < 8) *(LDS3 f32x4*)(sp + 3072 + (gq * 16 + h * 8 + r) * 16) = (f32x4){k1[4 * gq], k1[4 * gq + 1], k1[4 * gq + 2], k1[4 * gq + 3]};
            }
            asm volatile("s_waitcnt vmcnt(0) lgkmcnt(0)" ::: "memory");
            unsigned arr = 0;
            if (lane == 0) arr = LDS_CNT_ADD(C_KV);
            if (__builtin_amdgcn_readfirstlane(arr) == 3) {
                __builtin_amdgcn_fence(__ATOMIC_ACQUIRE, "wavefront");
#pragma unroll
                for (int i = 0; i < 4; ++i) {
                    const int qd = lane + 64 * i;
                    LDS3 const char* s0 = lds + X_SLOT + qd * 16;
                    const f32x4 s = (*(LDS3 const f32x4*)s0 + *(LDS3 const f32x4*)(s0 + 4096)) + (*(LDS3 const f32x4*)(s0 + 8192) + *(LDS3 const f32x4*)(s0 + 12288));
                    if (qd < 240) ST16(__builtin_bit_cast(u32x4, s), WS_KVP + (head * 8 + qb) * 4096 + qd * 16);
                }
                DRAIN();
                if (lane == 0) set_flag(ws, WS_F_KV + (head * 8 + qb) * 8, tag);
            }
#pragma unroll
            for (int i = 0; i < 2; ++i) feature_map(phm, pht, eq[i], eqt[i], h, qf[i][0], qf[i][1], qf[i][2]);
        }
        STAMP(4);
        POLL_ONE(hw == 0, 4, WS_F_KV + (head * 8 + (lane & 7)) * 8, WS_F_KV + (head * 8 + (lane & 7)) * 8, C_P2);
        {
            const int qd = hw * 60 + (lane < 60 ? lane : 59);
            f32x4 pp[8];
#pragma unroll
            for (int p = 0; p < 8; ++p) pp[p] = __builtin_bit_cast(f32x4, LD16(WS_KVP + (head * 8 + p) * 4096 + qd * 16));
            const f32x4 s4 = ((pp[0] + pp[1]) + (pp[2] + pp[3])) + ((pp[4] + pp[5]) + (pp[6] + pp[7]));
            const int gq = qd < 192 ? qd >> 6 : (qd - 192) >> 4, hs = qd < 192 ? (qd >> 5) & 1 : (qd >> 3) & 1, sg = qd < 192 ? qd & 31 : 32 + (qd & 7);
            const unsigned a = pk2(s4[0], s4[1]), b = pk2(s4[2], s4[3]);
            LDS3 u16* kp = (LDS3 u16*)(lds + X_KVF + (8 * gq + 4 * hs) * 96 + sg * 2);
            kp[0] = (u16)a; kp[48] = (u16)(a >> 16); kp[96] = (u16)b; kp[144] = (u16)(b >> 16);
            asm volatile("s_waitcnt lgkmcnt(0)" ::: "memory");
            if (lane == 0) LDS_CNT_ADD(C_KVF);
            lds_wait<2>(lds + C_KVF, 4);
        }
        {
            LDS3 const char* kp = lds + X_KVF + (r < 24 ? r : 23) * 96 + h * 16;
            const u32x4 a0 = *(LDS3 const u32x4*)kp, a1 = *(LDS3 const u32x4*)(kp + 32), a2 = *(LDS3 const u32x4*)(kp + 64);
#pragma unroll
            for (int i = 0; i < 2; ++i) {
                f32x16 d = MFMA32_16(a0, qf[i][0], z16); d = MFMA32_16(a1, qf[i][1], d); d = MFMA32_16(a2, qf[i][2], d);
                const float den = upper_half(d[8]) + 1e-6f;
                float o[12];
#pragma unroll
                for (int j = 0; j < 12; ++j) o[j] = d[j];
                store_o(rs, o, (1.f - gS[i]) / den, tokS + i * 32 + r, 640 + head * 20, h);
            }
            DRAIN();
            unsigned arr = 0;
            if (lane == 0) arr = LDS_CNT_ADD(C_OB);
            if (__builtin_amdgcn_readfirstlane(arr) == 3 && lane == 0) set_flag(ws, WS_F_OB + (qb * 32 + head) * 8, tag);
        }
        STAMP(5);
        f32x4 c[5];
#pragma unroll
        for (int n = 0; n < 5; ++n) c[n] = (f32x4){0.f, 0.f, 0.f, 0.f};
        const char* wsrc = ws + WS_WT + (size_t)ntO * 5 * 40960;
        {
            if (hw == 0) wait_flags<8>(ws, WS_F_KV + lane * 8, WS_F_KV + 512 + lane * 8, tag);
            POLL_ONE(hw == 0, 8, WS_F_KV + 1024 + lane * 8, WS_F_KV + 1536 + lane * 8, C_P5);
#pragma unroll 1
            for (int t = 0; t < 5; ++t) {
                if (t == 0) prog_wait<8>(lds + C_PR, lane, 4); else if (t == 3) prog_wait<4>(lds + C_PR, lane, 5);
                const int ks = hw + 4 * t < 19 ? hw + 4 * t : 18;
#pragma unroll
                for (int n = 0; n < 5; ++n) glds16s(lane * 16, wsrc + (n * 40 + 20 + ks) * 1024, lds0 + 4096 + (ks * 5 + n) * 1024);
            }
            POLL_ONE(hw == 0, 4, WS_F_OB + ((mtO >> 2) * 32 + r) * 8, WS_F_OB + ((mtO >> 2) * 32 + r) * 8, C_P3);
            const unsigned ab = WS_AO + ((mtO * 4 + hw) * 40 + 20) * 1024 + lane * 16;
            u32x4 af[20];
#pragma unroll
            for (int ks = 0; ks < 20; ++ks) af[ks] = LD16(ab + ks * 1024);
            asm volatile("s_waitcnt vmcnt(20)" ::: "memory");
            if (lane == 0) LDS_CNT_ADD(C_BX);
            lds_wait<2>(lds + C_BX, 4);
            u32x4 wf[3][5], wl[5];
            LDS3 const char* wp = lds + 4096 + lane * 16;
#pragma unroll
            for (int ks = 0; ks < 2; ++ks)
#pragma unroll
                for (int n = 0; n < 5; ++n) wf[ks][n] = *(LDS3 const u32x4*)(wp + (ks * 5 + n) * 1024);
#pragma unroll
            for (int ks = 0; ks < 20; ++ks) {
                if (ks + 2 < 19) {
#pragma unroll
                    for (int n = 0; n < 5; ++n) wf[(ks + 2) % 3][n] = *(LDS3 const u32x4*)(wp + ((ks + 2) * 5 + n) * 1024);
                }
                if (ks == 8) {
#pragma unroll
                    for (int n = 0; n < 5; ++n) wl[n] = LD16(WS_WT + ((ntO * 5 + n) * 40 + 39) * 1024 + lane * 16);
                }
                SBAR();
#pragma unroll
                for (int n = 0; n < 5; ++n) c[n] = MFMA16_32(ks < 19 ? wf[ks % 3][n] : wl[n], af[ks], c[n]);
                SBAR();
            }
            if (lane == 0) LDS_CNT_ADD(C_BX);
            lds_wait<2>(lds + C_BX, 8);
        }
        STAMP(6);
        prog_wait<4>(lds + C_PR, lane, 7);
#pragma unroll 1
        for (int n = 0; n < 5; ++n)
#pragma unroll
            for (int t = 0; t < 5; ++t) { const int ks = hw + 4 * t; glds16s(lane * 16, wsrc + (n * 40 + ks) * 1024, lds0 + L_WA + (n * 20 + ks) * 1024); }
#pragma unroll
        for (int n = 0; n < 3; ++n) *(LDS3 f32x4*)(lds + X_FIN + hw * 3072 + n * 1024 + lane * 16) = c[n];
        DRAIN();
        if (lane == 0) LDS_CNT_ADD(C_WA);
        prog_wait<4>(lds + C_PR, lane, 8);
#pragma unroll
        for (int n = 3; n < 5; ++n) *(LDS3 f32x4*)(lds + 143360 + hw * 2048 + (n - 3) * 1024 + lane * 16) = c[n];
        asm volatile("s_waitcnt lgkmcnt(0)" ::: "memory");
        if (lane == 0) __hip_atomic_store((LDS3 unsigned*)(lds + C_BP + hw * 4), 1u, __ATOMIC_RELAXED, __HIP_MEMORY_SCOPE_WORKGROUP);
        STAMP(7);
        asm volatile("mega_code_end2:");
    } else {
        const int qs = w & 3, kh = w >> 2;
        const int q0 = qb * 256 + qs * 64;
        {
            unsigned sink;
            if (w == 0 && (WARM_MODE & 1)) {
                const char* pc0; int len, len2;
                asm volatile("s_getpc_b64 %0\nmega_code_mark:\n\ts_mov_b32 %1, mega_code_end-mega_code_mark\n\ts_mov_b32 %2, mega_code_end2-mega_code_mark" : "=s"(pc0), "=s"(len), "=s"(len2));
                len = len > len2 ? len : len2;
                const int o = ((bid >> 3) * 64 + lane) * 64;
                if (o + 4 <= len) asm volatile("global_load_dword %0, %1, %2" : "=v"(sink) : "v"(o), "s"(pc0) : "memory");
            } else if (w >= 1 && w <= 6 && (WARM_MODE & 2)) {
                const unsigned o = ((w - 1) * 64 + lane) * 65536 + 65532;
                if (o < WS_END) asm volatile("global_load_dword %0, %1, %2 sc1" : "=v"(sink) : "v"(o), "s"(ws) : "memory");
            } else if (w == 7 && (WARM_MODE & 2)) {
                if (lane < 40) asm volatile("global_load_dword %0, %1, %2 sc1" : "=v"(sink) : "v"(lane * 65536), "s"(P.out) : "memory");
            }
            asm volatile("s_waitcnt vmcnt(0)" ::: "memory");
        }
        GET_TAG();
        if (bid < 240) {
            const int part = nt / 5, c0 = (nt - part * 5) * 128;
            const int wm = w & 1, wn = w >> 1;
            constexpr int RB = 32768;
            const int xr = wm * 64 + r;
            const unsigned xa = RB + xr * 128 + ((h ^ ((xr >> 1) & 7)) << 4);
            unsigned wt[2];
#pragma unroll
            for (int t = 0; t < 2; ++t) {
                const int q = (lane & 15) >> 2, p = lane & 3, blk = (lane >> 4) & 1, row = 8 * h + 4 * t + q;
                wt[t] = RB + 16384 + row * 256 + (((4 * wn + 2 * blk + (p >> 1)) ^ ((q << 2) | (2 * h + t))) << 4) + 8 * (p & 1);
            }
            auto gbody = [&](auto trc) {
                constexpr bool TR = decltype(trc)::value;
                u32x4 xf[2][8], wf[2][4];
                auto rd = [&](auto bc, int s) {
                    constexpr int B = decltype(bc)::value;
                    LDS3 const char* sb = lds + (s & 3) * 32768;
#pragma unroll
                    for (int ks = 0; ks < 4; ++ks) {
                        xf[B][2 * ks] = *(LDS3 const u32x4*)(sb + (xa ^ (ks * 32)));
                        xf[B][2 * ks + 1] = *(LDS3 const u32x4*)(sb + ((xa + 4096) ^ (ks * 32)));
                        const u32x2 lo = trread(sb + wt[0] + ks * 4096), hi = trread(sb + wt[1] + ks * 4096);
                        wf[B][ks] = (u32x4){lo[0], lo[1], hi[0], hi[1]};
                    }
                };
                f32x16 acc0, acc1;
                auto step = [&](auto bc, int s) {
                    constexpr int B = decltype(bc)::value;
                    if (s == 12) DRAIN();
                    __builtin_amdgcn_s_waitcnt(0xC07F);
                    BAR(); SBAR();
                    rd(std::integral_constant<int, B ^ 1>{}, s + 1);
#pragma unroll
                    for (int ks = 0; ks < 4; ++ks) {
                        if constexpr (TR) { acc0 = MFMA32_16(wf[B][ks], xf[B][2 * ks], acc0); acc1 = MFMA32_16(wf[B][ks], xf[B][2 * ks + 1], acc1); }
                        else              { acc0 = MFMA32_16(xf[B][2 * ks], wf[B][ks], acc0); acc1 = MFMA32_16(xf[B][2 * ks + 1], wf[B][ks], acc1); }
                    }
#pragma unroll
                    for (int i = 0; i < 4; ++i) { __builtin_amdgcn_sched_group_barrier(0x8, 1, 0); __builtin_amdgcn_sched_group_barrier(0x2, 2, 0); __builtin_amdgcn_sched_group_barrier(0x100, 4, 0); }
                    __builtin_amdgcn_sched_group_barrier(0x8, 4, 0);
                    SBAR();
                };
                WAIT_BAR(63);
                rd(std::integral_constant<int, 0>{}, 0);
#pragma unroll 1
                for (int mat = 0; mat < 2; ++mat) {
                    acc0 = (f32x16){}; acc1 = (f32x16){};
#pragma unroll 1
                    for (int kk = 0; kk < 10; kk += 2) {
                        step(std::integral_constant<int, 0>{}, mat * 10 + kk);
                        step(std::integral_constant<int, 1>{}, mat * 10 + kk + 1);
                    }
                    const int cpos = 2 * (mt & 7) + (mt >> 3);
                    if constexpr (TR) {
                        const float sc = (part == 0 && mat == 0) ? QSCALE : 1.f;
                        const unsigned base = part == 0 ? WS_Q + mat * NH * 81920 + mt * 5120 : WS_KV + mat * NH * 163840 + cpos * 10240;
                        const unsigned hs = part == 0 ? 81920 : 163840;
#pragma unroll
                        for (int pr = 0; pr < 2; ++pr) {
                            const int G8 = ((c0 + 32 * wn) >> 3) + 2 * pr + h, pp = G8 / 5, gi = G8 - pp * 5;
                            const unsigned hb0 = base + (2 * pp + (gi > 2)) * hs;
#pragma unroll
                            for (int i = 0; i < 2; ++i) {
                                const f32x16& a = i ? acc1 : acc0;
                                const int tl = wm * 64 + i * 32 + r;
                                const auto s0 = __builtin_amdgcn_permlane32_swap(pk2(a[8 * pr] * sc, a[8 * pr + 1] * sc), pk2(a[8 * pr + 4] * sc, a[8 * pr + 5] * sc), false, false);
                                const auto s1 = __builtin_amdgcn_permlane32_swap(pk2(a[8 * pr + 2] * sc, a[8 * pr + 3] * sc), pk2(a[8 * pr + 6] * sc, a[8 * pr + 7] * sc), false, false);
                                if (gi != 2) { const u32x4 ov = {s0[0], s1[0], s0[1], s1[1]}; ST16(ov, hb0 + (tl >> 5) * 1024 + (gi > 2 ? gi - 3 : gi) * 512 + r * 16); }
                                else { const u32x2 o0 = {s0[0], s1[0]}, o1 = {s0[1], s1[1]}; ST8(o0, hb0 + 4096 + tl * 8); ST8(o1, hb0 + hs + 4096 + tl * 8); }
                            }
                        }
                    } else {
                        const int n = c0 + 32 * wn + r, hd = n / 20, d = n - hd * 20, rho = (hd & 1) ? (d < 4 ? d + 16 : d - 4) : d;
                        const unsigned base = WS_KV + (mat * NH + hd) * 163840 + cpos * 10240 + 5120 + h * 320 + rho * 16;
#pragma unroll
                        for (int i = 0; i < 2; ++i)
#pragma unroll
                            for (int s2 = 0; s2 < 2; ++s2) {
                                const f32x16& a = i ? acc1 : acc0;
                                u32x4 ov = {pk2(a[8 * s2], a[8 * s2 + 1]), pk2(a[8 * s2 + 2], a[8 * s2 + 3]), pk2(a[8 * s2 + 4], a[8 * s2 + 5]), pk2(a[8 * s2 + 6], a[8 * s2 + 7])};
                                ST16(ov, base + (wm * 4 + i * 2 + s2) * 640);
                            }
                    }
                    if (mat == 0) STAMP(1);
                }
            };
            if (part < 2) gbody(std::true_type{}); else gbody(std::false_type{});
        } else {
            const int sj = bid - 240;
            WAIT_BAR(63);
            {
                const u32x4 ga = *(const u32x4*)(P.gw + lane * 8), gb = *(const u32x4*)(P.gw + 512 + (lane & 15) * 8);
                u32x4 xa[16], xb[4];
                const u16* xr = P.x + (size_t)(sj * 128 + w * 16) * DM;
#pragma unroll
                for (int rr = 0; rr < 16; ++rr) xa[rr] = *(const u32x4*)(xr + rr * DM + lane * 8);
#pragma unroll
                for (int i = 0; i < 4; ++i) xb[i] = *(const u32x4*)(xr + (i * 4 + (lane >> 4)) * DM + 512 + (lane & 15) * 8);
                float res = 0.f;
#pragma unroll
                for (int rr = 0; rr < 16; ++rr) {
                    float s = 0.f, s2 = 0.f;
#pragma unroll
                    for (int j = 0; j < 4; ++j) { s += bf_lo(xa[rr][j]) * bf_lo(ga[j]) + bf_hi(xa[rr][j]) * bf_hi(ga[j]); s2 += bf_lo(xb[rr >> 2][j]) * bf_lo(gb[j]) + bf_hi(xb[rr >> 2][j]) * bf_hi(gb[j]); }
                    if ((lane >> 4) == (rr & 3)) s += s2;
#pragma unroll
                    for (int o = 32; o >= 1; o >>= 1) s += __shfl_xor(s, o);
                    if (lane == rr) res = s;
                }
                if (lane < 16) ST4(__float_as_uint(1.f / (1.f + __expf(-res))), WS_G + (sj * 128 + w * 16 + lane) * 4);
            }
            DRAIN();
            unsigned arr = 0;
            if (lane == 0) arr = LDS_CNT_ADD(G_ST);
            if (__builtin_amdgcn_readfirstlane(arr) == 7 && lane == 0) set_flag(ws, WS_F_SD + sj * 8, tag);
        }
        STAMP(2);
        const unsigned hb = lds0 + kh * 10240;
        const unsigned laneK = hb + lane * 16, laneT = hb + 4096 + r * 8 + h * 4, laneV = hb + 5120 + h * 320 + (r < 20 ? r : 19) * 16;
        const u64 vmask = 0x000fffff000fffffull;
        u32x4 v0a = {one, one, one, one}, v0b = v0a, v1 = v0a;
        u32x4 qA, qB, ka; unsigned qtA, qtB, kt;
        {
            lds_wait(lds + G_Q, 4);
            LDS3 const char* qp = lds + L_Q + (qs >> 1) * 5120;
            const unsigned om = (qs & 1) * 2048 + lane * 16, ot = 4096 + ((qs & 1) * 64 + r) * 8 + 4 * h;
            qA = *(LDS3 const u32x4*)(qp + om); qB = *(LDS3 const u32x4*)(qp + om + 1024);
            qtA = *(LDS3 const unsigned*)(qp + ot); qtB = *(LDS3 const unsigned*)(qp + ot + 256);
            DSR128(ka, laneK, 0); DSR32(kt, laneT, 0); DSR128M2(v0a, laneV, 0, v1, laneV, 640);
            asm volatile("s_waitcnt lgkmcnt(0)\n\ts_barrier" : "+v"(ka), "+v"(kt), "+v"(v0a), "+v"(v1), "+v"(qA), "+v"(qB), "+v"(qtA), "+v"(qtB) :: "memory");
        }
        STAMP(5);
        float mA, mB; u32x2 qTA, qTB; f32x16 sA, sB;
        {
            const u32x2 t0 = {kt, 0x3f80u}, a0 = {qtA, 0u}, b0 = {qtB, 0u};
            const f32x16 s = MFMA32_16(ka, qA, z16);
            const f32x16 s0 = MFMA32_8(t0, a0, s);
            f32x16 s1 = MFMA32_16(ka, qB, z16); s1 = MFMA32_8(t0, b0, s1);
            float x0 = s0[0], x1 = s1[0];
#pragma unroll
            for (int i = 1; i < 16; ++i) { x0 = fmaxf(x0, s0[i]); x1 = fmaxf(x1, s1[i]); }
            x0 = -xhalf_max(x0); x1 = -xhalf_max(x1);
            const unsigned hh = pk2(x0, x1);
            const unsigned ll = pk2(x0 - bf_lo(hh), x1 - bf_hi(hh));
            mA = -(bf_lo(hh) + bf_lo(ll)); mB = -(bf_hi(hh) + bf_hi(ll));
            const unsigned sel = h ? ll : hh;
            qTA = (u32x2){qtA, sel & 0xffffu}; qTB = (u32x2){qtB, sel >> 16};
            sA = MFMA32_8(t0, qTA, s);
        }
        f32x16 oA = {}, oB = {};
        u32x4 pa, pb = {0u, 0u, 0u, 0u};
#define EX4(S, i, Pv, j) do { Pv[j] = pk2(ex2(S[i]), ex2(S[i + 1])); Pv[j + 1] = pk2(ex2(S[i + 2]), ex2(S[i + 3])); PIN(Pv); SBAR(); } while (0)
#define KT2 ((u32x2){kt, 0x3f80u})
#define TILE(V0C, V0N, aK1, oK1, aT1, oT1, oV1, aV0, oV0) do { \
            sB = MFMA32_16(ka, qB, z16); SBAR(); DSR128(ka, aK1, oK1); EX4(sA, 0, pa, 0); \
            oB = MFMA32_16(v1, pb, oB); SBAR(); DSR128M2(v1, aV, oV1, V0N, aV0, oV0); EX4(sA, 4, pa, 2); \
            sB = MFMA32_8(KT2, qTB, sB); SBAR(); DSR32(kt, aT1, oT1); EX4(sA, 8, pb, 0); \
            oA = MFMA32_16(V0C, pa, oA); SBAR(); EX4(sA, 12, pb, 2); \
            LWAIT(1); sA = MFMA32_16(ka, qA, z16); SBAR(); EX4(sB, 0, pa, 0); \
            oA = MFMA32_16(v1, pb, oA); SBAR(); EX4(sB, 4, pa, 2); \
            LWAIT(0); sA = MFMA32_8(KT2, qTA, sA); SBAR(); EX4(sB, 8, pb, 0); \
            oB = MFMA32_16(V0C, pa, oB); SBAR(); EX4(sB, 12, pb, 2); \
        } while (0)
        unsigned aK = laneK, aT = laneT, aV = laneV;
#pragma unroll 1
        for (int c = 0; c < 8; ++c) {
            if (c == 1) lds_wait(lds + C_KVL, 4);
            if (c >= 1) {
                unsigned pp;
                asm volatile("ds_write_b32 %1, %2\n\tds_read_b32 %0, %3\n\ts_waitcnt lgkmcnt(0)" : "=&v"(pp) : "v"(lds0 + C_PR + w * 4), "v"(c), "v"(lds0 + C_PR + (w ^ 4) * 4) : "memory");
                const int pc = __builtin_amdgcn_readfirstlane(pp);
                if (pc > c || (pc == c && ((c ^ kh) & 1))) __builtin_amdgcn_s_setprio(1); else __builtin_amdgcn_s_setprio(0);
            } else if (kh) __builtin_amdgcn_s_setprio(1);
            const unsigned nx = (c < 7 ? c + 1 : 7) * 20480;
            const unsigned nK = laneK + nx, nT = laneT + nx, nV = laneV + nx;
            SBAR();
            TILE(v0a, v0b, aK, 1024, aT, 256, 640,  aV, 1280);
            TILE(v0b, v0a, aK, 2048, aT, 512, 1920, aV, 2560);
            if (c == 0) { WAIT_BAR(0); SBAR(); }
            TILE(v0a, v0b, aK, 3072, aT, 768, 3200, aV, 3840);
            TILE(v0b, v0a, nK, 0,    nT, 0,   4480, nV, 0);
            aK = nK; aT = nT; aV = nV;
        }
        oB = MFMA32_16(v1, pb, oB);
#undef TILE
#undef EX4
        if (lane == 0) __hip_atomic_store((LDS3 unsigned*)(lds + C_PR + w * 4), 8u, __ATOMIC_RELAXED, __HIP_MEMORY_SCOPE_WORKGROUP);
        STAMP(6);
        {
            float chk = 0.f;
#pragma unroll
            for (int i = 0; i < 12; ++i) chk += fabsf(oA[i]) * 0.f + fabsf(oB[i]) * 0.f;
            bool bad = !(chk == 0.f);
            if (h) bad = bad || !(oA[8] >= 0x1p-60f && oA[8] <= 0x1p60f && oB[8] >= 0x1p-60f && oB[8] <= 0x1p60f);
            if (__builtin_expect(__any(bad), 0)) {
                mA = -INFINITY; mB = -INFINITY;
#pragma unroll
                for (int i = 0; i < 16; ++i) { oA[i] = 0.f; oB[i] = 0.f; }
                const u32x2 a0 = {qtA, 0u}, b0 = {qtB, 0u};
#pragma unroll 1
                for (int t = 0; t < 32; ++t) {
                    const unsigned kb = WS_KV + head * 163840 + (2 * (t >> 2) + kh) * 10240, t4 = t & 3;
                    const u32x4 k0 = LD16(kb + t4 * 1024 + lane * 16);
                    const u32x2 t0 = {LD4(kb + 4096 + (t4 * 32 + r) * 8 + 4 * h), 0u};
                    u32x4 w0 = LD16(kb + 5120 + t4 * 1280 + h * 320 + (r < 20 ? r : 19) * 16), w1 = LD16(kb + 5120 + t4 * 1280 + 640 + h * 320 + (r < 20 ? r : 19) * 16);
                    if (r >= 20) { w0 = (u32x4){one, one, one, one}; w1 = w0; }
                    f32x16 s0 = MFMA32_16(k0, qA, z16); s0 = MFMA32_8(t0, a0, s0);
                    f32x16 s1 = MFMA32_16(k0, qB, z16); s1 = MFMA32_8(t0, b0, s1);
                    float x0 = s0[0], x1 = s1[0];
#pragma unroll
                    for (int i = 1; i < 16; ++i) { x0 = fmaxf(x0, s0[i]); x1 = fmaxf(x1, s1[i]); }
                    x0 = fmaxf(xhalf_max(x0), mA); x1 = fmaxf(xhalf_max(x1), mB);
                    const float g0 = (mA == x0) ? 1.f : ex2(mA - x0), g1 = (mB == x1) ? 1.f : ex2(mB - x1);
                    mA = x0; mB = x1;
#pragma unroll
                    for (int i = 0; i < 16; ++i) { oA[i] *= g0; oB[i] *= g1; }
#pragma unroll
                    for (int i = 0; i < 4; ++i) { pa[i] = pk2(ex2(s0[2 * i] - x0), ex2(s0[2 * i + 1] - x0)); pb[i] = pk2(ex2(s0[8 + 2 * i] - x0), ex2(s0[9 + 2 * i] - x0)); }
                    oA = MFMA32_16(w0, pa, oA); oA = MFMA32_16(w1, pb, oA);
#pragma unroll
                    for (int i = 0; i < 4; ++i) { pa[i] = pk2(ex2(s1[2 * i] - x1), ex2(s1[2 * i + 1] - x1)); pb[i] = pk2(ex2(s1[8 + 2 * i] - x1), ex2(s1[9 + 2 * i] - x1)); }
                    oB = MFMA32_16(w0, pa, oB); oB = MFMA32_16(w1, pb, oB);
                }
            }
        }
        lds_wait<4>(lds + C_BX, 8);
        const float gO = *(LDS3 const float*)(lds + X_G + (qs * 64 + kh * 32 + r) * 4);
        {
            LDS3 char* xp = lds + X_MRG + w * 2816;
            const f32x16& s = kh ? oA : oB;
            *(LDS3 f32x4*)(xp + lane * 16) = (f32x4){s[0], s[1], s[2], s[3]};
            *(LDS3 f32x4*)(xp + 1024 + lane * 16) = (f32x4){s[4], s[5], s[6], s[7]};
            if (!h) *(LDS3 f32x4*)(xp + 2048 + r * 16) = (f32x4){s[8], s[9], s[10], s[11]};
            else *(LDS3 f32x2*)(xp + 2560 + r * 8) = (f32x2){s[8], kh ? mA : mB};
            asm volatile("s_waitcnt lgkmcnt(0)" ::: "memory");
            if (lane == 0) __hip_atomic_store((LDS3 unsigned*)(lds + C_MRG + w * 4), 1u, __ATOMIC_RELAXED, __HIP_MEMORY_SCOPE_WORKGROUP);
        }
        lds_wait<2>(lds + C_MRG + (w ^ 4) * 4, 1);
        bool lastw;
        {
            LDS3 const char* xp = lds + X_MRG + (w ^ 4) * 2816;
            const f32x4 p0 = *(LDS3 const f32x4*)(xp + lane * 16), p1 = *(LDS3 const f32x4*)(xp + 1024 + lane * 16), p2 = *(LDS3 const f32x4*)(xp + 2048 + r * 16);
            const f32x2 lm = *(LDS3 const f32x2*)(xp + 2560 + r * 8);
            const f32x16& s = kh ? oB : oA;
            const float mo = kh ? mB : mA, mp = lm[1];
            const float mx = fmaxf(mo, mp);
            const float fo = (mo == mx) ? 1.f : ex2(mo - mx), fq = (mp == mx) ? 1.f : ex2(mp - mx);
            float o[12];
#pragma unroll
            for (int i = 0; i < 4; ++i) { o[i] = s[i] * fo + p0[i] * fq; o[4 + i] = s[4 + i] * fo + p1[i] * fq; o[8 + i] = s[8 + i] * fo + p2[i] * fq; }
            const float l = upper_half(s[8]) * fo + lm[0] * fq;
            store_o(rs, o, gO / l, q0 + kh * 32 + r, head * 20, h);
            DRAIN();
            unsigned arr = 0;
            if (lane == 0) arr = LDS_CNT_ADD(C_OA);
            lastw = __builtin_amdgcn_readfirstlane(arr) == 7;
            if (lastw && lane == 0) set_flag(ws, WS_F_OA + (qb * 32 + head) * 8, tag);
        }
        STAMP(7);
        {
            const int c16 = lane & 15, g = lane >> 4, tt = w & 3, k0 = kh * 10;
            POLL_ONE(lastw, 4, WS_F_OA + ((mtO >> 2) * 32 + r) * 8, WS_F_OA + ((mtO >> 2) * 32 + r) * 8, C_P4);
            STAMP(9);
            const unsigned ab = WS_AO + ((mtO * 4 + tt) * 40 + k0) * 1024 + lane * 16;
            u32x4 af[10];
#pragma unroll
            for (int ks = 0; ks < 10; ++ks) af[ks] = LD16(ab + ks * 1024);
            lds_wait(lds + C_WA, 4);
            lds_wait(lds + C_OA, 8);
            f32x4 c[5];
#pragma unroll
            for (int n = 0; n < 5; ++n) c[n] = (f32x4){0.f, 0.f, 0.f, 0.f};
#pragma unroll
            for (int ks = 0; ks < 10; ++ks)
#pragma unroll
                for (int n = 0; n < 5; ++n) c[n] = MFMA16_32(*(LDS3 const u32x4*)(lds + L_WA + (n * 20 + k0 + ks) * 1024 + lane * 16), af[ks], c[n]);
            LDS3 char* xp = lds + X_MRG + tt * 5120 + lane * 16;
            if (w < 4) { *(LDS3 f32x4*)(xp + 3072) = c[3]; *(LDS3 f32x4*)(xp + 4096) = c[4]; }
            else { *(LDS3 f32x4*)(xp) = c[0]; *(LDS3 f32x4*)(xp + 1024) = c[1]; *(LDS3 f32x4*)(xp + 2048) = c[2]; }
            asm volatile("s_waitcnt lgkmcnt(0)" ::: "memory");
            if (lane == 0) __hip_atomic_store((LDS3 unsigned*)(lds + C_FIN + w * 4), 1u, __ATOMIC_RELAXED, __HIP_MEMORY_SCOPE_WORKGROUP);
            lds_wait(lds + C_FIN + (w ^ 4) * 4, 1);
            lds_wait(lds + C_BP + tt * 4, 1);
            u16* op = P.out + (size_t)(mtO * 64 + tt * 16 + c16) * DM + ntO * 80 + g * 4;
#pragma unroll
            for (int n = 0; n < 5; ++n) {
                if ((w < 4) == (n < 3)) {
                    const f32x4 bp = *(LDS3 const f32x4*)(n < 3 ? lds + X_FIN + tt * 3072 + n * 1024 + lane * 16 : lds + 143360 + tt * 2048 + (n - 3) * 1024 + lane * 16);
                    const f32x4 s = c[n] + *(LDS3 const f32x4*)(xp + n * 1024) + bp;
                    *(u32x2*)(op + n * 16) = (u32x2){pk2(s[0], s[1]), pk2(s[2], s[3])};
                }
            }
            if (bid == 0 && mtid == 0) set_flag(ws, WS_EPOCH, tag);
        }
        STAMP(10);
        asm volatile("mega_code_end:");
    }
}

extern "C" void kernel_launch(void* const* d_in, const int* in_sizes, int n_in, void* d_out, int out_size, void* d_ws, size_t ws_size,
                              hipStream_t stream) {
    Params P;
    P.x = (const u16*)d_in[0]; P.wa = (const u16*)d_in[1]; P.woa = (const u16*)d_in[2]; P.wb = (const u16*)d_in[4];
    P.wphi = (const u16*)d_in[5]; P.wob = (const u16*)d_in[6]; P.gw = (const u16*)d_in[7]; P.ws = (char*)d_ws; P.out = (u16*)d_out;
    mega<<<256, 768, 0, stream>>>(P);
}
```

```cpp
#include <hip/hip_runtime.h>
#include <hip/hip_bf16.h>
#include <stdint.h>
#include <type_traits>

typedef __attribute__((ext_vector_type(8))) short bf16x8;
typedef __attribute__((ext_vector_type(4))) short bf16x4;
typedef __attribute__((ext_vector_type(16))) float f32x16;
typedef __attribute__((ext_vector_type(4))) float f32x4;
typedef __attribute__((ext_vector_type(2))) float f32x2;
typedef __attribute__((ext_vector_type(4))) unsigned u32x4;
typedef __attribute__((ext_vector_type(2))) unsigned u32x2;
typedef __attribute__((ext_vector_type(2))) __bf16 bf2_t;
typedef unsigned short u16;
typedef unsigned long long u64;
#define LDS3 __attribute__((address_space(3)))
typedef __attribute__((address_space(1))) u64 gu64;
#define RLX_AGENT __ATOMIC_RELAXED, __HIP_MEMORY_SCOPE_AGENT

#define PRIO_MODE 3
#define WARM_MODE 1
#define GA_PUB 11
#define PRE_SLEEP 2
constexpr int SEQ = 2048, DM = 640, NH = 32;
constexpr float LOG2E = 1.4426950408889634f;
constexpr float QSCALE = 0.22360679774997896f * LOG2E;

constexpr unsigned WS_EPOCH = 0;
constexpr unsigned WS_F_G   = 256;
constexpr unsigned WS_F_SD  = WS_F_G + 4096;
constexpr unsigned WS_F_KV  = WS_F_SD + 256;
constexpr unsigned WS_F_OB  = WS_F_KV + 2048;
constexpr unsigned WS_F_OA  = WS_F_OB + 2048;
constexpr unsigned WS_G     = 16384;
constexpr unsigned WS_Q     = 32768;
constexpr unsigned WS_KV    = WS_Q + 2 * NH * 81920;
constexpr unsigned WS_KVP   = WS_KV + 2 * NH * 163840;
constexpr unsigned WS_AO    = WS_KVP + NH * 8 * 4096;
constexpr unsigned WS_WT    = WS_AO + SEQ * 1280 * 2;
constexpr unsigned WS_END   = WS_WT + 1280 * 640 * 2;

#define STAMP(i)

__device__ __forceinline__ unsigned pk2(float a, float b) { f32x2 v = {a, b}; bf2_t r = __builtin_convertvector(v, bf2_t); return __builtin_bit_cast(unsigned, r); }
__device__ __forceinline__ float bf_lo(unsigned u) { return __uint_as_float(u << 16); }
__device__ __forceinline__ float bf_hi(unsigned u) { return __uint_as_float(u & 0xffff0000u); }
__device__ __forceinline__ float ex2(float x) { return __builtin_amdgcn_exp2f(x); }
#define MFMA32_16(a, b, c) __builtin_amdgcn_mfma_f32_32x32x16_bf16(__builtin_bit_cast(bf16x8, a), __builtin_bit_cast(bf16x8, b), c, 0, 0, 0)
#define MFMA32_8(a, b, c) __builtin_amdgcn_mfma_f32_32x32x8bf16_1k(__builtin_bit_cast(bf16x4, a), __builtin_bit_cast(bf16x4, b), c, 0, 0, 0)
#define MFMA16_32(a, b, c) __builtin_amdgcn_mfma_f32_16x16x32_bf16(__builtin_bit_cast(bf16x8, a), __builtin_bit_cast(bf16x8, b), c, 0, 0, 0)

__device__ __forceinline__ void glds16(unsigned voff, const void* sbase, unsigned lds_base) {
    unsigned sv;
    asm volatile("s_mov_b32 %0, m0\n\ts_mov_b32 m0, %3\n\ts_nop 0\n\tglobal_load_lds_dwordx4 %1, %2\n\ts_mov_b32 m0, %0"
                 : "=&s"(sv) : "v"(voff), "s"(sbase), "s"(__builtin_amdgcn_readfirstlane(lds_base)) : "memory");
}
__device__ __forceinline__ void glds16s(unsigned voff, const void* sbase, unsigned lds_base) {
    unsigned sv;
    asm volatile("s_mov_b32 %0, m0\n\ts_mov_b32 m0, %3\n\ts_nop 0\n\tglobal_load_lds_dwordx4 %1, %2 sc1\n\ts_mov_b32 m0, %0"
                 : "=&s"(sv) : "v"(voff), "s"(sbase), "s"(__builtin_amdgcn_readfirstlane(lds_base)) : "memory");
}
__device__ __forceinline__ void glds4s(unsigned voff, const void* sbase, unsigned lds_base) {
    unsigned sv;
    asm volatile("s_mov_b32 %0, m0\n\ts_mov_b32 m0, %3\n\ts_nop 0\n\tglobal_load_lds_dword %1, %2 sc1\n\ts_mov_b32 m0, %0"
                 : "=&s"(sv) : "v"(voff), "s"(sbase), "s"(__builtin_amdgcn_readfirstlane(lds_base)) : "memory");
}
typedef short v4i16_t __attribute__((ext_vector_type(4)));
__device__ __forceinline__ u32x2 trread(LDS3 const char* p) {
    return __builtin_bit_cast(u32x2, __builtin_amdgcn_ds_read_tr16_b64_v4i16((LDS3 v4i16_t*)p));
}
#define WAIT_BAR(N) asm volatile("s_waitcnt vmcnt(" #N ") lgkmcnt(0)\n\ts_barrier" ::: "memory")
#define DRAIN() asm volatile("s_waitcnt vmcnt(0)" ::: "memory")
#define SBAR() __builtin_amdgcn_sched_barrier(0)
#define PIN(x) asm volatile("" : "+v"(x))

__device__ __forceinline__ float xhalf_max(float x) {
    auto r = __builtin_amdgcn_permlane32_swap(__float_as_uint(x), __float_as_uint(x), false, false);
    return fmaxf(__uint_as_float(r[0]), __uint_as_float(r[1]));
}
__device__ __forceinline__ float xhalf_sum(float x) {
    auto r = __builtin_amdgcn_permlane32_swap(__float_as_uint(x), __float_as_uint(x), false, false);
    return __uint_as_float(r[0]) + __uint_as_float(r[1]);
}
__device__ __forceinline__ float upper_half(float x) {
    auto r = __builtin_amdgcn_permlane32_swap(__float_as_uint(x), __float_as_uint(x), false, false);
    return __uint_as_float(r[1]);
}

typedef __amdgpu_buffer_rsrc_t rsrc_t;
#define ST8(v, off)  __builtin_amdgcn_raw_buffer_store_b64(v, rs, (int)(off), 0, 16)
#define ST16(v, off) __builtin_amdgcn_raw_buffer_store_b128(v, rs, (int)(off), 0, 16)
#define ST4(v, off)  __builtin_amdgcn_raw_buffer_store_b32(v, rs, (int)(off), 0, 16)
#define LD16(off) __builtin_amdgcn_raw_buffer_load_b128(rs, (int)(off), 0, 16)
#define LD4(off)  __builtin_amdgcn_raw_buffer_load_b32(rs, (int)(off), 0, 16)

template <int SLP = 2>
__device__ __forceinline__ void wait_flags(const char* ws, unsigned off0, unsigned off1, u64 tag) {
    gu64* p0 = (gu64*)(ws + off0); gu64* p1 = (gu64*)(ws + off1);
#pragma unroll 1
    for (unsigned spins = 0; spins < (1u << 16); ++spins) {
        const u64 a = __hip_atomic_load(p0, RLX_AGENT), b = __hip_atomic_load(p1, RLX_AGENT);
        if (__all(a == tag && b == tag)) break;
        __builtin_amdgcn_s_sleep(SLP);
    }
    __builtin_amdgcn_fence(__ATOMIC_ACQUIRE, "wavefront");
}
__device__ __forceinline__ void set_flag(char* ws, unsigned off, u64 tag) { __hip_atomic_store((gu64*)(ws + off), tag, RLX_AGENT); }

struct Params { const u16* x; const u16* wa; const u16* wb; const u16* wphi; const u16* woa; const u16* wob; const u16* gw; char* ws; u16* out; };

__device__ __forceinline__ void feature_map(const u32x4& pm, unsigned pt, const u32x4& tm, unsigned tt, int h, u32x4& fp, u32x4& fn, u32x4& ft) {
    const f32x16 z16 = {};
    const u32x2 a8 = {pt, 0u}, b8 = {tt, 0u};
    f32x16 s = MFMA32_16(pm, tm, z16); s = MFMA32_8(a8, b8, s);
    float v[12];
#pragma unroll
    for (int i = 0; i < 12; i += 2) { const unsigned u = pk2(s[i], s[i + 1]); v[i] = bf_lo(u) * LOG2E; v[i + 1] = bf_hi(u) * LOG2E; }
    float mx = v[0], mn = v[0];
#pragma unroll
    for (int i = 1; i < 8; ++i) { mx = fmaxf(mx, v[i]); mn = fminf(mn, v[i]); }
    const float tx = fmaxf(fmaxf(v[8], v[9]), fmaxf(v[10], v[11])), tn = fminf(fminf(v[8], v[9]), fminf(v[10], v[11]));
    if (!h) { mx = fmaxf(mx, tx); mn = fminf(mn, tn); }
    mx = xhalf_max(mx); mn = -xhalf_max(-mn);
    float ep[12], en[12], sp = 0.f, sn = 0.f;
#pragma unroll
    for (int i = 0; i < 12; ++i) {
        ep[i] = ex2(v[i] - mx); en[i] = ex2(mn - v[i]);
        if (i >= 8 && h) { ep[i] = 0.f; en[i] = 0.f; }
        sp += ep[i]; sn += en[i];
    }
    sp = __builtin_amdgcn_rcpf(xhalf_sum(sp)); sn = __builtin_amdgcn_rcpf(xhalf_sum(sn));
#pragma unroll
    for (int i = 0; i < 4; ++i) { fp[i] = pk2(ep[2 * i] * sp, ep[2 * i + 1] * sp); fn[i] = pk2(en[2 * i] * sn, en[2 * i + 1] * sn); }
    ft = (u32x4){pk2(ep[8] * sp, ep[9] * sp), pk2(ep[10] * sp, ep[11] * sp), pk2(en[8] * sn, en[9] * sn), pk2(en[10] * sn, en[11] * sn)};
}

__device__ __forceinline__ void store_o(rsrc_t rs, const float (&o)[12], float sc, int tok, int kbase, int h) {
#pragma unroll
    for (int gq = 0; gq < 3; ++gq) {
        const int d = 8 * gq + 4 * h;
        if (d < 20) {
            const int k = kbase + ((kbase & 4) ? (d == 16 ? 0 : d + 4) : d);
            u32x2 ov = {pk2(o[4 * gq] * sc, o[4 * gq + 1] * sc), pk2(o[4 * gq + 2] * sc, o[4 * gq + 3] * sc)};
            ST8(ov, WS_AO + (((tok >> 4) * 40 + (k >> 5)) * 64 + ((k >> 3) & 3) * 16 + (tok & 15)) * 16 + (k & 7) * 2);
        }
    }
}

#define DSR128(dst, addr, off) asm volatile("ds_read_b128 %0, %1 offset:%2" : "=v"(dst) : "v"(addr), "i"(off))
#define DSR32(dst, addr, off) asm volatile("ds_read_b32 %0, %1 offset:%2" : "=v"(dst) : "v"(addr), "i"(off))
#define DSR128M2(d0, a0, o0, d1, a1, o1) asm volatile("s_mov_b64 exec, %6\n\tds_read_b128 %0, %2 offset:%3\n\tds_read_b128 %1, %4 offset:%5\n\ts_mov_b64 exec, -1" \
                                                      : "+v"(d0), "+v"(d1) : "v"(a0), "i"(o0), "v"(a1), "i"(o1), "s"(vmask))
#define LWAIT(n) do { asm volatile("s_waitcnt lgkmcnt(" #n ")" ::: "memory"); SBAR(); } while (0)

#define BAR() asm volatile("s_barrier" ::: "memory")
#define LDS_CNT_ADD(off) __hip_atomic_fetch_add((LDS3 unsigned*)(lds + (off)), 1u, __ATOMIC_RELAXED, __HIP_MEMORY_SCOPE_WORKGROUP)

template <int SLP = 1>
__device__ __forceinline__ void lds_wait(LDS3 char* p, unsigned want) {
#pragma unroll 1
    for (unsigned spins = 0; spins < (1u << 20); ++spins) {
        if ((unsigned)__builtin_amdgcn_readfirstlane(__hip_atomic_load((LDS3 unsigned*)p, __ATOMIC_RELAXED, __HIP_MEMORY_SCOPE_WORKGROUP)) >= want) break;
        __builtin_amdgcn_s_sleep(SLP);
    }
    __builtin_amdgcn_fence(__ATOMIC_ACQUIRE, "wavefront");
}

template <int SLP>
__device__ __forceinline__ void prog_wait(LDS3 char* p, int lane, unsigned want) {
#pragma unroll 1
    for (unsigned spins = 0; spins < (1u << 20); ++spins) {
        if (__all(__hip_atomic_load((LDS3 unsigned*)p + (lane & 7), __ATOMIC_RELAXED, __HIP_MEMORY_SCOPE_WORKGROUP) >= want)) break;
        __builtin_amdgcn_s_sleep(SLP);
    }
    __builtin_amdgcn_fence(__ATOMIC_ACQUIRE, "wavefront");
}
#define POLL_ONE(cond, SLP, o0, o1, word) do { if (cond) { wait_flags<SLP>(ws, o0, o1, tag); \
        if (lane == 0) __hip_atomic_store((LDS3 unsigned*)(lds + (word)), 1u, __ATOMIC_RELAXED, __HIP_MEMORY_SCOPE_WORKGROUP); } else lds_wait<SLP>(lds + (word), 1); } while (0)
constexpr int C_KV = 0, C_KVF = 4, C_OB = 8, C_PROG = 12, C_WA = 16, C_OA = 20, C_MRG = 32  , C_FIN = 64  , C_BP = 96  ;
constexpr int C_PROG2 = 24, C_BX = 28, C_P1 = 112, C_P2 = 116, C_P3 = 120, C_P4 = 124, C_PR = 128  ;
constexpr int G_W = 31744;
constexpr int G_FULL = G_W, G_FREE = G_W + 16, G_Q = G_W + 32, G_KV0 = G_W + 36, G_ST = G_W + 40;
constexpr int C_KVL = 160, C_P5 = 164, X_G = 256;
constexpr int X_KVF = 1280, X_SLOT = 4096;
constexpr int X_MRG = 1280;
constexpr int L_WA = 24576;
constexpr int L_Q = 65536;
constexpr int X_FIN = 126976;

__global__ __launch_bounds__(768) void mega(Params P) {
    __shared__ __attribute__((aligned(16))) char smem[163840];
    LDS3 char* lds = (LDS3 char*)smem;
    char* ws = P.ws;
    const int tid = threadIdx.x, lane = tid & 63, r = lane & 31, h = lane >> 5;
    const int hw = __builtin_amdgcn_readfirstlane(tid >> 6);
    const bool helper = hw < 4;
    const int w = hw - 4, mtid = tid - 256;
    const int bid = blockIdx.x;
    const unsigned lds0 = (unsigned)(uintptr_t)smem;
    const rsrc_t rs = __builtin_amdgcn_make_buffer_rsrc(ws, 0, WS_END, 0x00020000);
    STAMP(0);
    u64 tag = 0;
#define GET_TAG() do { asm volatile("s_load_dwordx2 %0, %1, 0x0\n\ts_waitcnt lgkmcnt(0)" : "=s"(tag) : "s"(ws) : "memory"); tag += 1; } while (0)
    const int head = (bid & 7) * 4 + ((bid >> 3) & 3), qb = bid >> 5;
    const int nlo = (head * 20) >> 7, nhi = (head * 20 + 19) >> 7;
    const int mtO = (bid & 7) * 4 + ((bid >> 3) & 3), ntO = bid >> 5;
    const int lin = (bid & 7) * 30 + (bid >> 3), lh = lin >= 120, lrem = lin - 120 * lh;
    const int mt = lh * 8 + (lrem & 7), nt = lrem >> 3;
    const unsigned fG = WS_F_G + (mt * 15 + nt) * 8;
    const unsigned one = (r == 20) ? 0x3f803f80u : 0u;
    const f32x16 z16 = {};

    if (helper) {
        const unsigned fA0 = WS_F_G + ((lane & 15) * 15 + (lane < 32 ? 5 : 10) + ((lane & 16) ? nhi : nlo)) * 8;
        const unsigned fA1 = WS_F_G + ((qb * 2 + (lane & 1)) * 15 + ((lane & 2) ? nhi : nlo)) * 8;
        __builtin_amdgcn_s_setprio(3);
        const char* kvb = ws + WS_KV + head * 163840;
        auto kv_issue = [&](int grp) {
#pragma unroll
            for (int t = 0; t < 8; ++t) { int pc = grp * 32 + hw + 4 * t; if (pc == 31) pc = 30; glds16s(lane * 16, kvb + pc * 1024, lds0 + pc * 1024); }
        };
        auto q_issue = [&]() {
            const char* qbase = ws + WS_Q + head * 81920 + qb * 10240;
#pragma unroll
            for (int t = 0; t < 3; ++t) { const int pc = hw + 4 * t < 10 ? hw + 4 * t : 9; glds16s(lane * 16, qbase + pc * 1024, lds0 + L_Q + pc * 1024); }
        };
        if (bid < 240) {
            const int rot = (nt * 3 + mt * 5 + (mt >> 1)) % 10;
            const unsigned xo = (mt * 128 + 8 * hw + (lane >> 3)) * 1280 + (((lane & 7) ^ ((4 * hw + (lane >> 4)) & 7)) << 4);
            const unsigned wo = (4 * hw + (lane >> 4)) * 3840 + nt * 256 + (((lane & 15) ^ (((lane >> 4) << 2) | hw)) << 4);
            const unsigned dX = lds0 + 32768 + hw * 1024;
            auto issue = [&](int s) {
                int kt = (s >= 10 ? 19 - s : s) + rot; if (kt >= 10) kt -= 10;
                const char* xb = (const char*)P.x + kt * 128;
                const char* wb = (const char*)(s >= 10 ? P.wb : P.wa) + (size_t)kt * (64 * 3840);
                const unsigned dw = dX + 16384 + (s & 3) * 32768, dx = dX + ((s >= 10 ? 19 - s : s) & 3) * 32768;
#pragma unroll
                for (int i = 0; i < 4; ++i) glds16(wo, wb + i * 61440, dw + i * 4096);
                if (s < 10 || s >= 14) {
#pragma unroll
                    for (int i = 0; i < 4; ++i) glds16(xo, xb + i * 40960, dx + i * 4096);
                }
            };
#pragma unroll 1
            for (int n = 0; n < 20; ++n) {
                if (n == 2) { if (hw == 0 && lane < 16) *(LDS3 unsigned*)(lds + G_W + lane * 4) = 0u; GET_TAG(); WAIT_BAR(8); }
                else if (n == 3 || (n >= 11 && n <= 15)) { WAIT_BAR(8); }
                else if (n >= 5) { WAIT_BAR(16); }
                issue(n);
            }
            WAIT_BAR(16);
            WAIT_BAR(0);
        } else {
            if (hw == 0 && lane < 16) *(LDS3 unsigned*)(lds + G_W + lane * 4) = lane == 5 ? 16u : 0u;
            GET_TAG();
            WAIT_BAR(63);
            if (hw == 0) {
#pragma unroll 1
                for (int t = 0; t < PRE_SLEEP; ++t) __builtin_amdgcn_s_sleep(127);
            }
        }
        POLL_ONE(hw == 0, 6, fA0, fA1, G_FREE);
        kv_issue(0);
        lds_wait<2>(lds + G_FREE + 4, 16);
        q_issue(); kv_issue(1);
        asm volatile("s_waitcnt vmcnt(6)" ::: "memory");
        if (lane == 0) LDS_CNT_ADD(G_Q);
        kv_issue(3); kv_issue(4);
        WAIT_BAR(63);
        if (hw == 0) *(LDS3 u32x4*)(lds + lane * 16) = (u32x4){0u, 0u, 0u, 0u};
        if (hw == 3) glds16s(lane * 16, kvb + 31 * 1024, lds0 + 31 * 1024);
        kv_issue(2);
        WAIT_BAR(63);
        if (tid == 0 && bid < 240) set_flag(ws, fG + 2048, tag);
        STAMP(3);
        u32x4 phm; unsigned pht;
        {
            const u16* wp = P.wphi + head * 400 + (r < 20 ? r : 0);
            unsigned e[10];
#pragma unroll
            for (int j = 0; j < 8; ++j) e[j] = wp[(8 * h + j + 4 * (head & 1)) * 20];
            e[8] = wp[((head & 1 ? 0 : 16) + 2 * h) * 20]; e[9] = wp[((head & 1 ? 1 : 17) + 2 * h) * 20];
            const bool cp = bid < 160 && tid < 160;
            const int k0 = cp ? bid * 8 : 0, n = cp ? tid * 4 : 0;
            const u16* wsrc = (k0 < 640 ? P.woa + (size_t)k0 * DM : P.wob + (size_t)(k0 - 640) * DM) + n;
            u32x2 f[8];
#pragma unroll
            for (int j = 0; j < 8; ++j) f[j] = *(const u32x2*)(wsrc + (size_t)j * DM);
            DRAIN();
            if (lane == 0) LDS_CNT_ADD(C_KVL);
            phm = (u32x4){e[0] | (e[1] << 16), e[2] | (e[3] << 16), e[4] | (e[5] << 16), e[6] | (e[7] << 16)}; pht = e[8] | (e[9] << 16);
            if (r >= 20) { phm = (u32x4){0u, 0u, 0u, 0u}; pht = 0u; }
            const unsigned dp = WS_WT + (((n >> 4) * 40 + (k0 >> 5)) * 64 + ((k0 >> 3) & 3) * 16 + (n & 15)) * 16;
            if (cp) {
#pragma unroll
                for (int q = 0; q < 2; ++q) {
                    const u32x4 lo = {(f[0][q] & 0xffffu) | (f[1][q] << 16), (f[2][q] & 0xffffu) | (f[3][q] << 16), (f[4][q] & 0xffffu) | (f[5][q] << 16), (f[6][q] & 0xffffu) | (f[7][q] << 16)};
                    const u32x4 hi = {(f[0][q] >> 16) | (f[1][q] & 0xffff0000u), (f[2][q] >> 16) | (f[3][q] & 0xffff0000u), (f[4][q] >> 16) | (f[5][q] & 0xffff0000u), (f[6][q] >> 16) | (f[7][q] & 0xffff0000u)};
                    ST16(lo, dp + q * 32); ST16(hi, dp + q * 32 + 16);
                }
            }
        }
        const int tokS = qb * 256 + hw * 64, m7 = tokS >> 7, t4 = (tokS >> 5) & 3;
        {
            const unsigned pt3 = (lane >> 1) & 3;
            const unsigned tb = WS_F_G + 2048 + ((qb * 2 + ((lane >> 3) & 1)) * 15 + ((lane & 1) ? nhi : nlo) + (pt3 < 3 ? pt3 : 0) * 5) * 8;
            POLL_ONE(hw == 0, 4, tb, WS_F_SD + (qb * 2 + (lane & 1)) * 8, C_P1);
        }
        const unsigned om = t4 * 1024 + lane * 16, ot = 4096 + (t4 * 32 + r) * 8 + 4 * h;
        u32x4 qf[2][3]; float gS[2];
        {
            const unsigned kb = WS_KV + (NH + head) * 163840 + (2 * (m7 & 7) + (m7 >> 3)) * 10240, qbb = WS_Q + (NH + head) * 81920 + m7 * 5120;
            const unsigned ov = 5120 + t4 * 1280 + h * 320 + (r < 20 ? r : 19) * 16;
            u32x4 e0[2], w0[2], w1[2], eq[2]; unsigned et[2], eqt[2];
#pragma unroll
            for (int i = 0; i < 2; ++i) {
                e0[i] = LD16(kb + om + i * 1024); et[i] = LD4(kb + ot + i * 256); w0[i] = LD16(kb + ov + i * 1280); w1[i] = LD16(kb + ov + i * 1280 + 640);
                eq[i] = LD16(qbb + om + i * 1024); eqt[i] = LD4(qbb + ot + i * 256); gS[i] = __uint_as_float(LD4(WS_G + (tokS + i * 32 + r) * 4));
            }
            u32x4 i0, i1;
#pragma unroll
            for (int j = 0; j < 4; ++j) {
                const int e = r - 8 * h - 2 * j;
                i0[j] = e == 0 ? 0x3f80u : (e == 1 ? 0x3f800000u : 0u);
                i1[j] = e == 16 ? 0x3f80u : (e == 17 ? 0x3f800000u : 0u);
            }
            f32x16 k0 = {}, k1 = {};
#pragma unroll
            for (int i = 0; i < 2; ++i) {
                if (r >= 20) { w0[i] = (u32x4){one, one, one, one}; w1[i] = w0[i]; }
                u32x4 fp, fn, ft;
                feature_map(phm, pht, e0[i], et[i], h, fp, fn, ft);
                f32x16 T0 = MFMA32_16(fp, i0, z16); T0 = MFMA32_16(fn, i1, T0);
                f32x16 T1 = MFMA32_16(ft, i0, z16);
                u32x4 b00, b01, b10, b11;
#pragma unroll
                for (int j = 0; j < 4; ++j) {
                    b00[j] = pk2(T0[2 * j], T0[2 * j + 1]); b01[j] = pk2(T0[8 + 2 * j], T0[9 + 2 * j]);
                    b10[j] = pk2(T1[2 * j], T1[2 * j + 1]); b11[j] = pk2(T1[8 + 2 * j], T1[9 + 2 * j]);
                }
                k0 = MFMA32_16(w0[i], b00, k0); k0 = MFMA32_16(w1[i], b01, k0);
                k1 = MFMA32_16(w0[i], b10, k1); k1 = MFMA32_16(w1[i], b11, k1);
            }
            prog_wait<8>(lds + C_PR, lane, 1);
            *(LDS3 float*)(lds + X_G + (hw * 64 + lane) * 4) = h ? gS[1] : gS[0];
            if (hw * 64 + lane < 144) *(LDS3 u32x4*)(lds + X_KVF + (hw * 64 + lane) * 16) = (u32x4){0u, 0u, 0u, 0u};
            LDS3 char* sp = lds + X_SLOT + hw * 4096;
#pragma unroll
            for (int gq = 0; gq < 3; ++gq) {
                *(LDS3 f32x4*)(sp + gq * 1024 + lane * 16) = (f32x4){k0[4 * gq], k0[4 * gq + 1], k0[4 * gq + 2], k0[4 * gq + 3]};
                if (r < 8) *(LDS3 f32x4*)(sp + 3072 + (gq * 16 + h * 8 + r) * 16) = (f32x4){k1[4 * gq], k1[4 * gq + 1], k1[4 * gq + 2], k1[4 * gq + 3]};
            }
            asm volatile("s_waitcnt vmcnt(0) lgkmcnt(0)" ::: "memory");
            unsigned arr = 0;
            if (lane == 0) arr = LDS_CNT_ADD(C_KV);
            if (__builtin_amdgcn_readfirstlane(arr) == 3) {
                __builtin_amdgcn_fence(__ATOMIC_ACQUIRE, "wavefront");
#pragma unroll
                for (int i = 0; i < 4; ++i) {
                    const int qd = lane + 64 * i;
                    LDS3 const char* s0 = lds + X_SLOT + qd * 16;
                    const f32x4 s = (*(LDS3 const f32x4*)s0 + *(LDS3 const f32x4*)(s0 + 4096)) + (*(LDS3 const f32x4*)(s0 + 8192) + *(LDS3 const f32x4*)(s0 + 12288));
                    if (qd < 240) ST16(__builtin_bit_cast(u32x4, s), WS_KVP + (head * 8 + qb) * 4096 + qd * 16);
                }
                DRAIN();
                if (lane == 0) set_flag(ws, WS_F_KV + (head * 8 + qb) * 8, tag);
            }
#pragma unroll
            for (int i = 0; i < 2; ++i) feature_map(phm, pht, eq[i], eqt[i], h, qf[i][0], qf[i][1], qf[i][2]);
        }
        STAMP(4);
        POLL_ONE(hw == 0, 4, WS_F_KV + (head * 8 + (lane & 7)) * 8, WS_F_KV + (head * 8 + (lane & 7)) * 8, C_P2);
        {
            const int qd = hw * 60 + (lane < 60 ? lane : 59);
            f32x4 pp[8];
#pragma unroll
            for (int p = 0; p < 8; ++p) pp[p] = __builtin_bit_cast(f32x4, LD16(WS_KVP + (head * 8 + p) * 4096 + qd * 16));
            const f32x4 s4 = ((pp[0] + pp[1]) + (pp[2] + pp[3])) + ((pp[4] + pp[5]) + (pp[6] + pp[7]));
            const int gq = qd < 192 ? qd >> 6 : (qd - 192) >> 4, hs = qd < 192 ? (qd >> 5) & 1 : (qd >> 3) & 1, sg = qd < 192 ? qd & 31 : 32 + (qd & 7);
            const unsigned a = pk2(s4[0], s4[1]), b = pk2(s4[2], s4[3]);
            LDS3 u16* kp = (LDS3 u16*)(lds + X_KVF + (8 * gq + 4 * hs) * 96 + sg * 2);
            kp[0] = (u16)a; kp[48] = (u16)(a >> 16); kp[96] = (u16)b; kp[144] = (u16)(b >> 16);
            asm volatile("s_waitcnt lgkmcnt(0)" ::: "memory");
            if (lane == 0) LDS_CNT_ADD(C_KVF);
            lds_wait<2>(lds + C_KVF, 4);
        }
        {
            LDS3 const char* kp = lds + X_KVF + (r < 24 ? r : 23) * 96 + h * 16;
            const u32x4 a0 = *(LDS3 const u32x4*)kp, a1 = *(LDS3 const u32x4*)(kp + 32), a2 = *(LDS3 const u32x4*)(kp + 64);
#pragma unroll
            for (int i = 0; i < 2; ++i) {
                f32x16 d = MFMA32_16(a0, qf[i][0], z16); d = MFMA32_16(a1, qf[i][1], d); d = MFMA32_16(a2, qf[i][2], d);
                const float den = upper_half(d[8]) + 1e-6f;
                float o[12];
#pragma unroll
                for (int j = 0; j < 12; ++j) o[j] = d[j];
                store_o(rs, o, (1.f - gS[i]) / den, tokS + i * 32 + r, 640 + head * 20, h);
            }
            DRAIN();
            unsigned arr = 0;
            if (lane == 0) arr = LDS_CNT_ADD(C_OB);
            if (__builtin_amdgcn_readfirstlane(arr) == 3 && lane == 0) set_flag(ws, WS_F_OB + (qb * 32 + head) * 8, tag);
        }
        STAMP(5);
        f32x4 c[5];
#pragma unroll
        for (int n = 0; n < 5; ++n) c[n] = (f32x4){0.f, 0.f, 0.f, 0.f};
        const char* wsrc = ws + WS_WT + (size_t)ntO * 5 * 40960;
        {
            if (hw == 0) wait_flags<8>(ws, WS_F_KV + lane * 8, WS_F_KV + 512 + lane * 8, tag);
            POLL_ONE(hw == 0, 8, WS_F_KV + 1024 + lane * 8, WS_F_KV + 1536 + lane * 8, C_P5);
#pragma unroll 1
            for (int t = 0; t < 5; ++t) {
                if (t == 0) prog_wait<8>(lds + C_PR, lane, 4); else if (t == 3) prog_wait<4>(lds + C_PR, lane, 5);
                const int ks = hw + 4 * t < 19 ? hw + 4 * t : 18;
#pragma unroll
                for (int n = 0; n < 5; ++n) glds16s(lane * 16, wsrc + (n * 40 + 20 + ks) * 1024, lds0 + 4096 + (ks * 5 + n) * 1024);
            }
            POLL_ONE(hw == 0, 4, WS_F_OB + ((mtO >> 2) * 32 + r) * 8, WS_F_OB + ((mtO >> 2) * 32 + r) * 8, C_P3);
            const unsigned ab = WS_AO + ((mtO * 4 + hw) * 40 + 20) * 1024 + lane * 16;
            u32x4 af[20];
#pragma unroll
            for (int ks = 0; ks < 20; ++ks) af[ks] = LD16(ab + ks * 1024);
            asm volatile("s_waitcnt vmcnt(20)" ::: "memory");
            if (lane == 0) LDS_CNT_ADD(C_BX);
            lds_wait<2>(lds + C_BX, 4);
            u32x4 wf[3][5], wl[5];
            LDS3 const char* wp = lds + 4096 + lane * 16;
#pragma unroll
            for (int ks = 0; ks < 2; ++ks)
#pragma unroll
                for (int n = 0; n < 5; ++n) wf[ks][n] = *(LDS3 const u32x4*)(wp + (ks * 5 + n) * 1024);
#pragma unroll
            for (int ks = 0; ks < 20; ++ks) {
                if (ks + 2 < 19) {
#pragma unroll
                    for (int n = 0; n < 5; ++n) wf[(ks + 2) % 3][n] = *(LDS3 const u32x4*)(wp + ((ks + 2) * 5 + n) * 1024);
                }
                if (ks == 8) {
#pragma unroll
                    for (int n = 0; n < 5; ++n) wl[n] = LD16(WS_WT + ((ntO * 5 + n) * 40 + 39) * 1024 + lane * 16);
                }
                SBAR();
#pragma unroll
                for (int n = 0; n < 5; ++n) c[n] = MFMA16_32(ks < 19 ? wf[ks % 3][n] : wl[n], af[ks], c[n]);
                SBAR();
            }
            if (lane == 0) LDS_CNT_ADD(C_BX);
            lds_wait<2>(lds + C_BX, 8);
        }
        STAMP(6);
        prog_wait<4>(lds + C_PR, lane, 7);
#pragma unroll 1
        for (int n = 0; n < 5; ++n)
#pragma unroll
            for (int t = 0; t < 5; ++t) { const int ks = hw + 4 * t; glds16s(lane * 16, wsrc + (n * 40 + ks) * 1024, lds0 + L_WA + (n * 20 + ks) * 1024); }
#pragma unroll
        for (int n = 0; n < 3; ++n) *(LDS3 f32x4*)(lds + X_FIN + hw * 3072 + n * 1024 + lane * 16) = c[n];
        DRAIN();
        if (lane == 0) LDS_CNT_ADD(C_WA);
        prog_wait<4>(lds + C_PR, lane, 8);
#pragma unroll
        for (int n = 3; n < 5; ++n) *(LDS3 f32x4*)(lds + 143360 + hw * 2048 + (n - 3) * 1024 + lane * 16) = c[n];
        asm volatile("s_waitcnt lgkmcnt(0)" ::: "memory");
        if (lane == 0) __hip_atomic_store((LDS3 unsigned*)(lds + C_BP + hw * 4), 1u, __ATOMIC_RELAXED, __HIP_MEMORY_SCOPE_WORKGROUP);
        STAMP(7);
        asm volatile("mega_code_end2:");
    } else {
        const int qs = w & 3, kh = w >> 2;
        const int q0 = qb * 256 + qs * 64;
        {
            unsigned sink;
            if (w == 0 && (WARM_MODE & 1)) {
                const char* pc0; int len, len2;
                asm volatile("s_getpc_b64 %0\nmega_code_mark:\n\ts_mov_b32 %1, mega_code_end-mega_code_mark\n\ts_mov_b32 %2, mega_code_end2-mega_code_mark" : "=s"(pc0), "=s"(len), "=s"(len2));
                len = len > len2 ? len : len2;
                const int o = ((bid >> 3) * 64 + lane) * 64;
                if (o + 4 <= len) asm volatile("global_load_dword %0, %1, %2" : "=v"(sink) : "v"(o), "s"(pc0) : "memory");
            } else if (w >= 1 && w <= 6 && (WARM_MODE & 2)) {
                const unsigned o = ((w - 1) * 64 + lane) * 65536 + 65532;
                if (o < WS_END) asm volatile("global_load_dword %0, %1, %2 sc1" : "=v"(sink) : "v"(o), "s"(ws) : "memory");
            } else if (w == 7 && (WARM_MODE & 2)) {
                if (lane < 40) asm volatile("global_load_dword %0, %1, %2 sc1" : "=v"(sink) : "v"(lane * 65536), "s"(P.out) : "memory");
            }
            asm volatile("s_waitcnt vmcnt(0)" ::: "memory");
        }
        GET_TAG();
        if (bid < 240) {
            const int part = nt / 5, c0 = (nt - part * 5) * 128;
            const int wm = w & 1, wn = w >> 1;
            constexpr int RB = 32768;
            const int xr = wm * 64 + r;
            const unsigned xa = RB + xr * 128 + ((h ^ ((xr >> 1) & 7)) << 4);
            unsigned wt[2];
#pragma unroll
            for (int t = 0; t < 2; ++t) {
                const int q = (lane & 15) >> 2, p = lane & 3, blk = (lane >> 4) & 1, row = 8 * h + 4 * t + q;
                wt[t] = RB + 16384 + row * 256 + (((4 * wn + 2 * blk + (p >> 1)) ^ ((q << 2) | (2 * h + t))) << 4) + 8 * (p & 1);
            }
            auto gbody = [&](auto trc) {
                constexpr bool TR = decltype(trc)::value;
                u32x4 xf[2][8], wf[2][4];
                auto rd = [&](auto bc, int s) {
                    constexpr int B = decltype(bc)::value;
                    LDS3 const char* sb = lds + (s & 3) * 32768;
                    LDS3 const char* sx = lds + ((s >= 10 ? 19 - s : s) & 3) * 32768;
#pragma unroll
                    for (int ks = 0; ks < 4; ++ks) {
                        xf[B][2 * ks] = *(LDS3 const u32x4*)(sx + (xa ^ (ks * 32)));
                        xf[B][2 * ks + 1] = *(LDS3 const u32x4*)(sx + ((xa + 4096) ^ (ks * 32)));
                        const u32x2 lo = trread(sb + wt[0] + ks * 4096), hi = trread(sb + wt[1] + ks * 4096);
                        wf[B][ks] = (u32x4){lo[0], lo[1], hi[0], hi[1]};
                    }
                };
                f32x16 acc0, acc1;
                auto step = [&](auto bc, int s) {
                    constexpr int B = decltype(bc)::value;
                    if (s == GA_PUB) {
                        DRAIN();
                        unsigned arr = 0;
                        if (lane == 0) arr = LDS_CNT_ADD(G_ST);
                        if (__builtin_amdgcn_readfirstlane(arr) == 7 && lane == 0) set_flag(ws, fG, tag);
                    }
                    __builtin_amdgcn_s_waitcnt(0xC07F);
                    if (s < 18) BAR();
                    else if (lane == 0) LDS_CNT_ADD(G_FREE + 4);
                    SBAR();
                    rd(std::integral_constant<int, B ^ 1>{}, s + 1);
#pragma unroll
                    for (int ks = 0; ks < 4; ++ks) {
                        if constexpr (TR) { acc0 = MFMA32_16(wf[B][ks], xf[B][2 * ks], acc0); acc1 = MFMA32_16(wf[B][ks], xf[B][2 * ks + 1], acc1); }
                        else              { acc0 = MFMA32_16(xf[B][2 * ks], wf[B][ks], acc0); acc1 = MFMA32_16(xf[B][2 * ks + 1], wf[B][ks], acc1); }
                    }
#pragma unroll
                    for (int i = 0; i < 4; ++i) { __builtin_amdgcn_sched_group_barrier(0x8, 1, 0); __builtin_amdgcn_sched_group_barrier(0x2, 2, 0); __builtin_amdgcn_sched_group_barrier(0x100, 4, 0); }
                    __builtin_amdgcn_sched_group_barrier(0x8, 4, 0);
                    SBAR();
                };
                WAIT_BAR(63);
                rd(std::integral_constant<int, 0>{}, 0);
#pragma unroll 1
                for (int mat = 0; mat < 2; ++mat) {
                    acc0 = (f32x16){}; acc1 = (f32x16){};
#pragma unroll 1
                    for (int kk = 0; kk < 10; kk += 2) {
                        step(std::integral_constant<int, 0>{}, mat * 10 + kk);
                        step(std::integral_constant<int, 1>{}, mat * 10 + kk + 1);
                    }
                    const int cpos = 2 * (mt & 7) + (mt >> 3);
                    if constexpr (TR) {
                        const float sc = (part == 0 && mat == 0) ? QSCALE : 1.f;
                        const unsigned base = part == 0 ? WS_Q + mat * NH * 81920 + mt * 5120 : WS_KV + mat * NH * 163840 + cpos * 10240;
                        const unsigned hs = part == 0 ? 81920 : 163840;
#pragma unroll
                        for (int pr = 0; pr < 2; ++pr) {
                            const int G8 = ((c0 + 32 * wn) >> 3) + 2 * pr + h, pp = G8 / 5, gi = G8 - pp * 5;
                            const unsigned hb0 = base + (2 * pp + (gi > 2)) * hs;
#pragma unroll
                            for (int i = 0; i < 2; ++i) {
                                const f32x16& a = i ? acc1 : acc0;
                                const int tl = wm * 64 + i * 32 + r;
                                const auto s0 = __builtin_amdgcn_permlane32_swap(pk2(a[8 * pr] * sc, a[8 * pr + 1] * sc), pk2(a[8 * pr + 4] * sc, a[8 * pr + 5] * sc), false, false);
                                const auto s1 = __builtin_amdgcn_permlane32_swap(pk2(a[8 * pr + 2] * sc, a[8 * pr + 3] * sc), pk2(a[8 * pr + 6] * sc, a[8 * pr + 7] * sc), false, false);
                                if (gi != 2) { const u32x4 ov = {s0[0], s1[0], s0[1], s1[1]}; ST16(ov, hb0 + (tl >> 5) * 1024 + (gi > 2 ? gi - 3 : gi) * 512 + r * 16); }
                                else { const u32x2 o0 = {s0[0], s1[0]}, o1 = {s0[1], s1[1]}; ST8(o0, hb0 + 4096 + tl * 8); ST8(o1, hb0 + hs + 4096 + tl * 8); }
                            }
                        }
                    } else {
                        const int n = c0 + 32 * wn + r, hd = n / 20, d = n - hd * 20, rho = (hd & 1) ? (d < 4 ? d + 16 : d - 4) : d;
                        const unsigned base = WS_KV + (mat * NH + hd) * 163840 + cpos * 10240 + 5120 + h * 320 + rho * 16;
#pragma unroll
                        for (int i = 0; i < 2; ++i)
#pragma unroll
                            for (int s2 = 0; s2 < 2; ++s2) {
                                const f32x16& a = i ? acc1 : acc0;
                                u32x4 ov = {pk2(a[8 * s2], a[8 * s2 + 1]), pk2(a[8 * s2 + 2], a[8 * s2 + 3]), pk2(a[8 * s2 + 4], a[8 * s2 + 5]), pk2(a[8 * s2 + 6], a[8 * s2 + 7])};
                                ST16(ov, base + (wm * 4 + i * 2 + s2) * 640);
                            }
                    }
                    if (mat == 0) STAMP(1);
                }
            };
            if (part < 2) gbody(std::true_type{}); else gbody(std::false_type{});
        } else {
            const int sj = bid - 240;
            WAIT_BAR(63);
            {
                const u32x4 ga = *(const u32x4*)(P.gw + lane * 8), gb = *(const u32x4*)(P.gw + 512 + (lane & 15) * 8);
                u32x4 xa[16], xb[4];
                const u16* xr = P.x + (size_t)(sj * 128 + w * 16) * DM;
#pragma unroll
                for (int rr = 0; rr < 16; ++rr) xa[rr] = *(const u32x4*)(xr + rr * DM + lane * 8);
#pragma unroll
                for (int i = 0; i < 4; ++i) xb[i] = *(const u32x4*)(xr + (i * 4 + (lane >> 4)) * DM + 512 + (lane & 15) * 8);
                float res = 0.f;
#pragma unroll
                for (int rr = 0; rr < 16; ++rr) {
                    float s = 0.f, s2 = 0.f;
#pragma unroll
                    for (int j = 0; j < 4; ++j) { s += bf_lo(xa[rr][j]) * bf_lo(ga[j]) + bf_hi(xa[rr][j]) * bf_hi(ga[j]); s2 += bf_lo(xb[rr >> 2][j]) * bf_lo(gb[j]) + bf_hi(xb[rr >> 2][j]) * bf_hi(gb[j]); }
                    if ((lane >> 4) == (rr & 3)) s += s2;
#pragma unroll
                    for (int o = 32; o >= 1; o >>= 1) s += __shfl_xor(s, o);
                    if (lane == rr) res = s;
                }
                if (lane < 16) ST4(__float_as_uint(1.f / (1.f + __expf(-res))), WS_G + (sj * 128 + w * 16 + lane) * 4);
            }
            DRAIN();
            unsigned arr = 0;
            if (lane == 0) arr = LDS_CNT_ADD(G_ST);
            if (__builtin_amdgcn_readfirstlane(arr) == 7 && lane == 0) set_flag(ws, WS_F_SD + sj * 8, tag);
        }
        STAMP(2);
        const unsigned hb = lds0 + kh * 10240;
        const unsigned laneK = hb + lane * 16, laneT = hb + 4096 + r * 8 + h * 4, laneV = hb + 5120 + h * 320 + (r < 20 ? r : 19) * 16;
        const u64 vmask = 0x000fffff000fffffull;
        u32x4 v0a = {one, one, one, one}, v0b = v0a, v1 = v0a;
        u32x4 qA, qB, ka; unsigned qtA, qtB, kt;
        {
            lds_wait(lds + G_Q, 4);
            LDS3 const char* qp = lds + L_Q + (qs >> 1) * 5120;
            const unsigned om = (qs & 1) * 2048 + lane * 16, ot = 4096 + ((qs & 1) * 64 + r) * 8 + 4 * h;
            qA = *(LDS3 const u32x4*)(qp + om); qB = *(LDS3 const u32x4*)(qp + om + 1024);
            qtA = *(LDS3 const unsigned*)(qp + ot); qtB = *(LDS3 const unsigned*)(qp + ot + 256);
            DSR128(ka, laneK, 0); DSR32(kt, laneT, 0); DSR128M2(v0a, laneV, 0, v1, laneV, 640);
            asm volatile("s_waitcnt lgkmcnt(0)\n\ts_barrier" : "+v"(ka), "+v"(kt), "+v"(v0a), "+v"(v1), "+v"(qA), "+v"(qB), "+v"(qtA), "+v"(qtB) :: "memory");
        }
        STAMP(5);
        float mA, mB; u32x2 qTA, qTB; f32x16 sA, sB;
        {
            const u32x2 t0 = {kt, 0x3f80u}, a0 = {qtA, 0u}, b0 = {qtB, 0u};
            const f32x16 s = MFMA32_16(ka, qA, z16);
            const f32x16 s0 = MFMA32_8(t0, a0, s);
            f32x16 s1 = MFMA32_16(ka, qB, z16); s1 = MFMA32_8(t0, b0, s1);
            float x0 = s0[0], x1 = s1[0];
#pragma unroll
            for (int i = 1; i < 16; ++i) { x0 = fmaxf(x0, s0[i]); x1 = fmaxf(x1, s1[i]); }
            x0 = -xhalf_max(x0); x1 = -xhalf_max(x1);
            const unsigned hh = pk2(x0, x1);
            const unsigned ll = pk2(x0 - bf_lo(hh), x1 - bf_hi(hh));
            mA = -(bf_lo(hh) + bf_lo(ll)); mB = -(bf_hi(hh) + bf_hi(ll));
            const unsigned sel = h ? ll : hh;
            qTA = (u32x2){qtA, sel & 0xffffu}; qTB = (u32x2){qtB, sel >> 16};
            sA = MFMA32_8(t0, qTA, s);
        }
        f32x16 oA = {}, oB = {};
        u32x4 pa, pb = {0u, 0u, 0u, 0u};
#define EX4(S, i, Pv, j) do { Pv[j] = pk2(ex2(S[i]), ex2(S[i + 1])); Pv[j + 1] = pk2(ex2(S[i + 2]), ex2(S[i + 3])); PIN(Pv); SBAR(); } while (0)
#define KT2 ((u32x2){kt, 0x3f80u})
#define TILE(V0C, V0N, aK1, oK1, aT1, oT1, oV1, aV0, oV0) do { \
            sB = MFMA32_16(ka, qB, z16); SBAR(); DSR128(ka, aK1, oK1); EX4(sA, 0, pa, 0); \
            oB = MFMA32_16(v1, pb, oB); SBAR(); DSR128M2(v1, aV, oV1, V0N, aV0, oV0); EX4(sA, 4, pa, 2); \
            sB = MFMA32_8(KT2, qTB, sB); SBAR(); DSR32(kt, aT1, oT1); EX4(sA, 8, pb, 0); \
            oA = MFMA32_16(V0C, pa, oA); SBAR(); EX4(sA, 12, pb, 2); \
            LWAIT(1); sA = MFMA32_16(ka, qA, z16); SBAR(); EX4(sB, 0, pa, 0); \
            oA = MFMA32_16(v1, pb, oA); SBAR(); EX4(sB, 4, pa, 2); \
            LWAIT(0); sA = MFMA32_8(KT2, qTA, sA); SBAR(); EX4(sB, 8, pb, 0); \
            oB = MFMA32_16(V0C, pa, oB); SBAR(); EX4(sB, 12, pb, 2); \
        } while (0)
        unsigned aK = laneK, aT = laneT, aV = laneV;
#pragma unroll 1
        for (int c = 0; c < 8; ++c) {
            if (c == 1) lds_wait(lds + C_KVL, 4);
            if (c >= 1) {
                unsigned pp;
                asm volatile("ds_write_b32 %1, %2\n\tds_read_b32 %0, %3\n\ts_waitcnt lgkmcnt(0)" : "=&v"(pp) : "v"(lds0 + C_PR + w * 4), "v"(c), "v"(lds0 + C_PR + (w ^ 4) * 4) : "memory");
                const int pc = __builtin_amdgcn_readfirstlane(pp);
                if (pc > c || (pc == c && ((c ^ kh) & 1))) __builtin_amdgcn_s_setprio(1); else __builtin_amdgcn_s_setprio(0);
            } else if (kh) __builtin_amdgcn_s_setprio(1);
            const unsigned nx = (c < 7 ? c + 1 : 7) * 20480;
            const unsigned nK = laneK + nx, nT = laneT + nx, nV = laneV + nx;
            SBAR();
            TILE(v0a, v0b, aK, 1024, aT, 256, 640,  aV, 1280);
            TILE(v0b, v0a, aK, 2048, aT, 512, 1920, aV, 2560);
            if (c == 0) { WAIT_BAR(0); SBAR(); }
            TILE(v0a, v0b, aK, 3072, aT, 768, 3200, aV, 3840);
            TILE(v0b, v0a, nK, 0,    nT, 0,   4480, nV, 0);
            aK = nK; aT = nT; aV = nV;
        }
        oB = MFMA32_16(v1, pb, oB);
#undef TILE
#undef EX4
        if (lane == 0) __hip_atomic_store((LDS3 unsigned*)(lds + C_PR + w * 4), 8u, __ATOMIC_RELAXED, __HIP_MEMORY_SCOPE_WORKGROUP);
        STAMP(6);
        {
            float chk = 0.f;
#pragma unroll
            for (int i = 0; i < 12; ++i) chk += fabsf(oA[i]) * 0.f + fabsf(oB[i]) * 0.f;
            bool bad = !(chk == 0.f);
            if (h) bad = bad || !(oA[8] >= 0x1p-60f && oA[8] <= 0x1p60f && oB[8] >= 0x1p-60f && oB[8] <= 0x1p60f);
            if (__builtin_expect(__any(bad), 0)) {
                mA = -INFINITY; mB = -INFINITY;
#pragma unroll
                for (int i = 0; i < 16; ++i) { oA[i] = 0.f; oB[i] = 0.f; }
                const u32x2 a0 = {qtA, 0u}, b0 = {qtB, 0u};
#pragma unroll 1
                for (int t = 0; t < 32; ++t) {
                    const unsigned kb = WS_KV + head * 163840 + (2 * (t >> 2) + kh) * 10240, t4 = t & 3;
                    const u32x4 k0 = LD16(kb + t4 * 1024 + lane * 16);
                    const u32x2 t0 = {LD4(kb + 4096 + (t4 * 32 + r) * 8 + 4 * h), 0u};
                    u32x4 w0 = LD16(kb + 5120 + t4 * 1280 + h * 320 + (r < 20 ? r : 19) * 16), w1 = LD16(kb + 5120 + t4 * 1280 + 640 + h * 320 + (r < 20 ? r : 19) * 16);
                    if (r >= 20) { w0 = (u32x4){one, one, one, one}; w1 = w0; }
                    f32x16 s0 = MFMA32_16(k0, qA, z16); s0 = MFMA32_8(t0, a0, s0);
                    f32x16 s1 = MFMA32_16(k0, qB, z16); s1 = MFMA32_8(t0, b0, s1);
                    float x0 = s0[0], x1 = s1[0];
#pragma unroll
                    for (int i = 1; i < 16; ++i) { x0 = fmaxf(x0, s0[i]); x1 = fmaxf(x1, s1[i]); }
                    x0 = fmaxf(xhalf_max(x0), mA); x1 = fmaxf(xhalf_max(x1), mB);
                    const float g0 = (mA == x0) ? 1.f : ex2(mA - x0), g1 = (mB == x1) ? 1.f : ex2(mB - x1);
                    mA = x0; mB = x1;
#pragma unroll
                    for (int i = 0; i < 16; ++i) { oA[i] *= g0; oB[i] *= g1; }
#pragma unroll
                    for (int i = 0; i < 4; ++i) { pa[i] = pk2(ex2(s0[2 * i] - x0), ex2(s0[2 * i + 1] - x0)); pb[i] = pk2(ex2(s0[8 + 2 * i] - x0), ex2(s0[9 + 2 * i] - x0)); }
                    oA = MFMA32_16(w0, pa, oA); oA = MFMA32_16(w1, pb, oA);
#pragma unroll
                    for (int i = 0; i < 4; ++i) { pa[i] = pk2(ex2(s1[2 * i] - x1), ex2(s1[2 * i + 1] - x1)); pb[i] = pk2(ex2(s1[8 + 2 * i] - x1), ex2(s1[9 + 2 * i] - x1)); }
                    oB = MFMA32_16(w0, pa, oB); oB = MFMA32_16(w1, pb, oB);
                }
            }
        }
        lds_wait<4>(lds + C_BX, 8);
        const float gO = *(LDS3 const float*)(lds + X_G + (qs * 64 + kh * 32 + r) * 4);
        {
            LDS3 char* xp = lds + X_MRG + w * 2816;
            const f32x16& s = kh ? oA : oB;
            *(LDS3 f32x4*)(xp + lane * 16) = (f32x4){s[0], s[1], s[2], s[3]};
            *(LDS3 f32x4*)(xp + 1024 + lane * 16) = (f32x4){s[4], s[5], s[6], s[7]};
            if (!h) *(LDS3 f32x4*)(xp + 2048 + r * 16) = (f32x4){s[8], s[9], s[10], s[11]};
            else *(LDS3 f32x2*)(xp + 2560 + r * 8) = (f32x2){s[8], kh ? mA : mB};
            asm volatile("s_waitcnt lgkmcnt(0)" ::: "memory");
            if (lane == 0) __hip_atomic_store((LDS3 unsigned*)(lds + C_MRG + w * 4), 1u, __ATOMIC_RELAXED, __HIP_MEMORY_SCOPE_WORKGROUP);
        }
        lds_wait<2>(lds + C_MRG + (w ^ 4) * 4, 1);
        bool lastw;
        {
            LDS3 const char* xp = lds + X_MRG + (w ^ 4) * 2816;
            const f32x4 p0 = *(LDS3 const f32x4*)(xp + lane * 16), p1 = *(LDS3 const f32x4*)(xp + 1024 + lane * 16), p2 = *(LDS3 const f32x4*)(xp + 2048 + r * 16);
            const f32x2 lm = *(LDS3 const f32x2*)(xp + 2560 + r * 8);
            const f32x16& s = kh ? oB : oA;
            const float mo = kh ? mB : mA, mp = lm[1];
            const float mx = fmaxf(mo, mp);
            const float fo = (mo == mx) ? 1.f : ex2(mo - mx), fq = (mp == mx) ? 1.f : ex2(mp - mx);
            float o[12];
#pragma unroll
            for (int i = 0; i < 4; ++i) { o[i] = s[i] * fo + p0[i] * fq; o[4 + i] = s[4 + i] * fo + p1[i] * fq; o[8 + i] = s[8 + i] * fo + p2[i] * fq; }
            const float l = upper_half(s[8]) * fo + lm[0] * fq;
            store_o(rs, o, gO / l, q0 + kh * 32 + r, head * 20, h);
            DRAIN();
            unsigned arr = 0;
            if (lane == 0) arr = LDS_CNT_ADD(C_OA);
            lastw = __builtin_amdgcn_readfirstlane(arr) == 7;
            if (lastw && lane == 0) set_flag(ws, WS_F_OA + (qb * 32 + head) * 8, tag);
        }
        STAMP(7);
        {
            const int c16 = lane & 15, g = lane >> 4, tt = w & 3, k0 = kh * 10;
            POLL_ONE(lastw, 4, WS_F_OA + ((mtO >> 2) * 32 + r) * 8, WS_F_OA + ((mtO >> 2) * 32 + r) * 8, C_P4);
            STAMP(9);
            const unsigned ab = WS_AO + ((mtO * 4 + tt) * 40 + k0) * 1024 + lane * 16;
            u32x4 af[10];
#pragma unroll
            for (int ks = 0; ks < 10; ++ks) af[ks] = LD16(ab + ks * 1024);
            lds_wait(lds + C_WA, 4);
            lds_wait(lds + C_OA, 8);
            f32x4 c[5];
#pragma unroll
            for (int n = 0; n < 5; ++n) c[n] = (f32x4){0.f, 0.f, 0.f, 0.f};
#pragma unroll
            for (int ks = 0; ks < 10; ++ks)
#pragma unroll
                for (int n = 0; n < 5; ++n) c[n] = MFMA16_32(*(LDS3 const u32x4*)(lds + L_WA + (n * 20 + k0 + ks) * 1024 + lane * 16), af[ks], c[n]);
            LDS3 char* xp = lds + X_MRG + tt * 5120 + lane * 16;
            if (w < 4) { *(LDS3 f32x4*)(xp + 3072) = c[3]; *(LDS3 f32x4*)(xp + 4096) = c[4]; }
            else { *(LDS3 f32x4*)(xp) = c[0]; *(LDS3 f32x4*)(xp + 1024) = c[1]; *(LDS3 f32x4*)(xp + 2048) = c[2]; }
            asm volatile("s_waitcnt lgkmcnt(0)" ::: "memory");
            if (lane == 0) __hip_atomic_store((LDS3 unsigned*)(lds + C_FIN + w * 4), 1u, __ATOMIC_RELAXED, __HIP_MEMORY_SCOPE_WORKGROUP);
            lds_wait(lds + C_FIN + (w ^ 4) * 4, 1);
            lds_wait(lds + C_BP + tt * 4, 1);
            u16* op = P.out + (size_t)(mtO * 64 + tt * 16 + c16) * DM + ntO * 80 + g * 4;
#pragma unroll
            for (int n = 0; n < 5; ++n) {
                if ((w < 4) == (n < 3)) {
                    const f32x4 bp = *(LDS3 const f32x4*)(n < 3 ? lds + X_FIN + tt * 3072 + n * 1024 + lane * 16 : lds + 143360 + tt * 2048 + (n - 3) * 1024 + lane * 16);
                    const f32x4 s = c[n] + *(LDS3 const f32x4*)(xp + n * 1024) + bp;
                    *(u32x2*)(op + n * 16) = (u32x2){pk2(s[0], s[1]), pk2(s[2], s[3])};
                }
            }
            if (bid == 0 && mtid == 0) set_flag(ws, WS_EPOCH, tag);
        }
        STAMP(10);
        asm volatile("mega_code_end:");
    }
}

extern "C" void kernel_launch(void* const* d_in, const int* in_sizes, int n_in, void* d_out, int out_size, void* d_ws, size_t ws_size,
                              hipStream_t stream) {
    Params P;
    P.x = (const u16*)d_in[0]; P.wa = (const u16*)d_in[1]; P.woa = (const u16*)d_in[2]; P.wb = (const u16*)d_in[4];
    P.wphi = (const u16*)d_in[5]; P.wob = (const u16*)d_in[6]; P.gw = (const u16*)d_in[7]; P.ws = (char*)d_ws; P.out = (u16*)d_out;
    mega<<<256, 768, 0, stream>>>(P);
}
```

```cpp
#include <hip/hip_runtime.h>
#include <hip/hip_bf16.h>
#include <stdint.h>
#include <type_traits>

typedef __attribute__((ext_vector_type(8))) short bf16x8;
typedef __attribute__((ext_vector_type(4))) short bf16x4;
typedef __attribute__((ext_vector_type(16))) float f32x16;
typedef __attribute__((ext_vector_type(4))) float f32x4;
typedef __attribute__((ext_vector_type(2))) float f32x2;
typedef __attribute__((ext_vector_type(4))) unsigned u32x4;
typedef __attribute__((ext_vector_type(2))) unsigned u32x2;
typedef __attribute__((ext_vector_type(2))) __bf16 bf2_t;
typedef unsigned short u16;
typedef unsigned long long u64;
#define LDS3 __attribute__((address_space(3)))
typedef __attribute__((address_space(1))) u64 gu64;
#define RLX_AGENT __ATOMIC_RELAXED, __HIP_MEMORY_SCOPE_AGENT

#define PRIO_MODE 3
#define WARM_MODE 1
#define GA_PUB 11
#define PRE_SLEEP 2
constexpr int SEQ = 2048, DM = 640, NH = 32;
constexpr float LOG2E = 1.4426950408889634f;
constexpr float QSCALE = 0.22360679774997896f * LOG2E;

constexpr unsigned WS_EPOCH = 0;
constexpr unsigned WS_F_G   = 256;
constexpr unsigned WS_F_SD  = WS_F_G + 4096;
constexpr unsigned WS_F_KV  = WS_F_SD + 256;
constexpr unsigned WS_F_OB  = WS_F_KV + 2048;
constexpr unsigned WS_F_OA  = WS_F_OB + 2048;
constexpr unsigned WS_G     = 16384;
constexpr unsigned WS_Q     = 32768;
constexpr unsigned WS_KV    = WS_Q + 2 * NH * 81920;
constexpr unsigned WS_KVP   = WS_KV + 2 * NH * 163840;
constexpr unsigned WS_AO    = WS_KVP + NH * 8 * 4096;
constexpr unsigned WS_WT    = WS_AO + SEQ * 1280 * 2;
constexpr unsigned WS_END   = WS_WT + 1280 * 640 * 2;

#define STAMP(i)

__device__ __forceinline__ unsigned pk2(float a, float b) { f32x2 v = {a, b}; bf2_t r = __builtin_convertvector(v, bf2_t); return __builtin_bit_cast(unsigned, r); }
__device__ __forceinline__ float bf_lo(unsigned u) { return __uint_as_float(u << 16); }
__device__ __forceinline__ float bf_hi(unsigned u) { return __uint_as_float(u & 0xffff0000u); }
__device__ __forceinline__ float ex2(float x) { return __builtin_amdgcn_exp2f(x); }
#define MFMA32_16(a, b, c) __builtin_amdgcn_mfma_f32_32x32x16_bf16(__builtin_bit_cast(bf16x8, a), __builtin_bit_cast(bf16x8, b), c, 0, 0, 0)
#define MFMA32_8(a, b, c) __builtin_amdgcn_mfma_f32_32x32x8bf16_1k(__builtin_bit_cast(bf16x4, a), __builtin_bit_cast(bf16x4, b), c, 0, 0, 0)
#define MFMA16_32(a, b, c) __builtin_amdgcn_mfma_f32_16x16x32_bf16(__builtin_bit_cast(bf16x8, a), __builtin_bit_cast(bf16x8, b), c, 0, 0, 0)

__device__ __forceinline__ void glds16(unsigned voff, const void* sbase, unsigned lds_base) {
    unsigned sv;
    asm volatile("s_mov_b32 %0, m0\n\ts_mov_b32 m0, %3\n\ts_nop 0\n\tglobal_load_lds_dwordx4 %1, %2\n\ts_mov_b32 m0, %0"
                 : "=&s"(sv) : "v"(voff), "s"(sbase), "s"(__builtin_amdgcn_readfirstlane(lds_base)) : "memory");
}
__device__ __forceinline__ void glds16s(unsigned voff, const void* sbase, unsigned lds_base) {
    unsigned sv;
    asm volatile("s_mov_b32 %0, m0\n\ts_mov_b32 m0, %3\n\ts_nop 0\n\tglobal_load_lds_dwordx4 %1, %2 sc1\n\ts_mov_b32 m0, %0"
                 : "=&s"(sv) : "v"(voff), "s"(sbase), "s"(__builtin_amdgcn_readfirstlane(lds_base)) : "memory");
}
__device__ __forceinline__ void glds4s(unsigned voff, const void* sbase, unsigned lds_base) {
    unsigned sv;
    asm volatile("s_mov_b32 %0, m0\n\ts_mov_b32 m0, %3\n\ts_nop 0\n\tglobal_load_lds_dword %1, %2 sc1\n\ts_mov_b32 m0, %0"
                 : "=&s"(sv) : "v"(voff), "s"(sbase), "s"(__builtin_amdgcn_readfirstlane(lds_base)) : "memory");
}
typedef short v4i16_t __attribute__((ext_vector_type(4)));
__device__ __forceinline__ u32x2 trread(LDS3 const char* p) {
    return __builtin_bit_cast(u32x2, __builtin_amdgcn_ds_read_tr16_b64_v4i16((LDS3 v4i16_t*)p));
}
#define WAIT_BAR(N) asm volatile("s_waitcnt vmcnt(" #N ") lgkmcnt(0)\n\ts_barrier" ::: "memory")
#define DRAIN() asm volatile("s_waitcnt vmcnt(0)" ::: "memory")
#define SBAR() __builtin_amdgcn_sched_barrier(0)
#define PIN(x) asm volatile("" : "+v"(x))

__device__ __forceinline__ float xhalf_max(float x) {
    auto r = __builtin_amdgcn_permlane32_swap(__float_as_uint(x), __float_as_uint(x), false, false);
    return fmaxf(__uint_as_float(r[0]), __uint_as_float(r[1]));
}
__device__ __forceinline__ float xhalf_sum(float x) {
    auto r = __builtin_amdgcn_permlane32_swap(__float_as_uint(x), __float_as_uint(x), false, false);
    return __uint_as_float(r[0]) + __uint_as_float(r[1]);
}
__device__ __forceinline__ float upper_half(float x) {
    auto r = __builtin_amdgcn_permlane32_swap(__float_as_uint(x), __float_as_uint(x), false, false);
    return __uint_as_float(r[1]);
}

typedef __amdgpu_buffer_rsrc_t rsrc_t;
#define ST8(v, off)  __builtin_amdgcn_raw_buffer_store_b64(v, rs, (int)(off), 0, 16)
#define ST16(v, off) __builtin_amdgcn_raw_buffer_store_b128(v, rs, (int)(off), 0, 16)
#define ST4(v, off)  __builtin_amdgcn_raw_buffer_store_b32(v, rs, (int)(off), 0, 16)
#define LD16(off) __builtin_amdgcn_raw_buffer_load_b128(rs, (int)(off), 0, 16)
#define LD4(off)  __builtin_amdgcn_raw_buffer_load_b32(rs, (int)(off), 0, 16)

template <int SLP = 2>
__device__ __forceinline__ void wait_flags(const char* ws, unsigned off0, unsigned off1, u64 tag) {
    gu64* p0 = (gu64*)(ws + off0); gu64* p1 = (gu64*)(ws + off1);
#pragma unroll 1
    for (unsigned spins = 0; spins < (1u << 16); ++spins) {
        const u64 a = __hip_atomic_load(p0, RLX_AGENT), b = __hip_atomic_load(p1, RLX_AGENT);
        if (__all(a == tag && b == tag)) break;
        __builtin_amdgcn_s_sleep(SLP);
    }
    __builtin_amdgcn_fence(__ATOMIC_ACQUIRE, "wavefront");
}
__device__ __forceinline__ void set_flag(char* ws, unsigned off, u64 tag) { __hip_atomic_store((gu64*)(ws + off), tag, RLX_AGENT); }

struct Params { const u16* x; const u16* wa; const u16* wb; const u16* wphi; const u16* woa; const u16* wob; const u16* gw; char* ws; u16* out; };

__device__ __forceinline__ void feature_map(const u32x4& pm, unsigned pt, const u32x4& tm, unsigned tt, int h, u32x4& fp, u32x4& fn, u32x4& ft) {
    const f32x16 z16 = {};
    const u32x2 a8 = {pt, 0u}, b8 = {tt, 0u};
    f32x16 s = MFMA32_16(pm, tm, z16); s = MFMA32_8(a8, b8, s);
    float v[12];
#pragma unroll
    for (int i = 0; i < 12; i += 2) { const unsigned u = pk2(s[i], s[i + 1]); v[i] = bf_lo(u) * LOG2E; v[i + 1] = bf_hi(u) * LOG2E; }
    float mx = v[0], mn = v[0];
#pragma unroll
    for (int i = 1; i < 8; ++i) { mx = fmaxf(mx, v[i]); mn = fminf(mn, v[i]); }
    const float tx = fmaxf(fmaxf(v[8], v[9]), fmaxf(v[10], v[11])), tn = fminf(fminf(v[8], v[9]), fminf(v[10], v[11]));
    if (!h) { mx = fmaxf(mx, tx); mn = fminf(mn, tn); }
    mx = xhalf_max(mx); mn = -xhalf_max(-mn);
    float ep[12], en[12], sp = 0.f, sn = 0.f;
#pragma unroll
    for (int i = 0; i < 12; ++i) {
        ep[i] = ex2(v[i] - mx); en[i] = ex2(mn - v[i]);
        if (i >= 8 && h) { ep[i] = 0.f; en[i] = 0.f; }
        sp += ep[i]; sn += en[i];
    }
    sp = __builtin_amdgcn_rcpf(xhalf_sum(sp)); sn = __builtin_amdgcn_rcpf(xhalf_sum(sn));
#pragma unroll
    for (int i = 0; i < 4; ++i) { fp[i] = pk2(ep[2 * i] * sp, ep[2 * i + 1] * sp); fn[i] = pk2(en[2 * i] * sn, en[2 * i + 1] * sn); }
    ft = (u32x4){pk2(ep[8] * sp, ep[9] * sp), pk2(ep[10] * sp, ep[11] * sp), pk2(en[8] * sn, en[9] * sn), pk2(en[10] * sn, en[11] * sn)};
}

__device__ __forceinline__ void store_o(rsrc_t rs, const float (&o)[12], float sc, int tok, int kbase, int h) {
#pragma unroll
    for (int gq = 0; gq < 3; ++gq) {
        const int d = 8 * gq + 4 * h;
        if (d < 20) {
            const int k = kbase + ((kbase & 4) ? (d == 16 ? 0 : d + 4) : d);
            u32x2 ov = {pk2(o[4 * gq] * sc, o[4 * gq + 1] * sc), pk2(o[4 * gq + 2] * sc, o[4 * gq + 3] * sc)};
            ST8(ov, WS_AO + (((tok >> 4) * 40 + (k >> 5)) * 64 + ((k >> 3) & 3) * 16 + (tok & 15)) * 16 + (k & 7) * 2);
        }
    }
}

#define DSR128(dst, addr, off) asm volatile("ds_read_b128 %0, %1 offset:%2" : "=v"(dst) : "v"(addr), "i"(off))
#define DSR32(dst, addr, off) asm volatile("ds_read_b32 %0, %1 offset:%2" : "=v"(dst) : "v"(addr), "i"(off))
#define DSR128M2(d0, a0, o0, d1, a1, o1) asm volatile("s_mov_b64 exec, %6\n\tds_read_b128 %0, %2 offset:%3\n\tds_read_b128 %1, %4 offset:%5\n\ts_mov_b64 exec, -1" \
                                                      : "+v"(d0), "+v"(d1) : "v"(a0), "i"(o0), "v"(a1), "i"(o1), "s"(vmask))
#define LWAIT(n) do { asm volatile("s_waitcnt lgkmcnt(" #n ")" ::: "memory"); SBAR(); } while (0)

#define BAR() asm volatile("s_barrier" ::: "memory")
#define LDS_SET(off, v) __hip_atomic_store((LDS3 unsigned*)(lds + (off)), (unsigned)(v), __ATOMIC_RELAXED, __HIP_MEMORY_SCOPE_WORKGROUP)
#define LDS_CNT_ADD(off) __hip_atomic_fetch_add((LDS3 unsigned*)(lds + (off)), 1u, __ATOMIC_RELAXED, __HIP_MEMORY_SCOPE_WORKGROUP)

template <int SLP = 1>
__device__ __forceinline__ void lds_wait(LDS3 char* p, unsigned want) {
#pragma unroll 1
    for (unsigned spins = 0; spins < (1u << 20); ++spins) {
        if ((unsigned)__builtin_amdgcn_readfirstlane(__hip_atomic_load((LDS3 unsigned*)p, __ATOMIC_RELAXED, __HIP_MEMORY_SCOPE_WORKGROUP)) >= want) break;
        __builtin_amdgcn_s_sleep(SLP);
    }
    __builtin_amdgcn_fence(__ATOMIC_ACQUIRE, "wavefront");
}

template <int SLP>
__device__ __forceinline__ void prog_wait(LDS3 char* p, int lane, unsigned want) {
#pragma unroll 1
    for (unsigned spins = 0; spins < (1u << 20); ++spins) {
        if (__all(__hip_atomic_load((LDS3 unsigned*)p + (lane & 7), __ATOMIC_RELAXED, __HIP_MEMORY_SCOPE_WORKGROUP) >= want)) break;
        __builtin_amdgcn_s_sleep(SLP);
    }
    __builtin_amdgcn_fence(__ATOMIC_ACQUIRE, "wavefront");
}
#define POLL_ONE(cond, SLP, o0, o1, word) do { if (cond) { wait_flags<SLP>(ws, o0, o1, tag); \
        if (lane == 0) __hip_atomic_store((LDS3 unsigned*)(lds + (word)), 1u, __ATOMIC_RELAXED, __HIP_MEMORY_SCOPE_WORKGROUP); } else lds_wait<SLP>(lds + (word), 1); } while (0)
constexpr int C_KV = 0, C_KVF = 4, C_OB = 8, C_PROG = 12, C_WA = 16, C_OA = 20, C_MRG = 32  , C_FIN = 64  , C_BP = 96  ;
constexpr int C_PROG2 = 24, C_BX = 28, C_P1 = 112, C_P2 = 116, C_P3 = 120, C_P4 = 124, C_PR = 128  ;
constexpr int G_W = 31744;
constexpr int G_FULL = G_W  , G_PRG = G_W + 16  , G_Q = G_W + 48, G_P = G_W + 52, G_ST = G_W + 56, G_RD = G_W + 60, G_Z = G_W + 64, G_ST2 = G_W + 68;
constexpr int C_GB = 168, C_KVL = 160, C_P5 = 164, X_G = 256;
constexpr int X_KVF = 1280, X_SLOT = 4096;
constexpr int X_MRG = 1280;
constexpr int L_WA = 24576;
constexpr int L_Q = 65536;
constexpr int X_FIN = 126976;

__global__ __launch_bounds__(768) void mega(Params P) {
    __shared__ __attribute__((aligned(16))) char smem[163840];
    LDS3 char* lds = (LDS3 char*)smem;
    char* ws = P.ws;
    const int tid = threadIdx.x, lane = tid & 63, r = lane & 31, h = lane >> 5;
    const int hw = __builtin_amdgcn_readfirstlane(tid >> 6);
    const bool helper = hw < 4;
    const int w = hw - 4, mtid = tid - 256;
    const int bid = blockIdx.x;
    const unsigned lds0 = (unsigned)(uintptr_t)smem;
    const rsrc_t rs = __builtin_amdgcn_make_buffer_rsrc(ws, 0, WS_END, 0x00020000);
    STAMP(0);
    u64 tag = 0;
#define GET_TAG() do { asm volatile("s_load_dwordx2 %0, %1, 0x0\n\ts_waitcnt lgkmcnt(0)" : "=s"(tag) : "s"(ws) : "memory"); tag += 1; } while (0)
    const int head = (bid & 7) * 4 + ((bid >> 3) & 3), qb = bid >> 5;
    const int nlo = (head * 20) >> 7, nhi = (head * 20 + 19) >> 7;
    const int mtO = (bid & 7) * 4 + ((bid >> 3) & 3), ntO = bid >> 5;
    const int lin = (bid & 7) * 30 + (bid >> 3), lh = lin >= 120, lrem = lin - 120 * lh;
    const int mt = lh * 8 + (lrem & 7), nt = lrem >> 3;
    const unsigned fG = WS_F_G + (mt * 15 + nt) * 8;
    const unsigned one = (r == 20) ? 0x3f803f80u : 0u;
    const f32x16 z16 = {};

    if (helper) {
        const unsigned fA0 = WS_F_G + ((lane & 15) * 15 + (lane < 32 ? 5 : 10) + ((lane & 16) ? nhi : nlo)) * 8;
        const unsigned fA1 = WS_F_G + ((qb * 2 + (lane & 1)) * 15 + ((lane & 2) ? nhi : nlo)) * 8;
        __builtin_amdgcn_s_setprio(3);
        const char* kvb = ws + WS_KV + head * 163840;
        auto kv_issue = [&](int grp) {
#pragma unroll
            for (int t = 0; t < 8; ++t) { int pc = grp * 32 + hw + 4 * t; if (pc == 31) pc = 30; glds16s(lane * 16, kvb + pc * 1024, lds0 + pc * 1024); }
        };
        auto q_issue = [&]() {
            const char* qbase = ws + WS_Q + head * 81920 + qb * 10240;
#pragma unroll
            for (int t = 0; t < 3; ++t) { const int pc = hw + 4 * t < 10 ? hw + 4 * t : 9; glds16s(lane * 16, qbase + pc * 1024, lds0 + L_Q + pc * 1024); }
        };
        if (bid < 240) {
            const int rot = (nt * 3 + mt * 5 + (mt >> 1)) % 10;
            const unsigned xo = (mt * 128 + 8 * hw + (lane >> 3)) * 1280 + (((lane & 7) ^ ((4 * hw + (lane >> 4)) & 7)) << 4);
            const unsigned wo = (4 * hw + (lane >> 4)) * 3840 + nt * 256 + (((lane & 15) ^ (((lane >> 4) << 2) | hw)) << 4);
            const unsigned dX = lds0 + 32768 + hw * 1024;
            auto issue = [&](int s) {
                int kt = (s >= 10 ? 19 - s : s) + rot; if (kt >= 10) kt -= 10;
                const char* xb = (const char*)P.x + kt * 128;
                const char* wb = (const char*)(s >= 10 ? P.wb : P.wa) + (size_t)kt * (64 * 3840);
                const unsigned dw = dX + 16384 + (s & 3) * 32768, dx = dX + ((s >= 10 ? 19 - s : s) & 3) * 32768;
#pragma unroll
                for (int i = 0; i < 4; ++i) glds16(wo, wb + i * 61440, dw + i * 4096);
                if (s < 10 || s >= 14) {
#pragma unroll
                    for (int i = 0; i < 4; ++i) glds16(xo, xb + i * 40960, dx + i * 4096);
                }
            };
#pragma unroll 1
            for (int n = 0; n < 20; ++n) {
                if (n >= 4) prog_wait<1>(lds + G_PRG, lane, n - 3);
                issue(n);
                if (n == 1) { if (hw == 0 && lane < 32) *(LDS3 unsigned*)(lds + G_W + lane * 4) = 0u; GET_TAG(); WAIT_BAR(63); }
                if (n >= 1) {
                    if (n >= 10 && n < 14) asm volatile("s_waitcnt vmcnt(4)" ::: "memory"); else asm volatile("s_waitcnt vmcnt(8)" ::: "memory");
                    if (lane == 0) LDS_SET(G_FULL + hw * 4, n);
                }
            }
            DRAIN();
            if (lane == 0) LDS_SET(G_FULL + hw * 4, 20);
        } else {
            if (hw == 0 && lane < 32) *(LDS3 unsigned*)(lds + G_W + lane * 4) = lane >= 4 && lane < 12 ? 20u : 0u;
            GET_TAG();
            WAIT_BAR(63);
            if (hw == 0) {
#pragma unroll 1
                for (int t = 0; t < PRE_SLEEP; ++t) __builtin_amdgcn_s_sleep(127);
            }
        }
        POLL_ONE(hw == 0, 6, fA0, fA1, G_P);
        kv_issue(0);
        prog_wait<1>(lds + G_PRG, lane, 20);
        q_issue(); kv_issue(1);
        asm volatile("s_waitcnt vmcnt(6)" ::: "memory");
        if (lane == 0) LDS_CNT_ADD(G_Q);
        kv_issue(3); kv_issue(4);
        lds_wait<2>(lds + G_RD, 8);
        if (hw == 0) {
            *(LDS3 u32x4*)(lds + lane * 16) = (u32x4){0u, 0u, 0u, 0u};
            asm volatile("s_waitcnt lgkmcnt(0)" ::: "memory");
            if (lane == 0) LDS_SET(G_Z, 1);
        }
        kv_issue(2);
        lds_wait<2>(lds + G_Z, 1);
        if (lane == 0) LDS_CNT_ADD(G_ST2);
        if (hw == 3) {
            lds_wait<2>(lds + G_ST2, 12);
            glds16s(lane * 16, kvb + 31 * 1024, lds0 + 31 * 1024);
        }
        STAMP(3);
        u32x4 phm; unsigned pht;
        {
            const u16* wp = P.wphi + head * 400 + (r < 20 ? r : 0);
            unsigned e[10];
#pragma unroll
            for (int j = 0; j < 8; ++j) e[j] = wp[(8 * h + j + 4 * (head & 1)) * 20];
            e[8] = wp[((head & 1 ? 0 : 16) + 2 * h) * 20]; e[9] = wp[((head & 1 ? 1 : 17) + 2 * h) * 20];
            const bool cp = bid < 160 && tid < 160;
            const int k0 = cp ? bid * 8 : 0, n = cp ? tid * 4 : 0;
            const u16* wsrc = (k0 < 640 ? P.woa + (size_t)k0 * DM : P.wob + (size_t)(k0 - 640) * DM) + n;
            u32x2 f[8];
#pragma unroll
            for (int j = 0; j < 8; ++j) f[j] = *(const u32x2*)(wsrc + (size_t)j * DM);
            DRAIN();
            if (lane == 0) LDS_CNT_ADD(C_KVL);
            phm = (u32x4){e[0] | (e[1] << 16), e[2] | (e[3] << 16), e[4] | (e[5] << 16), e[6] | (e[7] << 16)}; pht = e[8] | (e[9] << 16);
            if (r >= 20) { phm = (u32x4){0u, 0u, 0u, 0u}; pht = 0u; }
            const unsigned dp = WS_WT + (((n >> 4) * 40 + (k0 >> 5)) * 64 + ((k0 >> 3) & 3) * 16 + (n & 15)) * 16;
            if (cp) {
#pragma unroll
                for (int q = 0; q < 2; ++q) {
                    const u32x4 lo = {(f[0][q] & 0xffffu) | (f[1][q] << 16), (f[2][q] & 0xffffu) | (f[3][q] << 16), (f[4][q] & 0xffffu) | (f[5][q] << 16), (f[6][q] & 0xffffu) | (f[7][q] << 16)};
                    const u32x4 hi = {(f[0][q] >> 16) | (f[1][q] & 0xffff0000u), (f[2][q] >> 16) | (f[3][q] & 0xffff0000u), (f[4][q] >> 16) | (f[5][q] & 0xffff0000u), (f[6][q] >> 16) | (f[7][q] & 0xffff0000u)};
                    ST16(lo, dp + q * 32); ST16(hi, dp + q * 32 + 16);
                }
            }
        }
        const int tokS = qb * 256 + hw * 64, m7 = tokS >> 7, t4 = (tokS >> 5) & 3;
        {
            const unsigned pt3 = (lane >> 1) & 3;
            const unsigned tb = WS_F_G + 2048 + ((qb * 2 + ((lane >> 3) & 1)) * 15 + ((lane & 1) ? nhi : nlo) + (pt3 < 3 ? pt3 : 0) * 5) * 8;
            POLL_ONE(hw == 0, 4, tb, WS_F_SD + (qb * 2 + (lane & 1)) * 8, C_P1);
        }
        const unsigned om = t4 * 1024 + lane * 16, ot = 4096 + (t4 * 32 + r) * 8 + 4 * h;
        u32x4 qf[2][3]; float gS[2];
        {
            const unsigned kb = WS_KV + (NH + head) * 163840 + (2 * (m7 & 7) + (m7 >> 3)) * 10240, qbb = WS_Q + (NH + head) * 81920 + m7 * 5120;
            const unsigned ov = 5120 + t4 * 1280 + h * 320 + (r < 20 ? r : 19) * 16;
            u32x4 e0[2], w0[2], w1[2], eq[2]; unsigned et[2], eqt[2];
#pragma unroll
            for (int i = 0; i < 2; ++i) {
                e0[i] = LD16(kb + om + i * 1024); et[i] = LD4(kb + ot + i * 256); w0[i] = LD16(kb + ov + i * 1280); w1[i] = LD16(kb + ov + i * 1280 + 640);
                eq[i] = LD16(qbb + om + i * 1024); eqt[i] = LD4(qbb + ot + i * 256); gS[i] = __uint_as_float(LD4(WS_G + (tokS + i * 32 + r) * 4));
            }
            u32x4 i0, i1;
#pragma unroll
            for (int j = 0; j < 4; ++j) {
                const int e = r - 8 * h - 2 * j;
                i0[j] = e == 0 ? 0x3f80u : (e == 1 ? 0x3f800000u : 0u);
                i1[j] = e == 16 ? 0x3f80u : (e == 17 ? 0x3f800000u : 0u);
            }
            f32x16 k0 = {}, k1 = {};
#pragma unroll
            for (int i = 0; i < 2; ++i) {
                if (r >= 20) { w0[i] = (u32x4){one, one, one, one}; w1[i] = w0[i]; }
                u32x4 fp, fn, ft;
                feature_map(phm, pht, e0[i], et[i], h, fp, fn, ft);
                f32x16 T0 = MFMA32_16(fp, i0, z16); T0 = MFMA32_16(fn, i1, T0);
                f32x16 T1 = MFMA32_16(ft, i0, z16);
                u32x4 b00, b01, b10, b11;
#pragma unroll
                for (int j = 0; j < 4; ++j) {
                    b00[j] = pk2(T0[2 * j], T0[2 * j + 1]); b01[j] = pk2(T0[8 + 2 * j], T0[9 + 2 * j]);
                    b10[j] = pk2(T1[2 * j], T1[2 * j + 1]); b11[j] = pk2(T1[8 + 2 * j], T1[9 + 2 * j]);
                }
                k0 = MFMA32_16(w0[i], b00, k0); k0 = MFMA32_16(w1[i], b01, k0);
                k1 = MFMA32_16(w0[i], b10, k1); k1 = MFMA32_16(w1[i], b11, k1);
            }
            prog_wait<8>(lds + C_PR, lane, 1);
            *(LDS3 float*)(lds + X_G + (hw * 64 + lane) * 4) = h ? gS[1] : gS[0];
            if (hw * 64 + lane < 144) *(LDS3 u32x4*)(lds + X_KVF + (hw * 64 + lane) * 16) = (u32x4){0u, 0u, 0u, 0u};
            LDS3 char* sp = lds + X_SLOT + hw * 4096;
#pragma unroll
            for (int gq = 0; gq < 3; ++gq) {
                *(LDS3 f32x4*)(sp + gq * 1024 + lane * 16) = (f32x4){k0[4 * gq], k0[4 * gq + 1], k0[4 * gq + 2], k0[4 * gq + 3]};
                if (r < 8) *(LDS3 f32x4*)(sp + 3072 + (gq * 16 + h * 8 + r) * 16) = (f32x4){k1[4 * gq], k1[4 * gq + 1], k1[4 * gq + 2], k1[4 * gq + 3]};
            }
            asm volatile("s_waitcnt vmcnt(0) lgkmcnt(0)" ::: "memory");
            unsigned arr = 0;
            if (lane == 0) arr = LDS_CNT_ADD(C_KV);
            if (__builtin_amdgcn_readfirstlane(arr) == 3) {
                __builtin_amdgcn_fence(__ATOMIC_ACQUIRE, "wavefront");
#pragma unroll
                for (int i = 0; i < 4; ++i) {
                    const int qd = lane + 64 * i;
                    LDS3 const char* s0 = lds + X_SLOT + qd * 16;
                    const f32x4 s = (*(LDS3 const f32x4*)s0 + *(LDS3 const f32x4*)(s0 + 4096)) + (*(LDS3 const f32x4*)(s0 + 8192) + *(LDS3 const f32x4*)(s0 + 12288));
                    if (qd < 240) ST16(__builtin_bit_cast(u32x4, s), WS_KVP + (head * 8 + qb) * 4096 + qd * 16);
                }
                DRAIN();
                if (lane == 0) set_flag(ws, WS_F_KV + (head * 8 + qb) * 8, tag);
            }
#pragma unroll
            for (int i = 0; i < 2; ++i) feature_map(phm, pht, eq[i], eqt[i], h, qf[i][0], qf[i][1], qf[i][2]);
        }
        STAMP(4);
        POLL_ONE(hw == 0, 4, WS_F_KV + (head * 8 + (lane & 7)) * 8, WS_F_KV + (head * 8 + (lane & 7)) * 8, C_P2);
        {
            const int qd = hw * 60 + (lane < 60 ? lane : 59);
            f32x4 pp[8];
#pragma unroll
            for (int p = 0; p < 8; ++p) pp[p] = __builtin_bit_cast(f32x4, LD16(WS_KVP + (head * 8 + p) * 4096 + qd * 16));
            const f32x4 s4 = ((pp[0] + pp[1]) + (pp[2] + pp[3])) + ((pp[4] + pp[5]) + (pp[6] + pp[7]));
            const int gq = qd < 192 ? qd >> 6 : (qd - 192) >> 4, hs = qd < 192 ? (qd >> 5) & 1 : (qd >> 3) & 1, sg = qd < 192 ? qd & 31 : 32 + (qd & 7);
            const unsigned a = pk2(s4[0], s4[1]), b = pk2(s4[2], s4[3]);
            LDS3 u16* kp = (LDS3 u16*)(lds + X_KVF + (8 * gq + 4 * hs) * 96 + sg * 2);
            kp[0] = (u16)a; kp[48] = (u16)(a >> 16); kp[96] = (u16)b; kp[144] = (u16)(b >> 16);
            asm volatile("s_waitcnt lgkmcnt(0)" ::: "memory");
            if (lane == 0) LDS_CNT_ADD(C_KVF);
            lds_wait<2>(lds + C_KVF, 4);
        }
        {
            LDS3 const char* kp = lds + X_KVF + (r < 24 ? r : 23) * 96 + h * 16;
            const u32x4 a0 = *(LDS3 const u32x4*)kp, a1 = *(LDS3 const u32x4*)(kp + 32), a2 = *(LDS3 const u32x4*)(kp + 64);
#pragma unroll
            for (int i = 0; i < 2; ++i) {
                f32x16 d = MFMA32_16(a0, qf[i][0], z16); d = MFMA32_16(a1, qf[i][1], d); d = MFMA32_16(a2, qf[i][2], d);
                const float den = upper_half(d[8]) + 1e-6f;
                float o[12];
#pragma unroll
                for (int j = 0; j < 12; ++j) o[j] = d[j];
                store_o(rs, o, (1.f - gS[i]) / den, tokS + i * 32 + r, 640 + head * 20, h);
            }
            DRAIN();
            unsigned arr = 0;
            if (lane == 0) arr = LDS_CNT_ADD(C_OB);
            if (__builtin_amdgcn_readfirstlane(arr) == 3 && lane == 0) set_flag(ws, WS_F_OB + (qb * 32 + head) * 8, tag);
        }
        STAMP(5);
        f32x4 c[5];
#pragma unroll
        for (int n = 0; n < 5; ++n) c[n] = (f32x4){0.f, 0.f, 0.f, 0.f};
        const char* wsrc = ws + WS_WT + (size_t)ntO * 5 * 40960;
        {
            if (hw == 0) wait_flags<8>(ws, WS_F_KV + lane * 8, WS_F_KV + 512 + lane * 8, tag);
            POLL_ONE(hw == 0, 8, WS_F_KV + 1024 + lane * 8, WS_F_KV + 1536 + lane * 8, C_P5);
#pragma unroll 1
            for (int t = 0; t < 5; ++t) {
                if (t == 0) prog_wait<8>(lds + C_PR, lane, 4); else if (t == 3) prog_wait<4>(lds + C_PR, lane, 5);
                const int ks = hw + 4 * t < 19 ? hw + 4 * t : 18;
#pragma unroll
                for (int n = 0; n < 5; ++n) glds16s(lane * 16, wsrc + (n * 40 + 20 + ks) * 1024, lds0 + 4096 + (ks * 5 + n) * 1024);
            }
            POLL_ONE(hw == 0, 4, WS_F_OB + ((mtO >> 2) * 32 + r) * 8, WS_F_OB + ((mtO >> 2) * 32 + r) * 8, C_P3);
            const unsigned ab = WS_AO + ((mtO * 4 + hw) * 40 + 20) * 1024 + lane * 16;
            u32x4 af[20];
#pragma unroll
            for (int ks = 0; ks < 20; ++ks) af[ks] = LD16(ab + ks * 1024);
            asm volatile("s_waitcnt vmcnt(20)" ::: "memory");
            if (lane == 0) LDS_CNT_ADD(C_BX);
            lds_wait<2>(lds + C_BX, 4);
            u32x4 wf[3][5], wl[5];
            LDS3 const char* wp = lds + 4096 + lane * 16;
#pragma unroll
            for (int ks = 0; ks < 2; ++ks)
#pragma unroll
                for (int n = 0; n < 5; ++n) wf[ks][n] = *(LDS3 const u32x4*)(wp + (ks * 5 + n) * 1024);
#pragma unroll
            for (int ks = 0; ks < 20; ++ks) {
                if (ks + 2 < 19) {
#pragma unroll
                    for (int n = 0; n < 5; ++n) wf[(ks + 2) % 3][n] = *(LDS3 const u32x4*)(wp + ((ks + 2) * 5 + n) * 1024);
                }
                if (ks == 8) {
#pragma unroll
                    for (int n = 0; n < 5; ++n) wl[n] = LD16(WS_WT + ((ntO * 5 + n) * 40 + 39) * 1024 + lane * 16);
                }
                SBAR();
#pragma unroll
                for (int n = 0; n < 5; ++n) c[n] = MFMA16_32(ks < 19 ? wf[ks % 3][n] : wl[n], af[ks], c[n]);
                SBAR();
            }
            if (lane == 0) LDS_CNT_ADD(C_BX);
            lds_wait<2>(lds + C_BX, 8);
        }
        STAMP(6);
        prog_wait<4>(lds + C_PR, lane, 7);
#pragma unroll 1
        for (int n = 0; n < 5; ++n)
#pragma unroll
            for (int t = 0; t < 5; ++t) { const int ks = hw + 4 * t; glds16s(lane * 16, wsrc + (n * 40 + ks) * 1024, lds0 + L_WA + (n * 20 + ks) * 1024); }
#pragma unroll
        for (int n = 0; n < 3; ++n) *(LDS3 f32x4*)(lds + X_FIN + hw * 3072 + n * 1024 + lane * 16) = c[n];
        DRAIN();
        if (lane == 0) LDS_CNT_ADD(C_WA);
        prog_wait<4>(lds + C_PR, lane, 8);
#pragma unroll
        for (int n = 3; n < 5; ++n) *(LDS3 f32x4*)(lds + 143360 + hw * 2048 + (n - 3) * 1024 + lane * 16) = c[n];
        asm volatile("s_waitcnt lgkmcnt(0)" ::: "memory");
        if (lane == 0) __hip_atomic_store((LDS3 unsigned*)(lds + C_BP + hw * 4), 1u, __ATOMIC_RELAXED, __HIP_MEMORY_SCOPE_WORKGROUP);
        STAMP(7);
        asm volatile("mega_code_end2:");
    } else {
        const int qs = w & 3, kh = w >> 2;
        const int q0 = qb * 256 + qs * 64;
        {
            unsigned sink;
            if (w == 0 && (WARM_MODE & 1)) {
                const char* pc0; int len, len2;
                asm volatile("s_getpc_b64 %0\nmega_code_mark:\n\ts_mov_b32 %1, mega_code_end-mega_code_mark\n\ts_mov_b32 %2, mega_code_end2-mega_code_mark" : "=s"(pc0), "=s"(len), "=s"(len2));
                len = len > len2 ? len : len2;
                const int o = ((bid >> 3) * 64 + lane) * 64;
                if (o + 4 <= len) asm volatile("global_load_dword %0, %1, %2" : "=v"(sink) : "v"(o), "s"(pc0) : "memory");
            } else if (w >= 1 && w <= 6 && (WARM_MODE & 2)) {
                const unsigned o = ((w - 1) * 64 + lane) * 65536 + 65532;
                if (o < WS_END) asm volatile("global_load_dword %0, %1, %2 sc1" : "=v"(sink) : "v"(o), "s"(ws) : "memory");
            } else if (w == 7 && (WARM_MODE & 2)) {
                if (lane < 40) asm volatile("global_load_dword %0, %1, %2 sc1" : "=v"(sink) : "v"(lane * 65536), "s"(P.out) : "memory");
            }
            asm volatile("s_waitcnt vmcnt(0)" ::: "memory");
        }
        GET_TAG();
        if (bid < 240) {
            const int part = nt / 5, c0 = (nt - part * 5) * 128;
            const int wm = w & 1, wn = w >> 1;
            constexpr int RB = 32768;
            const int xr = wm * 64 + r;
            const unsigned xa = RB + xr * 128 + ((h ^ ((xr >> 1) & 7)) << 4);
            unsigned wt[2];
#pragma unroll
            for (int t = 0; t < 2; ++t) {
                const int q = (lane & 15) >> 2, p = lane & 3, blk = (lane >> 4) & 1, row = 8 * h + 4 * t + q;
                wt[t] = RB + 16384 + row * 256 + (((4 * wn + 2 * blk + (p >> 1)) ^ ((q << 2) | (2 * h + t))) << 4) + 8 * (p & 1);
            }
            auto gbody = [&](auto trc) {
                constexpr bool TR = decltype(trc)::value;
                u32x4 xf[2][8], wf[2][4];
                auto rdk = [&](auto bc, LDS3 const char* sx, LDS3 const char* sb, int ks) {
                    constexpr int B = decltype(bc)::value;
                    xf[B][2 * ks] = *(LDS3 const u32x4*)(sx + (xa ^ (ks * 32)));
                    xf[B][2 * ks + 1] = *(LDS3 const u32x4*)(sx + ((xa + 4096) ^ (ks * 32)));
                    const u32x2 lo = trread(sb + wt[0] + ks * 4096), hi = trread(sb + wt[1] + ks * 4096);
                    wf[B][ks] = (u32x4){lo[0], lo[1], hi[0], hi[1]};
                };
                f32x16 acc0, acc1;
                u32x4 fl;
                auto full_wait = [&](unsigned want) {
#pragma unroll 1
                    for (unsigned spins = 0; spins < (1u << 20); ++spins) {
                        const u32x4 f = *(LDS3 const volatile u32x4*)(lds + G_FULL);
                        if ((unsigned)__builtin_amdgcn_readfirstlane(min(min(f[0], f[1]), min(f[2], f[3]))) >= want) break;
                        __builtin_amdgcn_s_sleep(1);
                    }
                    asm volatile("" ::: "memory");
                };
                auto step = [&](auto bc, int s) {
                    constexpr int B = decltype(bc)::value;
                    if (s == GA_PUB) {
                        DRAIN();
                        unsigned arr = 0;
                        if (lane == 0) arr = LDS_CNT_ADD(G_ST);
                        if (__builtin_amdgcn_readfirstlane(arr) == 7 && lane == 0) set_flag(ws, fG, tag);
                    }
                    if (s < 19 && __builtin_amdgcn_readfirstlane(min(min(fl[0], fl[1]), min(fl[2], fl[3]))) < s + 2) full_wait(s + 2);
                    SBAR();
                    const int s1 = s + 1;
                    LDS3 const char* sb = lds + __builtin_amdgcn_readfirstlane((s1 & 3) * 32768);
                    LDS3 const char* sx = lds + __builtin_amdgcn_readfirstlane(((s1 >= 10 ? 19 - s1 : s1) & 3) * 32768);
#define GM(A, i) do { if constexpr (TR) A = MFMA32_16(wf[B][(i) >> 1], xf[B][i], A); else A = MFMA32_16(xf[B][i], wf[B][(i) >> 1], A); SBAR(); } while (0)
#define GR(ks) do { rdk(std::integral_constant<int, B ^ 1>{}, sx, sb, ks); SBAR(); } while (0)
                    GM(acc0, 0); GR(0); GM(acc1, 1); GR(1); GM(acc0, 2); GR(2); GM(acc1, 3); GR(3);
                    GM(acc0, 4); GM(acc1, 5);
                    __builtin_amdgcn_s_waitcnt(0xC07F);
                    if (lane == 0) LDS_SET(G_PRG + w * 4, s + 2);
                    fl = *(LDS3 const volatile u32x4*)(lds + G_FULL);
                    SBAR();
                    GM(acc0, 6); GM(acc1, 7);
#undef GM
#undef GR
                };
                WAIT_BAR(63);
                full_wait(1);
#pragma unroll
                for (int ks = 0; ks < 4; ++ks) rdk(std::integral_constant<int, 0>{}, lds, lds, ks);
                __builtin_amdgcn_s_waitcnt(0xC07F);
                if (lane == 0) LDS_SET(G_PRG + w * 4, 1);
                fl = *(LDS3 const volatile u32x4*)(lds + G_FULL);
#pragma unroll 1
                for (int mat = 0; mat < 2; ++mat) {
                    acc0 = (f32x16){}; acc1 = (f32x16){};
#pragma unroll 1
                    for (int kk = 0; kk < 10; kk += 2) {
                        step(std::integral_constant<int, 0>{}, mat * 10 + kk);
                        step(std::integral_constant<int, 1>{}, mat * 10 + kk + 1);
                    }
                    const int cpos = 2 * (mt & 7) + (mt >> 3);
                    if constexpr (TR) {
                        const float sc = (part == 0 && mat == 0) ? QSCALE : 1.f;
                        const unsigned base = part == 0 ? WS_Q + mat * NH * 81920 + mt * 5120 : WS_KV + mat * NH * 163840 + cpos * 10240;
                        const unsigned hs = part == 0 ? 81920 : 163840;
#pragma unroll
                        for (int pr = 0; pr < 2; ++pr) {
                            const int G8 = ((c0 + 32 * wn) >> 3) + 2 * pr + h, pp = G8 / 5, gi = G8 - pp * 5;
                            const unsigned hb0 = base + (2 * pp + (gi > 2)) * hs;
#pragma unroll
                            for (int i = 0; i < 2; ++i) {
                                const f32x16& a = i ? acc1 : acc0;
                                const int tl = wm * 64 + i * 32 + r;
                                const auto s0 = __builtin_amdgcn_permlane32_swap(pk2(a[8 * pr] * sc, a[8 * pr + 1] * sc), pk2(a[8 * pr + 4] * sc, a[8 * pr + 5] * sc), false, false);
                                const auto s1 = __builtin_amdgcn_permlane32_swap(pk2(a[8 * pr + 2] * sc, a[8 * pr + 3] * sc), pk2(a[8 * pr + 6] * sc, a[8 * pr + 7] * sc), false, false);
                                if (gi != 2) { const u32x4 ov = {s0[0], s1[0], s0[1], s1[1]}; ST16(ov, hb0 + (tl >> 5) * 1024 + (gi > 2 ? gi - 3 : gi) * 512 + r * 16); }
                                else { const u32x2 o0 = {s0[0], s1[0]}, o1 = {s0[1], s1[1]}; ST8(o0, hb0 + 4096 + tl * 8); ST8(o1, hb0 + hs + 4096 + tl * 8); }
                            }
                        }
                    } else {
                        const int n = c0 + 32 * wn + r, hd = n / 20, d = n - hd * 20, rho = (hd & 1) ? (d < 4 ? d + 16 : d - 4) : d;
                        const unsigned base = WS_KV + (mat * NH + hd) * 163840 + cpos * 10240 + 5120 + h * 320 + rho * 16;
#pragma unroll
                        for (int i = 0; i < 2; ++i)
#pragma unroll
                            for (int s2 = 0; s2 < 2; ++s2) {
                                const f32x16& a = i ? acc1 : acc0;
                                u32x4 ov = {pk2(a[8 * s2], a[8 * s2 + 1]), pk2(a[8 * s2 + 2], a[8 * s2 + 3]), pk2(a[8 * s2 + 4], a[8 * s2 + 5]), pk2(a[8 * s2 + 6], a[8 * s2 + 7])};
                                ST16(ov, base + (wm * 4 + i * 2 + s2) * 640);
                            }
                    }
                    if (mat == 0) STAMP(1);
                }
            };
            if (part < 2) gbody(std::true_type{}); else gbody(std::false_type{});
        } else {
            const int sj = bid - 240;
            WAIT_BAR(63);
            {
                const u32x4 ga = *(const u32x4*)(P.gw + lane * 8), gb = *(const u32x4*)(P.gw + 512 + (lane & 15) * 8);
                u32x4 xa[16], xb[4];
                const u16* xr = P.x + (size_t)(sj * 128 + w * 16) * DM;
#pragma unroll
                for (int rr = 0; rr < 16; ++rr) xa[rr] = *(const u32x4*)(xr + rr * DM + lane * 8);
#pragma unroll
                for (int i = 0; i < 4; ++i) xb[i] = *(const u32x4*)(xr + (i * 4 + (lane >> 4)) * DM + 512 + (lane & 15) * 8);
                float res = 0.f;
#pragma unroll
                for (int rr = 0; rr < 16; ++rr) {
                    float s = 0.f, s2 = 0.f;
#pragma unroll
                    for (int j = 0; j < 4; ++j) { s += bf_lo(xa[rr][j]) * bf_lo(ga[j]) + bf_hi(xa[rr][j]) * bf_hi(ga[j]); s2 += bf_lo(xb[rr >> 2][j]) * bf_lo(gb[j]) + bf_hi(xb[rr >> 2][j]) * bf_hi(gb[j]); }
                    if ((lane >> 4) == (rr & 3)) s += s2;
#pragma unroll
                    for (int o = 32; o >= 1; o >>= 1) s += __shfl_xor(s, o);
                    if (lane == rr) res = s;
                }
                if (lane < 16) ST4(__float_as_uint(1.f / (1.f + __expf(-res))), WS_G + (sj * 128 + w * 16 + lane) * 4);
            }
            DRAIN();
            unsigned arr = 0;
            if (lane == 0) arr = LDS_CNT_ADD(G_ST);
            if (__builtin_amdgcn_readfirstlane(arr) == 7 && lane == 0) set_flag(ws, WS_F_SD + sj * 8, tag);
        }
        STAMP(2);
        const unsigned hb = lds0 + kh * 10240;
        const unsigned laneK = hb + lane * 16, laneT = hb + 4096 + r * 8 + h * 4, laneV = hb + 5120 + h * 320 + (r < 20 ? r : 19) * 16;
        const u64 vmask = 0x000fffff000fffffull;
        u32x4 v0a = {one, one, one, one}, v0b = v0a, v1 = v0a;
        u32x4 qA, qB, ka; unsigned qtA, qtB, kt;
        {
            lds_wait(lds + G_Q, 4);
            LDS3 const char* qp = lds + L_Q + (qs >> 1) * 5120;
            const unsigned om = (qs & 1) * 2048 + lane * 16, ot = 4096 + ((qs & 1) * 64 + r) * 8 + 4 * h;
            qA = *(LDS3 const u32x4*)(qp + om); qB = *(LDS3 const u32x4*)(qp + om + 1024);
            qtA = *(LDS3 const unsigned*)(qp + ot); qtB = *(LDS3 const unsigned*)(qp + ot + 256);
            DSR128(ka, laneK, 0); DSR32(kt, laneT, 0); DSR128M2(v0a, laneV, 0, v1, laneV, 640);
            asm volatile("s_waitcnt lgkmcnt(0)" : "+v"(ka), "+v"(kt), "+v"(v0a), "+v"(v1), "+v"(qA), "+v"(qB), "+v"(qtA), "+v"(qtB) :: "memory");
            if (lane == 0) LDS_CNT_ADD(G_RD);
        }
        STAMP(5);
        float mA, mB; u32x2 qTA, qTB; f32x16 sA, sB;
        {
            const u32x2 t0 = {kt, 0x3f80u}, a0 = {qtA, 0u}, b0 = {qtB, 0u};
            const f32x16 s = MFMA32_16(ka, qA, z16);
            const f32x16 s0 = MFMA32_8(t0, a0, s);
            f32x16 s1 = MFMA32_16(ka, qB, z16); s1 = MFMA32_8(t0, b0, s1);
            float x0 = s0[0], x1 = s1[0];
#pragma unroll
            for (int i = 1; i < 16; ++i) { x0 = fmaxf(x0, s0[i]); x1 = fmaxf(x1, s1[i]); }
            x0 = -xhalf_max(x0); x1 = -xhalf_max(x1);
            const unsigned hh = pk2(x0, x1);
            const unsigned ll = pk2(x0 - bf_lo(hh), x1 - bf_hi(hh));
            mA = -(bf_lo(hh) + bf_lo(ll)); mB = -(bf_hi(hh) + bf_hi(ll));
            const unsigned sel = h ? ll : hh;
            qTA = (u32x2){qtA, sel & 0xffffu}; qTB = (u32x2){qtB, sel >> 16};
            sA = MFMA32_8(t0, qTA, s);
        }
        f32x16 oA = {}, oB = {};
        u32x4 pa, pb = {0u, 0u, 0u, 0u};
#define EX4(S, i, Pv, j) do { Pv[j] = pk2(ex2(S[i]), ex2(S[i + 1])); Pv[j + 1] = pk2(ex2(S[i + 2]), ex2(S[i + 3])); PIN(Pv); SBAR(); } while (0)
#define KT2 ((u32x2){kt, 0x3f80u})
#define TILE(V0C, V0N, aK1, oK1, aT1, oT1, oV1, aV0, oV0) do { \
            sB = MFMA32_16(ka, qB, z16); SBAR(); DSR128(ka, aK1, oK1); EX4(sA, 0, pa, 0); \
            oB = MFMA32_16(v1, pb, oB); SBAR(); DSR128M2(v1, aV, oV1, V0N, aV0, oV0); EX4(sA, 4, pa, 2); \
            sB = MFMA32_8(KT2, qTB, sB); SBAR(); DSR32(kt, aT1, oT1); EX4(sA, 8, pb, 0); \
            oA = MFMA32_16(V0C, pa, oA); SBAR(); EX4(sA, 12, pb, 2); \
            LWAIT(1); sA = MFMA32_16(ka, qA, z16); SBAR(); EX4(sB, 0, pa, 0); \
            oA = MFMA32_16(v1, pb, oA); SBAR(); EX4(sB, 4, pa, 2); \
            LWAIT(0); sA = MFMA32_8(KT2, qTA, sA); SBAR(); EX4(sB, 8, pb, 0); \
            oB = MFMA32_16(V0C, pa, oB); SBAR(); EX4(sB, 12, pb, 2); \
        } while (0)
        unsigned aK = laneK, aT = laneT, aV = laneV;
#pragma unroll 1
        for (int c = 0; c < 8; ++c) {
            if (c == 1) lds_wait(lds + C_KVL, 4);
            if (c >= 1) {
                unsigned pp;
                asm volatile("ds_write_b32 %1, %2\n\tds_read_b32 %0, %3\n\ts_waitcnt lgkmcnt(0)" : "=&v"(pp) : "v"(lds0 + C_PR + w * 4), "v"(c), "v"(lds0 + C_PR + (w ^ 4) * 4) : "memory");
                const int pc = __builtin_amdgcn_readfirstlane(pp);
                if (pc > c || (pc == c && ((c ^ kh) & 1))) __builtin_amdgcn_s_setprio(1); else __builtin_amdgcn_s_setprio(0);
            } else if (kh) __builtin_amdgcn_s_setprio(1);
            const unsigned nx = (c < 7 ? c + 1 : 7) * 20480;
            const unsigned nK = laneK + nx, nT = laneT + nx, nV = laneV + nx;
            SBAR();
            TILE(v0a, v0b, aK, 1024, aT, 256, 640,  aV, 1280);
            TILE(v0b, v0a, aK, 2048, aT, 512, 1920, aV, 2560);
            if (c == 0) {
                DRAIN();
                lds_wait(lds + G_Z, 1);
                unsigned arr = 0;
                if (lane == 0) { arr = LDS_CNT_ADD(C_GB); LDS_CNT_ADD(G_ST2); }
                if (__builtin_amdgcn_readfirstlane(arr) == 7 && lane == 0 && bid < 240) set_flag(ws, fG + 2048, tag);
                asm volatile("s_waitcnt lgkmcnt(0)" ::: "memory");
                SBAR();
            }
            TILE(v0a, v0b, aK, 3072, aT, 768, 3200, aV, 3840);
            TILE(v0b, v0a, nK, 0,    nT, 0,   4480, nV, 0);
            aK = nK; aT = nT; aV = nV;
        }
        oB = MFMA32_16(v1, pb, oB);
#undef TILE
#undef EX4
        if (lane == 0) __hip_atomic_store((LDS3 unsigned*)(lds + C_PR + w * 4), 8u, __ATOMIC_RELAXED, __HIP_MEMORY_SCOPE_WORKGROUP);
        STAMP(6);
        {
            float chk = 0.f;
#pragma unroll
            for (int i = 0; i < 12; ++i) chk += fabsf(oA[i]) * 0.f + fabsf(oB[i]) * 0.f;
            bool bad = !(chk == 0.f);
            if (h) bad = bad || !(oA[8] >= 0x1p-60f && oA[8] <= 0x1p60f && oB[8] >= 0x1p-60f && oB[8] <= 0x1p60f);
            if (__builtin_expect(__any(bad), 0)) {
                mA = -INFINITY; mB = -INFINITY;
#pragma unroll
                for (int i = 0; i < 16; ++i) { oA[i] = 0.f; oB[i] = 0.f; }
                const u32x2 a0 = {qtA, 0u}, b0 = {qtB, 0u};
#pragma unroll 1
                for (int t = 0; t < 32; ++t) {
                    const unsigned kb = WS_KV + head * 163840 + (2 * (t >> 2) + kh) * 10240, t4 = t & 3;
                    const u32x4 k0 = LD16(kb + t4 * 1024 + lane * 16);
                    const u32x2 t0 = {LD4(kb + 4096 + (t4 * 32 + r) * 8 + 4 * h), 0u};
                    u32x4 w0 = LD16(kb + 5120 + t4 * 1280 + h * 320 + (r < 20 ? r : 19) * 16), w1 = LD16(kb + 5120 + t4 * 1280 + 640 + h * 320 + (r < 20 ? r : 19) * 16);
                    if (r >= 20) { w0 = (u32x4){one, one, one, one}; w1 = w0; }
                    f32x16 s0 = MFMA32_16(k0, qA, z16); s0 = MFMA32_8(t0, a0, s0);
                    f32x16 s1 = MFMA32_16(k0, qB, z16); s1 = MFMA32_8(t0, b0, s1);
                    float x0 = s0[0], x1 = s1[0];
#pragma unroll
                    for (int i = 1; i < 16; ++i) { x0 = fmaxf(x0, s0[i]); x1 = fmaxf(x1, s1[i]); }
                    x0 = fmaxf(xhalf_max(x0), mA); x1 = fmaxf(xhalf_max(x1), mB);
                    const float g0 = (mA == x0) ? 1.f : ex2(mA - x0), g1 = (mB == x1) ? 1.f : ex2(mB - x1);
                    mA = x0; mB = x1;
#pragma unroll
                    for (int i = 0; i < 16; ++i) { oA[i] *= g0; oB[i] *= g1; }
#pragma unroll
                    for (int i = 0; i < 4; ++i) { pa[i] = pk2(ex2(s0[2 * i] - x0), ex2(s0[2 * i + 1] - x0)); pb[i] = pk2(ex2(s0[8 + 2 * i] - x0), ex2(s0[9 + 2 * i] - x0)); }
                    oA = MFMA32_16(w0, pa, oA); oA = MFMA32_16(w1, pb, oA);
#pragma unroll
                    for (int i = 0; i < 4; ++i) { pa[i] = pk2(ex2(s1[2 * i] - x1), ex2(s1[2 * i + 1] - x1)); pb[i] = pk2(ex2(s1[8 + 2 * i] - x1), ex2(s1[9 + 2 * i] - x1)); }
                    oB = MFMA32_16(w0, pa, oB); oB = MFMA32_16(w1, pb, oB);
                }
            }
        }
        lds_wait<4>(lds + C_BX, 8);
        const float gO = *(LDS3 const float*)(lds + X_G + (qs * 64 + kh * 32 + r) * 4);
        {
            LDS3 char* xp = lds + X_MRG + w * 2816;
            const f32x16& s = kh ? oA : oB;
            *(LDS3 f32x4*)(xp + lane * 16) = (f32x4){s[0], s[1], s[2], s[3]};
            *(LDS3 f32x4*)(xp + 1024 + lane * 16) = (f32x4){s[4], s[5], s[6], s[7]};
            if (!h) *(LDS3 f32x4*)(xp + 2048 + r * 16) = (f32x4){s[8], s[9], s[10], s[11]};
            else *(LDS3 f32x2*)(xp + 2560 + r * 8) = (f32x2){s[8], kh ? mA : mB};
            asm volatile("s_waitcnt lgkmcnt(0)" ::: "memory");
            if (lane == 0) __hip_atomic_store((LDS3 unsigned*)(lds + C_MRG + w * 4), 1u, __ATOMIC_RELAXED, __HIP_MEMORY_SCOPE_WORKGROUP);
        }
        lds_wait<2>(lds + C_MRG + (w ^ 4) * 4, 1);
        bool lastw;
        {
            LDS3 const char* xp = lds + X_MRG + (w ^ 4) * 2816;
            const f32x4 p0 = *(LDS3 const f32x4*)(xp + lane * 16), p1 = *(LDS3 const f32x4*)(xp + 1024 + lane * 16), p2 = *(LDS3 const f32x4*)(xp + 2048 + r * 16);
            const f32x2 lm = *(LDS3 const f32x2*)(xp + 2560 + r * 8);
            const f32x16& s = kh ? oB : oA;
            const float mo = kh ? mB : mA, mp = lm[1];
            const float mx = fmaxf(mo, mp);
            const float fo = (mo == mx) ? 1.f : ex2(mo - mx), fq = (mp == mx) ? 1.f : ex2(mp - mx);
            float o[12];
#pragma unroll
            for (int i = 0; i < 4; ++i) { o[i] = s[i] * fo + p0[i] * fq; o[4 + i] = s[4 + i] * fo + p1[i] * fq; o[8 + i] = s[8 + i] * fo + p2[i] * fq; }
            const float l = upper_half(s[8]) * fo + lm[0] * fq;
            store_o(rs, o, gO / l, q0 + kh * 32 + r, head * 20, h);
            DRAIN();
            unsigned arr = 0;
            if (lane == 0) arr = LDS_CNT_ADD(C_OA);
            lastw = __builtin_amdgcn_readfirstlane(arr) == 7;
            if (lastw && lane == 0) set_flag(ws, WS_F_OA + (qb * 32 + head) * 8, tag);
        }
        STAMP(7);
        {
            const int c16 = lane & 15, g = lane >> 4, tt = w & 3, k0 = kh * 10;
            POLL_ONE(lastw, 4, WS_F_OA + ((mtO >> 2) * 32 + r) * 8, WS_F_OA + ((mtO >> 2) * 32 + r) * 8, C_P4);
            STAMP(9);
            const unsigned ab = WS_AO + ((mtO * 4 + tt) * 40 + k0) * 1024 + lane * 16;
            u32x4 af[10];
#pragma unroll
            for (int ks = 0; ks < 10; ++ks) af[ks] = LD16(ab + ks * 1024);
            lds_wait(lds + C_WA, 4);
            lds_wait(lds + C_OA, 8);
            f32x4 c[5];
#pragma unroll
            for (int n = 0; n < 5; ++n) c[n] = (f32x4){0.f, 0.f, 0.f, 0.f};
#pragma unroll
            for (int ks = 0; ks < 10; ++ks)
#pragma unroll
                for (int n = 0; n < 5; ++n) c[n] = MFMA16_32(*(LDS3 const u32x4*)(lds + L_WA + (n * 20 + k0 + ks) * 1024 + lane * 16), af[ks], c[n]);
            LDS3 char* xp = lds + X_MRG + tt * 5120 + lane * 16;
            if (w < 4) { *(LDS3 f32x4*)(xp + 3072) = c[3]; *(LDS3 f32x4*)(xp + 4096) = c[4]; }
            else { *(LDS3 f32x4*)(xp) = c[0]; *(LDS3 f32x4*)(xp + 1024) = c[1]; *(LDS3 f32x4*)(xp + 2048) = c[2]; }
            asm volatile("s_waitcnt lgkmcnt(0)" ::: "memory");
            if (lane == 0) __hip_atomic_store((LDS3 unsigned*)(lds + C_FIN + w * 4), 1u, __ATOMIC_RELAXED, __HIP_MEMORY_SCOPE_WORKGROUP);
            lds_wait(lds + C_FIN + (w ^ 4) * 4, 1);
            lds_wait(lds + C_BP + tt * 4, 1);
            u16* op = P.out + (size_t)(mtO * 64 + tt * 16 + c16) * DM + ntO * 80 + g * 4;
#pragma unroll
            for (int n = 0; n < 5; ++n) {
                if ((w < 4) == (n < 3)) {
                    const f32x4 bp = *(LDS3 const f32x4*)(n < 3 ? lds + X_FIN + tt * 3072 + n * 1024 + lane * 16 : lds + 143360 + tt * 2048 + (n - 3) * 1024 + lane * 16);
                    const f32x4 s = c[n] + *(LDS3 const f32x4*)(xp + n * 1024) + bp;
                    *(u32x2*)(op + n * 16) = (u32x2){pk2(s[0], s[1]), pk2(s[2], s[3])};
                }
            }
            if (bid == 0 && mtid == 0) set_flag(ws, WS_EPOCH, tag);
        }
        STAMP(10);
        asm volatile("mega_code_end:");
    }
}

extern "C" void kernel_launch(void* const* d_in, const int* in_sizes, int n_in, void* d_out, int out_size, void* d_ws, size_t ws_size,
                              hipStream_t stream) {
    Params P;
    P.x = (const u16*)d_in[0]; P.wa = (const u16*)d_in[1]; P.woa = (const u16*)d_in[2]; P.wb = (const u16*)d_in[4];
    P.wphi = (const u16*)d_in[5]; P.wob = (const u16*)d_in[6]; P.gw = (const u16*)d_in[7]; P.ws = (char*)d_ws; P.out = (u16*)d_out;
    mega<<<256, 768, 0, stream>>>(P);
}
```

```cpp
#include <hip/hip_runtime.h>
#include <hip/hip_bf16.h>
#include <stdint.h>
#include <type_traits>

typedef __attribute__((ext_vector_type(8))) short bf16x8;
typedef __attribute__((ext_vector_type(4))) short bf16x4;
typedef __attribute__((ext_vector_type(16))) float f32x16;
typedef __attribute__((ext_vector_type(4))) float f32x4;
typedef __attribute__((ext_vector_type(2))) float f32x2;
typedef __attribute__((ext_vector_type(4))) unsigned u32x4;
typedef __attribute__((ext_vector_type(2))) unsigned u32x2;
typedef __attribute__((ext_vector_type(2))) __bf16 bf2_t;
typedef unsigned short u16;
typedef unsigned long long u64;
#define LDS3 __attribute__((address_space(3)))
typedef __attribute__((address_space(1))) u64 gu64;
#define RLX_AGENT __ATOMIC_RELAXED, __HIP_MEMORY_SCOPE_AGENT

#define PRIO_MODE 3
#define WARM_MODE 1
#define GA_PUB 11
#define PHI_ROUND 1
#define PRE_SLEEP 2
constexpr int SEQ = 2048, DM = 640, NH = 32;
constexpr float LOG2E = 1.4426950408889634f;
constexpr float QSCALE = 0.22360679774997896f * LOG2E;

constexpr unsigned WS_EPOCH = 0;
constexpr unsigned WS_F_G   = 256;
constexpr unsigned WS_F_SD  = WS_F_G + 4096;
constexpr unsigned WS_F_KV  = WS_F_SD + 256;
constexpr unsigned WS_F_OB  = WS_F_KV + 2048;
constexpr unsigned WS_F_OA  = WS_F_OB + 2048;
constexpr unsigned WS_G     = 16384;
constexpr unsigned WS_Q     = 32768;
constexpr unsigned WS_KV    = WS_Q + 2 * NH * 81920;
constexpr unsigned WS_KVP   = WS_KV + 2 * NH * 163840;
constexpr unsigned WS_AO    = WS_KVP + NH * 8 * 4096;
constexpr unsigned WS_WT    = WS_AO + SEQ * 1280 * 2;
constexpr unsigned WS_END   = WS_WT + 1280 * 640 * 2;

#define STAMP(i)

__device__ __forceinline__ unsigned pk2(float a, float b) { f32x2 v = {a, b}; bf2_t r = __builtin_convertvector(v, bf2_t); return __builtin_bit_cast(unsigned, r); }
__device__ __forceinline__ float bf_lo(unsigned u) { return __uint_as_float(u << 16); }
__device__ __forceinline__ float bf_hi(unsigned u) { return __uint_as_float(u & 0xffff0000u); }
__device__ __forceinline__ float ex2(float x) { return __builtin_amdgcn_exp2f(x); }
#define MFMA32_16(a, b, c) __builtin_amdgcn_mfma_f32_32x32x16_bf16(__builtin_bit_cast(bf16x8, a), __builtin_bit_cast(bf16x8, b), c, 0, 0, 0)
#define MFMA32_8(a, b, c) __builtin_amdgcn_mfma_f32_32x32x8bf16_1k(__builtin_bit_cast(bf16x4, a), __builtin_bit_cast(bf16x4, b), c, 0, 0, 0)
#define MFMA16_32(a, b, c) __builtin_amdgcn_mfma_f32_16x16x32_bf16(__builtin_bit_cast(bf16x8, a), __builtin_bit_cast(bf16x8, b), c, 0, 0, 0)

__device__ __forceinline__ void glds16(unsigned voff, const void* sbase, unsigned lds_base) {
    unsigned sv;
    asm volatile("s_mov_b32 %0, m0\n\ts_mov_b32 m0, %3\n\ts_nop 0\n\tglobal_load_lds_dwordx4 %1, %2\n\ts_mov_b32 m0, %0"
                 : "=&s"(sv) : "v"(voff), "s"(sbase), "s"(__builtin_amdgcn_readfirstlane(lds_base)) : "memory");
}
__device__ __forceinline__ void glds16s(unsigned voff, const void* sbase, unsigned lds_base) {
    unsigned sv;
    asm volatile("s_mov_b32 %0, m0\n\ts_mov_b32 m0, %3\n\ts_nop 0\n\tglobal_load_lds_dwordx4 %1, %2 sc1\n\ts_mov_b32 m0, %0"
                 : "=&s"(sv) : "v"(voff), "s"(sbase), "s"(__builtin_amdgcn_readfirstlane(lds_base)) : "memory");
}
__device__ __forceinline__ void glds4s(unsigned voff, const void* sbase, unsigned lds_base) {
    unsigned sv;
    asm volatile("s_mov_b32 %0, m0\n\ts_mov_b32 m0, %3\n\ts_nop 0\n\tglobal_load_lds_dword %1, %2 sc1\n\ts_mov_b32 m0, %0"
                 : "=&s"(sv) : "v"(voff), "s"(sbase), "s"(__builtin_amdgcn_readfirstlane(lds_base)) : "memory");
}
typedef short v4i16_t __attribute__((ext_vector_type(4)));
__device__ __forceinline__ u32x2 trread(LDS3 const char* p) {
    return __builtin_bit_cast(u32x2, __builtin_amdgcn_ds_read_tr16_b64_v4i16((LDS3 v4i16_t*)p));
}
#define WAIT_BAR(N) asm volatile("s_waitcnt vmcnt(" #N ") lgkmcnt(0)\n\ts_barrier" ::: "memory")
#define DRAIN() asm volatile("s_waitcnt vmcnt(0)" ::: "memory")
#define SBAR() __builtin_amdgcn_sched_barrier(0)
#define PIN(x) asm volatile("" : "+v"(x))

__device__ __forceinline__ float xhalf_max(float x) {
    auto r = __builtin_amdgcn_permlane32_swap(__float_as_uint(x), __float_as_uint(x), false, false);
    return fmaxf(__uint_as_float(r[0]), __uint_as_float(r[1]));
}
__device__ __forceinline__ float xhalf_sum(float x) {
    auto r = __builtin_amdgcn_permlane32_swap(__float_as_uint(x), __float_as_uint(x), false, false);
    return __uint_as_float(r[0]) + __uint_as_float(r[1]);
}
__device__ __forceinline__ float upper_half(float x) {
    auto r = __builtin_amdgcn_permlane32_swap(__float_as_uint(x), __float_as_uint(x), false, false);
    return __uint_as_float(r[1]);
}

typedef __amdgpu_buffer_rsrc_t rsrc_t;
#define ST8(v, off)  __builtin_amdgcn_raw_buffer_store_b64(v, rs, (int)(off), 0, 16)
#define ST16(v, off) __builtin_amdgcn_raw_buffer_store_b128(v, rs, (int)(off), 0, 16)
#define ST4(v, off)  __builtin_amdgcn_raw_buffer_store_b32(v, rs, (int)(off), 0, 16)
#define LD16(off) __builtin_amdgcn_raw_buffer_load_b128(rs, (int)(off), 0, 16)
#define LD4(off)  __builtin_amdgcn_raw_buffer_load_b32(rs, (int)(off), 0, 16)

template <int SLP = 2>
__device__ __forceinline__ void wait_flags(const char* ws, unsigned off0, unsigned off1, u64 tag) {
    gu64* p0 = (gu64*)(ws + off0); gu64* p1 = (gu64*)(ws + off1);
#pragma unroll 1
    for (unsigned spins = 0; spins < (1u << 16); ++spins) {
        const u64 a = __hip_atomic_load(p0, RLX_AGENT), b = __hip_atomic_load(p1, RLX_AGENT);
        if (__all(a == tag && b == tag)) break;
        __builtin_amdgcn_s_sleep(SLP);
    }
    __builtin_amdgcn_fence(__ATOMIC_ACQUIRE, "wavefront");
}
__device__ __forceinline__ void set_flag(char* ws, unsigned off, u64 tag) { __hip_atomic_store((gu64*)(ws + off), tag, RLX_AGENT); }

struct Params { const u16* x; const u16* wa; const u16* wb; const u16* wphi; const u16* woa; const u16* wob; const u16* gw; char* ws; u16* out; };

__device__ __forceinline__ void feature_map(const u32x4& pm, unsigned pt, const u32x4& tm, unsigned tt, int h, u32x4& fp, u32x4& fn, u32x4& ft) {
    const f32x16 z16 = {};
    const u32x2 a8 = {pt, 0u}, b8 = {tt, 0u};
    f32x16 s = MFMA32_16(pm, tm, z16); s = MFMA32_8(a8, b8, s);
    f32x2 v[6];
#pragma unroll
    for (int i = 0; i < 6; ++i) {
        const unsigned u = pk2(s[2 * i], s[2 * i + 1]); v[i] = (f32x2){bf_lo(u), bf_hi(u)};
    }
    float mx = fmaxf(v[0][0], v[0][1]), mn = fminf(v[0][0], v[0][1]);
#pragma unroll
    for (int i = 1; i < 6; ++i) { mx = fmaxf(fmaxf(mx, v[i][0]), v[i][1]); mn = fminf(fminf(mn, v[i][0]), v[i][1]); }
    mx = xhalf_max(mx) * -LOG2E; mn = xhalf_max(-mn) * -LOG2E;
    f32x2 ep[6], en[6];
#pragma unroll
    for (int i = 0; i < 6; ++i) {
        const f32x2 a = v[i] * LOG2E + mx, b = v[i] * -LOG2E + mn;
        ep[i] = (f32x2){ex2(a[0]), ex2(a[1])}; en[i] = (f32x2){ex2(b[0]), ex2(b[1])};
    }
    const float tw = h ? 0.f : 1.f;
    const f32x2 s2p = ((ep[0] + ep[1]) + (ep[2] + ep[3])) + (ep[4] + ep[5]) * tw, s2n = ((en[0] + en[1]) + (en[2] + en[3])) + (en[4] + en[5]) * tw;
    const float sp = __builtin_amdgcn_rcpf(xhalf_sum(s2p[0] + s2p[1])), sn = __builtin_amdgcn_rcpf(xhalf_sum(s2n[0] + s2n[1]));
    const float tp = sp * tw, tn = sn * tw;
#pragma unroll
    for (int i = 0; i < 4; ++i) { const f32x2 a = ep[i] * sp, b = en[i] * sn; fp[i] = pk2(a[0], a[1]); fn[i] = pk2(b[0], b[1]); }
    const f32x2 c0 = ep[4] * tp, c1 = ep[5] * tp, c2 = en[4] * tn, c3 = en[5] * tn;
    ft = (u32x4){pk2(c0[0], c0[1]), pk2(c1[0], c1[1]), pk2(c2[0], c2[1]), pk2(c3[0], c3[1])};
}

__device__ __forceinline__ void store_o(rsrc_t rs, const float (&o)[12], float sc, int tok, int kbase, int h) {
#pragma unroll
    for (int gq = 0; gq < 3; ++gq) {
        const int d = 8 * gq + 4 * h;
        if (d < 20) {
            const int k = kbase + ((kbase & 4) ? (d == 16 ? 0 : d + 4) : d);
            u32x2 ov = {pk2(o[4 * gq] * sc, o[4 * gq + 1] * sc), pk2(o[4 * gq + 2] * sc, o[4 * gq + 3] * sc)};
            ST8(ov, WS_AO + (((tok >> 4) * 40 + (k >> 5)) * 64 + ((k >> 3) & 3) * 16 + (tok & 15)) * 16 + (k & 7) * 2);
        }
    }
}

#define DSR128(dst, addr, off) asm volatile("ds_read_b128 %0, %1 offset:%2" : "=v"(dst) : "v"(addr), "i"(off))
#define DSR32(dst, addr, off) asm volatile("ds_read_b32 %0, %1 offset:%2" : "=v"(dst) : "v"(addr), "i"(off))
#define DSR128M2(d0, a0, o0, d1, a1, o1) asm volatile("s_mov_b64 exec, %6\n\tds_read_b128 %0, %2 offset:%3\n\tds_read_b128 %1, %4 offset:%5\n\ts_mov_b64 exec, -1" \
                                                      : "+v"(d0), "+v"(d1) : "v"(a0), "i"(o0), "v"(a1), "i"(o1), "s"(vmask))
#define LWAIT(n) do { asm volatile("s_waitcnt lgkmcnt(" #n ")" ::: "memory"); SBAR(); } while (0)

#define BAR() asm volatile("s_barrier" ::: "memory")
#define LDS_SET(off, v) __hip_atomic_store((LDS3 unsigned*)(lds + (off)), (unsigned)(v), __ATOMIC_RELAXED, __HIP_MEMORY_SCOPE_WORKGROUP)
#define LDS_CNT_ADD(off) __hip_atomic_fetch_add((LDS3 unsigned*)(lds + (off)), 1u, __ATOMIC_RELAXED, __HIP_MEMORY_SCOPE_WORKGROUP)

template <int SLP = 1>
__device__ __forceinline__ void lds_wait(LDS3 char* p, unsigned want) {
#pragma unroll 1
    for (unsigned spins = 0; spins < (1u << 20); ++spins) {
        if ((unsigned)__builtin_amdgcn_readfirstlane(__hip_atomic_load((LDS3 unsigned*)p, __ATOMIC_RELAXED, __HIP_MEMORY_SCOPE_WORKGROUP)) >= want) break;
        __builtin_amdgcn_s_sleep(SLP);
    }
    __builtin_amdgcn_fence(__ATOMIC_ACQUIRE, "wavefront");
}

template <int SLP>
__device__ __forceinline__ void prog_wait(LDS3 char* p, int lane, unsigned want) {
#pragma unroll 1
    for (unsigned spins = 0; spins < (1u << 20); ++spins) {
        if (__all(__hip_atomic_load((LDS3 unsigned*)p + (lane & 7), __ATOMIC_RELAXED, __HIP_MEMORY_SCOPE_WORKGROUP) >= want)) break;
        __builtin_amdgcn_s_sleep(SLP);
    }
    __builtin_amdgcn_fence(__ATOMIC_ACQUIRE, "wavefront");
}
#define POLL_ONE(cond, SLP, o0, o1, word) do { if (cond) { wait_flags<SLP>(ws, o0, o1, tag); \
        if (lane == 0) __hip_atomic_store((LDS3 unsigned*)(lds + (word)), 1u, __ATOMIC_RELAXED, __HIP_MEMORY_SCOPE_WORKGROUP); } else lds_wait<SLP>(lds + (word), 1); } while (0)
constexpr int C_KV = 0, C_KVF = 4, C_OB = 8, C_PROG = 12, C_WA = 16, C_OA = 20, C_MRG = 32  , C_FIN = 64  , C_BP = 96  ;
constexpr int C_PROG2 = 24, C_BX = 28, C_P1 = 112, C_P2 = 116, C_P3 = 120, C_P4 = 124, C_PR = 128  ;
constexpr int G_W = 31744;
constexpr int G_FULL = G_W  , G_PRG = G_W + 16  , G_Q = G_W + 48, G_P = G_W + 52, G_ST = G_W + 56, G_RD = G_W + 60, G_Z = G_W + 64, G_ST2 = G_W + 68;
constexpr int C_GB = 168, C_KVL = 160, C_P5 = 164, X_G = 256;
constexpr int X_KVF = 1280, X_SLOT = 4096;
constexpr int X_MRG = 1280;
constexpr int L_WA = 24576;
constexpr int L_Q = 81920;
constexpr int X_FIN = 126976;

__global__ __launch_bounds__(768) void mega(Params P) {
    __shared__ __attribute__((aligned(16))) char smem[163840];
    LDS3 char* lds = (LDS3 char*)smem;
    char* ws = P.ws;
    const int tid = threadIdx.x, lane = tid & 63, r = lane & 31, h = lane >> 5;
    const int hw = __builtin_amdgcn_readfirstlane(tid >> 6);
    const bool helper = hw < 4;
    const int w = hw - 4, mtid = tid - 256;
    const int bid = blockIdx.x;
    const unsigned lds0 = (unsigned)(uintptr_t)smem;
    const rsrc_t rs = __builtin_amdgcn_make_buffer_rsrc(ws, 0, WS_END, 0x00020000);
    STAMP(0);
    u64 tag = 0;
#define GET_TAG() do { asm volatile("s_load_dwordx2 %0, %1, 0x0\n\ts_waitcnt lgkmcnt(0)" : "=s"(tag) : "s"(ws) : "memory"); tag += 1; } while (0)
    const int head = (bid & 7) * 4 + ((bid >> 3) & 3), qb = bid >> 5;
    const int nlo = (head * 20) >> 7, nhi = (head * 20 + 19) >> 7;
    const int mtO = (bid & 7) * 4 + ((bid >> 3) & 3), ntO = bid >> 5;
    const int lin = (bid & 7) * 30 + (bid >> 3), lh = lin >= 120, lrem = lin - 120 * lh;
    const int mt = lh * 8 + (lrem & 7), nt = lrem >> 3;
    const unsigned fG = WS_F_G + (mt * 15 + nt) * 8;
    const unsigned one = (r == 20) ? 0x3f803f80u : 0u;
    const f32x16 z16 = {};

    if (helper) {
        const unsigned fA0 = WS_F_G + ((lane & 15) * 15 + (lane < 32 ? 5 : 10) + ((lane & 16) ? nhi : nlo)) * 8;
        const unsigned fA1 = WS_F_G + ((qb * 2 + (lane & 1)) * 15 + ((lane & 2) ? nhi : nlo)) * 8;
        __builtin_amdgcn_s_setprio(3);
        const char* kvb = ws + WS_KV + head * 163840;
        auto kv_issue = [&](int grp) {
#pragma unroll
            for (int t = 0; t < 8; ++t) { int pc = grp * 32 + hw + 4 * t; if (pc == 31) pc = 30; glds16s(lane * 16, kvb + pc * 1024, lds0 + pc * 1024); }
        };
        auto q_issue = [&]() {
            const char* qbase = ws + WS_Q + head * 81920 + qb * 10240;
#pragma unroll
            for (int t = 0; t < 3; ++t) { const int pc = hw + 4 * t < 10 ? hw + 4 * t : 9; glds16s(lane * 16, qbase + pc * 1024, lds0 + L_Q + pc * 1024); }
        };
        if (bid < 240) {
            const int rot = (nt * 3 + mt * 5 + (mt >> 1)) % 10;
            const unsigned xo = (mt * 128 + 8 * hw + (lane >> 3)) * 1280 + (((lane & 7) ^ ((4 * hw + (lane >> 4)) & 7)) << 4);
            const unsigned wo = (4 * hw + (lane >> 4)) * 3840 + nt * 256 + (((lane & 15) ^ (((lane >> 4) << 2) | hw)) << 4);
            const unsigned dX = lds0 + 32768 + hw * 1024;
            auto issue = [&](int s) {
                int kt = (s >= 10 ? 19 - s : s) + rot; if (kt >= 10) kt -= 10;
                const char* xb = (const char*)P.x + kt * 128;
                const char* wb = (const char*)(s >= 10 ? P.wb : P.wa) + (size_t)kt * (64 * 3840);
                const unsigned dw = dX + 16384 + (s & 3) * 32768, dx = dX + ((s >= 10 ? 20 - s : s + 1) & 3) * 32768;
#pragma unroll
                for (int i = 0; i < 4; ++i) glds16(wo, wb + i * 61440, dw + i * 4096);
                if (s < 10 || s >= 14) {
#pragma unroll
                    for (int i = 0; i < 4; ++i) glds16(xo, xb + i * 40960, dx + i * 4096);
                }
            };
#pragma unroll 1
            for (int n = 0; n < 20; ++n) {
                if (n == 2) { if (hw == 0 && lane < 32) *(LDS3 unsigned*)(lds + G_W + lane * 4) = 0u; GET_TAG(); WAIT_BAR(8); }
                else if (n == 3 || (n >= 11 && n <= 15)) { WAIT_BAR(8); }
                else if (n >= 5) { WAIT_BAR(16); }
                issue(n);
            }
            WAIT_BAR(16);
            WAIT_BAR(0);
        } else {
            if (hw == 0 && lane < 32) *(LDS3 unsigned*)(lds + G_W + lane * 4) = lane >= 4 && lane < 12 ? 20u : 0u;
            GET_TAG();
            WAIT_BAR(63);
            if (hw == 0) {
#pragma unroll 1
                for (int t = 0; t < PRE_SLEEP; ++t) __builtin_amdgcn_s_sleep(127);
            }
        }
        POLL_ONE(hw == 0, 6, fA0, fA1, G_P);
        kv_issue(0); q_issue(); kv_issue(1);
        asm volatile("s_waitcnt vmcnt(6)" ::: "memory");
        if (lane == 0) LDS_CNT_ADD(G_Q);
        prog_wait<1>(lds + G_PRG, lane, 19);
        kv_issue(3);
        prog_wait<1>(lds + G_PRG, lane, 20);
        kv_issue(4);
        lds_wait<2>(lds + G_RD, 8);
        if (hw == 0) {
            *(LDS3 u32x4*)(lds + lane * 16) = (u32x4){0u, 0u, 0u, 0u};
            asm volatile("s_waitcnt lgkmcnt(0)" ::: "memory");
            if (lane == 0) LDS_SET(G_Z, 1);
        }
        kv_issue(2);
        lds_wait<2>(lds + G_Z, 1);
        if (lane == 0) LDS_CNT_ADD(G_ST2);
        if (hw == 3) {
            lds_wait<2>(lds + G_ST2, 12);
            glds16s(lane * 16, kvb + 31 * 1024, lds0 + 31 * 1024);
        }
        STAMP(3);
        u32x4 phm; unsigned pht;
        {
            const u16* wp = P.wphi + head * 400 + (r < 20 ? r : (r & 3));
            unsigned e[10];
#pragma unroll
            for (int j = 0; j < 8; ++j) e[j] = wp[(8 * h + j + 4 * (head & 1)) * 20];
            e[8] = wp[((head & 1 ? 0 : 16) + 2 * h) * 20]; e[9] = wp[((head & 1 ? 1 : 17) + 2 * h) * 20];
            const bool cp = bid < 160 && tid < 160;
            const int k0 = cp ? bid * 8 : 0, n = cp ? tid * 4 : 0;
            const u16* wsrc = (k0 < 640 ? P.woa + (size_t)k0 * DM : P.wob + (size_t)(k0 - 640) * DM) + n;
            u32x2 f[8];
#pragma unroll
            for (int j = 0; j < 8; ++j) f[j] = *(const u32x2*)(wsrc + (size_t)j * DM);
            DRAIN();
            if (lane == 0) LDS_CNT_ADD(C_KVL);
            phm = (u32x4){e[0] | (e[1] << 16), e[2] | (e[3] << 16), e[4] | (e[5] << 16), e[6] | (e[7] << 16)}; pht = e[8] | (e[9] << 16);
            const unsigned dp = WS_WT + (((n >> 4) * 40 + (k0 >> 5)) * 64 + ((k0 >> 3) & 3) * 16 + (n & 15)) * 16;
            if (cp) {
#pragma unroll
                for (int q = 0; q < 2; ++q) {
                    const u32x4 lo = {(f[0][q] & 0xffffu) | (f[1][q] << 16), (f[2][q] & 0xffffu) | (f[3][q] << 16), (f[4][q] & 0xffffu) | (f[5][q] << 16), (f[6][q] & 0xffffu) | (f[7][q] << 16)};
                    const u32x4 hi = {(f[0][q] >> 16) | (f[1][q] & 0xffff0000u), (f[2][q] >> 16) | (f[3][q] & 0xffff0000u), (f[4][q] >> 16) | (f[5][q] & 0xffff0000u), (f[6][q] >> 16) | (f[7][q] & 0xffff0000u)};
                    ST16(lo, dp + q * 32); ST16(hi, dp + q * 32 + 16);
                }
            }
        }
        const int tokS = qb * 256 + hw * 64, m7 = tokS >> 7, t4 = (tokS >> 5) & 3;
        {
            const unsigned pt3 = (lane >> 1) & 3;
            const unsigned tb = WS_F_G + 2048 + ((qb * 2 + ((lane >> 3) & 1)) * 15 + ((lane & 1) ? nhi : nlo) + (pt3 < 3 ? pt3 : 0) * 5) * 8;
            POLL_ONE(hw == 0, 4, tb, WS_F_SD + (qb * 2 + (lane & 1)) * 8, C_P1);
        }
        const unsigned om = t4 * 1024 + lane * 16, ot = 4096 + (t4 * 32 + r) * 8 + 4 * h;
        u32x4 qf[2][3]; float gS[2];
        {
            const unsigned kb = WS_KV + (NH + head) * 163840 + (2 * (m7 & 7) + (m7 >> 3)) * 10240, qbb = WS_Q + (NH + head) * 81920 + m7 * 5120;
            const unsigned ov = 5120 + t4 * 1280 + h * 320 + (r < 20 ? r : 19) * 16;
            u32x4 e0[2], w0[2], w1[2], eq[2]; unsigned et[2], eqt[2];
#pragma unroll
            for (int i = 0; i < 2; ++i) {
                e0[i] = LD16(kb + om + i * 1024); et[i] = LD4(kb + ot + i * 256); w0[i] = LD16(kb + ov + i * 1280); w1[i] = LD16(kb + ov + i * 1280 + 640);
                eq[i] = LD16(qbb + om + i * 1024); eqt[i] = LD4(qbb + ot + i * 256); gS[i] = __uint_as_float(LD4(WS_G + (tokS + i * 32 + r) * 4));
            }
            u32x4 i0, i1;
#pragma unroll
            for (int j = 0; j < 4; ++j) {
                const int e = r - 8 * h - 2 * j;
                i0[j] = e == 0 ? 0x3f80u : (e == 1 ? 0x3f800000u : 0u);
                i1[j] = e == 16 ? 0x3f80u : (e == 17 ? 0x3f800000u : 0u);
            }
            f32x16 k0 = {}, k1 = {};
#pragma unroll
            for (int i = 0; i < 2; ++i) {
                if (r >= 20) { w0[i] = (u32x4){one, one, one, one}; w1[i] = w0[i]; }
                u32x4 fp, fn, ft;
                feature_map(phm, pht, e0[i], et[i], h, fp, fn, ft);
                f32x16 T0 = MFMA32_16(fp, i0, z16); T0 = MFMA32_16(fn, i1, T0);
                f32x16 T1 = MFMA32_16(ft, i0, z16);
                u32x4 b00, b01, b10, b11;
#pragma unroll
                for (int j = 0; j < 4; ++j) {
                    b00[j] = pk2(T0[2 * j], T0[2 * j + 1]); b01[j] = pk2(T0[8 + 2 * j], T0[9 + 2 * j]);
                    b10[j] = pk2(T1[2 * j], T1[2 * j + 1]); b11[j] = pk2(T1[8 + 2 * j], T1[9 + 2 * j]);
                }
                k0 = MFMA32_16(w0[i], b00, k0); k0 = MFMA32_16(w1[i], b01, k0);
                k1 = MFMA32_16(w0[i], b10, k1); k1 = MFMA32_16(w1[i], b11, k1);
            }
            prog_wait<8>(lds + C_PR, lane, 1);
            *(LDS3 float*)(lds + X_G + (hw * 64 + lane) * 4) = h ? gS[1] : gS[0];
            if (hw * 64 + lane < 144) *(LDS3 u32x4*)(lds + X_KVF + (hw * 64 + lane) * 16) = (u32x4){0u, 0u, 0u, 0u};
            LDS3 char* sp = lds + X_SLOT + hw * 4096;
#pragma unroll
            for (int gq = 0; gq < 3; ++gq) {
                *(LDS3 f32x4*)(sp + gq * 1024 + lane * 16) = (f32x4){k0[4 * gq], k0[4 * gq + 1], k0[4 * gq + 2], k0[4 * gq + 3]};
                if (r < 8) *(LDS3 f32x4*)(sp + 3072 + (gq * 16 + h * 8 + r) * 16) = (f32x4){k1[4 * gq], k1[4 * gq + 1], k1[4 * gq + 2], k1[4 * gq + 3]};
            }
            asm volatile("s_waitcnt vmcnt(0) lgkmcnt(0)" ::: "memory");
            unsigned arr = 0;
            if (lane == 0) arr = LDS_CNT_ADD(C_KV);
            if (__builtin_amdgcn_readfirstlane(arr) == 3) {
                __builtin_amdgcn_fence(__ATOMIC_ACQUIRE, "wavefront");
#pragma unroll
                for (int i = 0; i < 4; ++i) {
                    const int qd = lane + 64 * i;
                    LDS3 const char* s0 = lds + X_SLOT + qd * 16;
                    const f32x4 s = (*(LDS3 const f32x4*)s0 + *(LDS3 const f32x4*)(s0 + 4096)) + (*(LDS3 const f32x4*)(s0 + 8192) + *(LDS3 const f32x4*)(s0 + 12288));
                    if (qd < 240) ST16(__builtin_bit_cast(u32x4, s), WS_KVP + (head * 8 + qb) * 4096 + qd * 16);
                }
                DRAIN();
                if (lane == 0) set_flag(ws, WS_F_KV + (head * 8 + qb) * 8, tag);
            }
#pragma unroll
            for (int i = 0; i < 2; ++i) feature_map(phm, pht, eq[i], eqt[i], h, qf[i][0], qf[i][1], qf[i][2]);
        }
        STAMP(4);
        POLL_ONE(hw == 0, 4, WS_F_KV + (head * 8 + (lane & 7)) * 8, WS_F_KV + (head * 8 + (lane & 7)) * 8, C_P2);
        {
            const int qd = hw * 60 + (lane < 60 ? lane : 59);
            f32x4 pp[8];
#pragma unroll
            for (int p = 0; p < 8; ++p) pp[p] = __builtin_bit_cast(f32x4, LD16(WS_KVP + (head * 8 + p) * 4096 + qd * 16));
            const f32x4 s4 = ((pp[0] + pp[1]) + (pp[2] + pp[3])) + ((pp[4] + pp[5]) + (pp[6] + pp[7]));
            const int gq = qd < 192 ? qd >> 6 : (qd - 192) >> 4, hs = qd < 192 ? (qd >> 5) & 1 : (qd >> 3) & 1, sg = qd < 192 ? qd & 31 : 32 + (qd & 7);
            const unsigned a = pk2(s4[0], s4[1]), b = pk2(s4[2], s4[3]);
            LDS3 u16* kp = (LDS3 u16*)(lds + X_KVF + (8 * gq + 4 * hs) * 96 + sg * 2);
            kp[0] = (u16)a; kp[48] = (u16)(a >> 16); kp[96] = (u16)b; kp[144] = (u16)(b >> 16);
            asm volatile("s_waitcnt lgkmcnt(0)" ::: "memory");
            if (lane == 0) LDS_CNT_ADD(C_KVF);
            lds_wait<2>(lds + C_KVF, 4);
        }
        {
            LDS3 const char* kp = lds + X_KVF + (r < 24 ? r : 23) * 96 + h * 16;
            const u32x4 a0 = *(LDS3 const u32x4*)kp, a1 = *(LDS3 const u32x4*)(kp + 32), a2 = *(LDS3 const u32x4*)(kp + 64);
#pragma unroll
            for (int i = 0; i < 2; ++i) {
                f32x16 d = MFMA32_16(a0, qf[i][0], z16); d = MFMA32_16(a1, qf[i][1], d); d = MFMA32_16(a2, qf[i][2], d);
                const float den = upper_half(d[8]) + 1e-6f;
                float o[12];
#pragma unroll
                for (int j = 0; j < 12; ++j) o[j] = d[j];
                store_o(rs, o, (1.f - gS[i]) / den, tokS + i * 32 + r, 640 + head * 20, h);
            }
            DRAIN();
            unsigned arr = 0;
            if (lane == 0) arr = LDS_CNT_ADD(C_OB);
            if (__builtin_amdgcn_readfirstlane(arr) == 3 && lane == 0) set_flag(ws, WS_F_OB + (qb * 32 + head) * 8, tag);
        }
        STAMP(5);
        f32x4 c[5];
#pragma unroll
        for (int n = 0; n < 5; ++n) c[n] = (f32x4){0.f, 0.f, 0.f, 0.f};
        const char* wsrc = ws + WS_WT + (size_t)ntO * 5 * 40960;
        {
            if (hw == 0) wait_flags<8>(ws, WS_F_KV + lane * 8, WS_F_KV + 512 + lane * 8, tag);
            POLL_ONE(hw == 0, 8, WS_F_KV + 1024 + lane * 8, WS_F_KV + 1536 + lane * 8, C_P5);
#pragma unroll 1
            for (int t = 0; t < 5; ++t) {
                if (t == 0) prog_wait<8>(lds + C_PR, lane, 4); else if (t == 3) prog_wait<4>(lds + C_PR, lane, 5);
                const int ks = hw + 4 * t < 19 ? hw + 4 * t : 18;
#pragma unroll
                for (int n = 0; n < 5; ++n) glds16s(lane * 16, wsrc + (n * 40 + 20 + ks) * 1024, lds0 + 4096 + (ks * 5 + n) * 1024);
            }
            POLL_ONE(hw == 0, 4, WS_F_OB + ((mtO >> 2) * 32 + r) * 8, WS_F_OB + ((mtO >> 2) * 32 + r) * 8, C_P3);
            const unsigned ab = WS_AO + ((mtO * 4 + hw) * 40 + 20) * 1024 + lane * 16;
            u32x4 af[20];
#pragma unroll
            for (int ks = 0; ks < 20; ++ks) af[ks] = LD16(ab + ks * 1024);
            asm volatile("s_waitcnt vmcnt(20)" ::: "memory");
            if (lane == 0) LDS_CNT_ADD(C_BX);
            lds_wait<2>(lds + C_BX, 4);
            u32x4 wf[3][5], wl[5];
            LDS3 const char* wp = lds + 4096 + lane * 16;
#pragma unroll
            for (int ks = 0; ks < 2; ++ks)
#pragma unroll
                for (int n = 0; n < 5; ++n) wf[ks][n] = *(LDS3 const u32x4*)(wp + (ks * 5 + n) * 1024);
#pragma unroll
            for (int ks = 0; ks < 20; ++ks) {
                if (ks + 2 < 19) {
#pragma unroll
                    for (int n = 0; n < 5; ++n) wf[(ks + 2) % 3][n] = *(LDS3 const u32x4*)(wp + ((ks + 2) * 5 + n) * 1024);
                }
                if (ks == 8) {
#pragma unroll
                    for (int n = 0; n < 5; ++n) wl[n] = LD16(WS_WT + ((ntO * 5 + n) * 40 + 39) * 1024 + lane * 16);
                }
                SBAR();
#pragma unroll
                for (int n = 0; n < 5; ++n) c[n] = MFMA16_32(ks < 19 ? wf[ks % 3][n] : wl[n], af[ks], c[n]);
                SBAR();
            }
            if (lane == 0) LDS_CNT_ADD(C_BX);
            lds_wait<2>(lds + C_BX, 8);
        }
        STAMP(6);
        prog_wait<4>(lds + C_PR, lane, 7);
#pragma unroll 1
        for (int n = 0; n < 5; ++n)
#pragma unroll
            for (int t = 0; t < 5; ++t) { const int ks = hw + 4 * t; glds16s(lane * 16, wsrc + (n * 40 + ks) * 1024, lds0 + L_WA + (n * 20 + ks) * 1024); }
#pragma unroll
        for (int n = 0; n < 3; ++n) *(LDS3 f32x4*)(lds + X_FIN + hw * 3072 + n * 1024 + lane * 16) = c[n];
        DRAIN();
        if (lane == 0) LDS_CNT_ADD(C_WA);
        prog_wait<4>(lds + C_PR, lane, 8);
#pragma unroll
        for (int n = 3; n < 5; ++n) *(LDS3 f32x4*)(lds + 143360 + hw * 2048 + (n - 3) * 1024 + lane * 16) = c[n];
        asm volatile("s_waitcnt lgkmcnt(0)" ::: "memory");
        if (lane == 0) __hip_atomic_store((LDS3 unsigned*)(lds + C_BP + hw * 4), 1u, __ATOMIC_RELAXED, __HIP_MEMORY_SCOPE_WORKGROUP);
        STAMP(7);
        asm volatile("mega_code_end2:");
    } else {
        const int qs = w & 3, kh = w >> 2;
        const int q0 = qb * 256 + qs * 64;
        {
            unsigned sink;
            if (w == 0 && (WARM_MODE & 1)) {
                const char* pc0; int len, len2;
                asm volatile("s_getpc_b64 %0\nmega_code_mark:\n\ts_mov_b32 %1, mega_code_end-mega_code_mark\n\ts_mov_b32 %2, mega_code_end2-mega_code_mark" : "=s"(pc0), "=s"(len), "=s"(len2));
                len = len > len2 ? len : len2;
                const int o = ((bid >> 3) * 64 + lane) * 64;
                if (o + 4 <= len) asm volatile("global_load_dword %0, %1, %2" : "=v"(sink) : "v"(o), "s"(pc0) : "memory");
            } else if (w >= 1 && w <= 6 && (WARM_MODE & 2)) {
                const unsigned o = ((w - 1) * 64 + lane) * 65536 + 65532;
                if (o < WS_END) asm volatile("global_load_dword %0, %1, %2 sc1" : "=v"(sink) : "v"(o), "s"(ws) : "memory");
            } else if (w == 7 && (WARM_MODE & 2)) {
                if (lane < 40) asm volatile("global_load_dword %0, %1, %2 sc1" : "=v"(sink) : "v"(lane * 65536), "s"(P.out) : "memory");
            }
            asm volatile("s_waitcnt vmcnt(0)" ::: "memory");
        }
        GET_TAG();
        if (bid < 240) {
            const int part = nt / 5, c0 = (nt - part * 5) * 128;
            const int wm = w & 1, wn = w >> 1;
            constexpr int RB = 32768;
            const int xr = wm * 64 + r;
            const unsigned xa = RB + xr * 128 + ((h ^ ((xr >> 1) & 7)) << 4);
            unsigned wt[2];
#pragma unroll
            for (int t = 0; t < 2; ++t) {
                const int q = (lane & 15) >> 2, p = lane & 3, blk = (lane >> 4) & 1, row = 8 * h + 4 * t + q;
                wt[t] = RB + 16384 + row * 256 + (((4 * wn + 2 * blk + (p >> 1)) ^ ((q << 2) | (2 * h + t))) << 4) + 8 * (p & 1);
            }
            auto gbody = [&](auto trc) {
                constexpr bool TR = decltype(trc)::value;
                u32x4 xf[2][8], wf[2][4];
                auto rdk = [&](auto bc, LDS3 const char* sx, LDS3 const char* sb, int ks) {
                    constexpr int B = decltype(bc)::value;
                    xf[B][2 * ks] = *(LDS3 const u32x4*)(sx + (xa ^ (ks * 32)));
                    xf[B][2 * ks + 1] = *(LDS3 const u32x4*)(sx + ((xa + 4096) ^ (ks * 32)));
                    const u32x2 lo = trread(sb + wt[0] + ks * 4096), hi = trread(sb + wt[1] + ks * 4096);
                    wf[B][ks] = (u32x4){lo[0], lo[1], hi[0], hi[1]};
                };
                f32x16 acc0, acc1;
                auto step = [&](auto bc, int s) {
                    constexpr int B = decltype(bc)::value;
                    if (s == GA_PUB) {
                        DRAIN();
                        unsigned arr = 0;
                        if (lane == 0) arr = LDS_CNT_ADD(G_ST);
                        if (__builtin_amdgcn_readfirstlane(arr) == 7 && lane == 0) set_flag(ws, fG, tag);
                    }
                    __builtin_amdgcn_s_waitcnt(0xC07F);
                    if (s < 18) BAR();
                    else if (lane == 0) LDS_SET(G_PRG + w * 4, s + 1);
                    SBAR();
                    const int s1 = s + 1;
                    LDS3 const char* sb = lds + __builtin_amdgcn_readfirstlane((s1 & 3) * 32768);
                    LDS3 const char* sx = lds + __builtin_amdgcn_readfirstlane(((s1 >= 10 ? 20 - s1 : s1 + 1) & 3) * 32768);
#define GM(A, i) do { if constexpr (TR) A = MFMA32_16(wf[B][(i) >> 1], xf[B][i], A); else A = MFMA32_16(xf[B][i], wf[B][(i) >> 1], A); SBAR(); } while (0)
#define GX(i) do { xf[B ^ 1][i] = *(LDS3 const u32x4*)(sx + ((xa + ((i) & 1) * 4096) ^ (((i) >> 1) * 32))); } while (0)
#define GW(ks) do { const u32x2 lo = trread(sb + wt[0] + (ks) * 4096), hi = trread(sb + wt[1] + (ks) * 4096); wf[B ^ 1][ks] = (u32x4){lo[0], lo[1], hi[0], hi[1]}; } while (0)
                    GM(acc0, 0); GX(0); GW(0); SBAR(); GM(acc1, 1); GX(1); GX(2); SBAR(); GM(acc0, 2); GW(1); GX(3); SBAR(); GM(acc1, 3); GX(4); GW(2); SBAR();
                    GM(acc0, 4); GX(5); GX(6); SBAR(); GM(acc1, 5); GW(3); GX(7); SBAR(); GM(acc0, 6); GM(acc1, 7);
#undef GX
#undef GW
#undef GM
                };
                WAIT_BAR(63);
#pragma unroll
                for (int ks = 0; ks < 4; ++ks) rdk(std::integral_constant<int, 0>{}, lds + 32768, lds, ks);
#pragma unroll 1
                for (int mat = 0; mat < 2; ++mat) {
                    acc0 = (f32x16){}; acc1 = (f32x16){};
#pragma unroll 1
                    for (int kk = 0; kk < 10; kk += 2) {
                        step(std::integral_constant<int, 0>{}, mat * 10 + kk);
                        step(std::integral_constant<int, 1>{}, mat * 10 + kk + 1);
                    }
                    const int cpos = 2 * (mt & 7) + (mt >> 3);
                    if constexpr (TR) {
                        const float sc = (part == 0 && mat == 0) ? QSCALE : 1.f;
                        const unsigned base = part == 0 ? WS_Q + mat * NH * 81920 + mt * 5120 : WS_KV + mat * NH * 163840 + cpos * 10240;
                        const unsigned hs = part == 0 ? 81920 : 163840;
#pragma unroll
                        for (int pr = 0; pr < 2; ++pr) {
                            const int G8 = ((c0 + 32 * wn) >> 3) + 2 * pr + h, pp = G8 / 5, gi = G8 - pp * 5;
                            const unsigned hb0 = base + (2 * pp + (gi > 2)) * hs;
#pragma unroll
                            for (int i = 0; i < 2; ++i) {
                                const f32x16& a = i ? acc1 : acc0;
                                const int tl = wm * 64 + i * 32 + r;
                                const auto s0 = __builtin_amdgcn_permlane32_swap(pk2(a[8 * pr] * sc, a[8 * pr + 1] * sc), pk2(a[8 * pr + 4] * sc, a[8 * pr + 5] * sc), false, false);
                                const auto s1 = __builtin_amdgcn_permlane32_swap(pk2(a[8 * pr + 2] * sc, a[8 * pr + 3] * sc), pk2(a[8 * pr + 6] * sc, a[8 * pr + 7] * sc), false, false);
                                if (gi != 2) { const u32x4 ov = {s0[0], s1[0], s0[1], s1[1]}; ST16(ov, hb0 + (tl >> 5) * 1024 + (gi > 2 ? gi - 3 : gi) * 512 + r * 16); }
                                else { const u32x2 o0 = {s0[0], s1[0]}, o1 = {s0[1], s1[1]}; ST8(o0, hb0 + 4096 + tl * 8); ST8(o1, hb0 + hs + 4096 + tl * 8); }
                            }
                        }
                    } else {
                        const int n = c0 + 32 * wn + r, hd = n / 20, d = n - hd * 20, rho = (hd & 1) ? (d < 4 ? d + 16 : d - 4) : d;
                        const unsigned base = WS_KV + (mat * NH + hd) * 163840 + cpos * 10240 + 5120 + h * 320 + rho * 16;
#pragma unroll
                        for (int i = 0; i < 2; ++i)
#pragma unroll
                            for (int s2 = 0; s2 < 2; ++s2) {
                                const f32x16& a = i ? acc1 : acc0;
                                u32x4 ov = {pk2(a[8 * s2], a[8 * s2 + 1]), pk2(a[8 * s2 + 2], a[8 * s2 + 3]), pk2(a[8 * s2 + 4], a[8 * s2 + 5]), pk2(a[8 * s2 + 6], a[8 * s2 + 7])};
                                ST16(ov, base + (wm * 4 + i * 2 + s2) * 640);
                            }
                    }
                    if (mat == 0) STAMP(1);
                }
            };
            if (part < 2) gbody(std::true_type{}); else gbody(std::false_type{});
        } else {
            const int sj = bid - 240;
            WAIT_BAR(63);
            {
                const u32x4 ga = *(const u32x4*)(P.gw + lane * 8), gb = *(const u32x4*)(P.gw + 512 + (lane & 15) * 8);
                u32x4 xa[16], xb[4];
                const u16* xr = P.x + (size_t)(sj * 128 + w * 16) * DM;
#pragma unroll
                for (int rr = 0; rr < 16; ++rr) xa[rr] = *(const u32x4*)(xr + rr * DM + lane * 8);
#pragma unroll
                for (int i = 0; i < 4; ++i) xb[i] = *(const u32x4*)(xr + (i * 4 + (lane >> 4)) * DM + 512 + (lane & 15) * 8);
                float res = 0.f;
#pragma unroll
                for (int rr = 0; rr < 16; ++rr) {
                    float s = 0.f, s2 = 0.f;
#pragma unroll
                    for (int j = 0; j < 4; ++j) { s += bf_lo(xa[rr][j]) * bf_lo(ga[j]) + bf_hi(xa[rr][j]) * bf_hi(ga[j]); s2 += bf_lo(xb[rr >> 2][j]) * bf_lo(gb[j]) + bf_hi(xb[rr >> 2][j]) * bf_hi(gb[j]); }
                    if ((lane >> 4) == (rr & 3)) s += s2;
#pragma unroll
                    for (int o = 32; o >= 1; o >>= 1) s += __shfl_xor(s, o);
                    if (lane == rr) res = s;
                }
                if (lane < 16) ST4(__float_as_uint(1.f / (1.f + __expf(-res))), WS_G + (sj * 128 + w * 16 + lane) * 4);
            }
            DRAIN();
            unsigned arr = 0;
            if (lane == 0) arr = LDS_CNT_ADD(G_ST);
            if (__builtin_amdgcn_readfirstlane(arr) == 7 && lane == 0) set_flag(ws, WS_F_SD + sj * 8, tag);
        }
        STAMP(2);
        const unsigned hb = lds0 + kh * 10240;
        const unsigned laneK = hb + lane * 16, laneT = hb + 4096 + r * 8 + h * 4, laneV = hb + 5120 + h * 320 + (r < 20 ? r : 19) * 16;
        const u64 vmask = 0x000fffff000fffffull;
        u32x4 v0a = {one, one, one, one}, v0b = v0a, v1 = v0a;
        u32x4 qA, qB, ka; unsigned qtA, qtB, kt;
        {
            lds_wait(lds + G_Q, 4);
            LDS3 const char* qp = lds + L_Q + (qs >> 1) * 5120;
            const unsigned om = (qs & 1) * 2048 + lane * 16, ot = 4096 + ((qs & 1) * 64 + r) * 8 + 4 * h;
            qA = *(LDS3 const u32x4*)(qp + om); qB = *(LDS3 const u32x4*)(qp + om + 1024);
            qtA = *(LDS3 const unsigned*)(qp + ot); qtB = *(LDS3 const unsigned*)(qp + ot + 256);
            DSR128(ka, laneK, 0); DSR32(kt, laneT, 0); DSR128M2(v0a, laneV, 0, v1, laneV, 640);
            asm volatile("s_waitcnt lgkmcnt(0)" : "+v"(ka), "+v"(kt), "+v"(v0a), "+v"(v1), "+v"(qA), "+v"(qB), "+v"(qtA), "+v"(qtB) :: "memory");
            if (lane == 0) LDS_CNT_ADD(G_RD);
        }
        STAMP(5);
        float mA, mB; u32x2 qTA, qTB; f32x16 sA, sB;
        {
            const u32x2 t0 = {kt, 0x3f80u}, a0 = {qtA, 0u}, b0 = {qtB, 0u};
            const f32x16 s = MFMA32_16(ka, qA, z16);
            const f32x16 s0 = MFMA32_8(t0, a0, s);
            f32x16 s1 = MFMA32_16(ka, qB, z16); s1 = MFMA32_8(t0, b0, s1);
            float x0 = s0[0], x1 = s1[0];
#pragma unroll
            for (int i = 1; i < 16; ++i) { x0 = fmaxf(x0, s0[i]); x1 = fmaxf(x1, s1[i]); }
            x0 = -xhalf_max(x0); x1 = -xhalf_max(x1);
            const unsigned hh = pk2(x0, x1);
            const unsigned ll = pk2(x0 - bf_lo(hh), x1 - bf_hi(hh));
            mA = -(bf_lo(hh) + bf_lo(ll)); mB = -(bf_hi(hh) + bf_hi(ll));
            const unsigned sel = h ? ll : hh;
            qTA = (u32x2){qtA, sel & 0xffffu}; qTB = (u32x2){qtB, sel >> 16};
            sA = MFMA32_8(t0, qTA, s);
        }
        f32x16 oA = {}, oB = {};
        u32x4 pa, pb = {0u, 0u, 0u, 0u};
#define EX4(S, i, Pv, j) do { Pv[j] = pk2(ex2(S[i]), ex2(S[i + 1])); Pv[j + 1] = pk2(ex2(S[i + 2]), ex2(S[i + 3])); PIN(Pv); SBAR(); } while (0)
#define KT2 ((u32x2){kt, 0x3f80u})
#define TILE(V0C, V0N, aK1, oK1, aT1, oT1, oV1, aV0, oV0) do { \
            sB = MFMA32_16(ka, qB, z16); SBAR(); DSR128(ka, aK1, oK1); EX4(sA, 0, pa, 0); \
            oB = MFMA32_16(v1, pb, oB); SBAR(); DSR128M2(v1, aV, oV1, V0N, aV0, oV0); EX4(sA, 4, pa, 2); \
            sB = MFMA32_8(KT2, qTB, sB); SBAR(); DSR32(kt, aT1, oT1); EX4(sA, 8, pb, 0); \
            oA = MFMA32_16(V0C, pa, oA); SBAR(); EX4(sA, 12, pb, 2); \
            LWAIT(1); sA = MFMA32_16(ka, qA, z16); SBAR(); EX4(sB, 0, pa, 0); \
            oA = MFMA32_16(v1, pb, oA); SBAR(); EX4(sB, 4, pa, 2); \
            LWAIT(0); sA = MFMA32_8(KT2, qTA, sA); SBAR(); EX4(sB, 8, pb, 0); \
            oB = MFMA32_16(V0C, pa, oB); SBAR(); EX4(sB, 12, pb, 2); \
        } while (0)
        unsigned aK = laneK, aT = laneT, aV = laneV;
#pragma unroll 1
        for (int c = 0; c < 8; ++c) {
            if (c == 1) lds_wait(lds + C_KVL, 4);
            if (c >= 1) {
                unsigned pp;
                asm volatile("ds_write_b32 %1, %2\n\tds_read_b32 %0, %3\n\ts_waitcnt lgkmcnt(0)" : "=&v"(pp) : "v"(lds0 + C_PR + w * 4), "v"(c), "v"(lds0 + C_PR + (w ^ 4) * 4) : "memory");
                const int pc = __builtin_amdgcn_readfirstlane(pp);
                if (pc > c || (pc == c && ((c ^ kh) & 1))) __builtin_amdgcn_s_setprio(1); else __builtin_amdgcn_s_setprio(0);
            } else if (kh) __builtin_amdgcn_s_setprio(1);
            const unsigned nx = (c < 7 ? c + 1 : 7) * 20480;
            const unsigned nK = laneK + nx, nT = laneT + nx, nV = laneV + nx;
            SBAR();
            TILE(v0a, v0b, aK, 1024, aT, 256, 640,  aV, 1280);
            TILE(v0b, v0a, aK, 2048, aT, 512, 1920, aV, 2560);
            if (c == 0) {
                DRAIN();
                lds_wait(lds + G_Z, 1);
                unsigned arr = 0;
                if (lane == 0) { arr = LDS_CNT_ADD(C_GB); LDS_CNT_ADD(G_ST2); }
                if (__builtin_amdgcn_readfirstlane(arr) == 7 && lane == 0 && bid < 240) set_flag(ws, fG + 2048, tag);
                asm volatile("s_waitcnt lgkmcnt(0)" ::: "memory");
                SBAR();
            }
            TILE(v0a, v0b, aK, 3072, aT, 768, 3200, aV, 3840);
            TILE(v0b, v0a, nK, 0,    nT, 0,   4480, nV, 0);
            aK = nK; aT = nT; aV = nV;
        }
        oB = MFMA32_16(v1, pb, oB);
#undef TILE
#undef EX4
        if (lane == 0) __hip_atomic_store((LDS3 unsigned*)(lds + C_PR + w * 4), 8u, __ATOMIC_RELAXED, __HIP_MEMORY_SCOPE_WORKGROUP);
        STAMP(6);
        {
            float chk = 0.f;
#pragma unroll
            for (int i = 0; i < 12; ++i) chk += fabsf(oA[i]) * 0.f + fabsf(oB[i]) * 0.f;
            bool bad = !(chk == 0.f);
            if (h) bad = bad || !(oA[8] >= 0x1p-60f && oA[8] <= 0x1p60f && oB[8] >= 0x1p-60f && oB[8] <= 0x1p60f);
            if (__builtin_expect(__any(bad), 0)) {
                mA = -INFINITY; mB = -INFINITY;
#pragma unroll
                for (int i = 0; i < 16; ++i) { oA[i] = 0.f; oB[i] = 0.f; }
                const u32x2 a0 = {qtA, 0u}, b0 = {qtB, 0u};
#pragma unroll 1
                for (int t = 0; t < 32; ++t) {
                    const unsigned kb = WS_KV + head * 163840 + (2 * (t >> 2) + kh) * 10240, t4 = t & 3;
                    const u32x4 k0 = LD16(kb + t4 * 1024 + lane * 16);
                    const u32x2 t0 = {LD4(kb + 4096 + (t4 * 32 + r) * 8 + 4 * h), 0u};
                    u32x4 w0 = LD16(kb + 5120 + t4 * 1280 + h * 320 + (r < 20 ? r : 19) * 16), w1 = LD16(kb + 5120 + t4 * 1280 + 640 + h * 320 + (r < 20 ? r : 19) * 16);
                    if (r >= 20) { w0 = (u32x4){one, one, one, one}; w1 = w0; }
                    f32x16 s0 = MFMA32_16(k0, qA, z16); s0 = MFMA32_8(t0, a0, s0);
                    f32x16 s1 = MFMA32_16(k0, qB, z16); s1 = MFMA32_8(t0, b0, s1);
                    float x0 = s0[0], x1 = s1[0];
#pragma unroll
                    for (int i = 1; i < 16; ++i) { x0 = fmaxf(x0, s0[i]); x1 = fmaxf(x1, s1[i]); }
                    x0 = fmaxf(xhalf_max(x0), mA); x1 = fmaxf(xhalf_max(x1), mB);
                    const float g0 = (mA == x0) ? 1.f : ex2(mA - x0), g1 = (mB == x1) ? 1.f : ex2(mB - x1);
                    mA = x0; mB = x1;
#pragma unroll
                    for (int i = 0; i < 16; ++i) { oA[i] *= g0; oB[i] *= g1; }
#pragma unroll
                    for (int i = 0; i < 4; ++i) { pa[i] = pk2(ex2(s0[2 * i] - x0), ex2(s0[2 * i + 1] - x0)); pb[i] = pk2(ex2(s0[8 + 2 * i] - x0), ex2(s0[9 + 2 * i] - x0)); }
                    oA = MFMA32_16(w0, pa, oA); oA = MFMA32_16(w1, pb, oA);
#pragma unroll
                    for (int i = 0; i < 4; ++i) { pa[i] = pk2(ex2(s1[2 * i] - x1), ex2(s1[2 * i + 1] - x1)); pb[i] = pk2(ex2(s1[8 + 2 * i] - x1), ex2(s1[9 + 2 * i] - x1)); }
                    oB = MFMA32_16(w0, pa, oB); oB = MFMA32_16(w1, pb, oB);
                }
            }
        }
        lds_wait<4>(lds + C_BX, 8);
        const float gO = *(LDS3 const float*)(lds + X_G + (qs * 64 + kh * 32 + r) * 4);
        {
            LDS3 char* xp = lds + X_MRG + w * 2816;
            const f32x16& s = kh ? oA : oB;
            *(LDS3 f32x4*)(xp + lane * 16) = (f32x4){s[0], s[1], s[2], s[3]};
            *(LDS3 f32x4*)(xp + 1024 + lane * 16) = (f32x4){s[4], s[5], s[6], s[7]};
            if (!h) *(LDS3 f32x4*)(xp + 2048 + r * 16) = (f32x4){s[8], s[9], s[10], s[11]};
            else *(LDS3 f32x2*)(xp + 2560 + r * 8) = (f32x2){s[8], kh ? mA : mB};
            asm volatile("s_waitcnt lgkmcnt(0)" ::: "memory");
            if (lane == 0) __hip_atomic_store((LDS3 unsigned*)(lds + C_MRG + w * 4), 1u, __ATOMIC_RELAXED, __HIP_MEMORY_SCOPE_WORKGROUP);
        }
        lds_wait<2>(lds + C_MRG + (w ^ 4) * 4, 1);
        bool lastw;
        {
            LDS3 const char* xp = lds + X_MRG + (w ^ 4) * 2816;
            const f32x4 p0 = *(LDS3 const f32x4*)(xp + lane * 16), p1 = *(LDS3 const f32x4*)(xp + 1024 + lane * 16), p2 = *(LDS3 const f32x4*)(xp + 2048 + r * 16);
            const f32x2 lm = *(LDS3 const f32x2*)(xp + 2560 + r * 8);
            const f32x16& s = kh ? oB : oA;
            const float mo = kh ? mB : mA, mp = lm[1];
            const float mx = fmaxf(mo, mp);
            const float fo = (mo == mx) ? 1.f : ex2(mo - mx), fq = (mp == mx) ? 1.f : ex2(mp - mx);
            float o[12];
#pragma unroll
            for (int i = 0; i < 4; ++i) { o[i] = s[i] * fo + p0[i] * fq; o[4 + i] = s[4 + i] * fo + p1[i] * fq; o[8 + i] = s[8 + i] * fo + p2[i] * fq; }
            const float l = upper_half(s[8]) * fo + lm[0] * fq;
            store_o(rs, o, gO / l, q0 + kh * 32 + r, head * 20, h);
            DRAIN();
            unsigned arr = 0;
            if (lane == 0) arr = LDS_CNT_ADD(C_OA);
            lastw = __builtin_amdgcn_readfirstlane(arr) == 7;
            if (lastw && lane == 0) set_flag(ws, WS_F_OA + (qb * 32 + head) * 8, tag);
        }
        STAMP(7);
        {
            const int c16 = lane & 15, g = lane >> 4, tt = w & 3, k0 = kh * 10;
            POLL_ONE(lastw, 4, WS_F_OA + ((mtO >> 2) * 32 + r) * 8, WS_F_OA + ((mtO >> 2) * 32 + r) * 8, C_P4);
            STAMP(9);
            const unsigned ab = WS_AO + ((mtO * 4 + tt) * 40 + k0) * 1024 + lane * 16;
            u32x4 af[10];
#pragma unroll
            for (int ks = 0; ks < 10; ++ks) af[ks] = LD16(ab + ks * 1024);
            lds_wait(lds + C_WA, 4);
            lds_wait(lds + C_OA, 8);
            f32x4 c[5];
#pragma unroll
            for (int n = 0; n < 5; ++n) c[n] = (f32x4){0.f, 0.f, 0.f, 0.f};
#pragma unroll
            for (int ks = 0; ks < 10; ++ks)
#pragma unroll
                for (int n = 0; n < 5; ++n) c[n] = MFMA16_32(*(LDS3 const u32x4*)(lds + L_WA + (n * 20 + k0 + ks) * 1024 + lane * 16), af[ks], c[n]);
            LDS3 char* xp = lds + X_MRG + tt * 5120 + lane * 16;
            if (w < 4) { *(LDS3 f32x4*)(xp + 3072) = c[3]; *(LDS3 f32x4*)(xp + 4096) = c[4]; }
            else { *(LDS3 f32x4*)(xp) = c[0]; *(LDS3 f32x4*)(xp + 1024) = c[1]; *(LDS3 f32x4*)(xp + 2048) = c[2]; }
            asm volatile("s_waitcnt lgkmcnt(0)" ::: "memory");
            if (lane == 0) __hip_atomic_store((LDS3 unsigned*)(lds + C_FIN + w * 4), 1u, __ATOMIC_RELAXED, __HIP_MEMORY_SCOPE_WORKGROUP);
            lds_wait(lds + C_FIN + (w ^ 4) * 4, 1);
            lds_wait(lds + C_BP + tt * 4, 1);
            u16* op = P.out + (size_t)(mtO * 64 + tt * 16 + c16) * DM + ntO * 80 + g * 4;
#pragma unroll
            for (int n = 0; n < 5; ++n) {
                if ((w < 4) == (n < 3)) {
                    const f32x4 bp = *(LDS3 const f32x4*)(n < 3 ? lds + X_FIN + tt * 3072 + n * 1024 + lane * 16 : lds + 143360 + tt * 2048 + (n - 3) * 1024 + lane * 16);
                    const f32x4 s = c[n] + *(LDS3 const f32x4*)(xp + n * 1024) + bp;
                    *(u32x2*)(op + n * 16) = (u32x2){pk2(s[0], s[1]), pk2(s[2], s[3])};
                }
            }
            if (bid == 0 && mtid == 0) set_flag(ws, WS_EPOCH, tag);
        }
        STAMP(10);
        asm volatile("mega_code_end:");
    }
}

extern "C" void kernel_launch(void* const* d_in, const int* in_sizes, int n_in, void* d_out, int out_size, void* d_ws, size_t ws_size,
                              hipStream_t stream) {
    Params P;
    P.x = (const u16*)d_in[0]; P.wa = (const u16*)d_in[1]; P.woa = (const u16*)d_in[2]; P.wb = (const u16*)d_in[4];
    P.wphi = (const u16*)d_in[5]; P.wob = (const u16*)d_in[6]; P.gw = (const u16*)d_in[7]; P.ws = (char*)d_ws; P.out = (u16*)d_out;
    mega<<<256, 768, 0, stream>>>(P);
}
```

```cpp
#include <hip/hip_runtime.h>
#include <hip/hip_bf16.h>
#include <stdint.h>
#include <type_traits>

typedef __attribute__((ext_vector_type(8))) short bf16x8;
typedef __attribute__((ext_vector_type(4))) short bf16x4;
typedef __attribute__((ext_vector_type(16))) float f32x16;
typedef __attribute__((ext_vector_type(4))) float f32x4;
typedef __attribute__((ext_vector_type(2))) float f32x2;
typedef __attribute__((ext_vector_type(4))) unsigned u32x4;
typedef __attribute__((ext_vector_type(2))) unsigned u32x2;
typedef __attribute__((ext_vector_type(2))) __bf16 bf2_t;
typedef unsigned short u16;
typedef unsigned long long u64;
#define LDS3 __attribute__((address_space(3)))
typedef __attribute__((address_space(1))) u64 gu64;
#define RLX_AGENT __ATOMIC_RELAXED, __HIP_MEMORY_SCOPE_AGENT

#define PRIO_MODE 3
#define WARM_MODE 1
#define GA_PUB 12
#define PHI_ROUND 0
#define PRE_SLEEP 2
constexpr int SEQ = 2048, DM = 640, NH = 32;
constexpr float LOG2E = 1.4426950408889634f;
constexpr float QSCALE = 0.22360679774997896f * LOG2E;

constexpr unsigned WS_EPOCH = 0;
constexpr unsigned WS_F_G   = 256;
constexpr unsigned WS_F_SD  = WS_F_G + 4096;
constexpr unsigned WS_F_KV  = WS_F_SD + 256;
constexpr unsigned WS_F_OB  = WS_F_KV + 2048;
constexpr unsigned WS_F_OA  = WS_F_OB + 2048;
constexpr unsigned WS_G     = 16384;
constexpr unsigned WS_Q     = 32768;
constexpr unsigned WS_KV    = WS_Q + 2 * NH * 81920;
constexpr unsigned WS_KVP   = WS_KV + 2 * NH * 163840;
constexpr unsigned WS_AO    = WS_KVP + NH * 8 * 4096;
constexpr unsigned WS_WT    = WS_AO + SEQ * 1280 * 2;
constexpr unsigned WS_END   = WS_WT + 1280 * 640 * 2;

#define STAMP(i)

__device__ __forceinline__ unsigned pk2(float a, float b) { f32x2 v = {a, b}; bf2_t r = __builtin_convertvector(v, bf2_t); return __builtin_bit_cast(unsigned, r); }
__device__ __forceinline__ float bf_lo(unsigned u) { return __uint_as_float(u << 16); }
__device__ __forceinline__ float bf_hi(unsigned u) { return __uint_as_float(u & 0xffff0000u); }
__device__ __forceinline__ float ex2(float x) { return __builtin_amdgcn_exp2f(x); }
#define MFMA32_16(a, b, c) __builtin_amdgcn_mfma_f32_32x32x16_bf16(__builtin_bit_cast(bf16x8, a), __builtin_bit_cast(bf16x8, b), c, 0, 0, 0)
#define MFMA32_8(a, b, c) __builtin_amdgcn_mfma_f32_32x32x8bf16_1k(__builtin_bit_cast(bf16x4, a), __builtin_bit_cast(bf16x4, b), c, 0, 0, 0)
#define MFMA16_32(a, b, c) __builtin_amdgcn_mfma_f32_16x16x32_bf16(__builtin_bit_cast(bf16x8, a), __builtin_bit_cast(bf16x8, b), c, 0, 0, 0)

__device__ __forceinline__ void glds16(unsigned voff, const void* sbase, unsigned lds_base) {
    unsigned sv;
    asm volatile("s_mov_b32 %0, m0\n\ts_mov_b32 m0, %3\n\ts_nop 0\n\tglobal_load_lds_dwordx4 %1, %2\n\ts_mov_b32 m0, %0"
                 : "=&s"(sv) : "v"(voff), "s"(sbase), "s"(__builtin_amdgcn_readfirstlane(lds_base)) : "memory");
}
__device__ __forceinline__ void glds16s(unsigned voff, const void* sbase, unsigned lds_base) {
    unsigned sv;
    asm volatile("s_mov_b32 %0, m0\n\ts_mov_b32 m0, %3\n\ts_nop 0\n\tglobal_load_lds_dwordx4 %1, %2 sc1\n\ts_mov_b32 m0, %0"
                 : "=&s"(sv) : "v"(voff), "s"(sbase), "s"(__builtin_amdgcn_readfirstlane(lds_base)) : "memory");
}
__device__ __forceinline__ void glds4s(unsigned voff, const void* sbase, unsigned lds_base) {
    unsigned sv;
    asm volatile("s_mov_b32 %0, m0\n\ts_mov_b32 m0, %3\n\ts_nop 0\n\tglobal_load_lds_dword %1, %2 sc1\n\ts_mov_b32 m0, %0"
                 : "=&s"(sv) : "v"(voff), "s"(sbase), "s"(__builtin_amdgcn_readfirstlane(lds_base)) : "memory");
}
typedef short v4i16_t __attribute__((ext_vector_type(4)));
__device__ __forceinline__ u32x2 trread(LDS3 const char* p) {
    return __builtin_bit_cast(u32x2, __builtin_amdgcn_ds_read_tr16_b64_v4i16((LDS3 v4i16_t*)p));
}
#define WAIT_BAR(N) asm volatile("s_waitcnt vmcnt(" #N ") lgkmcnt(0)\n\ts_barrier" ::: "memory")
#define DRAIN() asm volatile("s_waitcnt vmcnt(0)" ::: "memory")
#define SBAR() __builtin_amdgcn_sched_barrier(0)
#define PIN(x) asm volatile("" : "+v"(x))

__device__ __forceinline__ float xhalf_max(float x) {
    auto r = __builtin_amdgcn_permlane32_swap(__float_as_uint(x), __float_as_uint(x), false, false);
    return fmaxf(__uint_as_float(r[0]), __uint_as_float(r[1]));
}
__device__ __forceinline__ float xhalf_sum(float x) {
    auto r = __builtin_amdgcn_permlane32_swap(__float_as_uint(x), __float_as_uint(x), false, false);
    return __uint_as_float(r[0]) + __uint_as_float(r[1]);
}
__device__ __forceinline__ float upper_half(float x) {
    auto r = __builtin_amdgcn_permlane32_swap(__float_as_uint(x), __float_as_uint(x), false, false);
    return __uint_as_float(r[1]);
}

typedef __amdgpu_buffer_rsrc_t rsrc_t;
#define ST8(v, off)  __builtin_amdgcn_raw_buffer_store_b64(v, rs, (int)(off), 0, 16)
#define ST16(v, off) __builtin_amdgcn_raw_buffer_store_b128(v, rs, (int)(off), 0, 16)
#define ST4(v, off)  __builtin_amdgcn_raw_buffer_store_b32(v, rs, (int)(off), 0, 16)
#define LD16(off) __builtin_amdgcn_raw_buffer_load_b128(rs, (int)(off), 0, 16)
#define LD4(off)  __builtin_amdgcn_raw_buffer_load_b32(rs, (int)(off), 0, 16)

template <int SLP = 2>
__device__ __forceinline__ void wait_flags(const char* ws, unsigned off0, unsigned off1, u64 tag) {
    gu64* p0 = (gu64*)(ws + off0); gu64* p1 = (gu64*)(ws + off1);
#pragma unroll 1
    for (unsigned spins = 0; spins < (1u << 16); ++spins) {
        const u64 a = __hip_atomic_load(p0, RLX_AGENT), b = __hip_atomic_load(p1, RLX_AGENT);
        if (__all(a == tag && b == tag)) break;
        __builtin_amdgcn_s_sleep(SLP);
    }
    __builtin_amdgcn_fence(__ATOMIC_ACQUIRE, "wavefront");
}
__device__ __forceinline__ void set_flag(char* ws, unsigned off, u64 tag) { __hip_atomic_store((gu64*)(ws + off), tag, RLX_AGENT); }

struct Params { const u16* x; const u16* wa; const u16* wb; const u16* wphi; const u16* woa; const u16* wob; const u16* gw; char* ws; u16* out; };

__device__ __forceinline__ void feature_map(const u32x4& pm, unsigned pt, const u32x4& tm, unsigned tt, int h, u32x4& fp, u32x4& fn, u32x4& ft) {
    const f32x16 z16 = {};
    const u32x2 a8 = {pt, 0u}, b8 = {tt, 0u};
    f32x16 s = MFMA32_16(pm, tm, z16); s = MFMA32_8(a8, b8, s);
    f32x2 v[6];
#pragma unroll
    for (int i = 0; i < 6; ++i) {
        v[i] = (f32x2){s[2 * i], s[2 * i + 1]};
    }
    float mx = fmaxf(v[0][0], v[0][1]), mn = fminf(v[0][0], v[0][1]);
#pragma unroll
    for (int i = 1; i < 6; ++i) { mx = fmaxf(fmaxf(mx, v[i][0]), v[i][1]); mn = fminf(fminf(mn, v[i][0]), v[i][1]); }
    mx = xhalf_max(mx) * -LOG2E; mn = xhalf_max(-mn) * -LOG2E;
    f32x2 ep[6], en[6];
#pragma unroll
    for (int i = 0; i < 6; ++i) {
        const f32x2 a = v[i] * LOG2E + mx, b = v[i] * -LOG2E + mn;
        ep[i] = (f32x2){ex2(a[0]), ex2(a[1])}; en[i] = (f32x2){ex2(b[0]), ex2(b[1])};
    }
    const float tw = h ? 0.f : 1.f;
    const f32x2 s2p = ((ep[0] + ep[1]) + (ep[2] + ep[3])) + (ep[4] + ep[5]) * tw, s2n = ((en[0] + en[1]) + (en[2] + en[3])) + (en[4] + en[5]) * tw;
    const float sp = __builtin_amdgcn_rcpf(xhalf_sum(s2p[0] + s2p[1])), sn = __builtin_amdgcn_rcpf(xhalf_sum(s2n[0] + s2n[1]));
    const float tp = sp * tw, tn = sn * tw;
#pragma unroll
    for (int i = 0; i < 4; ++i) { const f32x2 a = ep[i] * sp, b = en[i] * sn; fp[i] = pk2(a[0], a[1]); fn[i] = pk2(b[0], b[1]); }
    const f32x2 c0 = ep[4] * tp, c1 = ep[5] * tp, c2 = en[4] * tn, c3 = en[5] * tn;
    ft = (u32x4){pk2(c0[0], c0[1]), pk2(c1[0], c1[1]), pk2(c2[0], c2[1]), pk2(c3[0], c3[1])};
}

__device__ __forceinline__ void store_o(rsrc_t rs, const float (&o)[12], float sc, int tok, int kbase, int h) {
#pragma unroll
    for (int gq = 0; gq < 3; ++gq) {
        const int d = 8 * gq + 4 * h;
        if (d < 20) {
            const int k = kbase + ((kbase & 4) ? (d == 16 ? 0 : d + 4) : d);
            u32x2 ov = {pk2(o[4 * gq] * sc, o[4 * gq + 1] * sc), pk2(o[4 * gq + 2] * sc, o[4 * gq + 3] * sc)};
            ST8(ov, WS_AO + (((tok >> 4) * 40 + (k >> 5)) * 64 + ((k >> 3) & 3) * 16 + (tok & 15)) * 16 + (k & 7) * 2);
        }
    }
}

#define DSR128(dst, addr, off) asm volatile("ds_read_b128 %0, %1 offset:%2" : "=v"(dst) : "v"(addr), "i"(off))
#define DSR32(dst, addr, off) asm volatile("ds_read_b32 %0, %1 offset:%2" : "=v"(dst) : "v"(addr), "i"(off))
#define DSR128M2(d0, a0, o0, d1, a1, o1) asm volatile("s_mov_b64 exec, %6\n\tds_read_b128 %0, %2 offset:%3\n\tds_read_b128 %1, %4 offset:%5\n\ts_mov_b64 exec, -1" \
                                                      : "+v"(d0), "+v"(d1) : "v"(a0), "i"(o0), "v"(a1), "i"(o1), "s"(vmask))
#define LWAIT(n) do { asm volatile("s_waitcnt lgkmcnt(" #n ")" ::: "memory"); SBAR(); } while (0)

#define BAR() asm volatile("s_barrier" ::: "memory")
#define LDS_SET(off, v) __hip_atomic_store((LDS3 unsigned*)(lds + (off)), (unsigned)(v), __ATOMIC_RELAXED, __HIP_MEMORY_SCOPE_WORKGROUP)
#define LDS_CNT_ADD(off) __hip_atomic_fetch_add((LDS3 unsigned*)(lds + (off)), 1u, __ATOMIC_RELAXED, __HIP_MEMORY_SCOPE_WORKGROUP)

template <int SLP = 1>
__device__ __forceinline__ void lds_wait(LDS3 char* p, unsigned want) {
#pragma unroll 1
    for (unsigned spins = 0; spins < (1u << 20); ++spins) {
        if ((unsigned)__builtin_amdgcn_readfirstlane(__hip_atomic_load((LDS3 unsigned*)p, __ATOMIC_RELAXED, __HIP_MEMORY_SCOPE_WORKGROUP)) >= want) break;
        __builtin_amdgcn_s_sleep(SLP);
    }
    __builtin_amdgcn_fence(__ATOMIC_ACQUIRE, "wavefront");
}

template <int SLP>
__device__ __forceinline__ void prog_wait(LDS3 char* p, int lane, unsigned want) {
#pragma unroll 1
    for (unsigned spins = 0; spins < (1u << 20); ++spins) {
        if (__all(__hip_atomic_load((LDS3 unsigned*)p + (lane & 7), __ATOMIC_RELAXED, __HIP_MEMORY_SCOPE_WORKGROUP) >= want)) break;
        __builtin_amdgcn_s_sleep(SLP);
    }
    __builtin_amdgcn_fence(__ATOMIC_ACQUIRE, "wavefront");
}
#define POLL_ONE(cond, SLP, o0, o1, word) do { if (cond) { wait_flags<SLP>(ws, o0, o1, tag); \
        if (lane == 0) __hip_atomic_store((LDS3 unsigned*)(lds + (word)), 1u, __ATOMIC_RELAXED, __HIP_MEMORY_SCOPE_WORKGROUP); } else lds_wait<SLP>(lds + (word), 1); } while (0)
constexpr int S_AREA = 131072, S_STR = 3840, G_W = S_AREA + 8 * S_STR, L_PHI = G_W + 1024;
constexpr int G_PRG = G_W  , G_Q = G_W + 32, G_P = G_W + 36, G_ST = G_W + 40, G_RD = G_W + 44, G_Z = G_W + 48, G_ST2 = G_W + 52, G_ST3 = G_W + 56,
              G_P1 = G_W + 60, G_B1 = G_W + 64, G_KV = G_W + 68, G_KVP = G_W + 72, G_WT = G_W + 76;
constexpr int C_KVF = 4, C_OB = 8, C_WA = 16, C_OA = 20, C_BX = 28, C_MRG = 32  , C_FIN = 64  , C_BP = 96  ;
constexpr int C_P2 = 116, C_P3 = 120, C_P4 = 124, C_PR = 128  , C_KVL = 160, C_P5 = 164, C_KVL2 = 168;
constexpr int X_G = 256, X_KVF = 1280;
constexpr int X_MRG = 1280;
constexpr int L_WA = 24576;
constexpr int X_FIN = 126976;

__global__ __launch_bounds__(768) void mega(Params P) {
    __shared__ __attribute__((aligned(16))) char smem[163840];
    LDS3 char* lds = (LDS3 char*)smem;
    char* ws = P.ws;
    const int tid = threadIdx.x, lane = tid & 63, r = lane & 31, h = lane >> 5;
    const int hw = __builtin_amdgcn_readfirstlane(tid >> 6);
    const bool helper = hw < 4;
    const int w = hw - 4, mtid = tid - 256;
    const int bid = blockIdx.x;
    const unsigned lds0 = (unsigned)(uintptr_t)smem;
    const rsrc_t rs = __builtin_amdgcn_make_buffer_rsrc(ws, 0, WS_END, 0x00020000);
    STAMP(0);
    u64 tag = 0;
#define GET_TAG() do { asm volatile("s_load_dwordx2 %0, %1, 0x0\n\ts_waitcnt lgkmcnt(0)" : "=s"(tag) : "s"(ws) : "memory"); tag += 1; } while (0)
    const int head = (bid & 7) * 4 + ((bid >> 3) & 3), qb = bid >> 5;
    const int nlo = (head * 20) >> 7, nhi = (head * 20 + 19) >> 7;
    const int mtO = (bid & 7) * 4 + ((bid >> 3) & 3), ntO = bid >> 5;
    const int lin = (bid & 7) * 30 + (bid >> 3), lh = lin >= 120, lrem = lin - 120 * lh;
    const int mt = lh * 8 + (lrem & 7), nt = lrem >> 3;
    const unsigned fG = WS_F_G + (mt * 15 + nt) * 8;
    const unsigned one = (r == 20) ? 0x3f803f80u : 0u;
    const f32x16 z16 = {};
    auto phi_frag = [&](u32x4& phm, unsigned& pht) {
        LDS3 const u16* wp = (LDS3 const u16*)(lds + L_PHI) + (r < 20 ? r : (r & 3));
        unsigned e[10];
#pragma unroll
        for (int j = 0; j < 8; ++j) e[j] = wp[(8 * h + j + 4 * (head & 1)) * 20];
        e[8] = wp[((head & 1 ? 0 : 16) + 2 * h) * 20]; e[9] = wp[((head & 1 ? 1 : 17) + 2 * h) * 20];
        phm = (u32x4){e[0] | (e[1] << 16), e[2] | (e[3] << 16), e[4] | (e[5] << 16), e[6] | (e[7] << 16)}; pht = e[8] | (e[9] << 16);
    };

    if (helper) {
        const unsigned fA0 = WS_F_G + ((lane & 15) * 15 + (lane < 32 ? 5 : 10) + ((lane & 16) ? nhi : nlo)) * 8;
        const unsigned fA1 = WS_F_G + ((qb * 2 + (lane & 1)) * 15 + ((lane & 2) ? nhi : nlo)) * 8;
        __builtin_amdgcn_s_setprio(3);
        const char* kvb = ws + WS_KV + head * 163840;
        auto kv_issue = [&](int grp) {
#pragma unroll
            for (int t = 0; t < 8; ++t) { int pc = grp * 32 + hw + 4 * t; if (pc > 157) pc = 157; glds16s(lane * 16, kvb + pc * 1024, lds0 + pc * 1024); }
        };
        if (hw == 3) glds16((lane < 50 ? lane : 49) * 16, (const char*)P.wphi + head * 800, lds0 + L_PHI);
        if (bid < 240) {
            const int rot = (nt * 3 + mt * 5 + (mt >> 1)) % 10;
            const unsigned xo = (mt * 128 + 8 * hw + (lane >> 3)) * 1280 + (((lane & 7) ^ ((4 * hw + (lane >> 4)) & 7)) << 4);
            const unsigned wo = (4 * hw + (lane >> 4)) * 3840 + nt * 256 + (((lane & 15) ^ (((lane >> 4) << 2) | hw)) << 4);
            const unsigned dX = lds0 + hw * 1024;
            auto issue = [&](int s) {
                int kt = (s >= 10 ? 19 - s : s) + rot; if (kt >= 10) kt -= 10;
                const char* xb = (const char*)P.x + kt * 128;
                const char* wb = (const char*)(s >= 10 ? P.wa : P.wb) + (size_t)kt * (64 * 3840);
                const unsigned dw = dX + 16384 + (s & 3) * 32768, dx = dX + ((s >= 10 ? 19 - s : s) & 3) * 32768;
#pragma unroll
                for (int i = 0; i < 4; ++i) glds16(wo, wb + i * 61440, dw + i * 4096);
                if (s < 10 || s >= 14) {
#pragma unroll
                    for (int i = 0; i < 4; ++i) glds16(xo, xb + i * 40960, dx + i * 4096);
                }
            };
#pragma unroll 1
            for (int n = 0; n < 20; ++n) {
                if (n == 2) { if (hw == 0 && lane < 32) *(LDS3 unsigned*)(lds + G_W + lane * 4) = 0u; GET_TAG(); WAIT_BAR(8); }
                else if (n == 3 || (n >= 11 && n <= 15)) { WAIT_BAR(8); }
                else if (n >= 5) { WAIT_BAR(16); }
                issue(n);
            }
            WAIT_BAR(16);
            WAIT_BAR(0);
        } else {
            if (hw == 0 && lane < 32) *(LDS3 unsigned*)(lds + G_W + lane * 4) = lane < 8 ? 20u : 0u;
            GET_TAG();
            WAIT_BAR(0);
            if (hw == 0) {
#pragma unroll 1
                for (int t = 0; t < PRE_SLEEP; ++t) __builtin_amdgcn_s_sleep(127);
            }
        }
        const int tokS = qb * 256 + hw * 64, m7 = tokS >> 7, t4 = (tokS >> 5) & 3;
        {
            const unsigned pt3 = (lane >> 1) & 3;
            const unsigned tb = WS_F_G + 2048 + ((qb * 2 + ((lane >> 3) & 1)) * 15 + ((lane & 1) ? nhi : nlo) + (pt3 < 3 ? pt3 : 0) * 5) * 8;
            POLL_ONE(hw == 0, 2, tb, WS_F_SD + (qb * 2 + (lane & 1)) * 8, G_P1);
        }
        u32x4 qf[2][3]; float gS[2];
        {
            const unsigned om = t4 * 1024 + lane * 16, ot = 4096 + (t4 * 32 + r) * 8 + 4 * h, ov = 5120 + t4 * 1280 + h * 320 + (r < 20 ? r : 19) * 16;
            const char* kb = ws + WS_KV + (NH + head) * 163840 + (2 * (m7 & 7) + (m7 >> 3)) * 10240;
#pragma unroll
            for (int i = 0; i < 2; ++i) {
                const unsigned d = lds0 + S_AREA + (2 * hw + i) * S_STR;
                glds16s(om + i * 1024, kb, d); glds16s(ov + i * 1280, kb, d + 1024); glds16s(ov + i * 1280 + 640, kb, d + 2048); glds4s(ot + i * 256, kb, d + 3072);
            }
            const unsigned qbb = WS_Q + (NH + head) * 81920 + m7 * 5120;
            u32x4 eq[2]; unsigned eqt[2];
#pragma unroll
            for (int i = 0; i < 2; ++i) { eq[i] = LD16(qbb + om + i * 1024); eqt[i] = LD4(qbb + ot + i * 256); gS[i] = __uint_as_float(LD4(WS_G + (tokS + i * 32 + r) * 4)); }
            const bool cp = bid < 160 && tid < 160;
            const int k0 = cp ? bid * 8 : 0, n = cp ? tid * 4 : 0;
            const u16* wsrc = (k0 < 640 ? P.woa + (size_t)k0 * DM : P.wob + (size_t)(k0 - 640) * DM) + n;
            u32x2 f[8];
#pragma unroll
            for (int j = 0; j < 8; ++j) f[j] = *(const u32x2*)(wsrc + (size_t)j * DM);
            asm volatile("s_waitcnt vmcnt(14)" ::: "memory");
            if (lane == 0) LDS_CNT_ADD(G_B1);
            const unsigned dp = WS_WT + (((n >> 4) * 40 + (k0 >> 5)) * 64 + ((k0 >> 3) & 3) * 16 + (n & 15)) * 16;
            if (cp) {
#pragma unroll
                for (int q = 0; q < 2; ++q) {
                    const u32x4 lo = {(f[0][q] & 0xffffu) | (f[1][q] << 16), (f[2][q] & 0xffffu) | (f[3][q] << 16), (f[4][q] & 0xffffu) | (f[5][q] << 16), (f[6][q] & 0xffffu) | (f[7][q] << 16)};
                    const u32x4 hi = {(f[0][q] >> 16) | (f[1][q] & 0xffff0000u), (f[2][q] >> 16) | (f[3][q] & 0xffff0000u), (f[4][q] >> 16) | (f[5][q] & 0xffff0000u), (f[6][q] >> 16) | (f[7][q] & 0xffff0000u)};
                    ST16(lo, dp + q * 32); ST16(hi, dp + q * 32 + 16);
                }
            }
            u32x4 phm; unsigned pht;
            phi_frag(phm, pht);
#pragma unroll
            for (int i = 0; i < 2; ++i) feature_map(phm, pht, eq[i], eqt[i], h, qf[i][0], qf[i][1], qf[i][2]);
            DRAIN();
            if (lane == 0) LDS_CNT_ADD(G_WT);
        }
        STAMP(3);
        POLL_ONE(hw == 0, 2, fA0, fA1, G_P);
        prog_wait<1>(lds + G_PRG, lane, 20);
        kv_issue(0); kv_issue(1);
        asm volatile("s_waitcnt vmcnt(6)" ::: "memory");
        if (lane == 0) LDS_CNT_ADD(G_Q);
        kv_issue(2); kv_issue(3);
        lds_wait<2>(lds + G_RD, 8);
        if (hw == 0) {
            *(LDS3 u32x4*)(lds + lane * 16) = (u32x4){0u, 0u, 0u, 0u};
            asm volatile("s_waitcnt lgkmcnt(0)" ::: "memory");
            if (lane == 0) LDS_SET(G_Z, 1);
        }
        lds_wait<2>(lds + G_Z, 1);
        DRAIN();
        if (lane == 0) LDS_CNT_ADD(C_KVL);
        lds_wait<4>(lds + G_KVP, 1);
        kv_issue(4);
        if (lane == 0) LDS_CNT_ADD(G_ST2);
        if (hw == 3) {
            lds_wait<4>(lds + G_ST2, 12);
            glds16s(lane * 16, kvb + 158 * 1024, lds0 + 158 * 1024); glds16s(lane * 16, kvb + 159 * 1024, lds0 + 159 * 1024);
        }
        DRAIN();
        if (lane == 0) LDS_CNT_ADD(C_KVL2);
        prog_wait<8>(lds + C_PR, lane, 1);
        *(LDS3 float*)(lds + X_G + (hw * 64 + lane) * 4) = h ? gS[1] : gS[0];
        STAMP(4);
        POLL_ONE(hw == 0, 4, WS_F_KV + (head * 8 + (lane & 7)) * 8, WS_F_KV + (head * 8 + (lane & 7)) * 8, C_P2);
        {
            const int qd = hw * 60 + (lane < 60 ? lane : 59);
            f32x4 pp[8];
#pragma unroll
            for (int p = 0; p < 8; ++p) pp[p] = __builtin_bit_cast(f32x4, LD16(WS_KVP + (head * 8 + p) * 4096 + qd * 16));
            const f32x4 s4 = ((pp[0] + pp[1]) + (pp[2] + pp[3])) + ((pp[4] + pp[5]) + (pp[6] + pp[7]));
            const int gq = qd < 192 ? qd >> 6 : (qd - 192) >> 4, hs = qd < 192 ? (qd >> 5) & 1 : (qd >> 3) & 1, sg = qd < 192 ? qd & 31 : 32 + (qd & 7);
            const unsigned a = pk2(s4[0], s4[1]), b = pk2(s4[2], s4[3]);
            LDS3 u16* kp = (LDS3 u16*)(lds + X_KVF + (8 * gq + 4 * hs) * 96 + sg * 2);
            kp[0] = (u16)a; kp[48] = (u16)(a >> 16); kp[96] = (u16)b; kp[144] = (u16)(b >> 16);
            if (qd >= 192) { kp[8] = 0; kp[56] = 0; kp[104] = 0; kp[152] = 0; }
            asm volatile("s_waitcnt lgkmcnt(0)" ::: "memory");
            if (lane == 0) LDS_CNT_ADD(C_KVF);
            lds_wait<2>(lds + C_KVF, 4);
        }
        {
            LDS3 const char* kp = lds + X_KVF + (r < 24 ? r : 23) * 96 + h * 16;
            const u32x4 a0 = *(LDS3 const u32x4*)kp, a1 = *(LDS3 const u32x4*)(kp + 32), a2 = *(LDS3 const u32x4*)(kp + 64);
#pragma unroll
            for (int i = 0; i < 2; ++i) {
                f32x16 d = MFMA32_16(a0, qf[i][0], z16); d = MFMA32_16(a1, qf[i][1], d); d = MFMA32_16(a2, qf[i][2], d);
                const float den = upper_half(d[8]) + 1e-6f;
                float o[12];
#pragma unroll
                for (int j = 0; j < 12; ++j) o[j] = d[j];
                store_o(rs, o, (1.f - gS[i]) / den, tokS + i * 32 + r, 640 + head * 20, h);
            }
            DRAIN();
            unsigned arr = 0;
            if (lane == 0) arr = LDS_CNT_ADD(C_OB);
            if (__builtin_amdgcn_readfirstlane(arr) == 3 && lane == 0) set_flag(ws, WS_F_OB + (qb * 32 + head) * 8, tag);
        }
        STAMP(5);
        f32x4 c[5];
#pragma unroll
        for (int n = 0; n < 5; ++n) c[n] = (f32x4){0.f, 0.f, 0.f, 0.f};
        const char* wsrc = ws + WS_WT + (size_t)ntO * 5 * 40960;
        {
            if (hw == 0) wait_flags<8>(ws, WS_F_KV + lane * 8, WS_F_KV + 512 + lane * 8, tag);
            POLL_ONE(hw == 0, 8, WS_F_KV + 1024 + lane * 8, WS_F_KV + 1536 + lane * 8, C_P5);
#pragma unroll 1
            for (int t = 0; t < 5; ++t) {
                if (t == 0) prog_wait<8>(lds + C_PR, lane, 4); else if (t == 3) prog_wait<4>(lds + C_PR, lane, 5);
                const int ks = hw + 4 * t < 19 ? hw + 4 * t : 18;
#pragma unroll
                for (int n = 0; n < 5; ++n) glds16s(lane * 16, wsrc + (n * 40 + 20 + ks) * 1024, lds0 + 4096 + (ks * 5 + n) * 1024);
            }
            POLL_ONE(hw == 0, 4, WS_F_OB + ((mtO >> 2) * 32 + r) * 8, WS_F_OB + ((mtO >> 2) * 32 + r) * 8, C_P3);
            const unsigned ab = WS_AO + ((mtO * 4 + hw) * 40 + 20) * 1024 + lane * 16;
            u32x4 af[20];
#pragma unroll
            for (int ks = 0; ks < 20; ++ks) af[ks] = LD16(ab + ks * 1024);
            asm volatile("s_waitcnt vmcnt(20)" ::: "memory");
            if (lane == 0) LDS_CNT_ADD(C_BX);
            lds_wait<2>(lds + C_BX, 4);
            u32x4 wf[3][5], wl[5];
            LDS3 const char* wp = lds + 4096 + lane * 16;
#pragma unroll
            for (int ks = 0; ks < 2; ++ks)
#pragma unroll
                for (int n = 0; n < 5; ++n) wf[ks][n] = *(LDS3 const u32x4*)(wp + (ks * 5 + n) * 1024);
#pragma unroll
            for (int ks = 0; ks < 20; ++ks) {
                if (ks + 2 < 19) {
#pragma unroll
                    for (int n = 0; n < 5; ++n) wf[(ks + 2) % 3][n] = *(LDS3 const u32x4*)(wp + ((ks + 2) * 5 + n) * 1024);
                }
                if (ks == 8) {
#pragma unroll
                    for (int n = 0; n < 5; ++n) wl[n] = LD16(WS_WT + ((ntO * 5 + n) * 40 + 39) * 1024 + lane * 16);
                }
                SBAR();
#pragma unroll
                for (int n = 0; n < 5; ++n) c[n] = MFMA16_32(ks < 19 ? wf[ks % 3][n] : wl[n], af[ks], c[n]);
                SBAR();
            }
            if (lane == 0) LDS_CNT_ADD(C_BX);
            lds_wait<2>(lds + C_BX, 8);
        }
        STAMP(6);
        prog_wait<4>(lds + C_PR, lane, 7);
#pragma unroll 1
        for (int n = 0; n < 5; ++n)
#pragma unroll
            for (int t = 0; t < 5; ++t) { const int ks = hw + 4 * t; glds16s(lane * 16, wsrc + (n * 40 + ks) * 1024, lds0 + L_WA + (n * 20 + ks) * 1024); }
#pragma unroll
        for (int n = 0; n < 3; ++n) *(LDS3 f32x4*)(lds + X_FIN + hw * 3072 + n * 1024 + lane * 16) = c[n];
        DRAIN();
        if (lane == 0) LDS_CNT_ADD(C_WA);
        prog_wait<4>(lds + C_PR, lane, 8);
#pragma unroll
        for (int n = 3; n < 5; ++n) *(LDS3 f32x4*)(lds + 143360 + hw * 2048 + (n - 3) * 1024 + lane * 16) = c[n];
        asm volatile("s_waitcnt lgkmcnt(0)" ::: "memory");
        if (lane == 0) __hip_atomic_store((LDS3 unsigned*)(lds + C_BP + hw * 4), 1u, __ATOMIC_RELAXED, __HIP_MEMORY_SCOPE_WORKGROUP);
        STAMP(7);
        asm volatile("mega_code_end2:");
    } else {
        const int qs = w & 3, kh = w >> 2;
        const int q0 = qb * 256 + qs * 64;
        {
            unsigned sink;
            if (w == 0 && (WARM_MODE & 1)) {
                const char* pc0; int len, len2;
                asm volatile("s_getpc_b64 %0\nmega_code_mark:\n\ts_mov_b32 %1, mega_code_end-mega_code_mark\n\ts_mov_b32 %2, mega_code_end2-mega_code_mark" : "=s"(pc0), "=s"(len), "=s"(len2));
                len = len > len2 ? len : len2;
                const int o = ((bid >> 3) * 64 + lane) * 64;
                if (o + 4 <= len) asm volatile("global_load_dword %0, %1, %2" : "=v"(sink) : "v"(o), "s"(pc0) : "memory");
            } else if (w >= 1 && w <= 6 && (WARM_MODE & 2)) {
                const unsigned o = ((w - 1) * 64 + lane) * 65536 + 65532;
                if (o < WS_END) asm volatile("global_load_dword %0, %1, %2 sc1" : "=v"(sink) : "v"(o), "s"(ws) : "memory");
            } else if (w == 7 && (WARM_MODE & 2)) {
                if (lane < 40) asm volatile("global_load_dword %0, %1, %2 sc1" : "=v"(sink) : "v"(lane * 65536), "s"(P.out) : "memory");
            }
            asm volatile("s_waitcnt vmcnt(0)" ::: "memory");
        }
        GET_TAG();
        if (bid < 240) {
            const int part = nt / 5, c0 = (nt - part * 5) * 128;
            const int wm = w & 1, wn = w >> 1;
            constexpr int RB = 0;
            const int xr = wm * 64 + r;
            const unsigned xa = RB + xr * 128 + ((h ^ ((xr >> 1) & 7)) << 4);
            unsigned wt[2];
#pragma unroll
            for (int t = 0; t < 2; ++t) {
                const int q = (lane & 15) >> 2, p = lane & 3, blk = (lane >> 4) & 1, row = 8 * h + 4 * t + q;
                wt[t] = RB + 16384 + row * 256 + (((4 * wn + 2 * blk + (p >> 1)) ^ ((q << 2) | (2 * h + t))) << 4) + 8 * (p & 1);
            }
            auto gbody = [&](auto trc) {
                constexpr bool TR = decltype(trc)::value;
                u32x4 xf[2][8], wf[2][4];
                auto rdk = [&](auto bc, LDS3 const char* sx, LDS3 const char* sb, int ks) {
                    constexpr int B = decltype(bc)::value;
                    xf[B][2 * ks] = *(LDS3 const u32x4*)(sx + (xa ^ (ks * 32)));
                    xf[B][2 * ks + 1] = *(LDS3 const u32x4*)(sx + ((xa + 4096) ^ (ks * 32)));
                    const u32x2 lo = trread(sb + wt[0] + ks * 4096), hi = trread(sb + wt[1] + ks * 4096);
                    wf[B][ks] = (u32x4){lo[0], lo[1], hi[0], hi[1]};
                };
                f32x16 acc0, acc1;
                auto step = [&](auto bc, int s) {
                    constexpr int B = decltype(bc)::value;
                    if (s == GA_PUB) {
                        DRAIN();
                        unsigned arr = 0;
                        if (lane == 0) arr = LDS_CNT_ADD(G_ST);
                        if (__builtin_amdgcn_readfirstlane(arr) == 7 && lane == 0) set_flag(ws, fG + 2048, tag);
                    }
                    __builtin_amdgcn_s_waitcnt(0xC07F);
                    if (s < 18) BAR();
                    else if (lane == 0) LDS_SET(G_PRG + w * 4, s + 1);
                    SBAR();
                    const int s1 = s + 1;
                    LDS3 const char* sb = lds + __builtin_amdgcn_readfirstlane((s1 & 3) * 32768);
                    LDS3 const char* sx = lds + __builtin_amdgcn_readfirstlane(((s1 >= 10 ? 19 - s1 : s1) & 3) * 32768);
#define GM(A, i) do { if constexpr (TR) A = MFMA32_16(wf[B][(i) >> 1], xf[B][i], A); else A = MFMA32_16(xf[B][i], wf[B][(i) >> 1], A); SBAR(); } while (0)
#define GX(i) do { xf[B ^ 1][i] = *(LDS3 const u32x4*)(sx + ((xa + ((i) & 1) * 4096) ^ (((i) >> 1) * 32))); } while (0)
#define GW(ks) do { const u32x2 lo = trread(sb + wt[0] + (ks) * 4096), hi = trread(sb + wt[1] + (ks) * 4096); wf[B ^ 1][ks] = (u32x4){lo[0], lo[1], hi[0], hi[1]}; } while (0)
                    GM(acc0, 0); GX(0); GW(0); SBAR(); GM(acc1, 1); GX(1); GX(2); SBAR(); GM(acc0, 2); GW(1); GX(3); SBAR(); GM(acc1, 3); GX(4); GW(2); SBAR();
                    GM(acc0, 4); GX(5); GX(6); SBAR(); GM(acc1, 5); GW(3); GX(7); SBAR(); GM(acc0, 6); GM(acc1, 7);
#undef GX
#undef GW
#undef GM
                };
                WAIT_BAR(63);
#pragma unroll
                for (int ks = 0; ks < 4; ++ks) rdk(std::integral_constant<int, 0>{}, lds, lds, ks);
#pragma unroll 1
                for (int pass = 0; pass < 2; ++pass) {
                    const int mat = pass ^ 1;
                    acc0 = (f32x16){}; acc1 = (f32x16){};
#pragma unroll 1
                    for (int kk = 0; kk < 10; kk += 2) {
                        step(std::integral_constant<int, 0>{}, pass * 10 + kk);
                        step(std::integral_constant<int, 1>{}, pass * 10 + kk + 1);
                    }
                    const int cpos = 2 * (mt & 7) + (mt >> 3);
                    if constexpr (TR) {
                        const float sc = (part == 0 && mat == 0) ? QSCALE : 1.f;
                        const unsigned base = part == 0 ? WS_Q + mat * NH * 81920 + mt * 5120 : WS_KV + mat * NH * 163840 + cpos * 10240;
                        const unsigned hs = part == 0 ? 81920 : 163840;
#pragma unroll
                        for (int pr = 0; pr < 2; ++pr) {
                            const int G8 = ((c0 + 32 * wn) >> 3) + 2 * pr + h, pp = G8 / 5, gi = G8 - pp * 5;
                            const unsigned hb0 = base + (2 * pp + (gi > 2)) * hs;
#pragma unroll
                            for (int i = 0; i < 2; ++i) {
                                const f32x16& a = i ? acc1 : acc0;
                                const int tl = wm * 64 + i * 32 + r;
                                const auto s0 = __builtin_amdgcn_permlane32_swap(pk2(a[8 * pr] * sc, a[8 * pr + 1] * sc), pk2(a[8 * pr + 4] * sc, a[8 * pr + 5] * sc), false, false);
                                const auto s1 = __builtin_amdgcn_permlane32_swap(pk2(a[8 * pr + 2] * sc, a[8 * pr + 3] * sc), pk2(a[8 * pr + 6] * sc, a[8 * pr + 7] * sc), false, false);
                                if (gi != 2) { const u32x4 ov = {s0[0], s1[0], s0[1], s1[1]}; ST16(ov, hb0 + (tl >> 5) * 1024 + (gi > 2 ? gi - 3 : gi) * 512 + r * 16); }
                                else { const u32x2 o0 = {s0[0], s1[0]}, o1 = {s0[1], s1[1]}; ST8(o0, hb0 + 4096 + tl * 8); ST8(o1, hb0 + hs + 4096 + tl * 8); }
                            }
                        }
                    } else {
                        const int n = c0 + 32 * wn + r, hd = n / 20, d = n - hd * 20, rho = (hd & 1) ? (d < 4 ? d + 16 : d - 4) : d;
                        const unsigned base = WS_KV + (mat * NH + hd) * 163840 + cpos * 10240 + 5120 + h * 320 + rho * 16;
#pragma unroll
                        for (int i = 0; i < 2; ++i)
#pragma unroll
                            for (int s2 = 0; s2 < 2; ++s2) {
                                const f32x16& a = i ? acc1 : acc0;
                                u32x4 ov = {pk2(a[8 * s2], a[8 * s2 + 1]), pk2(a[8 * s2 + 2], a[8 * s2 + 3]), pk2(a[8 * s2 + 4], a[8 * s2 + 5]), pk2(a[8 * s2 + 6], a[8 * s2 + 7])};
                                ST16(ov, base + (wm * 4 + i * 2 + s2) * 640);
                            }
                    }
                    if (pass == 0) STAMP(1);
                }
            };
            if (part < 2) gbody(std::true_type{}); else gbody(std::false_type{});
        } else {
            const int sj = bid - 240;
            WAIT_BAR(63);
            {
                const u32x4 ga = *(const u32x4*)(P.gw + lane * 8), gb = *(const u32x4*)(P.gw + 512 + (lane & 15) * 8);
                u32x4 xa[16], xb[4];
                const u16* xr = P.x + (size_t)(sj * 128 + w * 16) * DM;
#pragma unroll
                for (int rr = 0; rr < 16; ++rr) xa[rr] = *(const u32x4*)(xr + rr * DM + lane * 8);
#pragma unroll
                for (int i = 0; i < 4; ++i) xb[i] = *(const u32x4*)(xr + (i * 4 + (lane >> 4)) * DM + 512 + (lane & 15) * 8);
                float res = 0.f;
#pragma unroll
                for (int rr = 0; rr < 16; ++rr) {
                    float s = 0.f, s2 = 0.f;
#pragma unroll
                    for (int j = 0; j < 4; ++j) { s += bf_lo(xa[rr][j]) * bf_lo(ga[j]) + bf_hi(xa[rr][j]) * bf_hi(ga[j]); s2 += bf_lo(xb[rr >> 2][j]) * bf_lo(gb[j]) + bf_hi(xb[rr >> 2][j]) * bf_hi(gb[j]); }
                    if ((lane >> 4) == (rr & 3)) s += s2;
#pragma unroll
                    for (int o = 32; o >= 1; o >>= 1) s += __shfl_xor(s, o);
                    if (lane == rr) res = s;
                }
                if (lane < 16) ST4(__float_as_uint(1.f / (1.f + __expf(-res))), WS_G + (sj * 128 + w * 16 + lane) * 4);
            }
            DRAIN();
            unsigned arr = 0;
            if (lane == 0) arr = LDS_CNT_ADD(G_ST);
            if (__builtin_amdgcn_readfirstlane(arr) == 7 && lane == 0) set_flag(ws, WS_F_SD + sj * 8, tag);
        }
        STAMP(2);
        {
            lds_wait(lds + G_B1, 4);
            LDS3 char* sp = lds + S_AREA + w * S_STR;
            const u32x4 e0 = *(LDS3 const u32x4*)(sp + lane * 16);
            u32x4 w0 = *(LDS3 const u32x4*)(sp + 1024 + lane * 16), w1 = *(LDS3 const u32x4*)(sp + 2048 + lane * 16);
            const unsigned et = *(LDS3 const unsigned*)(sp + 3072 + lane * 4);
            if (r >= 20) { w0 = (u32x4){one, one, one, one}; w1 = w0; }
            u32x4 fp, fn, ft, phm; unsigned pht;
            phi_frag(phm, pht);
            feature_map(phm, pht, e0, et, h, fp, fn, ft);
            PIN(fp); PIN(fn); PIN(ft);
            {
                DRAIN();
                unsigned arr = 0;
                if (lane == 0) arr = LDS_CNT_ADD(G_ST3);
                if (__builtin_amdgcn_readfirstlane(arr) == 7 && lane == 0 && bid < 240) set_flag(ws, fG, tag);
            }
            u32x4 i0, i1;
#pragma unroll
            for (int j = 0; j < 4; ++j) {
                const int e = r - 8 * h - 2 * j;
                i0[j] = e == 0 ? 0x3f80u : (e == 1 ? 0x3f800000u : 0u);
                i1[j] = e == 16 ? 0x3f80u : (e == 17 ? 0x3f800000u : 0u);
            }
            f32x16 T0 = MFMA32_16(fp, i0, z16); T0 = MFMA32_16(fn, i1, T0);
            f32x16 T1 = MFMA32_16(ft, i0, z16);
            u32x4 b00, b01, b10, b11;
#pragma unroll
            for (int j = 0; j < 4; ++j) {
                b00[j] = pk2(T0[2 * j], T0[2 * j + 1]); b01[j] = pk2(T0[8 + 2 * j], T0[9 + 2 * j]);
                b10[j] = pk2(T1[2 * j], T1[2 * j + 1]); b11[j] = pk2(T1[8 + 2 * j], T1[9 + 2 * j]);
            }
            f32x16 k0 = MFMA32_16(w0, b00, z16); k0 = MFMA32_16(w1, b01, k0);
            f32x16 k1 = MFMA32_16(w0, b10, z16); k1 = MFMA32_16(w1, b11, k1);
#pragma unroll
            for (int gq = 0; gq < 3; ++gq) {
                *(LDS3 f32x4*)(sp + gq * 1024 + lane * 16) = (f32x4){k0[4 * gq], k0[4 * gq + 1], k0[4 * gq + 2], k0[4 * gq + 3]};
                if (r < 8) *(LDS3 f32x4*)(sp + 3072 + (gq * 16 + h * 8 + r) * 16) = (f32x4){k1[4 * gq], k1[4 * gq + 1], k1[4 * gq + 2], k1[4 * gq + 3]};
            }
            asm volatile("s_waitcnt lgkmcnt(0)" ::: "memory");
            unsigned arr = 0;
            if (lane == 0) arr = LDS_CNT_ADD(G_KV);
            if (__builtin_amdgcn_readfirstlane(arr) == 7) {
                __builtin_amdgcn_fence(__ATOMIC_ACQUIRE, "wavefront");
#pragma unroll
                for (int i = 0; i < 4; ++i) {
                    const int qd = lane + 64 * i < 240 ? lane + 64 * i : 239;
                    f32x4 s = {0.f, 0.f, 0.f, 0.f};
#pragma unroll
                    for (int p = 0; p < 8; ++p) s += *(LDS3 const f32x4*)(lds + S_AREA + p * S_STR + qd * 16);
                    ST16(__builtin_bit_cast(u32x4, s), WS_KVP + (head * 8 + qb) * 4096 + qd * 16);
                }
                asm volatile("s_waitcnt lgkmcnt(0)" ::: "memory");
                if (lane == 0) LDS_SET(G_KVP, 1);
                DRAIN();
                lds_wait(lds + G_WT, 4);
                if (lane == 0) set_flag(ws, WS_F_KV + (head * 8 + qb) * 8, tag);
            }
        }
        STAMP(3);
        const unsigned hb = lds0 + kh * 10240;
        const unsigned laneK = hb + lane * 16, laneT = hb + 4096 + r * 8 + h * 4, laneV = hb + 5120 + h * 320 + (r < 20 ? r : 19) * 16;
        const u64 vmask = 0x000fffff000fffffull;
        u32x4 v0a = {one, one, one, one}, v0b = v0a, v1 = v0a;
        u32x4 qA, qB, ka; unsigned qtA, qtB, kt;
        {
            lds_wait(lds + G_P, 1);
            const unsigned qp = WS_Q + head * 81920 + (q0 >> 7) * 5120;
            const unsigned om = (qs & 1) * 2048 + lane * 16, ot = 4096 + ((qs & 1) * 64 + r) * 8 + 4 * h;
            qA = LD16(qp + om); qB = LD16(qp + om + 1024); qtA = LD4(qp + ot); qtB = LD4(qp + ot + 256);
            lds_wait(lds + G_Q, 4);
            DSR128(ka, laneK, 0); DSR32(kt, laneT, 0); DSR128M2(v0a, laneV, 0, v1, laneV, 640);
            asm volatile("s_waitcnt lgkmcnt(0)" : "+v"(ka), "+v"(kt), "+v"(v0a), "+v"(v1) :: "memory");
            if (lane == 0) LDS_CNT_ADD(G_RD);
        }
        STAMP(5);
        float mA, mB; u32x2 qTA, qTB; f32x16 sA, sB;
        {
            const u32x2 t0 = {kt, 0x3f80u}, a0 = {qtA, 0u}, b0 = {qtB, 0u};
            const f32x16 s = MFMA32_16(ka, qA, z16);
            const f32x16 s0 = MFMA32_8(t0, a0, s);
            f32x16 s1 = MFMA32_16(ka, qB, z16); s1 = MFMA32_8(t0, b0, s1);
            float x0 = s0[0], x1 = s1[0];
#pragma unroll
            for (int i = 1; i < 16; ++i) { x0 = fmaxf(x0, s0[i]); x1 = fmaxf(x1, s1[i]); }
            x0 = -xhalf_max(x0); x1 = -xhalf_max(x1);
            const unsigned hh = pk2(x0, x1);
            const unsigned ll = pk2(x0 - bf_lo(hh), x1 - bf_hi(hh));
            mA = -(bf_lo(hh) + bf_lo(ll)); mB = -(bf_hi(hh) + bf_hi(ll));
            const unsigned sel = h ? ll : hh;
            qTA = (u32x2){qtA, sel & 0xffffu}; qTB = (u32x2){qtB, sel >> 16};
            sA = MFMA32_8(t0, qTA, s);
        }
        f32x16 oA = {}, oB = {};
        u32x4 pa, pb = {0u, 0u, 0u, 0u};
#define EX4(S, i, Pv, j) do { Pv[j] = pk2(ex2(S[i]), ex2(S[i + 1])); Pv[j + 1] = pk2(ex2(S[i + 2]), ex2(S[i + 3])); PIN(Pv); SBAR(); } while (0)
#define KT2 ((u32x2){kt, 0x3f80u})
#define TILE(V0C, V0N, aK1, oK1, aT1, oT1, oV1, aV0, oV0) do { \
            sB = MFMA32_16(ka, qB, z16); SBAR(); DSR128(ka, aK1, oK1); EX4(sA, 0, pa, 0); \
            oB = MFMA32_16(v1, pb, oB); SBAR(); DSR128M2(v1, aV, oV1, V0N, aV0, oV0); EX4(sA, 4, pa, 2); \
            sB = MFMA32_8(KT2, qTB, sB); SBAR(); DSR32(kt, aT1, oT1); EX4(sA, 8, pb, 0); \
            oA = MFMA32_16(V0C, pa, oA); SBAR(); EX4(sA, 12, pb, 2); \
            LWAIT(1); sA = MFMA32_16(ka, qA, z16); SBAR(); EX4(sB, 0, pa, 0); \
            oA = MFMA32_16(v1, pb, oA); SBAR(); EX4(sB, 4, pa, 2); \
            LWAIT(0); sA = MFMA32_8(KT2, qTA, sA); SBAR(); EX4(sB, 8, pb, 0); \
            oB = MFMA32_16(V0C, pa, oB); SBAR(); EX4(sB, 12, pb, 2); \
        } while (0)
        unsigned aK = laneK, aT = laneT, aV = laneV;
#pragma unroll 1
        for (int c = 0; c < 8; ++c) {
            if (c == 1) lds_wait(lds + C_KVL, 4);
            else if (c == 5) lds_wait(lds + C_KVL2, 4);
            if (c >= 1) {
                unsigned pp;
                asm volatile("ds_write_b32 %1, %2\n\tds_read_b32 %0, %3\n\ts_waitcnt lgkmcnt(0)" : "=&v"(pp) : "v"(lds0 + C_PR + w * 4), "v"(c), "v"(lds0 + C_PR + (w ^ 4) * 4) : "memory");
                const int pc = __builtin_amdgcn_readfirstlane(pp);
                if (pc > c || (pc == c && ((c ^ kh) & 1))) __builtin_amdgcn_s_setprio(1); else __builtin_amdgcn_s_setprio(0);
            } else if (kh) __builtin_amdgcn_s_setprio(1);
            const unsigned nx = (c < 7 ? c + 1 : 7) * 20480;
            const unsigned nK = laneK + nx, nT = laneT + nx, nV = laneV + nx;
            SBAR();
            TILE(v0a, v0b, aK, 1024, aT, 256, 640,  aV, 1280);
            TILE(v0b, v0a, aK, 2048, aT, 512, 1920, aV, 2560);
            if (c == 0) {
                lds_wait(lds + G_Z, 1);
                if (lane == 0) LDS_CNT_ADD(G_ST2);
                asm volatile("s_waitcnt lgkmcnt(0)" ::: "memory");
                SBAR();
            }
            TILE(v0a, v0b, aK, 3072, aT, 768, 3200, aV, 3840);
            TILE(v0b, v0a, nK, 0,    nT, 0,   4480, nV, 0);
            aK = nK; aT = nT; aV = nV;
        }
        oB = MFMA32_16(v1, pb, oB);
#undef TILE
#undef EX4
        if (lane == 0) __hip_atomic_store((LDS3 unsigned*)(lds + C_PR + w * 4), 8u, __ATOMIC_RELAXED, __HIP_MEMORY_SCOPE_WORKGROUP);
        STAMP(6);
        {
            float chk = 0.f;
#pragma unroll
            for (int i = 0; i < 12; ++i) chk += fabsf(oA[i]) * 0.f + fabsf(oB[i]) * 0.f;
            bool bad = !(chk == 0.f);
            if (h) bad = bad || !(oA[8] >= 0x1p-60f && oA[8] <= 0x1p60f && oB[8] >= 0x1p-60f && oB[8] <= 0x1p60f);
            if (__builtin_expect(__any(bad), 0)) {
                mA = -INFINITY; mB = -INFINITY;
#pragma unroll
                for (int i = 0; i < 16; ++i) { oA[i] = 0.f; oB[i] = 0.f; }
                const u32x2 a0 = {qtA, 0u}, b0 = {qtB, 0u};
#pragma unroll 1
                for (int t = 0; t < 32; ++t) {
                    const unsigned kb = WS_KV + head * 163840 + (2 * (t >> 2) + kh) * 10240, t4 = t & 3;
                    const u32x4 k0 = LD16(kb + t4 * 1024 + lane * 16);
                    const u32x2 t0 = {LD4(kb + 4096 + (t4 * 32 + r) * 8 + 4 * h), 0u};
                    u32x4 w0 = LD16(kb + 5120 + t4 * 1280 + h * 320 + (r < 20 ? r : 19) * 16), w1 = LD16(kb + 5120 + t4 * 1280 + 640 + h * 320 + (r < 20 ? r : 19) * 16);
                    if (r >= 20) { w0 = (u32x4){one, one, one, one}; w1 = w0; }
                    f32x16 s0 = MFMA32_16(k0, qA, z16); s0 = MFMA32_8(t0, a0, s0);
                    f32x16 s1 = MFMA32_16(k0, qB, z16); s1 = MFMA32_8(t0, b0, s1);
                    float x0 = s0[0], x1 = s1[0];
#pragma unroll
                    for (int i = 1; i < 16; ++i) { x0 = fmaxf(x0, s0[i]); x1 = fmaxf(x1, s1[i]); }
                    x0 = fmaxf(xhalf_max(x0), mA); x1 = fmaxf(xhalf_max(x1), mB);
                    const float g0 = (mA == x0) ? 1.f : ex2(mA - x0), g1 = (mB == x1) ? 1.f : ex2(mB - x1);
                    mA = x0; mB = x1;
#pragma unroll
                    for (int i = 0; i < 16; ++i) { oA[i] *= g0; oB[i] *= g1; }
#pragma unroll
                    for (int i = 0; i < 4; ++i) { pa[i] = pk2(ex2(s0[2 * i] - x0), ex2(s0[2 * i + 1] - x0)); pb[i] = pk2(ex2(s0[8 + 2 * i] - x0), ex2(s0[9 + 2 * i] - x0)); }
                    oA = MFMA32_16(w0, pa, oA); oA = MFMA32_16(w1, pb, oA);
#pragma unroll
                    for (int i = 0; i < 4; ++i) { pa[i] = pk2(ex2(s1[2 * i] - x1), ex2(s1[2 * i + 1] - x1)); pb[i] = pk2(ex2(s1[8 + 2 * i] - x1), ex2(s1[9 + 2 * i] - x1)); }
                    oB = MFMA32_16(w0, pa, oB); oB = MFMA32_16(w1, pb, oB);
                }
            }
        }
        lds_wait<4>(lds + C_BX, 8);
        const float gO = *(LDS3 const float*)(lds + X_G + (qs * 64 + kh * 32 + r) * 4);
        {
            LDS3 char* xp = lds + X_MRG + w * 2816;
            const f32x16& s = kh ? oA : oB;
            *(LDS3 f32x4*)(xp + lane * 16) = (f32x4){s[0], s[1], s[2], s[3]};
            *(LDS3 f32x4*)(xp + 1024 + lane * 16) = (f32x4){s[4], s[5], s[6], s[7]};
            if (!h) *(LDS3 f32x4*)(xp + 2048 + r * 16) = (f32x4){s[8], s[9], s[10], s[11]};
            else *(LDS3 f32x2*)(xp + 2560 + r * 8) = (f32x2){s[8], kh ? mA : mB};
            asm volatile("s_waitcnt lgkmcnt(0)" ::: "memory");
            if (lane == 0) __hip_atomic_store((LDS3 unsigned*)(lds + C_MRG + w * 4), 1u, __ATOMIC_RELAXED, __HIP_MEMORY_SCOPE_WORKGROUP);
        }
        lds_wait<2>(lds + C_MRG + (w ^ 4) * 4, 1);
        bool lastw;
        {
            LDS3 const char* xp = lds + X_MRG + (w ^ 4) * 2816;
            const f32x4 p0 = *(LDS3 const f32x4*)(xp + lane * 16), p1 = *(LDS3 const f32x4*)(xp + 1024 + lane * 16), p2 = *(LDS3 const f32x4*)(xp + 2048 + r * 16);
            const f32x2 lm = *(LDS3 const f32x2*)(xp + 2560 + r * 8);
            const f32x16& s = kh ? oB : oA;
            const float mo = kh ? mB : mA, mp = lm[1];
            const float mx = fmaxf(mo, mp);
            const float fo = (mo == mx) ? 1.f : ex2(mo - mx), fq = (mp == mx) ? 1.f : ex2(mp - mx);
            float o[12];
#pragma unroll
            for (int i = 0; i < 4; ++i) { o[i] = s[i] * fo + p0[i] * fq; o[4 + i] = s[4 + i] * fo + p1[i] * fq; o[8 + i] = s[8 + i] * fo + p2[i] * fq; }
            const float l = upper_half(s[8]) * fo + lm[0] * fq;
            store_o(rs, o, gO / l, q0 + kh * 32 + r, head * 20, h);
            DRAIN();
            unsigned arr = 0;
            if (lane == 0) arr = LDS_CNT_ADD(C_OA);
            lastw = __builtin_amdgcn_readfirstlane(arr) == 7;
            if (lastw && lane == 0) set_flag(ws, WS_F_OA + (qb * 32 + head) * 8, tag);
        }
        STAMP(7);
        {
            const int c16 = lane & 15, g = lane >> 4, tt = w & 3, k0 = kh * 10;
            POLL_ONE(lastw, 4, WS_F_OA + ((mtO >> 2) * 32 + r) * 8, WS_F_OA + ((mtO >> 2) * 32 + r) * 8, C_P4);
            STAMP(9);
            const unsigned ab = WS_AO + ((mtO * 4 + tt) * 40 + k0) * 1024 + lane * 16;
            u32x4 af[10];
#pragma unroll
            for (int ks = 0; ks < 10; ++ks) af[ks] = LD16(ab + ks * 1024);
            lds_wait(lds + C_WA, 4);
            lds_wait(lds + C_OA, 8);
            f32x4 c[5];
#pragma unroll
            for (int n = 0; n < 5; ++n) c[n] = (f32x4){0.f, 0.f, 0.f, 0.f};
#pragma unroll
            for (int ks = 0; ks < 10; ++ks)
#pragma unroll
                for (int n = 0; n < 5; ++n) c[n] = MFMA16_32(*(LDS3 const u32x4*)(lds + L_WA + (n * 20 + k0 + ks) * 1024 + lane * 16), af[ks], c[n]);
            LDS3 char* xp = lds + X_MRG + tt * 5120 + lane * 16;
            if (w < 4) { *(LDS3 f32x4*)(xp + 3072) = c[3]; *(LDS3 f32x4*)(xp + 4096) = c[4]; }
            else { *(LDS3 f32x4*)(xp) = c[0]; *(LDS3 f32x4*)(xp + 1024) = c[1]; *(LDS3 f32x4*)(xp + 2048) = c[2]; }
            asm volatile("s_waitcnt lgkmcnt(0)" ::: "memory");
            if (lane == 0) __hip_atomic_store((LDS3 unsigned*)(lds + C_FIN + w * 4), 1u, __ATOMIC_RELAXED, __HIP_MEMORY_SCOPE_WORKGROUP);
            lds_wait(lds + C_FIN + (w ^ 4) * 4, 1);
            lds_wait(lds + C_BP + tt * 4, 1);
            u16* op = P.out + (size_t)(mtO * 64 + tt * 16 + c16) * DM + ntO * 80 + g * 4;
#pragma unroll
            for (int n = 0; n < 5; ++n) {
                if ((w < 4) == (n < 3)) {
                    const f32x4 bp = *(LDS3 const f32x4*)(n < 3 ? lds + X_FIN + tt * 3072 + n * 1024 + lane * 16 : lds + 143360 + tt * 2048 + (n - 3) * 1024 + lane * 16);
                    const f32x4 s = c[n] + *(LDS3 const f32x4*)(xp + n * 1024) + bp;
                    *(u32x2*)(op + n * 16) = (u32x2){pk2(s[0], s[1]), pk2(s[2], s[3])};
                }
            }
            if (bid == 0 && mtid == 0) set_flag(ws, WS_EPOCH, tag);
        }
        STAMP(10);
        asm volatile("mega_code_end:");
    }
}

extern "C" void kernel_launch(void* const* d_in, const int* in_sizes, int n_in, void* d_out, int out_size, void* d_ws, size_t ws_size,
                              hipStream_t stream) {
    Params P;
    P.x = (const u16*)d_in[0]; P.wa = (const u16*)d_in[1]; P.woa = (const u16*)d_in[2]; P.wb = (const u16*)d_in[4];
    P.wphi = (const u16*)d_in[5]; P.wob = (const u16*)d_in[6]; P.gw = (const u16*)d_in[7]; P.ws = (char*)d_ws; P.out = (u16*)d_out;
    mega<<<256, 768, 0, stream>>>(P);
}
```

```cpp
#include <hip/hip_runtime.h>
#include <hip/hip_bf16.h>
#include <stdint.h>
#include <type_traits>

typedef __attribute__((ext_vector_type(8))) short bf16x8;
typedef __attribute__((ext_vector_type(4))) short bf16x4;
typedef __attribute__((ext_vector_type(16))) float f32x16;
typedef __attribute__((ext_vector_type(4))) float f32x4;
typedef __attribute__((ext_vector_type(2))) float f32x2;
typedef __attribute__((ext_vector_type(4))) unsigned u32x4;
typedef __attribute__((ext_vector_type(2))) unsigned u32x2;
typedef __attribute__((ext_vector_type(2))) __bf16 bf2_t;
typedef unsigned short u16;
typedef unsigned long long u64;
#define LDS3 __attribute__((address_space(3)))
typedef __attribute__((address_space(1))) u64 gu64;
#define RLX_AGENT __ATOMIC_RELAXED, __HIP_MEMORY_SCOPE_AGENT

#define PRIO_MODE 3
#define WARM_MODE 1
#define GA_PUB 12
#define PHI_ROUND 0
#define PA_SLP 6
#define PRE_SLEEP 2
constexpr int SEQ = 2048, DM = 640, NH = 32;
constexpr float LOG2E = 1.4426950408889634f;
constexpr float QSCALE = 0.22360679774997896f * LOG2E;

constexpr unsigned WS_EPOCH = 0;
constexpr unsigned WS_F_G   = 256;
constexpr unsigned WS_F_SD  = WS_F_G + 4096;
constexpr unsigned WS_F_KV  = WS_F_SD + 256;
constexpr unsigned WS_F_OB  = WS_F_KV + 2048;
constexpr unsigned WS_F_OA  = WS_F_OB + 2048;
constexpr unsigned WS_G     = 16384;
constexpr unsigned WS_Q     = 32768;
constexpr unsigned WS_KV    = WS_Q + 2 * NH * 81920;
constexpr unsigned WS_KVP   = WS_KV + 2 * NH * 163840;
constexpr unsigned WS_AO    = WS_KVP + NH * 8 * 4096;
constexpr unsigned WS_WT    = WS_AO + SEQ * 1280 * 2;
constexpr unsigned WS_END   = WS_WT + 1280 * 640 * 2;

#define STAMP(i)

__device__ __forceinline__ unsigned pk2(float a, float b) { f32x2 v = {a, b}; bf2_t r = __builtin_convertvector(v, bf2_t); return __builtin_bit_cast(unsigned, r); }
__device__ __forceinline__ float bf_lo(unsigned u) { return __uint_as_float(u << 16); }
__device__ __forceinline__ float bf_hi(unsigned u) { return __uint_as_float(u & 0xffff0000u); }
__device__ __forceinline__ float ex2(float x) { return __builtin_amdgcn_exp2f(x); }
#define MFMA32_16(a, b, c) __builtin_amdgcn_mfma_f32_32x32x16_bf16(__builtin_bit_cast(bf16x8, a), __builtin_bit_cast(bf16x8, b), c, 0, 0, 0)
#define MFMA32_8(a, b, c) __builtin_amdgcn_mfma_f32_32x32x8bf16_1k(__builtin_bit_cast(bf16x4, a), __builtin_bit_cast(bf16x4, b), c, 0, 0, 0)
#define MFMA16_32(a, b, c) __builtin_amdgcn_mfma_f32_16x16x32_bf16(__builtin_bit_cast(bf16x8, a), __builtin_bit_cast(bf16x8, b), c, 0, 0, 0)

__device__ __forceinline__ void glds16(unsigned voff, const void* sbase, unsigned lds_base) {
    unsigned sv;
    asm volatile("s_mov_b32 %0, m0\n\ts_mov_b32 m0, %3\n\ts_nop 0\n\tglobal_load_lds_dwordx4 %1, %2\n\ts_mov_b32 m0, %0"
                 : "=&s"(sv) : "v"(voff), "s"(sbase), "s"(__builtin_amdgcn_readfirstlane(lds_base)) : "memory");
}
__device__ __forceinline__ void glds16s(unsigned voff, const void* sbase, unsigned lds_base) {
    unsigned sv;
    asm volatile("s_mov_b32 %0, m0\n\ts_mov_b32 m0, %3\n\ts_nop 0\n\tglobal_load_lds_dwordx4 %1, %2 sc1\n\ts_mov_b32 m0, %0"
                 : "=&s"(sv) : "v"(voff), "s"(sbase), "s"(__builtin_amdgcn_readfirstlane(lds_base)) : "memory");
}
__device__ __forceinline__ void glds4s(unsigned voff, const void* sbase, unsigned lds_base) {
    unsigned sv;
    asm volatile("s_mov_b32 %0, m0\n\ts_mov_b32 m0, %3\n\ts_nop 0\n\tglobal_load_lds_dword %1, %2 sc1\n\ts_mov_b32 m0, %0"
                 : "=&s"(sv) : "v"(voff), "s"(sbase), "s"(__builtin_amdgcn_readfirstlane(lds_base)) : "memory");
}
typedef short v4i16_t __attribute__((ext_vector_type(4)));
__device__ __forceinline__ u32x2 trread(LDS3 const char* p) {
    return __builtin_bit_cast(u32x2, __builtin_amdgcn_ds_read_tr16_b64_v4i16((LDS3 v4i16_t*)p));
}
#define WAIT_BAR(N) asm volatile("s_waitcnt vmcnt(" #N ") lgkmcnt(0)\n\ts_barrier" ::: "memory")
#define DRAIN() asm volatile("s_waitcnt vmcnt(0)" ::: "memory")
#define SBAR() __builtin_amdgcn_sched_barrier(0)
#define PIN(x) asm volatile("" : "+v"(x))

__device__ __forceinline__ float xhalf_max(float x) {
    auto r = __builtin_amdgcn_permlane32_swap(__float_as_uint(x), __float_as_uint(x), false, false);
    return fmaxf(__uint_as_float(r[0]), __uint_as_float(r[1]));
}
__device__ __forceinline__ float xhalf_sum(float x) {
    auto r = __builtin_amdgcn_permlane32_swap(__float_as_uint(x), __float_as_uint(x), false, false);
    return __uint_as_float(r[0]) + __uint_as_float(r[1]);
}
__device__ __forceinline__ float upper_half(float x) {
    auto r = __builtin_amdgcn_permlane32_swap(__float_as_uint(x), __float_as_uint(x), false, false);
    return __uint_as_float(r[1]);
}

typedef __amdgpu_buffer_rsrc_t rsrc_t;
#define ST8(v, off)  __builtin_amdgcn_raw_buffer_store_b64(v, rs, (int)(off), 0, 16)
#define ST16(v, off) __builtin_amdgcn_raw_buffer_store_b128(v, rs, (int)(off), 0, 16)
#define ST4(v, off)  __builtin_amdgcn_raw_buffer_store_b32(v, rs, (int)(off), 0, 16)
#define LD16(off) __builtin_amdgcn_raw_buffer_load_b128(rs, (int)(off), 0, 16)
#define LD4(off)  __builtin_amdgcn_raw_buffer_load_b32(rs, (int)(off), 0, 16)

template <int SLP = 2>
__device__ __forceinline__ void wait_flags(const char* ws, unsigned off0, unsigned off1, u64 tag) {
    gu64* p0 = (gu64*)(ws + off0); gu64* p1 = (gu64*)(ws + off1);
#pragma unroll 1
    for (unsigned spins = 0; spins < (1u << 16); ++spins) {
        const u64 a = __hip_atomic_load(p0, RLX_AGENT), b = __hip_atomic_load(p1, RLX_AGENT);
        if (__all(a == tag && b == tag)) break;
        __builtin_amdgcn_s_sleep(SLP);
    }
    __builtin_amdgcn_fence(__ATOMIC_ACQUIRE, "wavefront");
}
__device__ __forceinline__ void set_flag(char* ws, unsigned off, u64 tag) { __hip_atomic_store((gu64*)(ws + off), tag, RLX_AGENT); }

struct Params { const u16* x; const u16* wa; const u16* wb; const u16* wphi; const u16* woa; const u16* wob; const u16* gw; char* ws; u16* out; };

__device__ __forceinline__ void feature_map(const u32x4& pm, unsigned pt, const u32x4& tm, unsigned tt, int h, u32x4& fp, u32x4& fn, u32x4& ft) {
    const f32x16 z16 = {};
    const u32x2 a8 = {pt, 0u}, b8 = {tt, 0u};
    f32x16 s = MFMA32_16(pm, tm, z16); s = MFMA32_8(a8, b8, s);
    f32x2 v[6];
#pragma unroll
    for (int i = 0; i < 6; ++i) {
        v[i] = (f32x2){s[2 * i], s[2 * i + 1]};
    }
    float mx = fmaxf(v[0][0], v[0][1]), mn = fminf(v[0][0], v[0][1]);
#pragma unroll
    for (int i = 1; i < 6; ++i) { mx = fmaxf(fmaxf(mx, v[i][0]), v[i][1]); mn = fminf(fminf(mn, v[i][0]), v[i][1]); }
    mx = xhalf_max(mx) * -LOG2E; mn = xhalf_max(-mn) * -LOG2E;
    f32x2 ep[6], en[6];
#pragma unroll
    for (int i = 0; i < 6; ++i) {
        const f32x2 a = v[i] * LOG2E + mx, b = v[i] * -LOG2E + mn;
        ep[i] = (f32x2){ex2(a[0]), ex2(a[1])}; en[i] = (f32x2){ex2(b[0]), ex2(b[1])};
    }
    const float tw = h ? 0.f : 1.f;
    const f32x2 s2p = ((ep[0] + ep[1]) + (ep[2] + ep[3])) + (ep[4] + ep[5]) * tw, s2n = ((en[0] + en[1]) + (en[2] + en[3])) + (en[4] + en[5]) * tw;
    const float sp = __builtin_amdgcn_rcpf(xhalf_sum(s2p[0] + s2p[1])), sn = __builtin_amdgcn_rcpf(xhalf_sum(s2n[0] + s2n[1]));
    const float tp = sp * tw, tn = sn * tw;
#pragma unroll
    for (int i = 0; i < 4; ++i) { const f32x2 a = ep[i] * sp, b = en[i] * sn; fp[i] = pk2(a[0], a[1]); fn[i] = pk2(b[0], b[1]); }
    const f32x2 c0 = ep[4] * tp, c1 = ep[5] * tp, c2 = en[4] * tn, c3 = en[5] * tn;
    ft = (u32x4){pk2(c0[0], c0[1]), pk2(c1[0], c1[1]), pk2(c2[0], c2[1]), pk2(c3[0], c3[1])};
}

__device__ __forceinline__ void store_o(rsrc_t rs, const float (&o)[12], float sc, int tok, int half, int head, int h) {
    const auto s0 = __builtin_amdgcn_permlane32_swap(pk2(o[0] * sc, o[1] * sc), pk2(o[4] * sc, o[5] * sc), false, false);
    const auto s1 = __builtin_amdgcn_permlane32_swap(pk2(o[2] * sc, o[3] * sc), pk2(o[6] * sc, o[7] * sc), false, false);
    const unsigned tb = WS_AO + ((tok >> 4) * 40 + half * 20) * 1024;
    const u32x4 mv = {s0[0], s1[0], s0[1], s1[1]};
    ST16(mv, tb + (head >> 1) * 1024 + (((head & 1) * 2 + h) * 16 + (tok & 15)) * 16);
    const u32x2 tv = {pk2(o[8] * sc, o[9] * sc), pk2(o[10] * sc, o[11] * sc)};
    if (!h) ST8(tv, tb + (16 + (head >> 3)) * 1024 + (head & 7) * 128 + (tok & 15) * 8);
}
#define LD8(off)  __builtin_amdgcn_raw_buffer_load_b64(rs, (int)(off), 0, 16)
#define LD_TAIL(dst, base) do { const u32x2 a_ = LD8((base) + (lane >> 4) * 256 + (lane & 15) * 8), b_ = LD8((base) + (lane >> 4) * 256 + 128 + (lane & 15) * 8); dst = (u32x4){a_[0], a_[1], b_[0], b_[1]}; } while (0)

#define DSR128(dst, addr, off) asm volatile("ds_read_b128 %0, %1 offset:%2" : "=v"(dst) : "v"(addr), "i"(off))
#define DSR32(dst, addr, off) asm volatile("ds_read_b32 %0, %1 offset:%2" : "=v"(dst) : "v"(addr), "i"(off))
#define DSR128M2(d0, a0, o0, d1, a1, o1) asm volatile("s_mov_b64 exec, %6\n\tds_read_b128 %0, %2 offset:%3\n\tds_read_b128 %1, %4 offset:%5\n\ts_mov_b64 exec, -1" \
                                                      : "+v"(d0), "+v"(d1) : "v"(a0), "i"(o0), "v"(a1), "i"(o1), "s"(vmask))
#define LWAIT(n) do { asm volatile("s_waitcnt lgkmcnt(" #n ")" ::: "memory"); SBAR(); } while (0)

#define BAR() asm volatile("s_barrier" ::: "memory")
#define LDS_SET(off, v) __hip_atomic_store((LDS3 unsigned*)(lds + (off)), (unsigned)(v), __ATOMIC_RELAXED, __HIP_MEMORY_SCOPE_WORKGROUP)
#define LDS_CNT_ADD(off) __hip_atomic_fetch_add((LDS3 unsigned*)(lds + (off)), 1u, __ATOMIC_RELAXED, __HIP_MEMORY_SCOPE_WORKGROUP)

template <int SLP = 1>
__device__ __forceinline__ void lds_wait(LDS3 char* p, unsigned want) {
#pragma unroll 1
    for (unsigned spins = 0; spins < (1u << 20); ++spins) {
        if ((unsigned)__builtin_amdgcn_readfirstlane(__hip_atomic_load((LDS3 unsigned*)p, __ATOMIC_RELAXED, __HIP_MEMORY_SCOPE_WORKGROUP)) >= want) break;
        __builtin_amdgcn_s_sleep(SLP);
    }
    __builtin_amdgcn_fence(__ATOMIC_ACQUIRE, "wavefront");
}

template <int SLP>
__device__ __forceinline__ void prog_wait(LDS3 char* p, int lane, unsigned want) {
#pragma unroll 1
    for (unsigned spins = 0; spins < (1u << 20); ++spins) {
        if (__all(__hip_atomic_load((LDS3 unsigned*)p + (lane & 7), __ATOMIC_RELAXED, __HIP_MEMORY_SCOPE_WORKGROUP) >= want)) break;
        __builtin_amdgcn_s_sleep(SLP);
    }
    __builtin_amdgcn_fence(__ATOMIC_ACQUIRE, "wavefront");
}
#define POLL_ONE(cond, SLP, o0, o1, word) do { if (cond) { wait_flags<SLP>(ws, o0, o1, tag); \
        if (lane == 0) __hip_atomic_store((LDS3 unsigned*)(lds + (word)), 1u, __ATOMIC_RELAXED, __HIP_MEMORY_SCOPE_WORKGROUP); } else lds_wait<2>(lds + (word), 1); } while (0)
constexpr int S_AREA = 131072, S_STR = 3840, G_W = S_AREA + 8 * S_STR, L_PHI = G_W + 1024;
constexpr int G_PRG = G_W  , G_Q = G_W + 32, G_P = G_W + 36, G_ST = G_W + 40, G_RD = G_W + 44, G_Z = G_W + 48, G_ST2 = G_W + 52, G_ST3 = G_W + 56,
              G_P1 = G_W + 60, G_B1 = G_W + 64, G_KV = G_W + 68, G_KVP = G_W + 72, G_WT = G_W + 76;
constexpr int C_KVF = 4, C_OB = 8, C_WA = 16, C_OA = 20, C_BX = 28, C_MRG = 32  , C_FIN = 64  , C_BP = 96  ;
constexpr int C_P2 = 116, C_P3 = 120, C_P4 = 124, C_PR = 128  , C_KVL = 160, C_P5 = 164, C_KVL2 = 168;
constexpr int X_G = 256, X_KVF = 1280;
constexpr int X_MRG = 1280;
constexpr int L_WA = 24576;
constexpr int X_FIN = 126976;

__global__ __launch_bounds__(768) void mega(Params P) {
    __shared__ __attribute__((aligned(16))) char smem[163840];
    LDS3 char* lds = (LDS3 char*)smem;
    char* ws = P.ws;
    const int tid = threadIdx.x, lane = tid & 63, r = lane & 31, h = lane >> 5;
    const int hw = __builtin_amdgcn_readfirstlane(tid >> 6);
    const bool helper = hw < 4;
    const int w = hw - 4, mtid = tid - 256;
    const int bid = blockIdx.x;
    const unsigned lds0 = (unsigned)(uintptr_t)smem;
    const rsrc_t rs = __builtin_amdgcn_make_buffer_rsrc(ws, 0, WS_END, 0x00020000);
    STAMP(0);
    u64 tag = 0;
#define GET_TAG() do { asm volatile("s_load_dwordx2 %0, %1, 0x0\n\ts_waitcnt lgkmcnt(0)" : "=s"(tag) : "s"(ws) : "memory"); tag += 1; } while (0)
    const int head = (bid & 7) * 4 + ((bid >> 3) & 3), qb = bid >> 5;
    const int nlo = (head * 20) >> 7, nhi = (head * 20 + 19) >> 7;
    const int mtO = (bid & 7) * 4 + ((bid >> 3) & 3), ntO = bid >> 5;
    const int lin = (bid & 7) * 30 + (bid >> 3), lh = lin >= 120, lrem = lin - 120 * lh;
    const int mt = lh * 8 + (lrem & 7), nt = lrem >> 3;
    const unsigned fG = WS_F_G + (nt * 16 + mt) * 8;
    const unsigned one = (r == 20) ? 0x3f803f80u : 0u;
    const f32x16 z16 = {};
    auto phi_frag = [&](u32x4& phm, unsigned& pht) {
        LDS3 const u16* wp = (LDS3 const u16*)(lds + L_PHI) + (r < 20 ? r : (r & 3));
        unsigned e[10];
#pragma unroll
        for (int j = 0; j < 8; ++j) e[j] = wp[(8 * h + j + 4 * (head & 1)) * 20];
        e[8] = wp[((head & 1 ? 0 : 16) + 2 * h) * 20]; e[9] = wp[((head & 1 ? 1 : 17) + 2 * h) * 20];
        phm = (u32x4){e[0] | (e[1] << 16), e[2] | (e[3] << 16), e[4] | (e[5] << 16), e[6] | (e[7] << 16)}; pht = e[8] | (e[9] << 16);
    };

    if (helper) {
        const unsigned fA0 = WS_F_G + (((lane < 32 ? 5 : 10) + ((lane & 16) ? nhi : nlo)) * 16 + (lane & 15)) * 8;
        const unsigned fA1 = WS_F_G + (((lane & 2) ? nhi : nlo) * 16 + qb * 2 + (lane & 1)) * 8;
        __builtin_amdgcn_s_setprio(3);
        const char* kvb = ws + WS_KV + head * 163840;
        auto kv_issue = [&](int grp) {
#pragma unroll
            for (int t = 0; t < 8; ++t) { int pc = grp * 32 + hw + 4 * t; if (pc > 157) pc = 157; glds16s(lane * 16, kvb + pc * 1024, lds0 + pc * 1024); }
        };
        if (hw == 3) glds16((lane < 50 ? lane : 49) * 16, (const char*)P.wphi + head * 800, lds0 + L_PHI);
        if (bid < 240) {
            const int rot = (nt * 3 + mt * 5 + (mt >> 1)) % 10;
            const unsigned xo = (mt * 128 + 8 * hw + (lane >> 3)) * 1280 + (((lane & 7) ^ ((4 * hw + (lane >> 4)) & 7)) << 4);
            const unsigned wo = (4 * hw + (lane >> 4)) * 3840 + nt * 256 + (((lane & 15) ^ (((lane >> 4) << 2) | hw)) << 4);
            const unsigned dX = lds0 + hw * 1024;
            auto issue = [&](int s) {
                int kt = (s >= 10 ? 19 - s : s) + rot; if (kt >= 10) kt -= 10;
                const char* xb = (const char*)P.x + kt * 128;
                const char* wb = (const char*)(s >= 10 ? P.wa : P.wb) + (size_t)kt * (64 * 3840);
                const unsigned dw = dX + 16384 + (s & 3) * 32768, dx = dX + ((s >= 10 ? 19 - s : s) & 3) * 32768;
#pragma unroll
                for (int i = 0; i < 4; ++i) glds16(wo, wb + i * 61440, dw + i * 4096);
                if (s < 10 || s >= 14) {
#pragma unroll
                    for (int i = 0; i < 4; ++i) glds16(xo, xb + i * 40960, dx + i * 4096);
                }
            };
#pragma unroll 1
            for (int n = 0; n < 20; ++n) {
                if (n == 2) { if (hw == 0 && lane < 32) *(LDS3 unsigned*)(lds + G_W + lane * 4) = 0u; GET_TAG(); WAIT_BAR(8); }
                else if (n == 3 || (n >= 11 && n <= 15)) { WAIT_BAR(8); }
                else if (n >= 5) { WAIT_BAR(16); }
                issue(n);
            }
            WAIT_BAR(16);
            WAIT_BAR(0);
        } else {
            if (hw == 0 && lane < 32) *(LDS3 unsigned*)(lds + G_W + lane * 4) = lane < 8 ? 20u : 0u;
            GET_TAG();
            WAIT_BAR(0);
            if (hw == 0) {
#pragma unroll 1
                for (int t = 0; t < PRE_SLEEP; ++t) __builtin_amdgcn_s_sleep(127);
            }
        }
        const int tokS = qb * 256 + hw * 64, m7 = tokS >> 7, t4 = (tokS >> 5) & 3;
        {
            const unsigned pt3 = (lane >> 1) & 3;
            const unsigned tb = WS_F_G + 2048 + ((((lane & 1) ? nhi : nlo) + (pt3 < 3 ? pt3 : 0) * 5) * 16 + qb * 2 + ((lane >> 3) & 1)) * 8;
            POLL_ONE(hw == 0, 8, tb, WS_F_SD + (qb * 2 + (lane & 1)) * 8, G_P1);
        }
        u32x4 qf[2][3]; float gS[2];
        {
            const unsigned om = t4 * 1024 + lane * 16, ot = 4096 + (t4 * 32 + r) * 8 + 4 * h, ov = 5120 + t4 * 1280 + h * 16 + (r < 20 ? r : 19) * 32;
            const char* kb = ws + WS_KV + (NH + head) * 163840 + (2 * (m7 & 7) + (m7 >> 3)) * 10240;
#pragma unroll
            for (int i = 0; i < 2; ++i) {
                const unsigned d = lds0 + S_AREA + (2 * hw + i) * S_STR;
                glds16s(om + i * 1024, kb, d); glds16s(ov + i * 1280, kb, d + 1024); glds16s(ov + i * 1280 + 640, kb, d + 2048); glds4s(ot + i * 256, kb, d + 3072);
            }
            const unsigned qbb = WS_Q + (NH + head) * 81920 + m7 * 5120;
            u32x4 eq[2]; unsigned eqt[2];
#pragma unroll
            for (int i = 0; i < 2; ++i) { eq[i] = LD16(qbb + om + i * 1024); eqt[i] = LD4(qbb + ot + i * 256); gS[i] = __uint_as_float(LD4(WS_G + (tokS + i * 32 + r) * 4)); }
            asm volatile("s_waitcnt vmcnt(6)" ::: "memory");
            if (lane == 0) LDS_CNT_ADD(G_B1);
            u32x4 phm; unsigned pht;
            phi_frag(phm, pht);
#pragma unroll
            for (int i = 0; i < 2; ++i) feature_map(phm, pht, eq[i], eqt[i], h, qf[i][0], qf[i][1], qf[i][2]);
        }
        STAMP(3);
        if (bid >= 240 && hw == 0) __builtin_amdgcn_s_sleep(100);
        POLL_ONE(hw == 0, PA_SLP, fA0, fA1, G_P);
        prog_wait<1>(lds + G_PRG, lane, 20);
        kv_issue(0); kv_issue(1);
        asm volatile("s_waitcnt vmcnt(6)" ::: "memory");
        if (lane == 0) LDS_CNT_ADD(G_Q);
        kv_issue(2); kv_issue(3);
        lds_wait<2>(lds + G_RD, 8);
        if (hw == 0) {
            *(LDS3 u32x4*)(lds + lane * 16) = (u32x4){0u, 0u, 0u, 0u};
            asm volatile("s_waitcnt lgkmcnt(0)" ::: "memory");
            if (lane == 0) LDS_SET(G_Z, 1);
        }
        lds_wait<2>(lds + G_Z, 1);
        DRAIN();
        if (lane == 0) LDS_CNT_ADD(C_KVL);
        lds_wait<4>(lds + G_KVP, 1);
        kv_issue(4);
        if (lane == 0) LDS_CNT_ADD(G_ST2);
        if (hw == 3) {
            lds_wait<4>(lds + G_ST2, 12);
            glds16s(lane * 16, kvb + 158 * 1024, lds0 + 158 * 1024); glds16s(lane * 16, kvb + 159 * 1024, lds0 + 159 * 1024);
        }
        DRAIN();
        if (lane == 0) LDS_CNT_ADD(C_KVL2);
        prog_wait<8>(lds + C_PR, lane, 1);
        *(LDS3 float*)(lds + X_G + (hw * 64 + lane) * 4) = h ? gS[1] : gS[0];
        STAMP(4);
        POLL_ONE(hw == 0, 4, WS_F_KV + (head * 8 + (lane & 7)) * 8, WS_F_KV + (head * 8 + (lane & 7)) * 8, C_P2);
        {
            const int qd = hw * 60 + (lane < 60 ? lane : 59);
            f32x4 pp[8];
#pragma unroll
            for (int p = 0; p < 8; ++p) pp[p] = __builtin_bit_cast(f32x4, LD16(WS_KVP + (head * 8 + p) * 4096 + qd * 16));
            const f32x4 s4 = ((pp[0] + pp[1]) + (pp[2] + pp[3])) + ((pp[4] + pp[5]) + (pp[6] + pp[7]));
            const int gq = qd < 192 ? qd >> 6 : (qd - 192) >> 4, hs = qd < 192 ? (qd >> 5) & 1 : (qd >> 3) & 1, sg = qd < 192 ? qd & 31 : 32 + (qd & 7);
            const unsigned a = pk2(s4[0], s4[1]), b = pk2(s4[2], s4[3]);
            LDS3 u16* kp = (LDS3 u16*)(lds + X_KVF + (8 * gq + 4 * hs) * 96 + sg * 2);
            kp[0] = (u16)a; kp[48] = (u16)(a >> 16); kp[96] = (u16)b; kp[144] = (u16)(b >> 16);
            if (qd >= 192) { kp[8] = 0; kp[56] = 0; kp[104] = 0; kp[152] = 0; }
            asm volatile("s_waitcnt lgkmcnt(0)" ::: "memory");
            if (lane == 0) LDS_CNT_ADD(C_KVF);
            lds_wait<2>(lds + C_KVF, 4);
        }
        {
            LDS3 const char* kp = lds + X_KVF + (r < 24 ? r : 23) * 96 + h * 16;
            const u32x4 a0 = *(LDS3 const u32x4*)kp, a1 = *(LDS3 const u32x4*)(kp + 32), a2 = *(LDS3 const u32x4*)(kp + 64);
#pragma unroll
            for (int i = 0; i < 2; ++i) {
                f32x16 d = MFMA32_16(a0, qf[i][0], z16); d = MFMA32_16(a1, qf[i][1], d); d = MFMA32_16(a2, qf[i][2], d);
                const float den = upper_half(d[8]) + 1e-6f;
                float o[12];
#pragma unroll
                for (int j = 0; j < 12; ++j) o[j] = d[j];
                store_o(rs, o, (1.f - gS[i]) / den, tokS + i * 32 + r, 1, head, h);
            }
            DRAIN();
            unsigned arr = 0;
            if (lane == 0) arr = LDS_CNT_ADD(C_OB);
            if (__builtin_amdgcn_readfirstlane(arr) == 3 && lane == 0) set_flag(ws, WS_F_OB + (qb * 32 + head) * 8, tag);
        }
        STAMP(5);
        f32x4 c[5];
#pragma unroll
        for (int n = 0; n < 5; ++n) c[n] = (f32x4){0.f, 0.f, 0.f, 0.f};
        const char* wsrc = ws + WS_WT + (size_t)ntO * 5 * 40960;
        {
            if (hw == 0) wait_flags<8>(ws, WS_F_KV + lane * 8, WS_F_KV + 512 + lane * 8, tag);
            POLL_ONE(hw == 0, 8, WS_F_KV + 1024 + lane * 8, WS_F_KV + 1536 + lane * 8, C_P5);
#pragma unroll 1
            for (int t = 0; t < 5; ++t) {
                if (t == 0) prog_wait<8>(lds + C_PR, lane, 4); else if (t == 3) prog_wait<4>(lds + C_PR, lane, 5);
                const int ks = hw + 4 * t < 19 ? hw + 4 * t : 18;
#pragma unroll
                for (int n = 0; n < 5; ++n) glds16s(lane * 16, wsrc + (n * 40 + 20 + ks) * 1024, lds0 + 4096 + (ks * 5 + n) * 1024);
            }
            POLL_ONE(hw == 0, 4, WS_F_OB + ((mtO >> 2) * 32 + r) * 8, WS_F_OB + ((mtO >> 2) * 32 + r) * 8, C_P3);
            const unsigned ab = WS_AO + ((mtO * 4 + hw) * 40 + 20) * 1024;
            u32x4 af[20];
#pragma unroll
            for (int ks = 0; ks < 16; ++ks) af[ks] = LD16(ab + ks * 1024 + lane * 16);
#pragma unroll
            for (int ks = 16; ks < 20; ++ks) LD_TAIL(af[ks], ab + ks * 1024);
            asm volatile("s_waitcnt vmcnt(20)" ::: "memory");
            if (lane == 0) LDS_CNT_ADD(C_BX);
            lds_wait<2>(lds + C_BX, 4);
            u32x4 wf[3][5], wl[5];
            LDS3 const char* wp = lds + 4096 + lane * 16;
#pragma unroll
            for (int ks = 0; ks < 2; ++ks)
#pragma unroll
                for (int n = 0; n < 5; ++n) wf[ks][n] = *(LDS3 const u32x4*)(wp + (ks * 5 + n) * 1024);
#pragma unroll
            for (int ks = 0; ks < 20; ++ks) {
                if (ks + 2 < 19) {
#pragma unroll
                    for (int n = 0; n < 5; ++n) wf[(ks + 2) % 3][n] = *(LDS3 const u32x4*)(wp + ((ks + 2) * 5 + n) * 1024);
                }
                if (ks == 8) {
#pragma unroll
                    for (int n = 0; n < 5; ++n) wl[n] = LD16(WS_WT + ((ntO * 5 + n) * 40 + 39) * 1024 + lane * 16);
                }
                SBAR();
#pragma unroll
                for (int n = 0; n < 5; ++n) c[n] = MFMA16_32(ks < 19 ? wf[ks % 3][n] : wl[n], af[ks], c[n]);
                SBAR();
            }
            if (lane == 0) LDS_CNT_ADD(C_BX);
            lds_wait<2>(lds + C_BX, 8);
        }
        STAMP(6);
        prog_wait<4>(lds + C_PR, lane, 7);
#pragma unroll 1
        for (int n = 0; n < 5; ++n)
#pragma unroll
            for (int t = 0; t < 5; ++t) { const int ks = hw + 4 * t; glds16s(lane * 16, wsrc + (n * 40 + ks) * 1024, lds0 + L_WA + (n * 20 + ks) * 1024); }
#pragma unroll
        for (int n = 0; n < 3; ++n) *(LDS3 f32x4*)(lds + X_FIN + hw * 3072 + n * 1024 + lane * 16) = c[n];
        DRAIN();
        if (lane == 0) LDS_CNT_ADD(C_WA);
        prog_wait<4>(lds + C_PR, lane, 8);
#pragma unroll
        for (int n = 3; n < 5; ++n) *(LDS3 f32x4*)(lds + 143360 + hw * 2048 + (n - 3) * 1024 + lane * 16) = c[n];
        asm volatile("s_waitcnt lgkmcnt(0)" ::: "memory");
        if (lane == 0) __hip_atomic_store((LDS3 unsigned*)(lds + C_BP + hw * 4), 1u, __ATOMIC_RELAXED, __HIP_MEMORY_SCOPE_WORKGROUP);
        STAMP(7);
        asm volatile("mega_code_end2:");
    } else {
        const int qs = w & 3, kh = w >> 2;
        const int q0 = qb * 256 + qs * 64;
        {
            unsigned sink;
            if (w == 0 && (WARM_MODE & 1)) {
                const char* pc0; int len, len2;
                asm volatile("s_getpc_b64 %0\nmega_code_mark:\n\ts_mov_b32 %1, mega_code_end-mega_code_mark\n\ts_mov_b32 %2, mega_code_end2-mega_code_mark" : "=s"(pc0), "=s"(len), "=s"(len2));
                len = len > len2 ? len : len2;
                const int o = ((bid >> 3) * 64 + lane) * 64;
                if (o + 4 <= len) asm volatile("global_load_dword %0, %1, %2" : "=v"(sink) : "v"(o), "s"(pc0) : "memory");
            } else if (w >= 1 && w <= 6 && (WARM_MODE & 2)) {
                const unsigned o = ((w - 1) * 64 + lane) * 65536 + 65532;
                if (o < WS_END) asm volatile("global_load_dword %0, %1, %2 sc1" : "=v"(sink) : "v"(o), "s"(ws) : "memory");
            } else if (w == 7 && (WARM_MODE & 2)) {
                if (lane < 40) asm volatile("global_load_dword %0, %1, %2 sc1" : "=v"(sink) : "v"(lane * 65536), "s"(P.out) : "memory");
            }
            asm volatile("s_waitcnt vmcnt(0)" ::: "memory");
        }
        if (bid < 160) {
            const int hf = bid >= 80, k0 = (bid - 80 * hf) * 8, hd = k0 < 512 ? k0 >> 4 : (k0 - 512) >> 2;
            const int row = k0 < 512 ? 20 * hd + (k0 & 8) + 4 * (hd & 1) : 20 * hd + 16;
            const u16* wsrc = (hf ? P.wob : P.woa) + (size_t)row * DM;
#pragma unroll
            for (int q = 0; q < 2; ++q) {
                const int n = mtid + 512 * q;
                if (n < 640) {
                    unsigned f[8];
#pragma unroll
                    for (int j = 0; j < 8; ++j) f[j] = wsrc[j * DM + n];
                    const u32x4 v = {f[0] | (f[1] << 16), f[2] | (f[3] << 16), f[4] | (f[5] << 16), f[6] | (f[7] << 16)};
                    ST16(v, WS_WT + (((n >> 4) * 40 + hf * 20 + (k0 >> 5)) * 64 + ((k0 >> 3) & 3) * 16 + (n & 15)) * 16);
                }
            }
        }
        GET_TAG();
        if (bid < 240) {
            const int part = nt / 5, c0 = (nt - part * 5) * 128;
            const int wm = w & 1, wn = w >> 1;
            constexpr int RB = 0;
            const int xr = wm * 64 + r;
            const unsigned xa = RB + xr * 128 + ((h ^ ((xr >> 1) & 7)) << 4);
            unsigned wt[2];
#pragma unroll
            for (int t = 0; t < 2; ++t) {
                const int q = (lane & 15) >> 2, p = lane & 3, blk = (lane >> 4) & 1, row = 8 * h + 4 * t + q;
                wt[t] = RB + 16384 + row * 256 + (((4 * wn + 2 * blk + (p >> 1)) ^ ((q << 2) | (2 * h + t))) << 4) + 8 * (p & 1);
            }
            auto gbody = [&](auto trc) {
                constexpr bool TR = decltype(trc)::value;
                u32x4 xf[2][8], wf[2][4];
                auto rdk = [&](auto bc, LDS3 const char* sx, LDS3 const char* sb, int ks) {
                    constexpr int B = decltype(bc)::value;
                    xf[B][2 * ks] = *(LDS3 const u32x4*)(sx + (xa ^ (ks * 32)));
                    xf[B][2 * ks + 1] = *(LDS3 const u32x4*)(sx + ((xa + 4096) ^ (ks * 32)));
                    const u32x2 lo = trread(sb + wt[0] + ks * 4096), hi = trread(sb + wt[1] + ks * 4096);
                    wf[B][ks] = (u32x4){lo[0], lo[1], hi[0], hi[1]};
                };
                f32x16 acc0, acc1;
                auto step = [&](auto bc, int s) {
                    constexpr int B = decltype(bc)::value;
                    if (s == GA_PUB) {
                        DRAIN();
                        unsigned arr = 0;
                        if (lane == 0) arr = LDS_CNT_ADD(G_ST);
                        if (__builtin_amdgcn_readfirstlane(arr) == 7 && lane == 0) set_flag(ws, fG + 2048, tag);
                    }
                    __builtin_amdgcn_s_waitcnt(0xC07F);
                    if (s < 18) BAR();
                    else if (lane == 0) LDS_SET(G_PRG + w * 4, s + 1);
                    SBAR();
                    const int s1 = s + 1;
                    LDS3 const char* sb = lds + __builtin_amdgcn_readfirstlane((s1 & 3) * 32768);
                    LDS3 const char* sx = lds + __builtin_amdgcn_readfirstlane(((s1 >= 10 ? 19 - s1 : s1) & 3) * 32768);
#define GM(A, i) do { if constexpr (TR) A = MFMA32_16(wf[B][(i) >> 1], xf[B][i], A); else A = MFMA32_16(xf[B][i], wf[B][(i) >> 1], A); SBAR(); } while (0)
#define GX(i) do { xf[B ^ 1][i] = *(LDS3 const u32x4*)(sx + ((xa + ((i) & 1) * 4096) ^ (((i) >> 1) * 32))); } while (0)
#define GW(ks) do { const u32x2 lo = trread(sb + wt[0] + (ks) * 4096), hi = trread(sb + wt[1] + (ks) * 4096); wf[B ^ 1][ks] = (u32x4){lo[0], lo[1], hi[0], hi[1]}; } while (0)
                    GM(acc0, 0); GX(0); GW(0); SBAR(); GM(acc1, 1); GX(1); GX(2); SBAR(); GM(acc0, 2); GW(1); GX(3); SBAR(); GM(acc1, 3); GX(4); GW(2); SBAR();
                    GM(acc0, 4); GX(5); GX(6); SBAR(); GM(acc1, 5); GW(3); GX(7); SBAR(); GM(acc0, 6); GM(acc1, 7);
#undef GX
#undef GW
#undef GM
                };
                WAIT_BAR(63);
#pragma unroll
                for (int ks = 0; ks < 4; ++ks) rdk(std::integral_constant<int, 0>{}, lds, lds, ks);
#pragma unroll 1
                for (int pass = 0; pass < 2; ++pass) {
                    const int mat = pass ^ 1;
                    acc0 = (f32x16){}; acc1 = (f32x16){};
#pragma unroll 1
                    for (int kk = 0; kk < 10; kk += 2) {
                        step(std::integral_constant<int, 0>{}, pass * 10 + kk);
                        step(std::integral_constant<int, 1>{}, pass * 10 + kk + 1);
                    }
                    const int cpos = 2 * (mt & 7) + (mt >> 3);
                    if constexpr (TR) {
                        const float sc = (part == 0 && mat == 0) ? QSCALE : 1.f;
                        const unsigned base = part == 0 ? WS_Q + mat * NH * 81920 + mt * 5120 : WS_KV + mat * NH * 163840 + cpos * 10240;
                        const unsigned hs = part == 0 ? 81920 : 163840;
#pragma unroll
                        for (int pr = 0; pr < 2; ++pr) {
                            const int G8 = ((c0 + 32 * wn) >> 3) + 2 * pr + h, pp = G8 / 5, gi = G8 - pp * 5;
                            const unsigned hb0 = base + (2 * pp + (gi > 2)) * hs;
#pragma unroll
                            for (int i = 0; i < 2; ++i) {
                                const f32x16& a = i ? acc1 : acc0;
                                const int tl = wm * 64 + i * 32 + r;
                                const auto s0 = __builtin_amdgcn_permlane32_swap(pk2(a[8 * pr] * sc, a[8 * pr + 1] * sc), pk2(a[8 * pr + 4] * sc, a[8 * pr + 5] * sc), false, false);
                                const auto s1 = __builtin_amdgcn_permlane32_swap(pk2(a[8 * pr + 2] * sc, a[8 * pr + 3] * sc), pk2(a[8 * pr + 6] * sc, a[8 * pr + 7] * sc), false, false);
                                if (gi != 2) { const u32x4 ov = {s0[0], s1[0], s0[1], s1[1]}; ST16(ov, hb0 + (tl >> 5) * 1024 + (gi > 2 ? gi - 3 : gi) * 512 + r * 16); }
                                else { const u32x2 o0 = {s0[0], s1[0]}, o1 = {s0[1], s1[1]}; ST8(o0, hb0 + 4096 + tl * 8); ST8(o1, hb0 + hs + 4096 + tl * 8); }
                            }
                        }
                    } else {
                        const int n = c0 + 32 * wn + r, hd = n / 20, d = n - hd * 20, rho = (hd & 1) ? (d < 4 ? d + 16 : d - 4) : d;
                        const unsigned base = WS_KV + (mat * NH + hd) * 163840 + cpos * 10240 + 5120 + h * 16 + rho * 32;
#pragma unroll
                        for (int i = 0; i < 2; ++i)
#pragma unroll
                            for (int s2 = 0; s2 < 2; ++s2) {
                                const f32x16& a = i ? acc1 : acc0;
                                u32x4 ov = {pk2(a[8 * s2], a[8 * s2 + 1]), pk2(a[8 * s2 + 2], a[8 * s2 + 3]), pk2(a[8 * s2 + 4], a[8 * s2 + 5]), pk2(a[8 * s2 + 6], a[8 * s2 + 7])};
                                ST16(ov, base + (wm * 4 + i * 2 + s2) * 640);
                            }
                    }
                    if (pass == 0) STAMP(1);
                }
            };
            if (part < 2) gbody(std::true_type{}); else gbody(std::false_type{});
        } else {
            const int sj = bid - 240;
            WAIT_BAR(63);
            {
                const u32x4 ga = *(const u32x4*)(P.gw + lane * 8), gb = *(const u32x4*)(P.gw + 512 + (lane & 15) * 8);
                u32x4 xa[16], xb[4];
                const u16* xr = P.x + (size_t)(sj * 128 + w * 16) * DM;
#pragma unroll
                for (int rr = 0; rr < 16; ++rr) xa[rr] = *(const u32x4*)(xr + rr * DM + lane * 8);
#pragma unroll
                for (int i = 0; i < 4; ++i) xb[i] = *(const u32x4*)(xr + (i * 4 + (lane >> 4)) * DM + 512 + (lane & 15) * 8);
                float res = 0.f;
#pragma unroll
                for (int rr = 0; rr < 16; ++rr) {
                    float s = 0.f, s2 = 0.f;
#pragma unroll
                    for (int j = 0; j < 4; ++j) { s += bf_lo(xa[rr][j]) * bf_lo(ga[j]) + bf_hi(xa[rr][j]) * bf_hi(ga[j]); s2 += bf_lo(xb[rr >> 2][j]) * bf_lo(gb[j]) + bf_hi(xb[rr >> 2][j]) * bf_hi(gb[j]); }
                    if ((lane >> 4) == (rr & 3)) s += s2;
#pragma unroll
                    for (int o = 32; o >= 1; o >>= 1) s += __shfl_xor(s, o);
                    if (lane == rr) res = s;
                }
                if (lane < 16) ST4(__float_as_uint(1.f / (1.f + __expf(-res))), WS_G + (sj * 128 + w * 16 + lane) * 4);
            }
            DRAIN();
            unsigned arr = 0;
            if (lane == 0) arr = LDS_CNT_ADD(G_ST);
            if (__builtin_amdgcn_readfirstlane(arr) == 7 && lane == 0) set_flag(ws, WS_F_SD + sj * 8, tag);
        }
        STAMP(2);
        {
            {
                DRAIN();
                unsigned arr = 0;
                if (lane == 0) arr = LDS_CNT_ADD(G_ST3);
                if (__builtin_amdgcn_readfirstlane(arr) == 7 && lane == 0 && bid < 240) set_flag(ws, fG, tag);
            }
            lds_wait(lds + G_B1, 4);
            LDS3 char* sp = lds + S_AREA + w * S_STR;
            const u32x4 e0 = *(LDS3 const u32x4*)(sp + lane * 16);
            u32x4 w0 = *(LDS3 const u32x4*)(sp + 1024 + lane * 16), w1 = *(LDS3 const u32x4*)(sp + 2048 + lane * 16);
            const unsigned et = *(LDS3 const unsigned*)(sp + 3072 + lane * 4);
            if (r >= 20) { w0 = (u32x4){one, one, one, one}; w1 = w0; }
            u32x4 fp, fn, ft, phm; unsigned pht;
            phi_frag(phm, pht);
            feature_map(phm, pht, e0, et, h, fp, fn, ft);
            u32x4 i0, i1;
#pragma unroll
            for (int j = 0; j < 4; ++j) {
                const int e = r - 8 * h - 2 * j;
                i0[j] = e == 0 ? 0x3f80u : (e == 1 ? 0x3f800000u : 0u);
                i1[j] = e == 16 ? 0x3f80u : (e == 17 ? 0x3f800000u : 0u);
            }
            f32x16 T0 = MFMA32_16(fp, i0, z16); T0 = MFMA32_16(fn, i1, T0);
            f32x16 T1 = MFMA32_16(ft, i0, z16);
            u32x4 b00, b01, b10, b11;
#pragma unroll
            for (int j = 0; j < 4; ++j) {
                b00[j] = pk2(T0[2 * j], T0[2 * j + 1]); b01[j] = pk2(T0[8 + 2 * j], T0[9 + 2 * j]);
                b10[j] = pk2(T1[2 * j], T1[2 * j + 1]); b11[j] = pk2(T1[8 + 2 * j], T1[9 + 2 * j]);
            }
            f32x16 k0 = MFMA32_16(w0, b00, z16); k0 = MFMA32_16(w1, b01, k0);
            f32x16 k1 = MFMA32_16(w0, b10, z16); k1 = MFMA32_16(w1, b11, k1);
#pragma unroll
            for (int gq = 0; gq < 3; ++gq) {
                *(LDS3 f32x4*)(sp + gq * 1024 + lane * 16) = (f32x4){k0[4 * gq], k0[4 * gq + 1], k0[4 * gq + 2], k0[4 * gq + 3]};
                if (r < 8) *(LDS3 f32x4*)(sp + 3072 + (gq * 16 + h * 8 + r) * 16) = (f32x4){k1[4 * gq], k1[4 * gq + 1], k1[4 * gq + 2], k1[4 * gq + 3]};
            }
            asm volatile("s_waitcnt lgkmcnt(0)" ::: "memory");
            unsigned arr = 0;
            if (lane == 0) arr = LDS_CNT_ADD(G_KV);
            if (__builtin_amdgcn_readfirstlane(arr) == 7) {
                __builtin_amdgcn_fence(__ATOMIC_ACQUIRE, "wavefront");
#pragma unroll
                for (int i = 0; i < 4; ++i) {
                    const int qd = lane + 64 * i < 240 ? lane + 64 * i : 239;
                    f32x4 s = {0.f, 0.f, 0.f, 0.f};
#pragma unroll
                    for (int p = 0; p < 8; ++p) s += *(LDS3 const f32x4*)(lds + S_AREA + p * S_STR + qd * 16);
                    ST16(__builtin_bit_cast(u32x4, s), WS_KVP + (head * 8 + qb) * 4096 + qd * 16);
                }
                asm volatile("s_waitcnt lgkmcnt(0)" ::: "memory");
                if (lane == 0) LDS_SET(G_KVP, 1);
                DRAIN();
                if (lane == 0) set_flag(ws, WS_F_KV + (head * 8 + qb) * 8, tag);
            }
        }
        STAMP(3);
        const unsigned hb = lds0 + kh * 10240;
        const unsigned laneK = hb + lane * 16, laneT = hb + 4096 + r * 8 + h * 4, laneV = hb + 5120 + h * 16 + (r < 20 ? r : 19) * 32;
        const u64 vmask = 0x000fffff000fffffull;
        u32x4 v0a = {one, one, one, one}, v0b = v0a, v1 = v0a;
        u32x4 qA, qB, ka; unsigned qtA, qtB, kt;
        {
            lds_wait(lds + G_P, 1);
            const unsigned qp = WS_Q + head * 81920 + (q0 >> 7) * 5120;
            const unsigned om = (qs & 1) * 2048 + lane * 16, ot = 4096 + ((qs & 1) * 64 + r) * 8 + 4 * h;
            qA = LD16(qp + om); qB = LD16(qp + om + 1024); qtA = LD4(qp + ot); qtB = LD4(qp + ot + 256);
            lds_wait(lds + G_Q, 4);
            DSR128(ka, laneK, 0); DSR32(kt, laneT, 0); DSR128M2(v0a, laneV, 0, v1, laneV, 640);
            asm volatile("s_waitcnt lgkmcnt(0)" : "+v"(ka), "+v"(kt), "+v"(v0a), "+v"(v1) :: "memory");
            if (lane == 0) LDS_CNT_ADD(G_RD);
        }
        STAMP(5);
        float mA, mB; u32x2 qTA, qTB; f32x16 sA, sB;
        {
            const u32x2 t0 = {kt, 0x3f80u}, a0 = {qtA, 0u}, b0 = {qtB, 0u};
            const f32x16 s = MFMA32_16(ka, qA, z16);
            const f32x16 s0 = MFMA32_8(t0, a0, s);
            f32x16 s1 = MFMA32_16(ka, qB, z16); s1 = MFMA32_8(t0, b0, s1);
            float x0 = s0[0], x1 = s1[0];
#pragma unroll
            for (int i = 1; i < 16; ++i) { x0 = fmaxf(x0, s0[i]); x1 = fmaxf(x1, s1[i]); }
            x0 = -xhalf_max(x0); x1 = -xhalf_max(x1);
            const unsigned hh = pk2(x0, x1);
            const unsigned ll = pk2(x0 - bf_lo(hh), x1 - bf_hi(hh));
            mA = -(bf_lo(hh) + bf_lo(ll)); mB = -(bf_hi(hh) + bf_hi(ll));
            const unsigned sel = h ? ll : hh;
            qTA = (u32x2){qtA, sel & 0xffffu}; qTB = (u32x2){qtB, sel >> 16};
            sA = MFMA32_8(t0, qTA, s);
        }
        f32x16 oA = {}, oB = {};
        u32x4 pa, pb = {0u, 0u, 0u, 0u};
#define EX4(S, i, Pv, j) do { Pv[j] = pk2(ex2(S[i]), ex2(S[i + 1])); Pv[j + 1] = pk2(ex2(S[i + 2]), ex2(S[i + 3])); PIN(Pv); SBAR(); } while (0)
#define KT2 ((u32x2){kt, 0x3f80u})
#define TILE(V0C, V0N, aK1, oK1, aT1, oT1, oV1, aV0, oV0) do { \
            sB = MFMA32_16(ka, qB, z16); SBAR(); DSR128(ka, aK1, oK1); EX4(sA, 0, pa, 0); \
            oB = MFMA32_16(v1, pb, oB); SBAR(); DSR128M2(v1, aV, oV1, V0N, aV0, oV0); EX4(sA, 4, pa, 2); \
            sB = MFMA32_8(KT2, qTB, sB); SBAR(); DSR32(kt, aT1, oT1); EX4(sA, 8, pb, 0); \
            oA = MFMA32_16(V0C, pa, oA); SBAR(); EX4(sA, 12, pb, 2); \
            LWAIT(1); sA = MFMA32_16(ka, qA, z16); SBAR(); EX4(sB, 0, pa, 0); \
            oA = MFMA32_16(v1, pb, oA); SBAR(); EX4(sB, 4, pa, 2); \
            LWAIT(0); sA = MFMA32_8(KT2, qTA, sA); SBAR(); EX4(sB, 8, pb, 0); \
            oB = MFMA32_16(V0C, pa, oB); SBAR(); EX4(sB, 12, pb, 2); \
        } while (0)
        unsigned aK = laneK, aT = laneT, aV = laneV;
#pragma unroll 1
        for (int c = 0; c < 8; ++c) {
            if (c == 1) lds_wait(lds + C_KVL, 4);
            else if (c == 5) lds_wait(lds + C_KVL2, 4);
            if (c >= 1) {
                unsigned pp;
                asm volatile("ds_write_b32 %1, %2\n\tds_read_b32 %0, %3\n\ts_waitcnt lgkmcnt(0)" : "=&v"(pp) : "v"(lds0 + C_PR + w * 4), "v"(c), "v"(lds0 + C_PR + (w ^ 4) * 4) : "memory");
                const int pc = __builtin_amdgcn_readfirstlane(pp);
                if (pc > c || (pc == c && ((c ^ kh) & 1))) __builtin_amdgcn_s_setprio(1); else __builtin_amdgcn_s_setprio(0);
            } else if (kh) __builtin_amdgcn_s_setprio(1);
            const unsigned nx = (c < 7 ? c + 1 : 7) * 20480;
            const unsigned nK = laneK + nx, nT = laneT + nx, nV = laneV + nx;
            SBAR();
            TILE(v0a, v0b, aK, 1024, aT, 256, 640,  aV, 1280);
            TILE(v0b, v0a, aK, 2048, aT, 512, 1920, aV, 2560);
            if (c == 0) {
                lds_wait(lds + G_Z, 1);
                if (lane == 0) LDS_CNT_ADD(G_ST2);
                asm volatile("s_waitcnt lgkmcnt(0)" ::: "memory");
                SBAR();
            }
            TILE(v0a, v0b, aK, 3072, aT, 768, 3200, aV, 3840);
            TILE(v0b, v0a, nK, 0,    nT, 0,   4480, nV, 0);
            aK = nK; aT = nT; aV = nV;
        }
        oB = MFMA32_16(v1, pb, oB);
#undef TILE
#undef EX4
        if (lane == 0) __hip_atomic_store((LDS3 unsigned*)(lds + C_PR + w * 4), 8u, __ATOMIC_RELAXED, __HIP_MEMORY_SCOPE_WORKGROUP);
        STAMP(6);
        {
            float chk = 0.f;
#pragma unroll
            for (int i = 0; i < 12; ++i) chk += fabsf(oA[i]) * 0.f + fabsf(oB[i]) * 0.f;
            bool bad = !(chk == 0.f);
            if (h) bad = bad || !(oA[8] >= 0x1p-60f && oA[8] <= 0x1p60f && oB[8] >= 0x1p-60f && oB[8] <= 0x1p60f);
            if (__builtin_expect(__any(bad), 0)) {
                mA = -INFINITY; mB = -INFINITY;
#pragma unroll
                for (int i = 0; i < 16; ++i) { oA[i] = 0.f; oB[i] = 0.f; }
                const u32x2 a0 = {qtA, 0u}, b0 = {qtB, 0u};
#pragma unroll 1
                for (int t = 0; t < 32; ++t) {
                    const unsigned kb = WS_KV + head * 163840 + (2 * (t >> 2) + kh) * 10240, t4 = t & 3;
                    const u32x4 k0 = LD16(kb + t4 * 1024 + lane * 16);
                    const u32x2 t0 = {LD4(kb + 4096 + (t4 * 32 + r) * 8 + 4 * h), 0u};
                    u32x4 w0 = LD16(kb + 5120 + t4 * 1280 + h * 16 + (r < 20 ? r : 19) * 32), w1 = LD16(kb + 5120 + t4 * 1280 + 640 + h * 16 + (r < 20 ? r : 19) * 32);
                    if (r >= 20) { w0 = (u32x4){one, one, one, one}; w1 = w0; }
                    f32x16 s0 = MFMA32_16(k0, qA, z16); s0 = MFMA32_8(t0, a0, s0);
                    f32x16 s1 = MFMA32_16(k0, qB, z16); s1 = MFMA32_8(t0, b0, s1);
                    float x0 = s0[0], x1 = s1[0];
#pragma unroll
                    for (int i = 1; i < 16; ++i) { x0 = fmaxf(x0, s0[i]); x1 = fmaxf(x1, s1[i]); }
                    x0 = fmaxf(xhalf_max(x0), mA); x1 = fmaxf(xhalf_max(x1), mB);
                    const float g0 = (mA == x0) ? 1.f : ex2(mA - x0), g1 = (mB == x1) ? 1.f : ex2(mB - x1);
                    mA = x0; mB = x1;
#pragma unroll
                    for (int i = 0; i < 16; ++i) { oA[i] *= g0; oB[i] *= g1; }
#pragma unroll
                    for (int i = 0; i < 4; ++i) { pa[i] = pk2(ex2(s0[2 * i] - x0), ex2(s0[2 * i + 1] - x0)); pb[i] = pk2(ex2(s0[8 + 2 * i] - x0), ex2(s0[9 + 2 * i] - x0)); }
                    oA = MFMA32_16(w0, pa, oA); oA = MFMA32_16(w1, pb, oA);
#pragma unroll
                    for (int i = 0; i < 4; ++i) { pa[i] = pk2(ex2(s1[2 * i] - x1), ex2(s1[2 * i + 1] - x1)); pb[i] = pk2(ex2(s1[8 + 2 * i] - x1), ex2(s1[9 + 2 * i] - x1)); }
                    oB = MFMA32_16(w0, pa, oB); oB = MFMA32_16(w1, pb, oB);
                }
            }
        }
        lds_wait<4>(lds + C_BX, 8);
        const float gO = *(LDS3 const float*)(lds + X_G + (qs * 64 + kh * 32 + r) * 4);
        {
            LDS3 char* xp = lds + X_MRG + w * 2816;
            const f32x16& s = kh ? oA : oB;
            *(LDS3 f32x4*)(xp + lane * 16) = (f32x4){s[0], s[1], s[2], s[3]};
            *(LDS3 f32x4*)(xp + 1024 + lane * 16) = (f32x4){s[4], s[5], s[6], s[7]};
            if (!h) *(LDS3 f32x4*)(xp + 2048 + r * 16) = (f32x4){s[8], s[9], s[10], s[11]};
            else *(LDS3 f32x2*)(xp + 2560 + r * 8) = (f32x2){s[8], kh ? mA : mB};
            asm volatile("s_waitcnt lgkmcnt(0)" ::: "memory");
            if (lane == 0) __hip_atomic_store((LDS3 unsigned*)(lds + C_MRG + w * 4), 1u, __ATOMIC_RELAXED, __HIP_MEMORY_SCOPE_WORKGROUP);
        }
        lds_wait<2>(lds + C_MRG + (w ^ 4) * 4, 1);
        bool lastw;
        {
            LDS3 const char* xp = lds + X_MRG + (w ^ 4) * 2816;
            const f32x4 p0 = *(LDS3 const f32x4*)(xp + lane * 16), p1 = *(LDS3 const f32x4*)(xp + 1024 + lane * 16), p2 = *(LDS3 const f32x4*)(xp + 2048 + r * 16);
            const f32x2 lm = *(LDS3 const f32x2*)(xp + 2560 + r * 8);
            const f32x16& s = kh ? oB : oA;
            const float mo = kh ? mB : mA, mp = lm[1];
            const float mx = fmaxf(mo, mp);
            const float fo = (mo == mx) ? 1.f : ex2(mo - mx), fq = (mp == mx) ? 1.f : ex2(mp - mx);
            float o[12];
#pragma unroll
            for (int i = 0; i < 4; ++i) { o[i] = s[i] * fo + p0[i] * fq; o[4 + i] = s[4 + i] * fo + p1[i] * fq; o[8 + i] = s[8 + i] * fo + p2[i] * fq; }
            const float l = upper_half(s[8]) * fo + lm[0] * fq;
            store_o(rs, o, gO / l, q0 + kh * 32 + r, 0, head, h);
            DRAIN();
            unsigned arr = 0;
            if (lane == 0) arr = LDS_CNT_ADD(C_OA);
            lastw = __builtin_amdgcn_readfirstlane(arr) == 7;
            if (lastw && lane == 0) set_flag(ws, WS_F_OA + (qb * 32 + head) * 8, tag);
        }
        STAMP(7);
        {
            const int c16 = lane & 15, g = lane >> 4, tt = w & 3, k0 = kh * 10;
            POLL_ONE(lastw, 4, WS_F_OA + ((mtO >> 2) * 32 + r) * 8, WS_F_OA + ((mtO >> 2) * 32 + r) * 8, C_P4);
            STAMP(9);
            const unsigned ab = WS_AO + ((mtO * 4 + tt) * 40 + k0) * 1024;
            u32x4 af[10];
#pragma unroll
            for (int ks = 0; ks < 6; ++ks) af[ks] = LD16(ab + ks * 1024 + lane * 16);
            if (kh) {
#pragma unroll
                for (int ks = 6; ks < 10; ++ks) LD_TAIL(af[ks], ab + ks * 1024);
            } else {
#pragma unroll
                for (int ks = 6; ks < 10; ++ks) af[ks] = LD16(ab + ks * 1024 + lane * 16);
            }
            lds_wait(lds + C_WA, 4);
            lds_wait(lds + C_OA, 8);
            f32x4 c[5];
#pragma unroll
            for (int n = 0; n < 5; ++n) c[n] = (f32x4){0.f, 0.f, 0.f, 0.f};
#pragma unroll
            for (int ks = 0; ks < 10; ++ks)
#pragma unroll
                for (int n = 0; n < 5; ++n) c[n] = MFMA16_32(*(LDS3 const u32x4*)(lds + L_WA + (n * 20 + k0 + ks) * 1024 + lane * 16), af[ks], c[n]);
            LDS3 char* xp = lds + X_MRG + tt * 5120 + lane * 16;
            if (w < 4) { *(LDS3 f32x4*)(xp + 3072) = c[3]; *(LDS3 f32x4*)(xp + 4096) = c[4]; }
            else { *(LDS3 f32x4*)(xp) = c[0]; *(LDS3 f32x4*)(xp + 1024) = c[1]; *(LDS3 f32x4*)(xp + 2048) = c[2]; }
            asm volatile("s_waitcnt lgkmcnt(0)" ::: "memory");
            if (lane == 0) __hip_atomic_store((LDS3 unsigned*)(lds + C_FIN + w * 4), 1u, __ATOMIC_RELAXED, __HIP_MEMORY_SCOPE_WORKGROUP);
            lds_wait(lds + C_FIN + (w ^ 4) * 4, 1);
            lds_wait(lds + C_BP + tt * 4, 1);
            u16* op = P.out + (size_t)(mtO * 64 + tt * 16 + c16) * DM + ntO * 80 + g * 4;
#pragma unroll
            for (int n = 0; n < 5; ++n) {
                if ((w < 4) == (n < 3)) {
                    const f32x4 bp = *(LDS3 const f32x4*)(n < 3 ? lds + X_FIN + tt * 3072 + n * 1024 + lane * 16 : lds + 143360 + tt * 2048 + (n - 3) * 1024 + lane * 16);
                    const f32x4 s = c[n] + *(LDS3 const f32x4*)(xp + n * 1024) + bp;
                    *(u32x2*)(op + n * 16) = (u32x2){pk2(s[0], s[1]), pk2(s[2], s[3])};
                }
            }
            if (bid == 0 && mtid == 0) set_flag(ws, WS_EPOCH, tag);
        }
        STAMP(10);
        asm volatile("mega_code_end:");
    }
}

extern "C" void kernel_launch(void* const* d_in, const int* in_sizes, int n_in, void* d_out, int out_size, void* d_ws, size_t ws_size,
                              hipStream_t stream) {
    Params P;
    P.x = (const u16*)d_in[0]; P.wa = (const u16*)d_in[1]; P.woa = (const u16*)d_in[2]; P.wb = (const u16*)d_in[4];
    P.wphi = (const u16*)d_in[5]; P.wob = (const u16*)d_in[6]; P.gw = (const u16*)d_in[7]; P.ws = (char*)d_ws; P.out = (u16*)d_out;
    mega<<<256, 768, 0, stream>>>(P);
}
```

```cpp
#include <hip/hip_runtime.h>
#include <hip/hip_bf16.h>
#include <stdint.h>
#include <type_traits>

typedef __attribute__((ext_vector_type(8))) short bf16x8;
typedef __attribute__((ext_vector_type(4))) short bf16x4;
typedef __attribute__((ext_vector_type(16))) float f32x16;
typedef __attribute__((ext_vector_type(4))) float f32x4;
typedef __attribute__((ext_vector_type(2))) float f32x2;
typedef __attribute__((ext_vector_type(4))) unsigned u32x4;
typedef __attribute__((ext_vector_type(2))) unsigned u32x2;
typedef __attribute__((ext_vector_type(2))) __bf16 bf2_t;
typedef unsigned short u16;
typedef unsigned long long u64;
#define LDS3 __attribute__((address_space(3)))
typedef __attribute__((address_space(1))) u64 gu64;
#define RLX_AGENT __ATOMIC_RELAXED, __HIP_MEMORY_SCOPE_AGENT

#define PRIO_MODE 3
#define WARM_MODE 1
#define GA_PUB 11
#define PHI_ROUND 0
#define PRE_SLEEP 2
constexpr int SEQ = 2048, DM = 640, NH = 32;
constexpr float LOG2E = 1.4426950408889634f;
constexpr float QSCALE = 0.22360679774997896f * LOG2E;

constexpr unsigned WS_EPOCH = 0;
constexpr unsigned WS_F_G   = 256;
constexpr unsigned WS_F_SD  = WS_F_G + 4096;
constexpr unsigned WS_F_KV  = WS_F_SD + 256;
constexpr unsigned WS_F_OB  = WS_F_KV + 2048;
constexpr unsigned WS_F_OA  = WS_F_OB + 2048;
constexpr unsigned WS_G     = 16384;
constexpr unsigned WS_Q     = 32768;
constexpr unsigned WS_KV    = WS_Q + 2 * NH * 81920;
constexpr unsigned WS_KVP   = WS_KV + 2 * NH * 163840;
constexpr unsigned WS_AO    = WS_KVP + NH * 8 * 4096;
constexpr unsigned WS_WT    = WS_AO + SEQ * 1280 * 2;
constexpr unsigned WS_END   = WS_WT + 1280 * 640 * 2;

#define STAMP(i)

__device__ __forceinline__ unsigned pk2(float a, float b) { f32x2 v = {a, b}; bf2_t r = __builtin_convertvector(v, bf2_t); return __builtin_bit_cast(unsigned, r); }
__device__ __forceinline__ float bf_lo(unsigned u) { return __uint_as_float(u << 16); }
__device__ __forceinline__ float bf_hi(unsigned u) { return __uint_as_float(u & 0xffff0000u); }
__device__ __forceinline__ float ex2(float x) { return __builtin_amdgcn_exp2f(x); }
#define MFMA32_16(a, b, c) __builtin_amdgcn_mfma_f32_32x32x16_bf16(__builtin_bit_cast(bf16x8, a), __builtin_bit_cast(bf16x8, b), c, 0, 0, 0)
#define MFMA32_8(a, b, c) __builtin_amdgcn_mfma_f32_32x32x8bf16_1k(__builtin_bit_cast(bf16x4, a), __builtin_bit_cast(bf16x4, b), c, 0, 0, 0)
#define MFMA16_32(a, b, c) __builtin_amdgcn_mfma_f32_16x16x32_bf16(__builtin_bit_cast(bf16x8, a), __builtin_bit_cast(bf16x8, b), c, 0, 0, 0)

__device__ __forceinline__ void glds16(unsigned voff, const void* sbase, unsigned lds_base) {
    unsigned sv;
    asm volatile("s_mov_b32 %0, m0\n\ts_mov_b32 m0, %3\n\ts_nop 0\n\tglobal_load_lds_dwordx4 %1, %2\n\ts_mov_b32 m0, %0"
                 : "=&s"(sv) : "v"(voff), "s"(sbase), "s"(__builtin_amdgcn_readfirstlane(lds_base)) : "memory");
}
__device__ __forceinline__ void glds16s(unsigned voff, const void* sbase, unsigned lds_base) {
    unsigned sv;
    asm volatile("s_mov_b32 %0, m0\n\ts_mov_b32 m0, %3\n\ts_nop 0\n\tglobal_load_lds_dwordx4 %1, %2 sc1\n\ts_mov_b32 m0, %0"
                 : "=&s"(sv) : "v"(voff), "s"(sbase), "s"(__builtin_amdgcn_readfirstlane(lds_base)) : "memory");
}
__device__ __forceinline__ void glds4s(unsigned voff, const void* sbase, unsigned lds_base) {
    unsigned sv;
    asm volatile("s_mov_b32 %0, m0\n\ts_mov_b32 m0, %3\n\ts_nop 0\n\tglobal_load_lds_dword %1, %2 sc1\n\ts_mov_b32 m0, %0"
                 : "=&s"(sv) : "v"(voff), "s"(sbase), "s"(__builtin_amdgcn_readfirstlane(lds_base)) : "memory");
}
typedef short v4i16_t __attribute__((ext_vector_type(4)));
__device__ __forceinline__ u32x2 trread(LDS3 const char* p) {
    return __builtin_bit_cast(u32x2, __builtin_amdgcn_ds_read_tr16_b64_v4i16((LDS3 v4i16_t*)p));
}
#define WAIT_BAR(N) asm volatile("s_waitcnt vmcnt(" #N ") lgkmcnt(0)\n\ts_barrier" ::: "memory")
#define DRAIN() asm volatile("s_waitcnt vmcnt(0)" ::: "memory")
#define SBAR() __builtin_amdgcn_sched_barrier(0)
#define PIN(x) asm volatile("" : "+v"(x))

__device__ __forceinline__ float xhalf_max(float x) {
    auto r = __builtin_amdgcn_permlane32_swap(__float_as_uint(x), __float_as_uint(x), false, false);
    return fmaxf(__uint_as_float(r[0]), __uint_as_float(r[1]));
}
__device__ __forceinline__ float xhalf_sum(float x) {
    auto r = __builtin_amdgcn_permlane32_swap(__float_as_uint(x), __float_as_uint(x), false, false);
    return __uint_as_float(r[0]) + __uint_as_float(r[1]);
}
__device__ __forceinline__ float upper_half(float x) {
    auto r = __builtin_amdgcn_permlane32_swap(__float_as_uint(x), __float_as_uint(x), false, false);
    return __uint_as_float(r[1]);
}

typedef __amdgpu_buffer_rsrc_t rsrc_t;
#define ST8(v, off)  __builtin_amdgcn_raw_buffer_store_b64(v, rs, (int)(off), 0, 16)
#define ST16(v, off) __builtin_amdgcn_raw_buffer_store_b128(v, rs, (int)(off), 0, 16)
#define ST4(v, off)  __builtin_amdgcn_raw_buffer_store_b32(v, rs, (int)(off), 0, 16)
#define LD16(off) __builtin_amdgcn_raw_buffer_load_b128(rs, (int)(off), 0, 16)
#define LD4(off)  __builtin_amdgcn_raw_buffer_load_b32(rs, (int)(off), 0, 16)

template <int SLP = 2>
__device__ __forceinline__ void wait_flags(const char* ws, unsigned off0, unsigned off1, u64 tag) {
    gu64* p0 = (gu64*)(ws + off0); gu64* p1 = (gu64*)(ws + off1);
#pragma unroll 1
    for (unsigned spins = 0; spins < (1u << 16); ++spins) {
        const u64 a = __hip_atomic_load(p0, RLX_AGENT), b = __hip_atomic_load(p1, RLX_AGENT);
        if (__all(a == tag && b == tag)) break;
        __builtin_amdgcn_s_sleep(SLP);
    }
    __builtin_amdgcn_fence(__ATOMIC_ACQUIRE, "wavefront");
}
__device__ __forceinline__ void set_flag(char* ws, unsigned off, u64 tag) { __hip_atomic_store((gu64*)(ws + off), tag, RLX_AGENT); }

struct Params { const u16* x; const u16* wa; const u16* wb; const u16* wphi; const u16* woa; const u16* wob; const u16* gw; char* ws; u16* out; };

__device__ __forceinline__ void feature_map(const u32x4& pm, unsigned pt, const u32x4& tm, unsigned tt, int h, u32x4& fp, u32x4& fn, u32x4& ft) {
    const f32x16 z16 = {};
    const u32x2 a8 = {pt, 0u}, b8 = {tt, 0u};
    f32x16 s = MFMA32_16(pm, tm, z16); s = MFMA32_8(a8, b8, s);
    f32x2 v[6];
#pragma unroll
    for (int i = 0; i < 6; ++i) {
        v[i] = (f32x2){s[2 * i], s[2 * i + 1]};
    }
    float mx = fmaxf(v[0][0], v[0][1]), mn = fminf(v[0][0], v[0][1]);
#pragma unroll
    for (int i = 1; i < 6; ++i) { mx = fmaxf(fmaxf(mx, v[i][0]), v[i][1]); mn = fminf(fminf(mn, v[i][0]), v[i][1]); }
    mx = xhalf_max(mx) * -LOG2E; mn = xhalf_max(-mn) * -LOG2E;
    f32x2 ep[6], en[6];
#pragma unroll
    for (int i = 0; i < 6; ++i) {
        const f32x2 a = v[i] * LOG2E + mx, b = v[i] * -LOG2E + mn;
        ep[i] = (f32x2){ex2(a[0]), ex2(a[1])}; en[i] = (f32x2){ex2(b[0]), ex2(b[1])};
    }
    const float tw = h ? 0.f : 1.f;
    const f32x2 s2p = ((ep[0] + ep[1]) + (ep[2] + ep[3])) + (ep[4] + ep[5]) * tw, s2n = ((en[0] + en[1]) + (en[2] + en[3])) + (en[4] + en[5]) * tw;
    const float sp = __builtin_amdgcn_rcpf(xhalf_sum(s2p[0] + s2p[1])), sn = __builtin_amdgcn_rcpf(xhalf_sum(s2n[0] + s2n[1]));
    const float tp = sp * tw, tn = sn * tw;
#pragma unroll
    for (int i = 0; i < 4; ++i) { const f32x2 a = ep[i] * sp, b = en[i] * sn; fp[i] = pk2(a[0], a[1]); fn[i] = pk2(b[0], b[1]); }
    const f32x2 c0 = ep[4] * tp, c1 = ep[5] * tp, c2 = en[4] * tn, c3 = en[5] * tn;
    ft = (u32x4){pk2(c0[0], c0[1]), pk2(c1[0], c1[1]), pk2(c2[0], c2[1]), pk2(c3[0], c3[1])};
}

__device__ __forceinline__ void store_o(rsrc_t rs, const float (&o)[12], float sc, int tok, int half, int head, int h) {
    const auto s0 = __builtin_amdgcn_permlane32_swap(pk2(o[0] * sc, o[1] * sc), pk2(o[4] * sc, o[5] * sc), false, false);
    const auto s1 = __builtin_amdgcn_permlane32_swap(pk2(o[2] * sc, o[3] * sc), pk2(o[6] * sc, o[7] * sc), false, false);
    const unsigned tb = WS_AO + ((tok >> 4) * 40 + half * 20) * 1024;
    const u32x4 mv = {s0[0], s1[0], s0[1], s1[1]};
    ST16(mv, tb + (head >> 1) * 1024 + (((head & 1) * 2 + h) * 16 + (tok & 15)) * 16);
    const u32x2 tv = {pk2(o[8] * sc, o[9] * sc), pk2(o[10] * sc, o[11] * sc)};
    if (!h) ST8(tv, tb + (16 + (head >> 3)) * 1024 + (head & 7) * 128 + (tok & 15) * 8);
}
#define LD8(off)  __builtin_amdgcn_raw_buffer_load_b64(rs, (int)(off), 0, 16)
#define LD_TAIL(dst, base) do { const u32x2 a_ = LD8((base) + (lane >> 4) * 256 + (lane & 15) * 8), b_ = LD8((base) + (lane >> 4) * 256 + 128 + (lane & 15) * 8); dst = (u32x4){a_[0], a_[1], b_[0], b_[1]}; } while (0)

#define DSR128(dst, addr, off) asm volatile("ds_read_b128 %0, %1 offset:%2" : "=v"(dst) : "v"(addr), "i"(off))
#define DSR32(dst, addr, off) asm volatile("ds_read_b32 %0, %1 offset:%2" : "=v"(dst) : "v"(addr), "i"(off))
#define DSR128M2(d0, a0, o0, d1, a1, o1) asm volatile("s_mov_b64 exec, %6\n\tds_read_b128 %0, %2 offset:%3\n\tds_read_b128 %1, %4 offset:%5\n\ts_mov_b64 exec, -1" \
                                                      : "+v"(d0), "+v"(d1) : "v"(a0), "i"(o0), "v"(a1), "i"(o1), "s"(vmask))
#define LWAIT(n) do { asm volatile("s_waitcnt lgkmcnt(" #n ")" ::: "memory"); SBAR(); } while (0)

#define BAR() asm volatile("s_barrier" ::: "memory")
#define LDS_SET(off, v) __hip_atomic_store((LDS3 unsigned*)(lds + (off)), (unsigned)(v), __ATOMIC_RELAXED, __HIP_MEMORY_SCOPE_WORKGROUP)
#define LDS_CNT_ADD(off) __hip_atomic_fetch_add((LDS3 unsigned*)(lds + (off)), 1u, __ATOMIC_RELAXED, __HIP_MEMORY_SCOPE_WORKGROUP)

template <int SLP = 1>
__device__ __forceinline__ void lds_wait(LDS3 char* p, unsigned want) {
#pragma unroll 1
    for (unsigned spins = 0; spins < (1u << 20); ++spins) {
        if ((unsigned)__builtin_amdgcn_readfirstlane(__hip_atomic_load((LDS3 unsigned*)p, __ATOMIC_RELAXED, __HIP_MEMORY_SCOPE_WORKGROUP)) >= want) break;
        __builtin_amdgcn_s_sleep(SLP);
    }
    __builtin_amdgcn_fence(__ATOMIC_ACQUIRE, "wavefront");
}

template <int SLP>
__device__ __forceinline__ void prog_wait(LDS3 char* p, int lane, unsigned want) {
#pragma unroll 1
    for (unsigned spins = 0; spins < (1u << 20); ++spins) {
        if (__all(__hip_atomic_load((LDS3 unsigned*)p + (lane & 7), __ATOMIC_RELAXED, __HIP_MEMORY_SCOPE_WORKGROUP) >= want)) break;
        __builtin_amdgcn_s_sleep(SLP);
    }
    __builtin_amdgcn_fence(__ATOMIC_ACQUIRE, "wavefront");
}
#define POLL_ONE(cond, SLP, o0, o1, word) do { if (cond) { wait_flags<SLP>(ws, o0, o1, tag); \
        if (lane == 0) __hip_atomic_store((LDS3 unsigned*)(lds + (word)), 1u, __ATOMIC_RELAXED, __HIP_MEMORY_SCOPE_WORKGROUP); } else lds_wait<2>(lds + (word), 1); } while (0)
constexpr int C_KV = 0, C_KVF = 4, C_OB = 8, C_PROG = 12, C_WA = 16, C_OA = 20, C_MRG = 32  , C_FIN = 64  , C_BP = 96  ;
constexpr int C_PROG2 = 24, C_BX = 28, C_P1 = 112, C_P2 = 116, C_P3 = 120, C_P4 = 124, C_PR = 128  ;
constexpr int G_W = 31744;
constexpr int G_FULL = G_W  , G_PRG = G_W + 16  , G_Q = G_W + 48, G_P = G_W + 52, G_ST = G_W + 56, G_RD = G_W + 60, G_Z = G_W + 64, G_ST2 = G_W + 68;
constexpr int C_GB = 168, C_KVL = 160, C_P5 = 164, X_G = 256;
constexpr int X_KVF = 1280, X_SLOT = 4096;
constexpr int X_MRG = 1280;
constexpr int L_WA = 24576;
constexpr int L_Q = 81920;
constexpr int X_FIN = 126976;

__global__ __launch_bounds__(768) void mega(Params P) {
    __shared__ __attribute__((aligned(16))) char smem[163840];
    LDS3 char* lds = (LDS3 char*)smem;
    char* ws = P.ws;
    const int tid = threadIdx.x, lane = tid & 63, r = lane & 31, h = lane >> 5;
    const int hw = __builtin_amdgcn_readfirstlane(tid >> 6);
    const bool helper = hw < 4;
    const int w = hw - 4, mtid = tid - 256;
    const int bid = blockIdx.x;
    const unsigned lds0 = (unsigned)(uintptr_t)smem;
    const rsrc_t rs = __builtin_amdgcn_make_buffer_rsrc(ws, 0, WS_END, 0x00020000);
    STAMP(0);
    u64 tag = 0;
#define GET_TAG() do { asm volatile("s_load_dwordx2 %0, %1, 0x0\n\ts_waitcnt lgkmcnt(0)" : "=s"(tag) : "s"(ws) : "memory"); tag += 1; } while (0)
    const int head = (bid & 7) * 4 + ((bid >> 3) & 3), qb = bid >> 5;
    const int nlo = (head * 20) >> 7, nhi = (head * 20 + 19) >> 7;
    const int mtO = (bid & 7) * 4 + ((bid >> 3) & 3), ntO = bid >> 5;
    const int lin = (bid & 7) * 30 + (bid >> 3), lh = lin >= 120, lrem = lin - 120 * lh;
    const int mt = lh * 8 + (lrem & 7), nt = lrem >> 3;
    const unsigned fG = WS_F_G + (nt * 16 + mt) * 8;
    const unsigned one = (r == 20) ? 0x3f803f80u : 0u;
    const f32x16 z16 = {};

    if (helper) {
        const unsigned fA0 = WS_F_G + (((lane < 32 ? 5 : 10) + ((lane & 16) ? nhi : nlo)) * 16 + (lane & 15)) * 8;
        const unsigned fA1 = WS_F_G + (((lane & 2) ? nhi : nlo) * 16 + qb * 2 + (lane & 1)) * 8;
        __builtin_amdgcn_s_setprio(3);
        const char* kvb = ws + WS_KV + head * 163840;
        auto kv_issue = [&](int grp) {
#pragma unroll
            for (int t = 0; t < 8; ++t) { int pc = grp * 32 + hw + 4 * t; if (pc == 31) pc = 30; glds16s(lane * 16, kvb + pc * 1024, lds0 + pc * 1024); }
        };
        auto q_issue = [&]() {
            const char* qbase = ws + WS_Q + head * 81920 + qb * 10240;
#pragma unroll
            for (int t = 0; t < 3; ++t) { const int pc = hw + 4 * t < 10 ? hw + 4 * t : 9; glds16s(lane * 16, qbase + pc * 1024, lds0 + L_Q + pc * 1024); }
        };
        if (bid < 240) {
            const int rot = (nt * 3 + mt * 5 + (mt >> 1)) % 10;
            const unsigned xo = (mt * 128 + 8 * hw + (lane >> 3)) * 1280 + (((lane & 7) ^ ((4 * hw + (lane >> 4)) & 7)) << 4);
            const unsigned wo = (4 * hw + (lane >> 4)) * 3840 + nt * 256 + (((lane & 15) ^ (((lane >> 4) << 2) | hw)) << 4);
            const unsigned dX = lds0 + 32768 + hw * 1024;
            auto issue = [&](int s) {
                int kt = (s >= 10 ? 19 - s : s) + rot; if (kt >= 10) kt -= 10;
                const char* xb = (const char*)P.x + kt * 128;
                const char* wb = (const char*)(s >= 10 ? P.wb : P.wa) + (size_t)kt * (64 * 3840);
                const unsigned dw = dX + 16384 + (s & 3) * 32768, dx = dX + ((s >= 10 ? 20 - s : s + 1) & 3) * 32768;
#pragma unroll
                for (int i = 0; i < 4; ++i) glds16(wo, wb + i * 61440, dw + i * 4096);
                if (s < 10 || s >= 14) {
#pragma unroll
                    for (int i = 0; i < 4; ++i) glds16(xo, xb + i * 40960, dx + i * 4096);
                }
            };
#pragma unroll 1
            for (int n = 0; n < 20; ++n) {
                if (n == 2) { if (hw == 0 && lane < 32) *(LDS3 unsigned*)(lds + G_W + lane * 4) = 0u; GET_TAG(); WAIT_BAR(8); }
                else if (n == 3 || (n >= 11 && n <= 15)) { WAIT_BAR(8); }
                else if (n >= 5) { WAIT_BAR(16); }
                issue(n);
            }
            WAIT_BAR(16);
            WAIT_BAR(0);
        } else {
            if (hw == 0 && lane < 32) *(LDS3 unsigned*)(lds + G_W + lane * 4) = lane >= 4 && lane < 12 ? 20u : 0u;
            GET_TAG();
            WAIT_BAR(63);
            if (hw == 0) {
#pragma unroll 1
                for (int t = 0; t < PRE_SLEEP; ++t) __builtin_amdgcn_s_sleep(127);
            }
        }
        POLL_ONE(hw == 0, 6, fA0, fA1, G_P);
        kv_issue(0); q_issue(); kv_issue(1);
        asm volatile("s_waitcnt vmcnt(6)" ::: "memory");
        if (lane == 0) LDS_CNT_ADD(G_Q);
        prog_wait<1>(lds + G_PRG, lane, 19);
        kv_issue(3);
        prog_wait<1>(lds + G_PRG, lane, 20);
        kv_issue(4);
        lds_wait<2>(lds + G_RD, 8);
        if (hw == 0) {
            *(LDS3 u32x4*)(lds + lane * 16) = (u32x4){0u, 0u, 0u, 0u};
            asm volatile("s_waitcnt lgkmcnt(0)" ::: "memory");
            if (lane == 0) LDS_SET(G_Z, 1);
        }
        kv_issue(2);
        lds_wait<2>(lds + G_Z, 1);
        if (lane == 0) LDS_CNT_ADD(G_ST2);
        if (hw == 3) {
            lds_wait<2>(lds + G_ST2, 12);
            glds16s(lane * 16, kvb + 31 * 1024, lds0 + 31 * 1024);
        }
        STAMP(3);
        u32x4 phm; unsigned pht;
        {
            const u16* wp = P.wphi + head * 400 + (r < 20 ? r : (r & 3));
            unsigned e[10];
#pragma unroll
            for (int j = 0; j < 8; ++j) e[j] = wp[(8 * h + j + 4 * (head & 1)) * 20];
            e[8] = wp[((head & 1 ? 0 : 16) + 2 * h) * 20]; e[9] = wp[((head & 1 ? 1 : 17) + 2 * h) * 20];
            const int bu = bid < 160 ? bid : 0, hf = bu >= 80, k0 = (bu - 80 * hf) * 8, hd = k0 < 512 ? k0 >> 4 : (k0 - 512) >> 2;
            const int row = k0 < 512 ? 20 * hd + (k0 & 8) + 4 * (hd & 1) : 20 * hd + 16;
            const u16* wsrc = (hf ? P.wob : P.woa) + (size_t)row * DM;
            unsigned f[3][8];
            if (bid < 160) {
#pragma unroll
                for (int q = 0; q < 3; ++q) {
                    const int n = tid + 256 * q < 640 ? tid + 256 * q : 639;
#pragma unroll
                    for (int j = 0; j < 8; ++j) f[q][j] = wsrc[j * DM + n];
                }
            }
            DRAIN();
            if (lane == 0) LDS_CNT_ADD(C_KVL);
            phm = (u32x4){e[0] | (e[1] << 16), e[2] | (e[3] << 16), e[4] | (e[5] << 16), e[6] | (e[7] << 16)}; pht = e[8] | (e[9] << 16);
            if (bid < 160) {
#pragma unroll
                for (int q = 0; q < 3; ++q) {
                    const int n = tid + 256 * q;
                    const u32x4 v = {f[q][0] | (f[q][1] << 16), f[q][2] | (f[q][3] << 16), f[q][4] | (f[q][5] << 16), f[q][6] | (f[q][7] << 16)};
                    if (n < 640) ST16(v, WS_WT + (((n >> 4) * 40 + hf * 20 + (k0 >> 5)) * 64 + ((k0 >> 3) & 3) * 16 + (n & 15)) * 16);
                }
            }
        }
        const int tokS = qb * 256 + hw * 64, m7 = tokS >> 7, t4 = (tokS >> 5) & 3;
        {
            const unsigned pt3 = (lane >> 1) & 3;
            const unsigned tb = WS_F_G + 2048 + ((((lane & 1) ? nhi : nlo) + (pt3 < 3 ? pt3 : 0) * 5) * 16 + qb * 2 + ((lane >> 3) & 1)) * 8;
            POLL_ONE(hw == 0, 4, tb, WS_F_SD + (qb * 2 + (lane & 1)) * 8, C_P1);
        }
        const unsigned om = t4 * 1024 + lane * 16, ot = 4096 + (t4 * 32 + r) * 8 + 4 * h;
        u32x4 qf[2][3]; float gS[2];
        {
            const unsigned kb = WS_KV + (NH + head) * 163840 + (2 * (m7 & 7) + (m7 >> 3)) * 10240, qbb = WS_Q + (NH + head) * 81920 + m7 * 5120;
            const unsigned ov = 5120 + t4 * 1280 + h * 16 + (r < 20 ? r : 19) * 32;
            u32x4 e0[2], w0[2], w1[2], eq[2]; unsigned et[2], eqt[2];
#pragma unroll
            for (int i = 0; i < 2; ++i) {
                e0[i] = LD16(kb + om + i * 1024); et[i] = LD4(kb + ot + i * 256); w0[i] = LD16(kb + ov + i * 1280); w1[i] = LD16(kb + ov + i * 1280 + 640);
                eq[i] = LD16(qbb + om + i * 1024); eqt[i] = LD4(qbb + ot + i * 256); gS[i] = __uint_as_float(LD4(WS_G + (tokS + i * 32 + r) * 4));
            }
            u32x4 i0, i1;
#pragma unroll
            for (int j = 0; j < 4; ++j) {
                const int e = r - 8 * h - 2 * j;
                i0[j] = e == 0 ? 0x3f80u : (e == 1 ? 0x3f800000u : 0u);
                i1[j] = e == 16 ? 0x3f80u : (e == 17 ? 0x3f800000u : 0u);
            }
            f32x16 k0 = {}, k1 = {};
#pragma unroll
            for (int i = 0; i < 2; ++i) {
                if (r >= 20) { w0[i] = (u32x4){one, one, one, one}; w1[i] = w0[i]; }
                u32x4 fp, fn, ft;
                feature_map(phm, pht, e0[i], et[i], h, fp, fn, ft);
                f32x16 T0 = MFMA32_16(fp, i0, z16); T0 = MFMA32_16(fn, i1, T0);
                f32x16 T1 = MFMA32_16(ft, i0, z16);
                u32x4 b00, b01, b10, b11;
#pragma unroll
                for (int j = 0; j < 4; ++j) {
                    b00[j] = pk2(T0[2 * j], T0[2 * j + 1]); b01[j] = pk2(T0[8 + 2 * j], T0[9 + 2 * j]);
                    b10[j] = pk2(T1[2 * j], T1[2 * j + 1]); b11[j] = pk2(T1[8 + 2 * j], T1[9 + 2 * j]);
                }
                k0 = MFMA32_16(w0[i], b00, k0); k0 = MFMA32_16(w1[i], b01, k0);
                k1 = MFMA32_16(w0[i], b10, k1); k1 = MFMA32_16(w1[i], b11, k1);
            }
            prog_wait<8>(lds + C_PR, lane, 1);
            *(LDS3 float*)(lds + X_G + (hw * 64 + lane) * 4) = h ? gS[1] : gS[0];
            if (hw * 64 + lane < 144) *(LDS3 u32x4*)(lds + X_KVF + (hw * 64 + lane) * 16) = (u32x4){0u, 0u, 0u, 0u};
            LDS3 char* sp = lds + X_SLOT + hw * 4096;
#pragma unroll
            for (int gq = 0; gq < 3; ++gq) {
                *(LDS3 f32x4*)(sp + gq * 1024 + lane * 16) = (f32x4){k0[4 * gq], k0[4 * gq + 1], k0[4 * gq + 2], k0[4 * gq + 3]};
                if (r < 8) *(LDS3 f32x4*)(sp + 3072 + (gq * 16 + h * 8 + r) * 16) = (f32x4){k1[4 * gq], k1[4 * gq + 1], k1[4 * gq + 2], k1[4 * gq + 3]};
            }
            asm volatile("s_waitcnt vmcnt(0) lgkmcnt(0)" ::: "memory");
            unsigned arr = 0;
            if (lane == 0) arr = LDS_CNT_ADD(C_KV);
            if (__builtin_amdgcn_readfirstlane(arr) == 3) {
                __builtin_amdgcn_fence(__ATOMIC_ACQUIRE, "wavefront");
#pragma unroll
                for (int i = 0; i < 4; ++i) {
                    const int qd = lane + 64 * i;
                    LDS3 const char* s0 = lds + X_SLOT + qd * 16;
                    const f32x4 s = (*(LDS3 const f32x4*)s0 + *(LDS3 const f32x4*)(s0 + 4096)) + (*(LDS3 const f32x4*)(s0 + 8192) + *(LDS3 const f32x4*)(s0 + 12288));
                    if (qd < 240) ST16(__builtin_bit_cast(u32x4, s), WS_KVP + (head * 8 + qb) * 4096 + qd * 16);
                }
                DRAIN();
                if (lane == 0) set_flag(ws, WS_F_KV + (head * 8 + qb) * 8, tag);
            }
#pragma unroll
            for (int i = 0; i < 2; ++i) feature_map(phm, pht, eq[i], eqt[i], h, qf[i][0], qf[i][1], qf[i][2]);
        }
        STAMP(4);
        POLL_ONE(hw == 0, 4, WS_F_KV + (head * 8 + (lane & 7)) * 8, WS_F_KV + (head * 8 + (lane & 7)) * 8, C_P2);
        {
            const int qd = hw * 60 + (lane < 60 ? lane : 59);
            f32x4 pp[8];
#pragma unroll
            for (int p = 0; p < 8; ++p) pp[p] = __builtin_bit_cast(f32x4, LD16(WS_KVP + (head * 8 + p) * 4096 + qd * 16));
            const f32x4 s4 = ((pp[0] + pp[1]) + (pp[2] + pp[3])) + ((pp[4] + pp[5]) + (pp[6] + pp[7]));
            const int gq = qd < 192 ? qd >> 6 : (qd - 192) >> 4, hs = qd < 192 ? (qd >> 5) & 1 : (qd >> 3) & 1, sg = qd < 192 ? qd & 31 : 32 + (qd & 7);
            const unsigned a = pk2(s4[0], s4[1]), b = pk2(s4[2], s4[3]);
            LDS3 u16* kp = (LDS3 u16*)(lds + X_KVF + (8 * gq + 4 * hs) * 96 + sg * 2);
            kp[0] = (u16)a; kp[48] = (u16)(a >> 16); kp[96] = (u16)b; kp[144] = (u16)(b >> 16);
            asm volatile("s_waitcnt lgkmcnt(0)" ::: "memory");
            if (lane == 0) LDS_CNT_ADD(C_KVF);
            lds_wait<2>(lds + C_KVF, 4);
        }
        {
            LDS3 const char* kp = lds + X_KVF + (r < 24 ? r : 23) * 96 + h * 16;
            const u32x4 a0 = *(LDS3 const u32x4*)kp, a1 = *(LDS3 const u32x4*)(kp + 32), a2 = *(LDS3 const u32x4*)(kp + 64);
#pragma unroll
            for (int i = 0; i < 2; ++i) {
                f32x16 d = MFMA32_16(a0, qf[i][0], z16); d = MFMA32_16(a1, qf[i][1], d); d = MFMA32_16(a2, qf[i][2], d);
                const float den = upper_half(d[8]) + 1e-6f;
                float o[12];
#pragma unroll
                for (int j = 0; j < 12; ++j) o[j] = d[j];
                store_o(rs, o, (1.f - gS[i]) / den, tokS + i * 32 + r, 1, head, h);
            }
            DRAIN();
            unsigned arr = 0;
            if (lane == 0) arr = LDS_CNT_ADD(C_OB);
            if (__builtin_amdgcn_readfirstlane(arr) == 3 && lane == 0) set_flag(ws, WS_F_OB + (qb * 32 + head) * 8, tag);
        }
        STAMP(5);
        f32x4 c[5];
#pragma unroll
        for (int n = 0; n < 5; ++n) c[n] = (f32x4){0.f, 0.f, 0.f, 0.f};
        const char* wsrc = ws + WS_WT + (size_t)ntO * 5 * 40960;
        {
            if (hw == 0) wait_flags<8>(ws, WS_F_KV + lane * 8, WS_F_KV + 512 + lane * 8, tag);
            POLL_ONE(hw == 0, 8, WS_F_KV + 1024 + lane * 8, WS_F_KV + 1536 + lane * 8, C_P5);
#pragma unroll 1
            for (int t = 0; t < 5; ++t) {
                if (t == 0) prog_wait<8>(lds + C_PR, lane, 4); else if (t == 3) prog_wait<4>(lds + C_PR, lane, 5);
                const int ks = hw + 4 * t < 19 ? hw + 4 * t : 18;
#pragma unroll
                for (int n = 0; n < 5; ++n) glds16s(lane * 16, wsrc + (n * 40 + 20 + ks) * 1024, lds0 + 4096 + (ks * 5 + n) * 1024);
            }
            POLL_ONE(hw == 0, 4, WS_F_OB + ((mtO >> 2) * 32 + r) * 8, WS_F_OB + ((mtO >> 2) * 32 + r) * 8, C_P3);
            const unsigned ab = WS_AO + ((mtO * 4 + hw) * 40 + 20) * 1024;
            u32x4 af[20];
#pragma unroll
            for (int ks = 0; ks < 16; ++ks) af[ks] = LD16(ab + ks * 1024 + lane * 16);
#pragma unroll
            for (int ks = 16; ks < 20; ++ks) LD_TAIL(af[ks], ab + ks * 1024);
            asm volatile("s_waitcnt vmcnt(20)" ::: "memory");
            if (lane == 0) LDS_CNT_ADD(C_BX);
            lds_wait<2>(lds + C_BX, 4);
            u32x4 wf[3][5], wl[5];
            LDS3 const char* wp = lds + 4096 + lane * 16;
#pragma unroll
            for (int ks = 0; ks < 2; ++ks)
#pragma unroll
                for (int n = 0; n < 5; ++n) wf[ks][n] = *(LDS3 const u32x4*)(wp + (ks * 5 + n) * 1024);
#pragma unroll
            for (int ks = 0; ks < 20; ++ks) {
                if (ks + 2 < 19) {
#pragma unroll
                    for (int n = 0; n < 5; ++n) wf[(ks + 2) % 3][n] = *(LDS3 const u32x4*)(wp + ((ks + 2) * 5 + n) * 1024);
                }
                if (ks == 8) {
#pragma unroll
                    for (int n = 0; n < 5; ++n) wl[n] = LD16(WS_WT + ((ntO * 5 + n) * 40 + 39) * 1024 + lane * 16);
                }
                SBAR();
#pragma unroll
                for (int n = 0; n < 5; ++n) c[n] = MFMA16_32(ks < 19 ? wf[ks % 3][n] : wl[n], af[ks], c[n]);
                SBAR();
            }
            if (lane == 0) LDS_CNT_ADD(C_BX);
            lds_wait<2>(lds + C_BX, 8);
        }
        STAMP(6);
        prog_wait<4>(lds + C_PR, lane, 7);
#pragma unroll 1
        for (int n = 0; n < 5; ++n)
#pragma unroll
            for (int t = 0; t < 5; ++t) { const int ks = hw + 4 * t; glds16s(lane * 16, wsrc + (n * 40 + ks) * 1024, lds0 + L_WA + (n * 20 + ks) * 1024); }
#pragma unroll
        for (int n = 0; n < 3; ++n) *(LDS3 f32x4*)(lds + X_FIN + hw * 3072 + n * 1024 + lane * 16) = c[n];
        DRAIN();
        if (lane == 0) LDS_CNT_ADD(C_WA);
        prog_wait<4>(lds + C_PR, lane, 8);
#pragma unroll
        for (int n = 3; n < 5; ++n) *(LDS3 f32x4*)(lds + 143360 + hw * 2048 + (n - 3) * 1024 + lane * 16) = c[n];
        asm volatile("s_waitcnt lgkmcnt(0)" ::: "memory");
        if (lane == 0) __hip_atomic_store((LDS3 unsigned*)(lds + C_BP + hw * 4), 1u, __ATOMIC_RELAXED, __HIP_MEMORY_SCOPE_WORKGROUP);
        STAMP(7);
        asm volatile("mega_code_end2:");
    } else {
        const int qs = w & 3, kh = w >> 2;
        const int q0 = qb * 256 + qs * 64;
        {
            unsigned sink;
            if (w == 0 && (WARM_MODE & 1)) {
                const char* pc0; int len, len2;
                asm volatile("s_getpc_b64 %0\nmega_code_mark:\n\ts_mov_b32 %1, mega_code_end-mega_code_mark\n\ts_mov_b32 %2, mega_code_end2-mega_code_mark" : "=s"(pc0), "=s"(len), "=s"(len2));
                len = len > len2 ? len : len2;
                const int o = ((bid >> 3) * 64 + lane) * 64;
                if (o + 4 <= len) asm volatile("global_load_dword %0, %1, %2" : "=v"(sink) : "v"(o), "s"(pc0) : "memory");
            } else if (w >= 1 && w <= 6 && (WARM_MODE & 2)) {
                const unsigned o = ((w - 1) * 64 + lane) * 65536 + 65532;
                if (o < WS_END) asm volatile("global_load_dword %0, %1, %2 sc1" : "=v"(sink) : "v"(o), "s"(ws) : "memory");
            } else if (w == 7 && (WARM_MODE & 2)) {
                if (lane < 40) asm volatile("global_load_dword %0, %1, %2 sc1" : "=v"(sink) : "v"(lane * 65536), "s"(P.out) : "memory");
            }
            asm volatile("s_waitcnt vmcnt(0)" ::: "memory");
        }
        GET_TAG();
        if (bid < 240) {
            const int part = nt / 5, c0 = (nt - part * 5) * 128;
            const int wm = w & 1, wn = w >> 1;
            constexpr int RB = 32768;
            const int xr = wm * 64 + r;
            const unsigned xa = RB + xr * 128 + ((h ^ ((xr >> 1) & 7)) << 4);
            unsigned wt[2];
#pragma unroll
            for (int t = 0; t < 2; ++t) {
                const int q = (lane & 15) >> 2, p = lane & 3, blk = (lane >> 4) & 1, row = 8 * h + 4 * t + q;
                wt[t] = RB + 16384 + row * 256 + (((4 * wn + 2 * blk + (p >> 1)) ^ ((q << 2) | (2 * h + t))) << 4) + 8 * (p & 1);
            }
            auto gbody = [&](auto trc) {
                constexpr bool TR = decltype(trc)::value;
                u32x4 xf[2][8], wf[2][4];
                auto rdk = [&](auto bc, LDS3 const char* sx, LDS3 const char* sb, int ks) {
                    constexpr int B = decltype(bc)::value;
                    xf[B][2 * ks] = *(LDS3 const u32x4*)(sx + (xa ^ (ks * 32)));
                    xf[B][2 * ks + 1] = *(LDS3 const u32x4*)(sx + ((xa + 4096) ^ (ks * 32)));
                    const u32x2 lo = trread(sb + wt[0] + ks * 4096), hi = trread(sb + wt[1] + ks * 4096);
                    wf[B][ks] = (u32x4){lo[0], lo[1], hi[0], hi[1]};
                };
                f32x16 acc0, acc1;
                auto step = [&](auto bc, int s) {
                    constexpr int B = decltype(bc)::value;
                    if (s == GA_PUB) {
                        DRAIN();
                        unsigned arr = 0;
                        if (lane == 0) arr = LDS_CNT_ADD(G_ST);
                        if (__builtin_amdgcn_readfirstlane(arr) == 7 && lane == 0) set_flag(ws, fG, tag);
                    }
                    __builtin_amdgcn_s_waitcnt(0xC07F);
                    if (s < 18) BAR();
                    else if (lane == 0) LDS_SET(G_PRG + w * 4, s + 1);
                    SBAR();
                    const int s1 = s + 1;
                    LDS3 const char* sb = lds + __builtin_amdgcn_readfirstlane((s1 & 3) * 32768);
                    LDS3 const char* sx = lds + __builtin_amdgcn_readfirstlane(((s1 >= 10 ? 20 - s1 : s1 + 1) & 3) * 32768);
#define GM(A, i) do { if constexpr (TR) A = MFMA32_16(wf[B][(i) >> 1], xf[B][i], A); else A = MFMA32_16(xf[B][i], wf[B][(i) >> 1], A); SBAR(); } while (0)
#define GX(i) do { xf[B ^ 1][i] = *(LDS3 const u32x4*)(sx + ((xa + ((i) & 1) * 4096) ^ (((i) >> 1) * 32))); } while (0)
#define GW(ks) do { const u32x2 lo = trread(sb + wt[0] + (ks) * 4096), hi = trread(sb + wt[1] + (ks) * 4096); wf[B ^ 1][ks] = (u32x4){lo[0], lo[1], hi[0], hi[1]}; } while (0)
                    GM(acc0, 0); GX(0); GW(0); SBAR(); GM(acc1, 1); GX(1); GX(2); SBAR(); GM(acc0, 2); GW(1); GX(3); SBAR(); GM(acc1, 3); GX(4); GW(2); SBAR();
                    GM(acc0, 4); GX(5); GX(6); SBAR(); GM(acc1, 5); GW(3); GX(7); SBAR(); GM(acc0, 6); GM(acc1, 7);
#undef GX
#undef GW
#undef GM
                };
                WAIT_BAR(63);
#pragma unroll
                for (int ks = 0; ks < 4; ++ks) rdk(std::integral_constant<int, 0>{}, lds + 32768, lds, ks);
#pragma unroll 1
                for (int mat = 0; mat < 2; ++mat) {
                    acc0 = (f32x16){}; acc1 = (f32x16){};
#pragma unroll 1
                    for (int kk = 0; kk < 10; kk += 2) {
                        step(std::integral_constant<int, 0>{}, mat * 10 + kk);
                        step(std::integral_constant<int, 1>{}, mat * 10 + kk + 1);
                    }
                    const int cpos = 2 * (mt & 7) + (mt >> 3);
                    if constexpr (TR) {
                        const float sc = (part == 0 && mat == 0) ? QSCALE : 1.f;
                        const unsigned base = part == 0 ? WS_Q + mat * NH * 81920 + mt * 5120 : WS_KV + mat * NH * 163840 + cpos * 10240;
                        const unsigned hs = part == 0 ? 81920 : 163840;
#pragma unroll
                        for (int pr = 0; pr < 2; ++pr) {
                            const int G8 = ((c0 + 32 * wn) >> 3) + 2 * pr + h, pp = G8 / 5, gi = G8 - pp * 5;
                            const unsigned hb0 = base + (2 * pp + (gi > 2)) * hs;
#pragma unroll
                            for (int i = 0; i < 2; ++i) {
                                const f32x16& a = i ? acc1 : acc0;
                                const int tl = wm * 64 + i * 32 + r;
                                const auto s0 = __builtin_amdgcn_permlane32_swap(pk2(a[8 * pr] * sc, a[8 * pr + 1] * sc), pk2(a[8 * pr + 4] * sc, a[8 * pr + 5] * sc), false, false);
                                const auto s1 = __builtin_amdgcn_permlane32_swap(pk2(a[8 * pr + 2] * sc, a[8 * pr + 3] * sc), pk2(a[8 * pr + 6] * sc, a[8 * pr + 7] * sc), false, false);
                                if (gi != 2) { const u32x4 ov = {s0[0], s1[0], s0[1], s1[1]}; ST16(ov, hb0 + (tl >> 5) * 1024 + (gi > 2 ? gi - 3 : gi) * 512 + r * 16); }
                                else { const u32x2 o0 = {s0[0], s1[0]}, o1 = {s0[1], s1[1]}; ST8(o0, hb0 + 4096 + tl * 8); ST8(o1, hb0 + hs + 4096 + tl * 8); }
                            }
                        }
                    } else {
                        const int n = c0 + 32 * wn + r, hd = n / 20, d = n - hd * 20, rho = (hd & 1) ? (d < 4 ? d + 16 : d - 4) : d;
                        const unsigned base = WS_KV + (mat * NH + hd) * 163840 + cpos * 10240 + 5120 + h * 16 + rho * 32;
#pragma unroll
                        for (int i = 0; i < 2; ++i)
#pragma unroll
                            for (int s2 = 0; s2 < 2; ++s2) {
                                const f32x16& a = i ? acc1 : acc0;
                                u32x4 ov = {pk2(a[8 * s2], a[8 * s2 + 1]), pk2(a[8 * s2 + 2], a[8 * s2 + 3]), pk2(a[8 * s2 + 4], a[8 * s2 + 5]), pk2(a[8 * s2 + 6], a[8 * s2 + 7])};
                                ST16(ov, base + (wm * 4 + i * 2 + s2) * 640);
                            }
                    }
                    if (mat == 0) STAMP(1);
                }
            };
            if (part < 2) gbody(std::true_type{}); else gbody(std::false_type{});
        } else {
            const int sj = bid - 240;
            WAIT_BAR(63);
            {
                const u32x4 ga = *(const u32x4*)(P.gw + lane * 8), gb = *(const u32x4*)(P.gw + 512 + (lane & 15) * 8);
                u32x4 xa[16], xb[4];
                const u16* xr = P.x + (size_t)(sj * 128 + w * 16) * DM;
#pragma unroll
                for (int rr = 0; rr < 16; ++rr) xa[rr] = *(const u32x4*)(xr + rr * DM + lane * 8);
#pragma unroll
                for (int i = 0; i < 4; ++i) xb[i] = *(const u32x4*)(xr + (i * 4 + (lane >> 4)) * DM + 512 + (lane & 15) * 8);
                float res = 0.f;
#pragma unroll
                for (int rr = 0; rr < 16; ++rr) {
                    float s = 0.f, s2 = 0.f;
#pragma unroll
                    for (int j = 0; j < 4; ++j) { s += bf_lo(xa[rr][j]) * bf_lo(ga[j]) + bf_hi(xa[rr][j]) * bf_hi(ga[j]); s2 += bf_lo(xb[rr >> 2][j]) * bf_lo(gb[j]) + bf_hi(xb[rr >> 2][j]) * bf_hi(gb[j]); }
                    if ((lane >> 4) == (rr & 3)) s += s2;
#pragma unroll
                    for (int o = 32; o >= 1; o >>= 1) s += __shfl_xor(s, o);
                    if (lane == rr) res = s;
                }
                if (lane < 16) ST4(__float_as_uint(1.f / (1.f + __expf(-res))), WS_G + (sj * 128 + w * 16 + lane) * 4);
            }
            DRAIN();
            unsigned arr = 0;
            if (lane == 0) arr = LDS_CNT_ADD(G_ST);
            if (__builtin_amdgcn_readfirstlane(arr) == 7 && lane == 0) set_flag(ws, WS_F_SD + sj * 8, tag);
        }
        STAMP(2);
        const unsigned hb = lds0 + kh * 10240;
        const unsigned laneK = hb + lane * 16, laneT = hb + 4096 + r * 8 + h * 4, laneV = hb + 5120 + h * 16 + (r < 20 ? r : 19) * 32;
        const u64 vmask = 0x000fffff000fffffull;
        u32x4 v0a = {one, one, one, one}, v0b = v0a, v1 = v0a;
        u32x4 qA, qB, ka; unsigned qtA, qtB, kt;
        {
            lds_wait(lds + G_Q, 4);
            LDS3 const char* qp = lds + L_Q + (qs >> 1) * 5120;
            const unsigned om = (qs & 1) * 2048 + lane * 16, ot = 4096 + ((qs & 1) * 64 + r) * 8 + 4 * h;
            qA = *(LDS3 const u32x4*)(qp + om); qB = *(LDS3 const u32x4*)(qp + om + 1024);
            qtA = *(LDS3 const unsigned*)(qp + ot); qtB = *(LDS3 const unsigned*)(qp + ot + 256);
            DSR128(ka, laneK, 0); DSR32(kt, laneT, 0); DSR128M2(v0a, laneV, 0, v1, laneV, 640);
            asm volatile("s_waitcnt lgkmcnt(0)" : "+v"(ka), "+v"(kt), "+v"(v0a), "+v"(v1), "+v"(qA), "+v"(qB), "+v"(qtA), "+v"(qtB) :: "memory");
            if (lane == 0) LDS_CNT_ADD(G_RD);
        }
        STAMP(5);
        float mA, mB; u32x2 qTA, qTB; f32x16 sA, sB;
        {
            const u32x2 t0 = {kt, 0x3f80u}, a0 = {qtA, 0u}, b0 = {qtB, 0u};
            const f32x16 s = MFMA32_16(ka, qA, z16);
            const f32x16 s0 = MFMA32_8(t0, a0, s);
            f32x16 s1 = MFMA32_16(ka, qB, z16); s1 = MFMA32_8(t0, b0, s1);
            float x0 = s0[0], x1 = s1[0];
#pragma unroll
            for (int i = 1; i < 16; ++i) { x0 = fmaxf(x0, s0[i]); x1 = fmaxf(x1, s1[i]); }
            x0 = -xhalf_max(x0); x1 = -xhalf_max(x1);
            const unsigned hh = pk2(x0, x1);
            const unsigned ll = pk2(x0 - bf_lo(hh), x1 - bf_hi(hh));
            mA = -(bf_lo(hh) + bf_lo(ll)); mB = -(bf_hi(hh) + bf_hi(ll));
            const unsigned sel = h ? ll : hh;
            qTA = (u32x2){qtA, sel & 0xffffu}; qTB = (u32x2){qtB, sel >> 16};
            sA = MFMA32_8(t0, qTA, s);
        }
        f32x16 oA = {}, oB = {};
        u32x4 pa, pb = {0u, 0u, 0u, 0u};
#define EX4(S, i, Pv, j) do { Pv[j] = pk2(ex2(S[i]), ex2(S[i + 1])); Pv[j + 1] = pk2(ex2(S[i + 2]), ex2(S[i + 3])); PIN(Pv); SBAR(); } while (0)
#define KT2 ((u32x2){kt, 0x3f80u})
#define TILE(V0C, V0N, aK1, oK1, aT1, oT1, oV1, aV0, oV0) do { \
            sB = MFMA32_16(ka, qB, z16); SBAR(); DSR128(ka, aK1, oK1); EX4(sA, 0, pa, 0); \
            oB = MFMA32_16(v1, pb, oB); SBAR(); DSR128M2(v1, aV, oV1, V0N, aV0, oV0); EX4(sA, 4, pa, 2); \
            sB = MFMA32_8(KT2, qTB, sB); SBAR(); DSR32(kt, aT1, oT1); EX4(sA, 8, pb, 0); \
            oA = MFMA32_16(V0C, pa, oA); SBAR(); EX4(sA, 12, pb, 2); \
            LWAIT(1); sA = MFMA32_16(ka, qA, z16); SBAR(); EX4(sB, 0, pa, 0); \
            oA = MFMA32_16(v1, pb, oA); SBAR(); EX4(sB, 4, pa, 2); \
            LWAIT(0); sA = MFMA32_8(KT2, qTA, sA); SBAR(); EX4(sB, 8, pb, 0); \
            oB = MFMA32_16(V0C, pa, oB); SBAR(); EX4(sB, 12, pb, 2); \
        } while (0)
        unsigned aK = laneK, aT = laneT, aV = laneV;
#pragma unroll 1
        for (int c = 0; c < 8; ++c) {
            if (c == 1) lds_wait(lds + C_KVL, 4);
            if (c >= 1) {
                unsigned pp;
                asm volatile("ds_write_b32 %1, %2\n\tds_read_b32 %0, %3\n\ts_waitcnt lgkmcnt(0)" : "=&v"(pp) : "v"(lds0 + C_PR + w * 4), "v"(c), "v"(lds0 + C_PR + (w ^ 4) * 4) : "memory");
                const int pc = __builtin_amdgcn_readfirstlane(pp);
                if (pc > c || (pc == c && ((c ^ kh) & 1))) __builtin_amdgcn_s_setprio(1); else __builtin_amdgcn_s_setprio(0);
            } else if (kh) __builtin_amdgcn_s_setprio(1);
            const unsigned nx = (c < 7 ? c + 1 : 7) * 20480;
            const unsigned nK = laneK + nx, nT = laneT + nx, nV = laneV + nx;
            SBAR();
            TILE(v0a, v0b, aK, 1024, aT, 256, 640,  aV, 1280);
            TILE(v0b, v0a, aK, 2048, aT, 512, 1920, aV, 2560);
            if (c == 0) {
                DRAIN();
                lds_wait(lds + G_Z, 1);
                unsigned arr = 0;
                if (lane == 0) { arr = LDS_CNT_ADD(C_GB); LDS_CNT_ADD(G_ST2); }
                if (__builtin_amdgcn_readfirstlane(arr) == 7 && lane == 0 && bid < 240) set_flag(ws, fG + 2048, tag);
                asm volatile("s_waitcnt lgkmcnt(0)" ::: "memory");
                SBAR();
            }
            TILE(v0a, v0b, aK, 3072, aT, 768, 3200, aV, 3840);
            TILE(v0b, v0a, nK, 0,    nT, 0,   4480, nV, 0);
            aK = nK; aT = nT; aV = nV;
        }
        oB = MFMA32_16(v1, pb, oB);
#undef TILE
#undef EX4
        if (lane == 0) __hip_atomic_store((LDS3 unsigned*)(lds + C_PR + w * 4), 8u, __ATOMIC_RELAXED, __HIP_MEMORY_SCOPE_WORKGROUP);
        STAMP(6);
        {
            float chk = 0.f;
#pragma unroll
            for (int i = 0; i < 12; ++i) chk += fabsf(oA[i]) * 0.f + fabsf(oB[i]) * 0.f;
            bool bad = !(chk == 0.f);
            if (h) bad = bad || !(oA[8] >= 0x1p-60f && oA[8] <= 0x1p60f && oB[8] >= 0x1p-60f && oB[8] <= 0x1p60f);
            if (__builtin_expect(__any(bad), 0)) {
                mA = -INFINITY; mB = -INFINITY;
#pragma unroll
                for (int i = 0; i < 16; ++i) { oA[i] = 0.f; oB[i] = 0.f; }
                const u32x2 a0 = {qtA, 0u}, b0 = {qtB, 0u};
#pragma unroll 1
                for (int t = 0; t < 32; ++t) {
                    const unsigned kb = WS_KV + head * 163840 + (2 * (t >> 2) + kh) * 10240, t4 = t & 3;
                    const u32x4 k0 = LD16(kb + t4 * 1024 + lane * 16);
                    const u32x2 t0 = {LD4(kb + 4096 + (t4 * 32 + r) * 8 + 4 * h), 0u};
                    u32x4 w0 = LD16(kb + 5120 + t4 * 1280 + h * 16 + (r < 20 ? r : 19) * 32), w1 = LD16(kb + 5120 + t4 * 1280 + 640 + h * 16 + (r < 20 ? r : 19) * 32);
                    if (r >= 20) { w0 = (u32x4){one, one, one, one}; w1 = w0; }
                    f32x16 s0 = MFMA32_16(k0, qA, z16); s0 = MFMA32_8(t0, a0, s0);
                    f32x16 s1 = MFMA32_16(k0, qB, z16); s1 = MFMA32_8(t0, b0, s1);
                    float x0 = s0[0], x1 = s1[0];
#pragma unroll
                    for (int i = 1; i < 16; ++i) { x0 = fmaxf(x0, s0[i]); x1 = fmaxf(x1, s1[i]); }
                    x0 = fmaxf(xhalf_max(x0), mA); x1 = fmaxf(xhalf_max(x1), mB);
                    const float g0 = (mA == x0) ? 1.f : ex2(mA - x0), g1 = (mB == x1) ? 1.f : ex2(mB - x1);
                    mA = x0; mB = x1;
#pragma unroll
                    for (int i = 0; i < 16; ++i) { oA[i] *= g0; oB[i] *= g1; }
#pragma unroll
                    for (int i = 0; i < 4; ++i) { pa[i] = pk2(ex2(s0[2 * i] - x0), ex2(s0[2 * i + 1] - x0)); pb[i] = pk2(ex2(s0[8 + 2 * i] - x0), ex2(s0[9 + 2 * i] - x0)); }
                    oA = MFMA32_16(w0, pa, oA); oA = MFMA32_16(w1, pb, oA);
#pragma unroll
                    for (int i = 0; i < 4; ++i) { pa[i] = pk2(ex2(s1[2 * i] - x1), ex2(s1[2 * i + 1] - x1)); pb[i] = pk2(ex2(s1[8 + 2 * i] - x1), ex2(s1[9 + 2 * i] - x1)); }
                    oB = MFMA32_16(w0, pa, oB); oB = MFMA32_16(w1, pb, oB);
                }
            }
        }
        lds_wait<4>(lds + C_BX, 8);
        const float gO = *(LDS3 const float*)(lds + X_G + (qs * 64 + kh * 32 + r) * 4);
        {
            LDS3 char* xp = lds + X_MRG + w * 2816;
            const f32x16& s = kh ? oA : oB;
            *(LDS3 f32x4*)(xp + lane * 16) = (f32x4){s[0], s[1], s[2], s[3]};
            *(LDS3 f32x4*)(xp + 1024 + lane * 16) = (f32x4){s[4], s[5], s[6], s[7]};
            if (!h) *(LDS3 f32x4*)(xp + 2048 + r * 16) = (f32x4){s[8], s[9], s[10], s[11]};
            else *(LDS3 f32x2*)(xp + 2560 + r * 8) = (f32x2){s[8], kh ? mA : mB};
            asm volatile("s_waitcnt lgkmcnt(0)" ::: "memory");
            if (lane == 0) __hip_atomic_store((LDS3 unsigned*)(lds + C_MRG + w * 4), 1u, __ATOMIC_RELAXED, __HIP_MEMORY_SCOPE_WORKGROUP);
        }
        lds_wait<2>(lds + C_MRG + (w ^ 4) * 4, 1);
        bool lastw;
        {
            LDS3 const char* xp = lds + X_MRG + (w ^ 4) * 2816;
            const f32x4 p0 = *(LDS3 const f32x4*)(xp + lane * 16), p1 = *(LDS3 const f32x4*)(xp + 1024 + lane * 16), p2 = *(LDS3 const f32x4*)(xp + 2048 + r * 16);
            const f32x2 lm = *(LDS3 const f32x2*)(xp + 2560 + r * 8);
            const f32x16& s = kh ? oB : oA;
            const float mo = kh ? mB : mA, mp = lm[1];
            const float mx = fmaxf(mo, mp);
            const float fo = (mo == mx) ? 1.f : ex2(mo - mx), fq = (mp == mx) ? 1.f : ex2(mp - mx);
            float o[12];
#pragma unroll
            for (int i = 0; i < 4; ++i) { o[i] = s[i] * fo + p0[i] * fq; o[4 + i] = s[4 + i] * fo + p1[i] * fq; o[8 + i] = s[8 + i] * fo + p2[i] * fq; }
            const float l = upper_half(s[8]) * fo + lm[0] * fq;
            store_o(rs, o, gO / l, q0 + kh * 32 + r, 0, head, h);
            DRAIN();
            unsigned arr = 0;
            if (lane == 0) arr = LDS_CNT_ADD(C_OA);
            lastw = __builtin_amdgcn_readfirstlane(arr) == 7;
            if (lastw && lane == 0) set_flag(ws, WS_F_OA + (qb * 32 + head) * 8, tag);
        }
        STAMP(7);
        {
            const int c16 = lane & 15, g = lane >> 4, tt = w & 3, k0 = kh * 10;
            POLL_ONE(lastw, 4, WS_F_OA + ((mtO >> 2) * 32 + r) * 8, WS_F_OA + ((mtO >> 2) * 32 + r) * 8, C_P4);
            STAMP(9);
            const unsigned ab = WS_AO + ((mtO * 4 + tt) * 40 + k0) * 1024;
            u32x4 af[10];
#pragma unroll
            for (int ks = 0; ks < 6; ++ks) af[ks] = LD16(ab + ks * 1024 + lane * 16);
            if (kh) {
#pragma unroll
                for (int ks = 6; ks < 10; ++ks) LD_TAIL(af[ks], ab + ks * 1024);
            } else {
#pragma unroll
                for (int ks = 6; ks < 10; ++ks) af[ks] = LD16(ab + ks * 1024 + lane * 16);
            }
            lds_wait(lds + C_WA, 4);
            lds_wait(lds + C_OA, 8);
            f32x4 c[5];
#pragma unroll
            for (int n = 0; n < 5; ++n) c[n] = (f32x4){0.f, 0.f, 0.f, 0.f};
#pragma unroll
            for (int ks = 0; ks < 10; ++ks)
#pragma unroll
                for (int n = 0; n < 5; ++n) c[n] = MFMA16_32(*(LDS3 const u32x4*)(lds + L_WA + (n * 20 + k0 + ks) * 1024 + lane * 16), af[ks], c[n]);
            LDS3 char* xp = lds + X_MRG + tt * 5120 + lane * 16;
            if (w < 4) { *(LDS3 f32x4*)(xp + 3072) = c[3]; *(LDS3 f32x4*)(xp + 4096) = c[4]; }
            else { *(LDS3 f32x4*)(xp) = c[0]; *(LDS3 f32x4*)(xp + 1024) = c[1]; *(LDS3 f32x4*)(xp + 2048) = c[2]; }
            asm volatile("s_waitcnt lgkmcnt(0)" ::: "memory");
            if (lane == 0) __hip_atomic_store((LDS3 unsigned*)(lds + C_FIN + w * 4), 1u, __ATOMIC_RELAXED, __HIP_MEMORY_SCOPE_WORKGROUP);
            lds_wait(lds + C_FIN + (w ^ 4) * 4, 1);
            lds_wait(lds + C_BP + tt * 4, 1);
            u16* op = P.out + (size_t)(mtO * 64 + tt * 16 + c16) * DM + ntO * 80 + g * 4;
#pragma unroll
            for (int n = 0; n < 5; ++n) {
                if ((w < 4) == (n < 3)) {
                    const f32x4 bp = *(LDS3 const f32x4*)(n < 3 ? lds + X_FIN + tt * 3072 + n * 1024 + lane * 16 : lds + 143360 + tt * 2048 + (n - 3) * 1024 + lane * 16);
                    const f32x4 s = c[n] + *(LDS3 const f32x4*)(xp + n * 1024) + bp;
                    *(u32x2*)(op + n * 16) = (u32x2){pk2(s[0], s[1]), pk2(s[2], s[3])};
                }
            }
            if (bid == 0 && mtid == 0) set_flag(ws, WS_EPOCH, tag);
        }
        STAMP(10);
        asm volatile("mega_code_end:");
    }
}

extern "C" void kernel_launch(void* const* d_in, const int* in_sizes, int n_in, void* d_out, int out_size, void* d_ws, size_t ws_size,
                              hipStream_t stream) {
    Params P;
    P.x = (const u16*)d_in[0]; P.wa = (const u16*)d_in[1]; P.woa = (const u16*)d_in[2]; P.wb = (const u16*)d_in[4];
    P.wphi = (const u16*)d_in[5]; P.wob = (const u16*)d_in[6]; P.gw = (const u16*)d_in[7]; P.ws = (char*)d_ws; P.out = (u16*)d_out;
    mega<<<256, 768, 0, stream>>>(P);
}
```

```cpp
#include <hip/hip_runtime.h>
#include <hip/hip_bf16.h>
#include <stdint.h>
#include <type_traits>

typedef __attribute__((ext_vector_type(8))) short bf16x8;
typedef __attribute__((ext_vector_type(4))) short bf16x4;
typedef __attribute__((ext_vector_type(16))) float f32x16;
typedef __attribute__((ext_vector_type(4))) float f32x4;
typedef __attribute__((ext_vector_type(2))) float f32x2;
typedef __attribute__((ext_vector_type(4))) unsigned u32x4;
typedef __attribute__((ext_vector_type(2))) unsigned u32x2;
typedef __attribute__((ext_vector_type(2))) __bf16 bf2_t;
typedef unsigned short u16;
typedef unsigned long long u64;
#define LDS3 __attribute__((address_space(3)))
typedef __attribute__((address_space(1))) u64 gu64;
#define RLX_AGENT __ATOMIC_RELAXED, __HIP_MEMORY_SCOPE_AGENT

#define PRIO_MODE 3
#define WARM_MODE 1
#define GA_PUB 11
#define PHI_ROUND 0
#define PRE_SLEEP 2
constexpr int SEQ = 2048, DM = 640, NH = 32;
constexpr float LOG2E = 1.4426950408889634f;
constexpr float QSCALE = 0.22360679774997896f * LOG2E;

constexpr unsigned WS_EPOCH = 0;
constexpr unsigned WS_F_G   = 256;
constexpr unsigned WS_F_SD  = WS_F_G + 4096;
constexpr unsigned WS_F_KV  = WS_F_SD + 256;
constexpr unsigned WS_F_OB  = WS_F_KV + 2048;
constexpr unsigned WS_F_OA  = WS_F_OB + 2048;
constexpr unsigned WS_G     = 16384;
constexpr unsigned WS_Q     = 32768;
constexpr unsigned WS_KV    = WS_Q + 2 * NH * 81920;
constexpr unsigned WS_KVP   = WS_KV + 2 * NH * 163840;
constexpr unsigned WS_AO    = WS_KVP + NH * 8 * 4096;
constexpr unsigned WS_WT    = WS_AO + SEQ * 1280 * 2;
constexpr unsigned WS_END   = WS_WT + 1280 * 640 * 2;

#define STAMP(i)

__device__ __forceinline__ unsigned pk2(float a, float b) { f32x2 v = {a, b}; bf2_t r = __builtin_convertvector(v, bf2_t); return __builtin_bit_cast(unsigned, r); }
__device__ __forceinline__ float bf_lo(unsigned u) { return __uint_as_float(u << 16); }
__device__ __forceinline__ float bf_hi(unsigned u) { return __uint_as_float(u & 0xffff0000u); }
__device__ __forceinline__ float ex2(float x) { return __builtin_amdgcn_exp2f(x); }
#define MFMA32_16(a, b, c) __builtin_amdgcn_mfma_f32_32x32x16_bf16(__builtin_bit_cast(bf16x8, a), __builtin_bit_cast(bf16x8, b), c, 0, 0, 0)
#define MFMA32_8(a, b, c) __builtin_amdgcn_mfma_f32_32x32x8bf16_1k(__builtin_bit_cast(bf16x4, a), __builtin_bit_cast(bf16x4, b), c, 0, 0, 0)
#define MFMA16_32(a, b, c) __builtin_amdgcn_mfma_f32_16x16x32_bf16(__builtin_bit_cast(bf16x8, a), __builtin_bit_cast(bf16x8, b), c, 0, 0, 0)

__device__ __forceinline__ void glds16(unsigned voff, const void* sbase, unsigned lds_base) {
    unsigned sv;
    asm volatile("s_mov_b32 %0, m0\n\ts_mov_b32 m0, %3\n\ts_nop 0\n\tglobal_load_lds_dwordx4 %1, %2\n\ts_mov_b32 m0, %0"
                 : "=&s"(sv) : "v"(voff), "s"(sbase), "s"(__builtin_amdgcn_readfirstlane(lds_base)) : "memory");
}
__device__ __forceinline__ void glds16s(unsigned voff, const void* sbase, unsigned lds_base) {
    unsigned sv;
    asm volatile("s_mov_b32 %0, m0\n\ts_mov_b32 m0, %3\n\ts_nop 0\n\tglobal_load_lds_dwordx4 %1, %2 sc1\n\ts_mov_b32 m0, %0"
                 : "=&s"(sv) : "v"(voff), "s"(sbase), "s"(__builtin_amdgcn_readfirstlane(lds_base)) : "memory");
}
__device__ __forceinline__ void glds4s(unsigned voff, const void* sbase, unsigned lds_base) {
    unsigned sv;
    asm volatile("s_mov_b32 %0, m0\n\ts_mov_b32 m0, %3\n\ts_nop 0\n\tglobal_load_lds_dword %1, %2 sc1\n\ts_mov_b32 m0, %0"
                 : "=&s"(sv) : "v"(voff), "s"(sbase), "s"(__builtin_amdgcn_readfirstlane(lds_base)) : "memory");
}
__device__ __forceinline__ void glds16x4(const unsigned (&vo)[4], const void* sbase, unsigned lds_base) {
    unsigned sv;
    asm volatile("s_mov_b32 %0, m0\n\ts_mov_b32 m0, %6\n\ts_nop 0\n\tglobal_load_lds_dwordx4 %1, %5\n\tglobal_load_lds_dwordx4 %2, %5 offset:1024\n\t"
                 "global_load_lds_dwordx4 %3, %5 offset:2048\n\tglobal_load_lds_dwordx4 %4, %5 offset:3072\n\ts_mov_b32 m0, %0"
                 : "=&s"(sv) : "v"(vo[0]), "v"(vo[1]), "v"(vo[2]), "v"(vo[3]), "s"(sbase), "s"(lds_base) : "memory");
}
typedef short v4i16_t __attribute__((ext_vector_type(4)));
__device__ __forceinline__ u32x2 trread(LDS3 const char* p) {
    return __builtin_bit_cast(u32x2, __builtin_amdgcn_ds_read_tr16_b64_v4i16((LDS3 v4i16_t*)p));
}
#define WAIT_BAR(N) asm volatile("s_waitcnt vmcnt(" #N ") lgkmcnt(0)\n\ts_barrier" ::: "memory")
#define DRAIN() asm volatile("s_waitcnt vmcnt(0)" ::: "memory")
#define SBAR() __builtin_amdgcn_sched_barrier(0)
#define PIN(x) asm volatile("" : "+v"(x))

__device__ __forceinline__ float xhalf_max(float x) {
    auto r = __builtin_amdgcn_permlane32_swap(__float_as_uint(x), __float_as_uint(x), false, false);
    return fmaxf(__uint_as_float(r[0]), __uint_as_float(r[1]));
}
__device__ __forceinline__ float xhalf_sum(float x) {
    auto r = __builtin_amdgcn_permlane32_swap(__float_as_uint(x), __float_as_uint(x), false, false);
    return __uint_as_float(r[0]) + __uint_as_float(r[1]);
}
__device__ __forceinline__ float upper_half(float x) {
    auto r = __builtin_amdgcn_permlane32_swap(__float_as_uint(x), __float_as_uint(x), false, false);
    return __uint_as_float(r[1]);
}

typedef __amdgpu_buffer_rsrc_t rsrc_t;
#define ST8(v, off)  __builtin_amdgcn_raw_buffer_store_b64(v, rs, (int)(off), 0, 16)
#define ST16(v, off) __builtin_amdgcn_raw_buffer_store_b128(v, rs, (int)(off), 0, 16)
#define ST4(v, off)  __builtin_amdgcn_raw_buffer_store_b32(v, rs, (int)(off), 0, 16)
#define LD16(off) __builtin_amdgcn_raw_buffer_load_b128(rs, (int)(off), 0, 16)
#define LD4(off)  __builtin_amdgcn_raw_buffer_load_b32(rs, (int)(off), 0, 16)

template <int SLP = 2>
__device__ __forceinline__ void wait_flags(const char* ws, unsigned off0, unsigned off1, u64 tag) {
    gu64* p0 = (gu64*)(ws + off0); gu64* p1 = (gu64*)(ws + off1);
#pragma unroll 1
    for (unsigned spins = 0; spins < (1u << 16); ++spins) {
        const u64 a = __hip_atomic_load(p0, RLX_AGENT), b = __hip_atomic_load(p1, RLX_AGENT);
        if (__all(a == tag && b == tag)) break;
        __builtin_amdgcn_s_sleep(SLP);
    }
    __builtin_amdgcn_fence(__ATOMIC_ACQUIRE, "wavefront");
}
__device__ __forceinline__ void set_flag(char* ws, unsigned off, u64 tag) { __hip_atomic_store((gu64*)(ws + off), tag, RLX_AGENT); }

struct Params { const u16* x; const u16* wa; const u16* wb; const u16* wphi; const u16* woa; const u16* wob; const u16* gw; char* ws; u16* out; };

__device__ __forceinline__ void feature_map(const u32x4& pm, unsigned pt, const u32x4& tm, unsigned tt, int h, u32x4& fp, u32x4& fn, u32x4& ft) {
    const f32x16 z16 = {};
    const u32x2 a8 = {pt, 0u}, b8 = {tt, 0u};
    f32x16 s = MFMA32_16(pm, tm, z16); s = MFMA32_8(a8, b8, s);
    f32x2 v[6];
#pragma unroll
    for (int i = 0; i < 6; ++i) {
        v[i] = (f32x2){s[2 * i], s[2 * i + 1]};
    }
    float mx = fmaxf(v[0][0], v[0][1]), mn = fminf(v[0][0], v[0][1]);
#pragma unroll
    for (int i = 1; i < 6; ++i) { mx = fmaxf(fmaxf(mx, v[i][0]), v[i][1]); mn = fminf(fminf(mn, v[i][0]), v[i][1]); }
    mx = xhalf_max(mx) * -LOG2E; mn = xhalf_max(-mn) * -LOG2E;
    f32x2 ep[6], en[6];
#pragma unroll
    for (int i = 0; i < 6; ++i) {
        const f32x2 a = v[i] * LOG2E + mx, b = v[i] * -LOG2E + mn;
        ep[i] = (f32x2){ex2(a[0]), ex2(a[1])}; en[i] = (f32x2){ex2(b[0]), ex2(b[1])};
    }
    const float tw = h ? 0.f : 1.f;
    const f32x2 s2p = ((ep[0] + ep[1]) + (ep[2] + ep[3])) + (ep[4] + ep[5]) * tw, s2n = ((en[0] + en[1]) + (en[2] + en[3])) + (en[4] + en[5]) * tw;
    const float sp = __builtin_amdgcn_rcpf(xhalf_sum(s2p[0] + s2p[1])), sn = __builtin_amdgcn_rcpf(xhalf_sum(s2n[0] + s2n[1]));
    const float tp = sp * tw, tn = sn * tw;
#pragma unroll
    for (int i = 0; i < 4; ++i) { const f32x2 a = ep[i] * sp, b = en[i] * sn; fp[i] = pk2(a[0], a[1]); fn[i] = pk2(b[0], b[1]); }
    const f32x2 c0 = ep[4] * tp, c1 = ep[5] * tp, c2 = en[4] * tn, c3 = en[5] * tn;
    ft = (u32x4){pk2(c0[0], c0[1]), pk2(c1[0], c1[1]), pk2(c2[0], c2[1]), pk2(c3[0], c3[1])};
}

__device__ __forceinline__ void store_o(rsrc_t rs, const float (&o)[12], float sc, int tok, int half, int head, int h) {
    const auto s0 = __builtin_amdgcn_permlane32_swap(pk2(o[0] * sc, o[1] * sc), pk2(o[4] * sc, o[5] * sc), false, false);
    const auto s1 = __builtin_amdgcn_permlane32_swap(pk2(o[2] * sc, o[3] * sc), pk2(o[6] * sc, o[7] * sc), false, false);
    const unsigned tb = WS_AO + ((tok >> 4) * 40 + half * 20) * 1024;
    const u32x4 mv = {s0[0], s1[0], s0[1], s1[1]};
    ST16(mv, tb + (head >> 1) * 1024 + (((head & 1) * 2 + h) * 16 + (tok & 15)) * 16);
    const u32x2 tv = {pk2(o[8] * sc, o[9] * sc), pk2(o[10] * sc, o[11] * sc)};
    if (!h) ST8(tv, tb + (16 + (head >> 3)) * 1024 + (head & 7) * 128 + (tok & 15) * 8);
}
#define LD8(off)  __builtin_amdgcn_raw_buffer_load_b64(rs, (int)(off), 0, 16)
#define LD_TAIL(dst, base) do { const u32x2 a_ = LD8((base) + (lane >> 4) * 256 + (lane & 15) * 8), b_ = LD8((base) + (lane >> 4) * 256 + 128 + (lane & 15) * 8); dst = (u32x4){a_[0], a_[1], b_[0], b_[1]}; } while (0)

#define DSR128(dst, addr, off) asm volatile("ds_read_b128 %0, %1 offset:%2" : "=v"(dst) : "v"(addr), "i"(off))
#define DSR32(dst, addr, off) asm volatile("ds_read_b32 %0, %1 offset:%2" : "=v"(dst) : "v"(addr), "i"(off))
#define DSR128M2(d0, a0, o0, d1, a1, o1) asm volatile("s_mov_b64 exec, %6\n\tds_read_b128 %0, %2 offset:%3\n\tds_read_b128 %1, %4 offset:%5\n\ts_mov_b64 exec, -1" \
                                                      : "+v"(d0), "+v"(d1) : "v"(a0), "i"(o0), "v"(a1), "i"(o1), "s"(vmask))
#define LWAIT(n) do { asm volatile("s_waitcnt lgkmcnt(" #n ")" ::: "memory"); SBAR(); } while (0)

#define BAR() asm volatile("s_barrier" ::: "memory")
#define LDS_SET(off, v) __hip_atomic_store((LDS3 unsigned*)(lds + (off)), (unsigned)(v), __ATOMIC_RELAXED, __HIP_MEMORY_SCOPE_WORKGROUP)
#define LDS_CNT_ADD(off) __hip_atomic_fetch_add((LDS3 unsigned*)(lds + (off)), 1u, __ATOMIC_RELAXED, __HIP_MEMORY_SCOPE_WORKGROUP)

template <int SLP = 1>
__device__ __forceinline__ void lds_wait(LDS3 char* p, unsigned want) {
#pragma unroll 1
    for (unsigned spins = 0; spins < (1u << 20); ++spins) {
        if ((unsigned)__builtin_amdgcn_readfirstlane(__hip_atomic_load((LDS3 unsigned*)p, __ATOMIC_RELAXED, __HIP_MEMORY_SCOPE_WORKGROUP)) >= want) break;
        __builtin_amdgcn_s_sleep(SLP);
    }
    __builtin_amdgcn_fence(__ATOMIC_ACQUIRE, "wavefront");
}

template <int SLP>
__device__ __forceinline__ void prog_wait(LDS3 char* p, int lane, unsigned want) {
#pragma unroll 1
    for (unsigned spins = 0; spins < (1u << 20); ++spins) {
        if (__all(__hip_atomic_load((LDS3 unsigned*)p + (lane & 7), __ATOMIC_RELAXED, __HIP_MEMORY_SCOPE_WORKGROUP) >= want)) break;
        __builtin_amdgcn_s_sleep(SLP);
    }
    __builtin_amdgcn_fence(__ATOMIC_ACQUIRE, "wavefront");
}
#define POLL_ONE(cond, SLP, o0, o1, word) do { if (cond) { wait_flags<SLP>(ws, o0, o1, tag); \
        if (lane == 0) __hip_atomic_store((LDS3 unsigned*)(lds + (word)), 1u, __ATOMIC_RELAXED, __HIP_MEMORY_SCOPE_WORKGROUP); } else lds_wait<2>(lds + (word), 1); } while (0)
constexpr int C_KV = 0, C_KVF = 4, C_OB = 8, C_WA = 16, C_OA = 20, C_BX = 28, C_MRG = 32  , C_FIN = 64  , C_BP = 96  ;
constexpr int C_P1 = 112, C_P2 = 116, C_P3 = 120, C_P4 = 124, C_PR = 128  , C_KVL = 160, C_P5 = 164, C_GB = 168;
constexpr int G_W = 0, G_Q = 176, G_P = 180, G_ST = 184, G_RD = 188;
constexpr int X_G = 256;
constexpr int X_KVF = 1280, X_SLOT = 4096;
constexpr int X_MRG = 1280;
constexpr int L_WA = 24576;
constexpr int L_Q = 81920;
constexpr int X_FIN = 126976;

__global__ __launch_bounds__(768) void mega(Params P) {
    __shared__ __attribute__((aligned(16))) char smem[163840];
    LDS3 char* lds = (LDS3 char*)smem;
    char* ws = P.ws;
    const int tid = threadIdx.x, lane = tid & 63, r = lane & 31, h = lane >> 5;
    const int hw = __builtin_amdgcn_readfirstlane(tid >> 6);
    const bool helper = hw < 4;
    const int w = hw - 4, mtid = tid - 256;
    const int bid = blockIdx.x;
    const unsigned lds0 = (unsigned)(uintptr_t)smem;
    const rsrc_t rs = __builtin_amdgcn_make_buffer_rsrc(ws, 0, WS_END, 0x00020000);
    STAMP(0);
    u64 tag = 0;
#define GET_TAG() do { asm volatile("s_load_dwordx2 %0, %1, 0x0\n\ts_waitcnt lgkmcnt(0)" : "=s"(tag) : "s"(ws) : "memory"); tag += 1; } while (0)
    const int head = (bid & 7) * 4 + ((bid >> 3) & 3), qb = bid >> 5;
    const int nlo = (head * 20) >> 7, nhi = (head * 20 + 19) >> 7;
    const int mtO = (bid & 7) * 4 + ((bid >> 3) & 3), ntO = bid >> 5;
    const int lin = (bid & 7) * 30 + (bid >> 3), lh = lin >= 120, lrem = lin - 120 * lh;
    const int mt = lh * 8 + (lrem & 7), nt = lrem >> 3;
    const unsigned fG = WS_F_G + (nt * 16 + mt) * 8;
    const unsigned one = (r == 20) ? 0x3f803f80u : 0u;
    const f32x16 z16 = {};

    if (helper) {
        const unsigned fA0 = WS_F_G + (((lane < 32 ? 5 : 10) + ((lane & 16) ? nhi : nlo)) * 16 + (lane & 15)) * 8;
        const unsigned fA1 = WS_F_G + (((lane & 2) ? nhi : nlo) * 16 + qb * 2 + (lane & 1)) * 8;
        __builtin_amdgcn_s_setprio(3);
        const char* kvb = ws + WS_KV + head * 163840;
        auto kv_issue = [&](int grp, int t0 = 0, int t1 = 8) {
#pragma unroll
            for (int t = t0; t < t1; ++t) { int pc = grp * 32 + hw + 4 * t; if (pc == 0) pc = 4; glds16s(lane * 16, kvb + pc * 1024, lds0 + pc * 1024); }
        };
        auto q_issue = [&]() {
            const char* qbase = ws + WS_Q + head * 81920 + qb * 10240;
#pragma unroll
            for (int t = 0; t < 3; ++t) { const int pc = hw + 4 * t < 10 ? hw + 4 * t : 10; glds16s(lane * 16, pc < 10 ? qbase + pc * 1024 : kvb, lds0 + L_Q + pc * 1024); }
        };
        if (bid < 240) {
            const int rot = (nt * 3 + mt * 5 + (mt >> 1)) % 10;
            unsigned xo[4], wo[4];
#pragma unroll
            for (int i = 0; i < 4; ++i) {
                xo[i] = (mt * 128 + 32 * hw + 8 * i + (lane >> 3)) * 1280 + (((lane & 7) ^ ((4 * i + (lane >> 4)) & 7)) << 4) - i * 1024;
                wo[i] = (16 * hw + 4 * i + (lane >> 4)) * 3840 + nt * 256 + (((lane & 15) ^ (((lane >> 4) << 2) | i)) << 4) - i * 1024;
            }
            const unsigned dX = lds0 + 32768 + hw * 4096;
            int kt = rot;
#pragma unroll
            for (int n = 0; n < 20; ++n) {
                const char* xb = (const char*)P.x + kt * 128;
                const char* wb = (const char*)(n >= 10 ? P.wb : P.wa) + (size_t)kt * (64 * 3840);
                asm volatile("" : "+s"(xb), "+s"(wb));
                if (n == 2) { if (hw == 0) *(LDS3 unsigned*)(lds + lane * 4) = 0u; GET_TAG(); WAIT_BAR(8); }
                else if (n == 11 || n == 15) { WAIT_BAR(12); }
                else if (n == 3 || (n >= 12 && n <= 14)) { WAIT_BAR(8); }
                else if (n >= 5) { WAIT_BAR(16); }
                glds16x4(wo, wb, dX + 16384 + (n & 3) * 32768);
                if (n < 10 || n >= 14) glds16x4(xo, xb, dX + ((n >= 10 ? 20 - n : n + 1) & 3) * 32768);
                if (n < 9) kt = kt == 9 ? 0 : kt + 1; else if (n > 9) kt = kt == 0 ? 9 : kt - 1;
            }
            WAIT_BAR(16);
            WAIT_BAR(0);
        } else {
            if (hw == 0) *(LDS3 unsigned*)(lds + lane * 4) = 0u;
            GET_TAG();
            WAIT_BAR(63);
            if (hw == 0) {
#pragma unroll 1
                for (int t = 0; t < PRE_SLEEP; ++t) __builtin_amdgcn_s_sleep(127);
            }
        }
        POLL_ONE(hw == 0, 6, fA0, fA1, G_P);
        kv_issue(0); q_issue(); kv_issue(1, 0, 2);
        DRAIN();
        if (lane == 0) LDS_CNT_ADD(G_Q);
        lds_wait<2>(lds + G_RD, 8);
        kv_issue(1, 2, 8); kv_issue(2); kv_issue(3); kv_issue(4);
        asm volatile("s_waitcnt vmcnt(24)" ::: "memory"); if (lane == 0) LDS_CNT_ADD(C_KVL);
        asm volatile("s_waitcnt vmcnt(16)" ::: "memory"); if (lane == 0) LDS_CNT_ADD(C_KVL + 12);
        STAMP(3);
        u32x4 phm; unsigned pht;
        {
            const u16* wp = P.wphi + head * 400 + (r < 20 ? r : (r & 3));
            unsigned e[10];
#pragma unroll
            for (int j = 0; j < 8; ++j) e[j] = wp[(8 * h + j + 4 * (head & 1)) * 20];
            e[8] = wp[((head & 1 ? 0 : 16) + 2 * h) * 20]; e[9] = wp[((head & 1 ? 1 : 17) + 2 * h) * 20];
            const int bu = bid < 160 ? bid : 0, hf = bu >= 80, k0 = (bu - 80 * hf) * 8, hd = k0 < 512 ? k0 >> 4 : (k0 - 512) >> 2;
            const int row = k0 < 512 ? 20 * hd + (k0 & 8) + 4 * (hd & 1) : 20 * hd + 16;
            const u16* wsrc = (hf ? P.wob : P.woa) + (size_t)row * DM;
            unsigned f[3][8];
            if (bid < 160) {
#pragma unroll
                for (int q = 0; q < 3; ++q) {
                    const int n = tid + 256 * q < 640 ? tid + 256 * q : 639;
#pragma unroll
                    for (int j = 0; j < 8; ++j) f[q][j] = wsrc[j * DM + n];
                }
            }
            DRAIN();
            if (lane == 0) LDS_CNT_ADD(C_WA + 8);
            phm = (u32x4){e[0] | (e[1] << 16), e[2] | (e[3] << 16), e[4] | (e[5] << 16), e[6] | (e[7] << 16)}; pht = e[8] | (e[9] << 16);
            if (bid < 160) {
#pragma unroll
                for (int q = 0; q < 3; ++q) {
                    const int n = tid + 256 * q;
                    const u32x4 v = {f[q][0] | (f[q][1] << 16), f[q][2] | (f[q][3] << 16), f[q][4] | (f[q][5] << 16), f[q][6] | (f[q][7] << 16)};
                    if (n < 640) ST16(v, WS_WT + (((n >> 4) * 40 + hf * 20 + (k0 >> 5)) * 64 + ((k0 >> 3) & 3) * 16 + (n & 15)) * 16);
                }
            }
        }
        const int tokS = qb * 256 + hw * 64, m7 = tokS >> 7, t4 = (tokS >> 5) & 3;
        {
            const unsigned pt3 = (lane >> 1) & 3;
            const unsigned tb = WS_F_G + 2048 + ((((lane & 1) ? nhi : nlo) + (pt3 < 3 ? pt3 : 0) * 5) * 16 + qb * 2 + ((lane >> 3) & 1)) * 8;
            POLL_ONE(hw == 0, 4, tb, WS_F_SD + (qb * 2 + (lane & 1)) * 8, C_P1);
        }
        const unsigned om = t4 * 1024 + lane * 16, ot = 4096 + (t4 * 32 + r) * 8 + 4 * h;
        u32x4 qf[2][3]; float gS[2];
        {
            const unsigned kb = WS_KV + (NH + head) * 163840 + (2 * (m7 & 7) + (m7 >> 3)) * 10240, qbb = WS_Q + (NH + head) * 81920 + m7 * 5120;
            const unsigned ov = 5120 + t4 * 1280 + h * 16 + (r < 20 ? r : 19) * 32;
            u32x4 e0[2], w0[2], w1[2], eq[2]; unsigned et[2], eqt[2];
#pragma unroll
            for (int i = 0; i < 2; ++i) {
                e0[i] = LD16(kb + om + i * 1024); et[i] = LD4(kb + ot + i * 256); w0[i] = LD16(kb + ov + i * 1280); w1[i] = LD16(kb + ov + i * 1280 + 640);
                eq[i] = LD16(qbb + om + i * 1024); eqt[i] = LD4(qbb + ot + i * 256); gS[i] = __uint_as_float(LD4(WS_G + (tokS + i * 32 + r) * 4));
            }
            u32x4 i0, i1;
#pragma unroll
            for (int j = 0; j < 4; ++j) {
                const int e = r - 8 * h - 2 * j;
                i0[j] = e == 0 ? 0x3f80u : (e == 1 ? 0x3f800000u : 0u);
                i1[j] = e == 16 ? 0x3f80u : (e == 17 ? 0x3f800000u : 0u);
            }
            f32x16 k0 = {}, k1 = {};
#pragma unroll
            for (int i = 0; i < 2; ++i) {
                if (r >= 20) { w0[i] = (u32x4){one, one, one, one}; w1[i] = w0[i]; }
                u32x4 fp, fn, ft;
                feature_map(phm, pht, e0[i], et[i], h, fp, fn, ft);
                f32x16 T0 = MFMA32_16(fp, i0, z16); T0 = MFMA32_16(fn, i1, T0);
                f32x16 T1 = MFMA32_16(ft, i0, z16);
                u32x4 b00, b01, b10, b11;
#pragma unroll
                for (int j = 0; j < 4; ++j) {
                    b00[j] = pk2(T0[2 * j], T0[2 * j + 1]); b01[j] = pk2(T0[8 + 2 * j], T0[9 + 2 * j]);
                    b10[j] = pk2(T1[2 * j], T1[2 * j + 1]); b11[j] = pk2(T1[8 + 2 * j], T1[9 + 2 * j]);
                }
                k0 = MFMA32_16(w0[i], b00, k0); k0 = MFMA32_16(w1[i], b01, k0);
                k1 = MFMA32_16(w0[i], b10, k1); k1 = MFMA32_16(w1[i], b11, k1);
            }
            prog_wait<8>(lds + C_PR, lane, 1);
            *(LDS3 float*)(lds + X_G + (hw * 64 + lane) * 4) = h ? gS[1] : gS[0];
            if (hw * 64 + lane < 144) *(LDS3 u32x4*)(lds + X_KVF + (hw * 64 + lane) * 16) = (u32x4){0u, 0u, 0u, 0u};
            LDS3 char* sp = lds + X_SLOT + hw * 4096;
#pragma unroll
            for (int gq = 0; gq < 3; ++gq) {
                *(LDS3 f32x4*)(sp + gq * 1024 + lane * 16) = (f32x4){k0[4 * gq], k0[4 * gq + 1], k0[4 * gq + 2], k0[4 * gq + 3]};
                if (r < 8) *(LDS3 f32x4*)(sp + 3072 + (gq * 16 + h * 8 + r) * 16) = (f32x4){k1[4 * gq], k1[4 * gq + 1], k1[4 * gq + 2], k1[4 * gq + 3]};
            }
            asm volatile("s_waitcnt vmcnt(0) lgkmcnt(0)" ::: "memory");
            unsigned arr = 0;
            if (lane == 0) arr = LDS_CNT_ADD(C_KV);
            if (__builtin_amdgcn_readfirstlane(arr) == 3) {
                __builtin_amdgcn_fence(__ATOMIC_ACQUIRE, "wavefront");
#pragma unroll
                for (int i = 0; i < 4; ++i) {
                    const int qd = lane + 64 * i;
                    LDS3 const char* s0 = lds + X_SLOT + qd * 16;
                    const f32x4 s = (*(LDS3 const f32x4*)s0 + *(LDS3 const f32x4*)(s0 + 4096)) + (*(LDS3 const f32x4*)(s0 + 8192) + *(LDS3 const f32x4*)(s0 + 12288));
                    if (qd < 240) ST16(__builtin_bit_cast(u32x4, s), WS_KVP + (head * 8 + qb) * 4096 + qd * 16);
                }
                DRAIN();
                if (lane == 0) set_flag(ws, WS_F_KV + (head * 8 + qb) * 8, tag);
            }
#pragma unroll
            for (int i = 0; i < 2; ++i) feature_map(phm, pht, eq[i], eqt[i], h, qf[i][0], qf[i][1], qf[i][2]);
        }
        STAMP(4);
        POLL_ONE(hw == 0, 4, WS_F_KV + (head * 8 + (lane & 7)) * 8, WS_F_KV + (head * 8 + (lane & 7)) * 8, C_P2);
        {
            const int qd = hw * 60 + (lane < 60 ? lane : 59);
            f32x4 pp[8];
#pragma unroll
            for (int p = 0; p < 8; ++p) pp[p] = __builtin_bit_cast(f32x4, LD16(WS_KVP + (head * 8 + p) * 4096 + qd * 16));
            const f32x4 s4 = ((pp[0] + pp[1]) + (pp[2] + pp[3])) + ((pp[4] + pp[5]) + (pp[6] + pp[7]));
            const int gq = qd < 192 ? qd >> 6 : (qd - 192) >> 4, hs = qd < 192 ? (qd >> 5) & 1 : (qd >> 3) & 1, sg = qd < 192 ? qd & 31 : 32 + (qd & 7);
            const unsigned a = pk2(s4[0], s4[1]), b = pk2(s4[2], s4[3]);
            LDS3 u16* kp = (LDS3 u16*)(lds + X_KVF + (8 * gq + 4 * hs) * 96 + sg * 2);
            kp[0] = (u16)a; kp[48] = (u16)(a >> 16); kp[96] = (u16)b; kp[144] = (u16)(b >> 16);
            asm volatile("s_waitcnt lgkmcnt(0)" ::: "memory");
            if (lane == 0) LDS_CNT_ADD(C_KVF);
            lds_wait<2>(lds + C_KVF, 4);
        }
        {
            LDS3 const char* kp = lds + X_KVF + (r < 24 ? r : 23) * 96 + h * 16;
            const u32x4 a0 = *(LDS3 const u32x4*)kp, a1 = *(LDS3 const u32x4*)(kp + 32), a2 = *(LDS3 const u32x4*)(kp + 64);
#pragma unroll
            for (int i = 0; i < 2; ++i) {
                f32x16 d = MFMA32_16(a0, qf[i][0], z16); d = MFMA32_16(a1, qf[i][1], d); d = MFMA32_16(a2, qf[i][2], d);
                const float den = upper_half(d[8]) + 1e-6f;
                float o[12];
#pragma unroll
                for (int j = 0; j < 12; ++j) o[j] = d[j];
                store_o(rs, o, (1.f - gS[i]) / den, tokS + i * 32 + r, 1, head, h);
            }
            DRAIN();
            unsigned arr = 0;
            if (lane == 0) arr = LDS_CNT_ADD(C_OB);
            if (__builtin_amdgcn_readfirstlane(arr) == 3 && lane == 0) set_flag(ws, WS_F_OB + (qb * 32 + head) * 8, tag);
        }
        STAMP(5);
        f32x4 c[5];
#pragma unroll
        for (int n = 0; n < 5; ++n) c[n] = (f32x4){0.f, 0.f, 0.f, 0.f};
        const char* wsrc = ws + WS_WT + (size_t)ntO * 5 * 40960;
        {
            if (hw == 0) wait_flags<8>(ws, WS_F_KV + lane * 8, WS_F_KV + 512 + lane * 8, tag);
            POLL_ONE(hw == 0, 8, WS_F_KV + 1024 + lane * 8, WS_F_KV + 1536 + lane * 8, C_P5);
#pragma unroll 1
            for (int t = 0; t < 5; ++t) {
                if (t == 0) prog_wait<8>(lds + C_PR, lane, 4); else if (t == 3) prog_wait<4>(lds + C_PR, lane, 5);
                const int ks = hw + 4 * t < 19 ? hw + 4 * t : 18;
#pragma unroll
                for (int n = 0; n < 5; ++n) glds16s(lane * 16, wsrc + (n * 40 + 20 + ks) * 1024, lds0 + 4096 + (ks * 5 + n) * 1024);
            }
            POLL_ONE(hw == 0, 4, WS_F_OB + ((mtO >> 2) * 32 + r) * 8, WS_F_OB + ((mtO >> 2) * 32 + r) * 8, C_P3);
            const unsigned ab = WS_AO + ((mtO * 4 + hw) * 40 + 20) * 1024;
            u32x4 af[20];
#pragma unroll
            for (int ks = 0; ks < 16; ++ks) af[ks] = LD16(ab + ks * 1024 + lane * 16);
#pragma unroll
            for (int ks = 16; ks < 20; ++ks) LD_TAIL(af[ks], ab + ks * 1024);
            asm volatile("s_waitcnt vmcnt(20)" ::: "memory");
            if (lane == 0) LDS_CNT_ADD(C_BX);
            lds_wait<2>(lds + C_BX, 4);
            u32x4 wf[3][5], wl[5];
            LDS3 const char* wp = lds + 4096 + lane * 16;
#pragma unroll
            for (int ks = 0; ks < 2; ++ks)
#pragma unroll
                for (int n = 0; n < 5; ++n) wf[ks][n] = *(LDS3 const u32x4*)(wp + (ks * 5 + n) * 1024);
#pragma unroll
            for (int ks = 0; ks < 20; ++ks) {
                if (ks + 2 < 19) {
#pragma unroll
                    for (int n = 0; n < 5; ++n) wf[(ks + 2) % 3][n] = *(LDS3 const u32x4*)(wp + ((ks + 2) * 5 + n) * 1024);
                }
                if (ks == 8) {
#pragma unroll
                    for (int n = 0; n < 5; ++n) wl[n] = LD16(WS_WT + ((ntO * 5 + n) * 40 + 39) * 1024 + lane * 16);
                }
                SBAR();
#pragma unroll
                for (int n = 0; n < 5; ++n) c[n] = MFMA16_32(ks < 19 ? wf[ks % 3][n] : wl[n], af[ks], c[n]);
                SBAR();
            }
            if (lane == 0) LDS_CNT_ADD(C_BX);
            lds_wait<2>(lds + C_BX, 8);
        }
        STAMP(6);
        prog_wait<4>(lds + C_PR, lane, 7);
#pragma unroll 1
        for (int n = 0; n < 5; ++n)
#pragma unroll
            for (int t = 0; t < 5; ++t) { const int ks = hw + 4 * t; glds16s(lane * 16, wsrc + (n * 40 + ks) * 1024, lds0 + L_WA + (n * 20 + ks) * 1024); }
#pragma unroll
        for (int n = 0; n < 3; ++n) *(LDS3 f32x4*)(lds + X_FIN + hw * 3072 + n * 1024 + lane * 16) = c[n];
        DRAIN();
        if (lane == 0) LDS_CNT_ADD(C_WA);
        prog_wait<4>(lds + C_PR, lane, 8);
#pragma unroll
        for (int n = 3; n < 5; ++n) *(LDS3 f32x4*)(lds + 143360 + hw * 2048 + (n - 3) * 1024 + lane * 16) = c[n];
        asm volatile("s_waitcnt lgkmcnt(0)" ::: "memory");
        if (lane == 0) __hip_atomic_store((LDS3 unsigned*)(lds + C_BP + hw * 4), 1u, __ATOMIC_RELAXED, __HIP_MEMORY_SCOPE_WORKGROUP);
        STAMP(7);
        asm volatile("mega_code_end2:");
    } else {
        const int qs = w & 3, kh = w >> 2;
        const int q0 = qb * 256 + qs * 64;
        {
            unsigned sink;
            if (w == 0 && (WARM_MODE & 1)) {
                const char* pc0; int len, len2;
                asm volatile("s_getpc_b64 %0\nmega_code_mark:\n\ts_mov_b32 %1, mega_code_end-mega_code_mark\n\ts_mov_b32 %2, mega_code_end2-mega_code_mark" : "=s"(pc0), "=s"(len), "=s"(len2));
                len = len > len2 ? len : len2;
                const int o = ((bid >> 3) * 64 + lane) * 64;
                if (o + 4 <= len) asm volatile("global_load_dword %0, %1, %2" : "=v"(sink) : "v"(o), "s"(pc0) : "memory");
            } else if (w >= 1 && w <= 6 && (WARM_MODE & 2)) {
                const unsigned o = ((w - 1) * 64 + lane) * 65536 + 65532;
                if (o < WS_END) asm volatile("global_load_dword %0, %1, %2 sc1" : "=v"(sink) : "v"(o), "s"(ws) : "memory");
            } else if (w == 7 && (WARM_MODE & 2)) {
                if (lane < 40) asm volatile("global_load_dword %0, %1, %2 sc1" : "=v"(sink) : "v"(lane * 65536), "s"(P.out) : "memory");
            }
            asm volatile("s_waitcnt vmcnt(0)" ::: "memory");
        }
        GET_TAG();
        if (bid < 240) {
            const int part = nt / 5, c0 = (nt - part * 5) * 128;
            const int wm = w & 1, wn = w >> 1;
            constexpr int RB = 32768;
            const int xr = wm * 64 + r;
            const unsigned xa = RB + xr * 128 + ((h ^ ((xr >> 1) & 7)) << 4);
            unsigned wt[2];
#pragma unroll
            for (int t = 0; t < 2; ++t) {
                const int q = (lane & 15) >> 2, p = lane & 3, blk = (lane >> 4) & 1, row = 8 * h + 4 * t + q;
                wt[t] = RB + 16384 + row * 256 + (((4 * wn + 2 * blk + (p >> 1)) ^ ((q << 2) | (2 * h + t))) << 4) + 8 * (p & 1);
            }
            auto gbody = [&](auto trc) {
                constexpr bool TR = decltype(trc)::value;
                u32x4 xf[2][8], wf[2][4];
                auto rdk = [&](auto bc, LDS3 const char* sx, LDS3 const char* sb, int ks) {
                    constexpr int B = decltype(bc)::value;
                    xf[B][2 * ks] = *(LDS3 const u32x4*)(sx + (xa ^ (ks * 32)));
                    xf[B][2 * ks + 1] = *(LDS3 const u32x4*)(sx + ((xa + 4096) ^ (ks * 32)));
                    const u32x2 lo = trread(sb + wt[0] + ks * 4096), hi = trread(sb + wt[1] + ks * 4096);
                    wf[B][ks] = (u32x4){lo[0], lo[1], hi[0], hi[1]};
                };
                f32x16 acc0, acc1;
                auto step = [&](auto bc, int s) {
                    constexpr int B = decltype(bc)::value;
                    if (s == GA_PUB) {
                        DRAIN();
                        unsigned arr = 0;
                        if (lane == 0) arr = LDS_CNT_ADD(G_ST);
                        if (__builtin_amdgcn_readfirstlane(arr) == 7 && lane == 0) set_flag(ws, fG, tag);
                    }
                    __builtin_amdgcn_s_waitcnt(0xC07F);
                    if (s < 18) BAR();
                    SBAR();
                    const int s1 = s + 1;
                    LDS3 const char* sb = lds + __builtin_amdgcn_readfirstlane((s1 & 3) * 32768);
                    LDS3 const char* sx = lds + __builtin_amdgcn_readfirstlane(((s1 >= 10 ? 20 - s1 : s1 + 1) & 3) * 32768);
#define GM(A, i) do { if constexpr (TR) A = MFMA32_16(wf[B][(i) >> 1], xf[B][i], A); else A = MFMA32_16(xf[B][i], wf[B][(i) >> 1], A); SBAR(); } while (0)
#define GX(i) do { xf[B ^ 1][i] = *(LDS3 const u32x4*)(sx + ((xa + ((i) & 1) * 4096) ^ (((i) >> 1) * 32))); } while (0)
#define GW(ks) do { const u32x2 lo = trread(sb + wt[0] + (ks) * 4096), hi = trread(sb + wt[1] + (ks) * 4096); wf[B ^ 1][ks] = (u32x4){lo[0], lo[1], hi[0], hi[1]}; } while (0)
                    GM(acc0, 0); GX(0); GW(0); SBAR(); GM(acc1, 1); GX(1); GX(2); SBAR(); GM(acc0, 2); GW(1); GX(3); SBAR(); GM(acc1, 3); GX(4); GW(2); SBAR();
                    GM(acc0, 4); GX(5); GX(6); SBAR(); GM(acc1, 5); GW(3); GX(7); SBAR(); GM(acc0, 6); GM(acc1, 7);
#undef GX
#undef GW
#undef GM
                };
                WAIT_BAR(63);
#pragma unroll
                for (int ks = 0; ks < 4; ++ks) rdk(std::integral_constant<int, 0>{}, lds + 32768, lds, ks);
#pragma unroll 1
                for (int mat = 0; mat < 2; ++mat) {
                    acc0 = (f32x16){}; acc1 = (f32x16){};
#pragma unroll 1
                    for (int kk = 0; kk < 10; kk += 2) {
                        step(std::integral_constant<int, 0>{}, mat * 10 + kk);
                        step(std::integral_constant<int, 1>{}, mat * 10 + kk + 1);
                    }
                    const int cpos = 2 * (mt & 7) + (mt >> 3);
                    if constexpr (TR) {
                        const float sc = (part == 0 && mat == 0) ? QSCALE : 1.f;
                        const unsigned base = part == 0 ? WS_Q + mat * NH * 81920 + mt * 5120 : WS_KV + mat * NH * 163840 + cpos * 10240;
                        const unsigned hs = part == 0 ? 81920 : 163840;
#pragma unroll
                        for (int pr = 0; pr < 2; ++pr) {
                            const int G8 = ((c0 + 32 * wn) >> 3) + 2 * pr + h, pp = G8 / 5, gi = G8 - pp * 5;
                            const unsigned hb0 = base + (2 * pp + (gi > 2)) * hs;
#pragma unroll
                            for (int i = 0; i < 2; ++i) {
                                const f32x16& a = i ? acc1 : acc0;
                                const int tl = wm * 64 + i * 32 + r;
                                const auto s0 = __builtin_amdgcn_permlane32_swap(pk2(a[8 * pr] * sc, a[8 * pr + 1] * sc), pk2(a[8 * pr + 4] * sc, a[8 * pr + 5] * sc), false, false);
                                const auto s1 = __builtin_amdgcn_permlane32_swap(pk2(a[8 * pr + 2] * sc, a[8 * pr + 3] * sc), pk2(a[8 * pr + 6] * sc, a[8 * pr + 7] * sc), false, false);
                                if (gi != 2) { const u32x4 ov = {s0[0], s1[0], s0[1], s1[1]}; ST16(ov, hb0 + (tl >> 5) * 1024 + (gi > 2 ? gi - 3 : gi) * 512 + r * 16); }
                                else { const u32x2 o0 = {s0[0], s1[0]}, o1 = {s0[1], s1[1]}; ST8(o0, hb0 + 4096 + tl * 8); ST8(o1, hb0 + hs + 4096 + tl * 8); }
                            }
                        }
                    } else {
                        const int n = c0 + 32 * wn + r, hd = n / 20, d = n - hd * 20, rho = (hd & 1) ? (d < 4 ? d + 16 : d - 4) : d;
                        const unsigned base = WS_KV + (mat * NH + hd) * 163840 + cpos * 10240 + 5120 + h * 16 + rho * 32;
#pragma unroll
                        for (int i = 0; i < 2; ++i)
#pragma unroll
                            for (int s2 = 0; s2 < 2; ++s2) {
                                const f32x16& a = i ? acc1 : acc0;
                                u32x4 ov = {pk2(a[8 * s2], a[8 * s2 + 1]), pk2(a[8 * s2 + 2], a[8 * s2 + 3]), pk2(a[8 * s2 + 4], a[8 * s2 + 5]), pk2(a[8 * s2 + 6], a[8 * s2 + 7])};
                                ST16(ov, base + (wm * 4 + i * 2 + s2) * 640);
                            }
                    }
                    if (mat == 0) STAMP(1);
                }
            };
            if (part < 2) gbody(std::true_type{}); else gbody(std::false_type{});
        } else {
            const int sj = bid - 240;
            WAIT_BAR(63);
            {
                const u32x4 ga = *(const u32x4*)(P.gw + lane * 8), gb = *(const u32x4*)(P.gw + 512 + (lane & 15) * 8);
                u32x4 xa[16], xb[4];
                const u16* xr = P.x + (size_t)(sj * 128 + w * 16) * DM;
#pragma unroll
                for (int rr = 0; rr < 16; ++rr) xa[rr] = *(const u32x4*)(xr + rr * DM + lane * 8);
#pragma unroll
                for (int i = 0; i < 4; ++i) xb[i] = *(const u32x4*)(xr + (i * 4 + (lane >> 4)) * DM + 512 + (lane & 15) * 8);
                float res = 0.f;
#pragma unroll
                for (int rr = 0; rr < 16; ++rr) {
                    float s = 0.f, s2 = 0.f;
#pragma unroll
                    for (int j = 0; j < 4; ++j) { s += bf_lo(xa[rr][j]) * bf_lo(ga[j]) + bf_hi(xa[rr][j]) * bf_hi(ga[j]); s2 += bf_lo(xb[rr >> 2][j]) * bf_lo(gb[j]) + bf_hi(xb[rr >> 2][j]) * bf_hi(gb[j]); }
                    if ((lane >> 4) == (rr & 3)) s += s2;
#pragma unroll
                    for (int o = 32; o >= 1; o >>= 1) s += __shfl_xor(s, o);
                    if (lane == rr) res = s;
                }
                if (lane < 16) ST4(__float_as_uint(1.f / (1.f + __expf(-res))), WS_G + (sj * 128 + w * 16 + lane) * 4);
            }
            DRAIN();
            unsigned arr = 0;
            if (lane == 0) arr = LDS_CNT_ADD(G_ST);
            if (__builtin_amdgcn_readfirstlane(arr) == 7 && lane == 0) set_flag(ws, WS_F_SD + sj * 8, tag);
        }
        STAMP(2);
        const unsigned hb = lds0 + kh * 10240;
        const unsigned laneK = hb + lane * 16, laneT = hb + 4096 + r * 8 + h * 4, laneV = hb + 5120 + h * 16 + (r < 20 ? r : 19) * 32;
        const u64 vmask = 0x000fffff000fffffull;
        u32x4 v0a = {one, one, one, one}, v0b = v0a, v1 = v0a;
        u32x4 qA, qB, ka; unsigned qtA, qtB, kt;
        {
            lds_wait(lds + G_Q, 4);
            LDS3 const char* qp = lds + L_Q + (qs >> 1) * 5120;
            const unsigned om = (qs & 1) * 2048 + lane * 16, ot = 4096 + ((qs & 1) * 64 + r) * 8 + 4 * h;
            qA = *(LDS3 const u32x4*)(qp + om); qB = *(LDS3 const u32x4*)(qp + om + 1024);
            qtA = *(LDS3 const unsigned*)(qp + ot); qtB = *(LDS3 const unsigned*)(qp + ot + 256);
            DSR128(ka, kh ? laneK : lds0 + L_Q + 10240 + lane * 16, 0); DSR32(kt, laneT, 0); DSR128M2(v0a, laneV, 0, v1, laneV, 640);
            asm volatile("s_waitcnt lgkmcnt(0)" : "+v"(ka), "+v"(kt), "+v"(v0a), "+v"(v1), "+v"(qA), "+v"(qB), "+v"(qtA), "+v"(qtB) :: "memory");
            if (lane == 0) LDS_CNT_ADD(G_RD);
        }
        STAMP(5);
        float mA, mB; u32x2 qTA, qTB; f32x16 sA, sB;
        {
            const u32x2 t0 = {kt, 0x3f80u}, a0 = {qtA, 0u}, b0 = {qtB, 0u};
            const f32x16 s = MFMA32_16(ka, qA, z16);
            const f32x16 s0 = MFMA32_8(t0, a0, s);
            f32x16 s1 = MFMA32_16(ka, qB, z16); s1 = MFMA32_8(t0, b0, s1);
            float x0 = s0[0], x1 = s1[0];
#pragma unroll
            for (int i = 1; i < 16; ++i) { x0 = fmaxf(x0, s0[i]); x1 = fmaxf(x1, s1[i]); }
            x0 = -xhalf_max(x0); x1 = -xhalf_max(x1);
            const unsigned hh = pk2(x0, x1);
            const unsigned ll = pk2(x0 - bf_lo(hh), x1 - bf_hi(hh));
            mA = -(bf_lo(hh) + bf_lo(ll)); mB = -(bf_hi(hh) + bf_hi(ll));
            const unsigned sel = h ? ll : hh;
            qTA = (u32x2){qtA, sel & 0xffffu}; qTB = (u32x2){qtB, sel >> 16};
            sA = MFMA32_8(t0, qTA, s);
        }
        f32x16 oA = {}, oB = {};
        u32x4 pa, pb = {0u, 0u, 0u, 0u};
#define EX4(S, i, Pv, j) do { Pv[j] = pk2(ex2(S[i]), ex2(S[i + 1])); Pv[j + 1] = pk2(ex2(S[i + 2]), ex2(S[i + 3])); PIN(Pv); SBAR(); } while (0)
#define KT2 ((u32x2){kt, 0x3f80u})
#define TILE(V0C, V0N, aK1, oK1, aT1, oT1, oV1, aV0, oV0) do { \
            sB = MFMA32_16(ka, qB, z16); SBAR(); DSR128(ka, aK1, oK1); EX4(sA, 0, pa, 0); \
            oB = MFMA32_16(v1, pb, oB); SBAR(); DSR128M2(v1, aV, oV1, V0N, aV0, oV0); EX4(sA, 4, pa, 2); \
            sB = MFMA32_8(KT2, qTB, sB); SBAR(); DSR32(kt, aT1, oT1); EX4(sA, 8, pb, 0); \
            oA = MFMA32_16(V0C, pa, oA); SBAR(); EX4(sA, 12, pb, 2); \
            LWAIT(1); sA = MFMA32_16(ka, qA, z16); SBAR(); EX4(sB, 0, pa, 0); \
            oA = MFMA32_16(v1, pb, oA); SBAR(); EX4(sB, 4, pa, 2); \
            LWAIT(0); sA = MFMA32_8(KT2, qTA, sA); SBAR(); EX4(sB, 8, pb, 0); \
            oB = MFMA32_16(V0C, pa, oB); SBAR(); EX4(sB, 12, pb, 2); \
        } while (0)
        unsigned aK = laneK, aT = laneT, aV = laneV;
#pragma unroll 1
        for (int c = 0; c < 8; ++c) {
            if (c >= 1 && c <= 3) lds_wait(lds + (c == 1 ? C_KVL : (c == 2 ? C_KVL + 12 : C_WA + 8)), 4);
            if (c >= 1) {
                unsigned pp;
                asm volatile("ds_write_b32 %1, %2\n\tds_read_b32 %0, %3\n\ts_waitcnt lgkmcnt(0)" : "=&v"(pp) : "v"(lds0 + C_PR + w * 4), "v"(c), "v"(lds0 + C_PR + (w ^ 4) * 4) : "memory");
                const int pc = __builtin_amdgcn_readfirstlane(pp);
                if (pc > c || (pc == c && ((c ^ kh) & 1))) __builtin_amdgcn_s_setprio(1); else __builtin_amdgcn_s_setprio(0);
            } else if (kh) __builtin_amdgcn_s_setprio(1);
            const unsigned nx = (c < 7 ? c + 1 : 7) * 20480;
            const unsigned nK = laneK + nx, nT = laneT + nx, nV = laneV + nx;
            SBAR();
            TILE(v0a, v0b, aK, 1024, aT, 256, 640,  aV, 1280);
            TILE(v0b, v0a, aK, 2048, aT, 512, 1920, aV, 2560);
            if (c == 0) {
                DRAIN();
                unsigned arr = 0;
                if (lane == 0) arr = LDS_CNT_ADD(C_GB);
                if (__builtin_amdgcn_readfirstlane(arr) == 7 && lane == 0 && bid < 240) set_flag(ws, fG + 2048, tag);
                asm volatile("s_waitcnt lgkmcnt(0)" ::: "memory");
                SBAR();
            }
            TILE(v0a, v0b, aK, 3072, aT, 768, 3200, aV, 3840);
            TILE(v0b, v0a, nK, 0,    nT, 0,   4480, nV, 0);
            aK = nK; aT = nT; aV = nV;
        }
        oB = MFMA32_16(v1, pb, oB);
#undef TILE
#undef EX4
        if (lane == 0) __hip_atomic_store((LDS3 unsigned*)(lds + C_PR + w * 4), 8u, __ATOMIC_RELAXED, __HIP_MEMORY_SCOPE_WORKGROUP);
        STAMP(6);
        {
            float chk = 0.f;
#pragma unroll
            for (int i = 0; i < 12; ++i) chk += fabsf(oA[i]) * 0.f + fabsf(oB[i]) * 0.f;
            bool bad = !(chk == 0.f);
            if (h) bad = bad || !(oA[8] >= 0x1p-60f && oA[8] <= 0x1p60f && oB[8] >= 0x1p-60f && oB[8] <= 0x1p60f);
            if (__builtin_expect(__any(bad), 0)) {
                mA = -INFINITY; mB = -INFINITY;
#pragma unroll
                for (int i = 0; i < 16; ++i) { oA[i] = 0.f; oB[i] = 0.f; }
                const u32x2 a0 = {qtA, 0u}, b0 = {qtB, 0u};
#pragma unroll 1
                for (int t = 0; t < 32; ++t) {
                    const unsigned kb = WS_KV + head * 163840 + (2 * (t >> 2) + kh) * 10240, t4 = t & 3;
                    const u32x4 k0 = LD16(kb + t4 * 1024 + lane * 16);
                    const u32x2 t0 = {LD4(kb + 4096 + (t4 * 32 + r) * 8 + 4 * h), 0u};
                    u32x4 w0 = LD16(kb + 5120 + t4 * 1280 + h * 16 + (r < 20 ? r : 19) * 32), w1 = LD16(kb + 5120 + t4 * 1280 + 640 + h * 16 + (r < 20 ? r : 19) * 32);
                    if (r >= 20) { w0 = (u32x4){one, one, one, one}; w1 = w0; }
                    f32x16 s0 = MFMA32_16(k0, qA, z16); s0 = MFMA32_8(t0, a0, s0);
                    f32x16 s1 = MFMA32_16(k0, qB, z16); s1 = MFMA32_8(t0, b0, s1);
                    float x0 = s0[0], x1 = s1[0];
#pragma unroll
                    for (int i = 1; i < 16; ++i) { x0 = fmaxf(x0, s0[i]); x1 = fmaxf(x1, s1[i]); }
                    x0 = fmaxf(xhalf_max(x0), mA); x1 = fmaxf(xhalf_max(x1), mB);
                    const float g0 = (mA == x0) ? 1.f : ex2(mA - x0), g1 = (mB == x1) ? 1.f : ex2(mB - x1);
                    mA = x0; mB = x1;
#pragma unroll
                    for (int i = 0; i < 16; ++i) { oA[i] *= g0; oB[i] *= g1; }
#pragma unroll
                    for (int i = 0; i < 4; ++i) { pa[i] = pk2(ex2(s0[2 * i] - x0), ex2(s0[2 * i + 1] - x0)); pb[i] = pk2(ex2(s0[8 + 2 * i] - x0), ex2(s0[9 + 2 * i] - x0)); }
                    oA = MFMA32_16(w0, pa, oA); oA = MFMA32_16(w1, pb, oA);
#pragma unroll
                    for (int i = 0; i < 4; ++i) { pa[i] = pk2(ex2(s1[2 * i] - x1), ex2(s1[2 * i + 1] - x1)); pb[i] = pk2(ex2(s1[8 + 2 * i] - x1), ex2(s1[9 + 2 * i] - x1)); }
                    oB = MFMA32_16(w0, pa, oB); oB = MFMA32_16(w1, pb, oB);
                }
            }
        }
        lds_wait<4>(lds + C_BX, 8);
        const float gO = *(LDS3 const float*)(lds + X_G + (qs * 64 + kh * 32 + r) * 4);
        {
            LDS3 char* xp = lds + X_MRG + w * 2816;
            const f32x16& s = kh ? oA : oB;
            *(LDS3 f32x4*)(xp + lane * 16) = (f32x4){s[0], s[1], s[2], s[3]};
            *(LDS3 f32x4*)(xp + 1024 + lane * 16) = (f32x4){s[4], s[5], s[6], s[7]};
            if (!h) *(LDS3 f32x4*)(xp + 2048 + r * 16) = (f32x4){s[8], s[9], s[10], s[11]};
            else *(LDS3 f32x2*)(xp + 2560 + r * 8) = (f32x2){s[8], kh ? mA : mB};
            asm volatile("s_waitcnt lgkmcnt(0)" ::: "memory");
            if (lane == 0) __hip_atomic_store((LDS3 unsigned*)(lds + C_MRG + w * 4), 1u, __ATOMIC_RELAXED, __HIP_MEMORY_SCOPE_WORKGROUP);
        }
        lds_wait<2>(lds + C_MRG + (w ^ 4) * 4, 1);
        bool lastw;
        {
            LDS3 const char* xp = lds + X_MRG + (w ^ 4) * 2816;
            const f32x4 p0 = *(LDS3 const f32x4*)(xp + lane * 16), p1 = *(LDS3 const f32x4*)(xp + 1024 + lane * 16), p2 = *(LDS3 const f32x4*)(xp + 2048 + r * 16);
            const f32x2 lm = *(LDS3 const f32x2*)(xp + 2560 + r * 8);
            const f32x16& s = kh ? oB : oA;
            const float mo = kh ? mB : mA, mp = lm[1];
            const float mx = fmaxf(mo, mp);
            const float fo = (mo == mx) ? 1.f : ex2(mo - mx), fq = (mp == mx) ? 1.f : ex2(mp - mx);
            float o[12];
#pragma unroll
            for (int i = 0; i < 4; ++i) { o[i] = s[i] * fo + p0[i] * fq; o[4 + i] = s[4 + i] * fo + p1[i] * fq; o[8 + i] = s[8 + i] * fo + p2[i] * fq; }
            const float l = upper_half(s[8]) * fo + lm[0] * fq;
            store_o(rs, o, gO / l, q0 + kh * 32 + r, 0, head, h);
            DRAIN();
            unsigned arr = 0;
            if (lane == 0) arr = LDS_CNT_ADD(C_OA);
            lastw = __builtin_amdgcn_readfirstlane(arr) == 7;
            if (lastw && lane == 0) set_flag(ws, WS_F_OA + (qb * 32 + head) * 8, tag);
        }
        STAMP(7);
        {
            const int c16 = lane & 15, g = lane >> 4, tt = w & 3, k0 = kh * 10;
            lds_wait(lds + C_WA, 4);
            LDS3 const char* wp = lds + L_WA + k0 * 1024 + lane * 16;
            u32x4 wf[4][5];
#pragma unroll
            for (int ks = 0; ks < 4; ++ks)
#pragma unroll
                for (int n = 0; n < 5; ++n) wf[ks][n] = *(LDS3 const u32x4*)(wp + (n * 20 + ks) * 1024);
            POLL_ONE(lastw, 2, WS_F_OA + ((mtO >> 2) * 32 + r) * 8, WS_F_OA + ((mtO >> 2) * 32 + r) * 8, C_P4);
            STAMP(9);
            const unsigned ab = WS_AO + ((mtO * 4 + tt) * 40 + k0) * 1024;
            u32x4 af[10];
#pragma unroll
            for (int ks = 0; ks < 6; ++ks) af[ks] = LD16(ab + ks * 1024 + lane * 16);
            if (kh) {
#pragma unroll
                for (int ks = 6; ks < 10; ++ks) LD_TAIL(af[ks], ab + ks * 1024);
            } else {
#pragma unroll
                for (int ks = 6; ks < 10; ++ks) af[ks] = LD16(ab + ks * 1024 + lane * 16);
            }
            f32x4 c[5];
#pragma unroll
            for (int n = 0; n < 5; ++n) c[n] = (f32x4){0.f, 0.f, 0.f, 0.f};
            SBAR();
#pragma unroll
            for (int ks = 0; ks < 10; ++ks) {
#pragma unroll
                for (int n = 0; n < 5; ++n) c[n] = MFMA16_32(wf[ks & 3][n], af[ks], c[n]);
                SBAR();
                if (ks + 4 < 10) {
#pragma unroll
                    for (int n = 0; n < 5; ++n) wf[ks & 3][n] = *(LDS3 const u32x4*)(wp + (n * 20 + ks + 4) * 1024);
                }
                SBAR();
            }
            LDS3 char* xp = lds + X_MRG + tt * 5120 + lane * 16;
            if (w < 4) { *(LDS3 f32x4*)(xp + 3072) = c[3]; *(LDS3 f32x4*)(xp + 4096) = c[4]; }
            else { *(LDS3 f32x4*)(xp) = c[0]; *(LDS3 f32x4*)(xp + 1024) = c[1]; *(LDS3 f32x4*)(xp + 2048) = c[2]; }
            asm volatile("s_waitcnt lgkmcnt(0)" ::: "memory");
            if (lane == 0) __hip_atomic_store((LDS3 unsigned*)(lds + C_FIN + w * 4), 1u, __ATOMIC_RELAXED, __HIP_MEMORY_SCOPE_WORKGROUP);
            lds_wait(lds + C_FIN + (w ^ 4) * 4, 1);
            lds_wait(lds + C_BP + tt * 4, 1);
            u16* op = P.out + (size_t)(mtO * 64 + tt * 16 + c16) * DM + ntO * 80 + g * 4;
#pragma unroll
            for (int n = 0; n < 5; ++n) {
                if ((w < 4) == (n < 3)) {
                    const f32x4 bp = *(LDS3 const f32x4*)(n < 3 ? lds + X_FIN + tt * 3072 + n * 1024 + lane * 16 : lds + 143360 + tt * 2048 + (n - 3) * 1024 + lane * 16);
                    const f32x4 s = c[n] + *(LDS3 const f32x4*)(xp + n * 1024) + bp;
                    *(u32x2*)(op + n * 16) = (u32x2){pk2(s[0], s[1]), pk2(s[2], s[3])};
                }
            }
            if (bid == 0 && mtid == 0) set_flag(ws, WS_EPOCH, tag);
        }
        STAMP(10);
        asm volatile("mega_code_end:");
    }
}

extern "C" void kernel_launch(void* const* d_in, const int* in_sizes, int n_in, void* d_out, int out_size, void* d_ws, size_t ws_size,
                              hipStream_t stream) {
    Params P;
    P.x = (const u16*)d_in[0]; P.wa = (const u16*)d_in[1]; P.woa = (const u16*)d_in[2]; P.wb = (const u16*)d_in[4];
    P.wphi = (const u16*)d_in[5]; P.wob = (const u16*)d_in[6]; P.gw = (const u16*)d_in[7]; P.ws = (char*)d_ws; P.out = (u16*)d_out;
    mega<<<256, 768, 0, stream>>>(P);
}
```

```cpp
#include <hip/hip_runtime.h>
#include <hip/hip_bf16.h>
#include <stdint.h>
#include <type_traits>

typedef __attribute__((ext_vector_type(8))) short bf16x8;
typedef __attribute__((ext_vector_type(4))) short bf16x4;
typedef __attribute__((ext_vector_type(16))) float f32x16;
typedef __attribute__((ext_vector_type(4))) float f32x4;
typedef __attribute__((ext_vector_type(2))) float f32x2;
typedef __attribute__((ext_vector_type(4))) unsigned u32x4;
typedef __attribute__((ext_vector_type(2))) unsigned u32x2;
typedef __attribute__((ext_vector_type(2))) __bf16 bf2_t;
typedef unsigned short u16;
typedef unsigned long long u64;
#define LDS3 __attribute__((address_space(3)))
typedef __attribute__((address_space(1))) u64 gu64;
#define RLX_AGENT __ATOMIC_RELAXED, __HIP_MEMORY_SCOPE_AGENT

#define PRIO_MODE 3
#define WARM_MODE 1
#define GA_PUB 11
#define PHI_ROUND 0
#define HS 8
#define MS 2
#define ROT_MODE 3
#define PRE_SLEEP 2
constexpr int SEQ = 2048, DM = 640, NH = 32;
constexpr float LOG2E = 1.4426950408889634f;
constexpr float QSCALE = 0.22360679774997896f * LOG2E;

constexpr unsigned WS_EPOCH = 0;
constexpr unsigned WS_F_G   = 256;
constexpr unsigned WS_F_SD  = WS_F_G + 4096;
constexpr unsigned WS_F_KV  = WS_F_SD + 256;
constexpr unsigned WS_F_OB  = WS_F_KV + 2048;
constexpr unsigned WS_F_OA  = WS_F_OB + 2048;
constexpr unsigned WS_G     = 16384;
constexpr unsigned WS_Q     = 32768;
constexpr unsigned WS_KV    = WS_Q + 2 * NH * 81920;
constexpr unsigned WS_KVP   = WS_KV + 2 * NH * 163840;
constexpr unsigned WS_AO    = WS_KVP + NH * 8 * 4096;
constexpr unsigned WS_WT    = WS_AO + SEQ * 1280 * 2;
constexpr unsigned WS_END   = WS_WT + 1280 * 640 * 2;

#define STAMP(i)

__device__ __forceinline__ unsigned pk2(float a, float b) { f32x2 v = {a, b}; bf2_t r = __builtin_convertvector(v, bf2_t); return __builtin_bit_cast(unsigned, r); }
__device__ __forceinline__ float bf_lo(unsigned u) { return __uint_as_float(u << 16); }
__device__ __forceinline__ float bf_hi(unsigned u) { return __uint_as_float(u & 0xffff0000u); }
__device__ __forceinline__ float ex2(float x) { return __builtin_amdgcn_exp2f(x); }
#define MFMA32_16(a, b, c) __builtin_amdgcn_mfma_f32_32x32x16_bf16(__builtin_bit_cast(bf16x8, a), __builtin_bit_cast(bf16x8, b), c, 0, 0, 0)
#define MFMA32_8(a, b, c) __builtin_amdgcn_mfma_f32_32x32x8bf16_1k(__builtin_bit_cast(bf16x4, a), __builtin_bit_cast(bf16x4, b), c, 0, 0, 0)
#define MFMA16_32(a, b, c) __builtin_amdgcn_mfma_f32_16x16x32_bf16(__builtin_bit_cast(bf16x8, a), __builtin_bit_cast(bf16x8, b), c, 0, 0, 0)

__device__ __forceinline__ void glds16(unsigned voff, const void* sbase, unsigned lds_base) {
    unsigned sv;
    asm volatile("s_mov_b32 %0, m0\n\ts_mov_b32 m0, %3\n\ts_nop 0\n\tglobal_load_lds_dwordx4 %1, %2\n\ts_mov_b32 m0, %0"
                 : "=&s"(sv) : "v"(voff), "s"(sbase), "s"(__builtin_amdgcn_readfirstlane(lds_base)) : "memory");
}
__device__ __forceinline__ void glds16s(unsigned voff, const void* sbase, unsigned lds_base) {
    unsigned sv;
    asm volatile("s_mov_b32 %0, m0\n\ts_mov_b32 m0, %3\n\ts_nop 0\n\tglobal_load_lds_dwordx4 %1, %2 sc1\n\ts_mov_b32 m0, %0"
                 : "=&s"(sv) : "v"(voff), "s"(sbase), "s"(__builtin_amdgcn_readfirstlane(lds_base)) : "memory");
}
__device__ __forceinline__ void glds4s(unsigned voff, const void* sbase, unsigned lds_base) {
    unsigned sv;
    asm volatile("s_mov_b32 %0, m0\n\ts_mov_b32 m0, %3\n\ts_nop 0\n\tglobal_load_lds_dword %1, %2 sc1\n\ts_mov_b32 m0, %0"
                 : "=&s"(sv) : "v"(voff), "s"(sbase), "s"(__builtin_amdgcn_readfirstlane(lds_base)) : "memory");
}
__device__ __forceinline__ void glds16x4(const unsigned (&vo)[4], const void* sbase, unsigned lds_base) {
    unsigned sv;
    asm volatile("s_mov_b32 %0, m0\n\ts_mov_b32 m0, %6\n\ts_nop 0\n\tglobal_load_lds_dwordx4 %1, %5\n\tglobal_load_lds_dwordx4 %2, %5 offset:1024\n\t"
                 "global_load_lds_dwordx4 %3, %5 offset:2048\n\tglobal_load_lds_dwordx4 %4, %5 offset:3072\n\ts_mov_b32 m0, %0"
                 : "=&s"(sv) : "v"(vo[0]), "v"(vo[1]), "v"(vo[2]), "v"(vo[3]), "s"(sbase), "s"(lds_base) : "memory");
}
typedef short v4i16_t __attribute__((ext_vector_type(4)));
__device__ __forceinline__ u32x2 trread(LDS3 const char* p) {
    return __builtin_bit_cast(u32x2, __builtin_amdgcn_ds_read_tr16_b64_v4i16((LDS3 v4i16_t*)p));
}
#define WAIT_BAR(N) asm volatile("s_waitcnt vmcnt(" #N ") lgkmcnt(0)\n\ts_barrier" ::: "memory")
#define DRAIN() asm volatile("s_waitcnt vmcnt(0)" ::: "memory")
#define SBAR() __builtin_amdgcn_sched_barrier(0)
#define PIN(x) asm volatile("" : "+v"(x))

__device__ __forceinline__ float xhalf_max(float x) {
    auto r = __builtin_amdgcn_permlane32_swap(__float_as_uint(x), __float_as_uint(x), false, false);
    return fmaxf(__uint_as_float(r[0]), __uint_as_float(r[1]));
}
__device__ __forceinline__ float xhalf_sum(float x) {
    auto r = __builtin_amdgcn_permlane32_swap(__float_as_uint(x), __float_as_uint(x), false, false);
    return __uint_as_float(r[0]) + __uint_as_float(r[1]);
}
__device__ __forceinline__ float upper_half(float x) {
    auto r = __builtin_amdgcn_permlane32_swap(__float_as_uint(x), __float_as_uint(x), false, false);
    return __uint_as_float(r[1]);
}

typedef __amdgpu_buffer_rsrc_t rsrc_t;
#define ST8(v, off)  __builtin_amdgcn_raw_buffer_store_b64(v, rs, (int)(off), 0, 16)
#define ST16(v, off) __builtin_amdgcn_raw_buffer_store_b128(v, rs, (int)(off), 0, 16)
#define ST4(v, off)  __builtin_amdgcn_raw_buffer_store_b32(v, rs, (int)(off), 0, 16)
#define LD16(off) __builtin_amdgcn_raw_buffer_load_b128(rs, (int)(off), 0, 16)
#define LD4(off)  __builtin_amdgcn_raw_buffer_load_b32(rs, (int)(off), 0, 16)

template <int SLP = 2>
__device__ __forceinline__ void wait_flags(const char* ws, unsigned off0, unsigned off1, u64 tag) {
    gu64* p0 = (gu64*)(ws + off0); gu64* p1 = (gu64*)(ws + off1);
#pragma unroll 1
    for (unsigned spins = 0; spins < (1u << 16); ++spins) {
        const u64 a = __hip_atomic_load(p0, RLX_AGENT), b = __hip_atomic_load(p1, RLX_AGENT);
        if (__all(a == tag && b == tag)) break;
        __builtin_amdgcn_s_sleep(SLP);
    }
    __builtin_amdgcn_fence(__ATOMIC_ACQUIRE, "wavefront");
}
__device__ __forceinline__ void set_flag(char* ws, unsigned off, u64 tag) { __hip_atomic_store((gu64*)(ws + off), tag, RLX_AGENT); }

struct Params { const u16* x; const u16* wa; const u16* wb; const u16* wphi; const u16* woa; const u16* wob; const u16* gw; char* ws; u16* out; };

__device__ __forceinline__ void feature_map(const u32x4& pm, unsigned pt, const u32x4& tm, unsigned tt, int h, u32x4& fp, u32x4& fn, u32x4& ft) {
    const f32x16 z16 = {};
    const u32x2 a8 = {pt, 0u}, b8 = {tt, 0u};
    f32x16 s = MFMA32_16(pm, tm, z16); s = MFMA32_8(a8, b8, s);
    f32x2 v[6];
#pragma unroll
    for (int i = 0; i < 6; ++i) {
        v[i] = (f32x2){s[2 * i], s[2 * i + 1]};
    }
    float mx = fmaxf(v[0][0], v[0][1]), mn = fminf(v[0][0], v[0][1]);
#pragma unroll
    for (int i = 1; i < 6; ++i) { mx = fmaxf(fmaxf(mx, v[i][0]), v[i][1]); mn = fminf(fminf(mn, v[i][0]), v[i][1]); }
    mx = xhalf_max(mx) * -LOG2E; mn = xhalf_max(-mn) * -LOG2E;
    f32x2 ep[6], en[6];
#pragma unroll
    for (int i = 0; i < 6; ++i) {
        const f32x2 a = v[i] * LOG2E + mx, b = v[i] * -LOG2E + mn;
        ep[i] = (f32x2){ex2(a[0]), ex2(a[1])}; en[i] = (f32x2){ex2(b[0]), ex2(b[1])};
    }
    const float tw = h ? 0.f : 1.f;
    const f32x2 s2p = ((ep[0] + ep[1]) + (ep[2] + ep[3])) + (ep[4] + ep[5]) * tw, s2n = ((en[0] + en[1]) + (en[2] + en[3])) + (en[4] + en[5]) * tw;
    const float sp = __builtin_amdgcn_rcpf(xhalf_sum(s2p[0] + s2p[1])), sn = __builtin_amdgcn_rcpf(xhalf_sum(s2n[0] + s2n[1]));
    const float tp = sp * tw, tn = sn * tw;
#pragma unroll
    for (int i = 0; i < 4; ++i) { const f32x2 a = ep[i] * sp, b = en[i] * sn; fp[i] = pk2(a[0], a[1]); fn[i] = pk2(b[0], b[1]); }
    const f32x2 c0 = ep[4] * tp, c1 = ep[5] * tp, c2 = en[4] * tn, c3 = en[5] * tn;
    ft = (u32x4){pk2(c0[0], c0[1]), pk2(c1[0], c1[1]), pk2(c2[0], c2[1]), pk2(c3[0], c3[1])};
}

__device__ __forceinline__ void store_o(rsrc_t rs, const float (&o)[12], float sc, int tok, int half, int head, int h) {
    const auto s0 = __builtin_amdgcn_permlane32_swap(pk2(o[0] * sc, o[1] * sc), pk2(o[4] * sc, o[5] * sc), false, false);
    const auto s1 = __builtin_amdgcn_permlane32_swap(pk2(o[2] * sc, o[3] * sc), pk2(o[6] * sc, o[7] * sc), false, false);
    const unsigned tb = WS_AO + ((tok >> 4) * 40 + half * 20) * 1024;
    const u32x4 mv = {s0[0], s1[0], s0[1], s1[1]};
    ST16(mv, tb + (head >> 1) * 1024 + (((head & 1) * 2 + h) * 16 + (tok & 15)) * 16);
    const u32x2 tv = {pk2(o[8] * sc, o[9] * sc), pk2(o[10] * sc, o[11] * sc)};
    if (!h) ST8(tv, tb + (16 + (head >> 3)) * 1024 + (head & 7) * 128 + (tok & 15) * 8);
}
#define LD8(off)  __builtin_amdgcn_raw_buffer_load_b64(rs, (int)(off), 0, 16)
#define LD_TAIL(dst, base) do { const u32x2 a_ = LD8((base) + (lane >> 4) * 256 + (lane & 15) * 8), b_ = LD8((base) + (lane >> 4) * 256 + 128 + (lane & 15) * 8); dst = (u32x4){a_[0], a_[1], b_[0], b_[1]}; } while (0)

#define DSR128(dst, addr, off) asm volatile("ds_read_b128 %0, %1 offset:%2" : "=v"(dst) : "v"(addr), "i"(off))
#define DSR32(dst, addr, off) asm volatile("ds_read_b32 %0, %1 offset:%2" : "=v"(dst) : "v"(addr), "i"(off))
#define DSR128M2(d0, a0, o0, d1, a1, o1) asm volatile("s_mov_b64 exec, %6\n\tds_read_b128 %0, %2 offset:%3\n\tds_read_b128 %1, %4 offset:%5\n\ts_mov_b64 exec, -1" \
                                                      : "+v"(d0), "+v"(d1) : "v"(a0), "i"(o0), "v"(a1), "i"(o1), "s"(vmask))
#define LWAIT(n) do { asm volatile("s_waitcnt lgkmcnt(" #n ")" ::: "memory"); SBAR(); } while (0)

#define BAR() asm volatile("s_barrier" ::: "memory")
#define LDS_SET(off, v) __hip_atomic_store((LDS3 unsigned*)(lds + (off)), (unsigned)(v), __ATOMIC_RELAXED, __HIP_MEMORY_SCOPE_WORKGROUP)
#define LDS_CNT_ADD(off) __hip_atomic_fetch_add((LDS3 unsigned*)(lds + (off)), 1u, __ATOMIC_RELAXED, __HIP_MEMORY_SCOPE_WORKGROUP)

template <int SLP = 1>
__device__ __forceinline__ void lds_wait(LDS3 char* p, unsigned want) {
#pragma unroll 1
    for (unsigned spins = 0; spins < (1u << 20); ++spins) {
        if ((unsigned)__builtin_amdgcn_readfirstlane(__hip_atomic_load((LDS3 unsigned*)p, __ATOMIC_RELAXED, __HIP_MEMORY_SCOPE_WORKGROUP)) >= want) break;
        __builtin_amdgcn_s_sleep(SLP);
    }
    __builtin_amdgcn_fence(__ATOMIC_ACQUIRE, "wavefront");
}

template <int SLP>
__device__ __forceinline__ void prog_wait(LDS3 char* p, int lane, unsigned want) {
#pragma unroll 1
    for (unsigned spins = 0; spins < (1u << 20); ++spins) {
        if (__all(__hip_atomic_load((LDS3 unsigned*)p + (lane & 7), __ATOMIC_RELAXED, __HIP_MEMORY_SCOPE_WORKGROUP) >= want)) break;
        __builtin_amdgcn_s_sleep(SLP);
    }
    __builtin_amdgcn_fence(__ATOMIC_ACQUIRE, "wavefront");
}
#define POLL_ONE(cond, SLP, o0, o1, word) POLL_ONE2(cond, SLP, 2, o0, o1, word)
#define POLL_ONE2(cond, SLP, SLP2, o0, o1, word) do { if (cond) { wait_flags<SLP>(ws, o0, o1, tag); \
        if (lane == 0) __hip_atomic_store((LDS3 unsigned*)(lds + (word)), 1u, __ATOMIC_RELAXED, __HIP_MEMORY_SCOPE_WORKGROUP); } else lds_wait<SLP2>(lds + (word), 1); } while (0)
constexpr int C_KV = 0, C_KVF = 4, C_OB = 8, C_WA = 16, C_OA = 20, C_BX = 28, C_MRG = 32  , C_FIN = 64  , C_BP = 96  ;
constexpr int C_P1 = 112, C_P2 = 116, C_P3 = 120, C_P4 = 124, C_PR = 128  , C_KVL = 160, C_P5 = 164, C_GB = 168;
constexpr int G_W = 0, G_Q = 176, G_P = 180, G_ST = 184, G_RD = 188;
constexpr int X_G = 256;
constexpr int X_KVF = 1280, X_SLOT = 4096;
constexpr int X_MRG = 1280;
constexpr int L_WA = 24576;
constexpr int L_Q = 81920;
constexpr int X_FIN = 126976;

__global__ __launch_bounds__(768) void mega(Params P) {
    __shared__ __attribute__((aligned(16))) char smem[163840];
    LDS3 char* lds = (LDS3 char*)smem;
    char* ws = P.ws;
    const int tid = threadIdx.x, lane = tid & 63, r = lane & 31, h = lane >> 5;
    const int hw = __builtin_amdgcn_readfirstlane(tid >> 6);
    const bool helper = hw < 4;
    const int w = hw - 4, mtid = tid - 256;
    const int bid = blockIdx.x;
    const unsigned lds0 = (unsigned)(uintptr_t)smem;
    const rsrc_t rs = __builtin_amdgcn_make_buffer_rsrc(ws, 0, WS_END, 0x00020000);
    STAMP(0);
    u64 tag = 0;
#define GET_TAG() do { asm volatile("s_load_dwordx2 %0, %1, 0x0\n\ts_waitcnt lgkmcnt(0)" : "=s"(tag) : "s"(ws) : "memory"); tag += 1; } while (0)
    const int head = (bid & 7) * 4 + ((bid >> 3) & 3), qb = bid >> 5;
    const int nlo = (head * 20) >> 7, nhi = (head * 20 + 19) >> 7;
    const int mtO = (bid & 7) * 4 + ((bid >> 3) & 3), ntO = bid >> 5;
    const int lin = (bid & 7) * 30 + (bid >> 3), lh = lin >= 120, lrem = lin - 120 * lh;
    const int mt = lh * 8 + (lrem & 7), nt = lrem >> 3;
    const unsigned fG = WS_F_G + (nt * 16 + mt) * 8;
    const unsigned one = (r == 20) ? 0x3f803f80u : 0u;
    const f32x16 z16 = {};

    if (helper) {
        const unsigned fA0 = WS_F_G + (((lane < 32 ? 5 : 10) + ((lane & 16) ? nhi : nlo)) * 16 + (lane & 15)) * 8;
        const unsigned fA1 = WS_F_G + (((lane & 2) ? nhi : nlo) * 16 + qb * 2 + (lane & 1)) * 8;
        __builtin_amdgcn_s_setprio(3);
        const char* kvb = ws + WS_KV + head * 163840;
        auto kv_issue = [&](int grp, int t0 = 0, int t1 = 8) {
#pragma unroll
            for (int t = t0; t < t1; ++t) { int pc = grp * 32 + hw + 4 * t; if (pc == 0) pc = 4; glds16s(lane * 16, kvb + pc * 1024, lds0 + pc * 1024); }
        };
        auto q_issue = [&]() {
            const char* qbase = ws + WS_Q + head * 81920 + qb * 10240;
#pragma unroll
            for (int t = 0; t < 3; ++t) { const int pc = hw + 4 * t < 10 ? hw + 4 * t : 10; glds16s(lane * 16, pc < 10 ? qbase + pc * 1024 : kvb, lds0 + L_Q + pc * 1024); }
        };
        if (bid < 240) {
            const int rot = ((nt + mt) % 3) * 3;
            unsigned xo[4], wo[4];
#pragma unroll
            for (int i = 0; i < 4; ++i) {
                xo[i] = (mt * 128 + 32 * hw + 8 * i + (lane >> 3)) * 1280 + (((lane & 7) ^ ((4 * i + (lane >> 4)) & 7)) << 4) - i * 1024;
                wo[i] = (16 * hw + 4 * i + (lane >> 4)) * 3840 + nt * 256 + (((lane & 15) ^ (((lane >> 4) << 2) | i)) << 4) - i * 1024;
            }
            const unsigned dX = lds0 + 32768 + hw * 4096;
            int kt = rot;
#pragma unroll
            for (int n = 0; n < 20; ++n) {
                const char* xb = (const char*)P.x + kt * 128;
                const char* wb = (const char*)(n >= 10 ? P.wb : P.wa) + (size_t)kt * (64 * 3840);
                asm volatile("" : "+s"(xb), "+s"(wb));
                if (n == 2) { if (hw == 0) *(LDS3 unsigned*)(lds + lane * 4) = 0u; GET_TAG(); WAIT_BAR(8); }
                else if (n == 11 || n == 15) { WAIT_BAR(12); }
                else if (n == 3 || (n >= 12 && n <= 14)) { WAIT_BAR(8); }
                else if (n >= 5) { WAIT_BAR(16); }
                glds16x4(wo, wb, dX + 16384 + (n & 3) * 32768);
                if (n < 10 || n >= 14) glds16x4(xo, xb, dX + ((n >= 10 ? 20 - n : n + 1) & 3) * 32768);
                if (n < 9) kt = kt == 9 ? 0 : kt + 1; else if (n > 9) kt = kt == 0 ? 9 : kt - 1;
            }
            WAIT_BAR(16);
            WAIT_BAR(0);
        } else {
            if (hw == 0) *(LDS3 unsigned*)(lds + lane * 4) = 0u;
            GET_TAG();
            WAIT_BAR(63);
            if (hw == 0) {
#pragma unroll 1
                for (int t = 0; t < PRE_SLEEP; ++t) __builtin_amdgcn_s_sleep(127);
            }
        }
        POLL_ONE(hw == 0, 6, fA0, fA1, G_P);
        kv_issue(0); q_issue(); kv_issue(1, 0, 2);
        DRAIN();
        if (lane == 0) LDS_CNT_ADD(G_Q);
        lds_wait<2>(lds + G_RD, 8);
        kv_issue(1, 2, 8); kv_issue(2); kv_issue(3); kv_issue(4);
        asm volatile("s_waitcnt vmcnt(24)" ::: "memory"); if (lane == 0) LDS_CNT_ADD(C_KVL);
        asm volatile("s_waitcnt vmcnt(16)" ::: "memory"); if (lane == 0) LDS_CNT_ADD(C_KVL + 12);
        STAMP(3);
        u32x4 phm; unsigned pht;
        {
            const u16* wp = P.wphi + head * 400 + (r < 20 ? r : (r & 3));
            unsigned e[10];
#pragma unroll
            for (int j = 0; j < 8; ++j) e[j] = wp[(8 * h + j + 4 * (head & 1)) * 20];
            e[8] = wp[((head & 1 ? 0 : 16) + 2 * h) * 20]; e[9] = wp[((head & 1 ? 1 : 17) + 2 * h) * 20];
            const int bu = bid < 160 ? bid : 0, hf = bu >= 80, k0 = (bu - 80 * hf) * 8, hd = k0 < 512 ? k0 >> 4 : (k0 - 512) >> 2;
            const int row = k0 < 512 ? 20 * hd + (k0 & 8) + 4 * (hd & 1) : 20 * hd + 16;
            const u16* wsrc = (hf ? P.wob : P.woa) + (size_t)row * DM;
            unsigned f[3][8];
            if (bid < 160) {
#pragma unroll
                for (int q = 0; q < 3; ++q) {
                    const int n = tid + 256 * q < 640 ? tid + 256 * q : 639;
#pragma unroll
                    for (int j = 0; j < 8; ++j) f[q][j] = wsrc[j * DM + n];
                }
            }
            DRAIN();
            if (lane == 0) LDS_CNT_ADD(C_WA + 8);
            phm = (u32x4){e[0] | (e[1] << 16), e[2] | (e[3] << 16), e[4] | (e[5] << 16), e[6] | (e[7] << 16)}; pht = e[8] | (e[9] << 16);
            if (bid < 160) {
#pragma unroll
                for (int q = 0; q < 3; ++q) {
                    const int n = tid + 256 * q;
                    const u32x4 v = {f[q][0] | (f[q][1] << 16), f[q][2] | (f[q][3] << 16), f[q][4] | (f[q][5] << 16), f[q][6] | (f[q][7] << 16)};
                    if (n < 640) ST16(v, WS_WT + (((n >> 4) * 40 + hf * 20 + (k0 >> 5)) * 64 + ((k0 >> 3) & 3) * 16 + (n & 15)) * 16);
                }
            }
        }
        const int tokS = qb * 256 + hw * 64, m7 = tokS >> 7, t4 = (tokS >> 5) & 3;
        {
            const unsigned pt3 = (lane >> 1) & 3;
            const unsigned tb = WS_F_G + 2048 + ((((lane & 1) ? nhi : nlo) + (pt3 < 3 ? pt3 : 0) * 5) * 16 + qb * 2 + ((lane >> 3) & 1)) * 8;
            POLL_ONE2(hw == 0, 4, HS, tb, WS_F_SD + (qb * 2 + (lane & 1)) * 8, C_P1);
        }
        const unsigned om = t4 * 1024 + lane * 16, ot = 4096 + (t4 * 32 + r) * 8 + 4 * h;
        u32x4 qf[2][3]; float gS[2];
        {
            const unsigned kb = WS_KV + (NH + head) * 163840 + (2 * (m7 & 7) + (m7 >> 3)) * 10240, qbb = WS_Q + (NH + head) * 81920 + m7 * 5120;
            const unsigned ov = 5120 + t4 * 1280 + h * 16 + (r < 20 ? r : 19) * 32;
            u32x4 e0[2], w0[2], w1[2], eq[2]; unsigned et[2], eqt[2];
#pragma unroll
            for (int i = 0; i < 2; ++i) {
                e0[i] = LD16(kb + om + i * 1024); et[i] = LD4(kb + ot + i * 256); w0[i] = LD16(kb + ov + i * 1280); w1[i] = LD16(kb + ov + i * 1280 + 640);
                eq[i] = LD16(qbb + om + i * 1024); eqt[i] = LD4(qbb + ot + i * 256); gS[i] = __uint_as_float(LD4(WS_G + (tokS + i * 32 + r) * 4));
            }
            u32x4 i0, i1;
#pragma unroll
            for (int j = 0; j < 4; ++j) {
                const int e = r - 8 * h - 2 * j;
                i0[j] = e == 0 ? 0x3f80u : (e == 1 ? 0x3f800000u : 0u);
                i1[j] = e == 16 ? 0x3f80u : (e == 17 ? 0x3f800000u : 0u);
            }
            f32x16 k0 = {}, k1 = {};
#pragma unroll
            for (int i = 0; i < 2; ++i) {
                if (r >= 20) { w0[i] = (u32x4){one, one, one, one}; w1[i] = w0[i]; }
                u32x4 fp, fn, ft;
                feature_map(phm, pht, e0[i], et[i], h, fp, fn, ft);
                f32x16 T0 = MFMA32_16(fp, i0, z16); T0 = MFMA32_16(fn, i1, T0);
                f32x16 T1 = MFMA32_16(ft, i0, z16);
                u32x4 b00, b01, b10, b11;
#pragma unroll
                for (int j = 0; j < 4; ++j) {
                    b00[j] = pk2(T0[2 * j], T0[2 * j + 1]); b01[j] = pk2(T0[8 + 2 * j], T0[9 + 2 * j]);
                    b10[j] = pk2(T1[2 * j], T1[2 * j + 1]); b11[j] = pk2(T1[8 + 2 * j], T1[9 + 2 * j]);
                }
                k0 = MFMA32_16(w0[i], b00, k0); k0 = MFMA32_16(w1[i], b01, k0);
                k1 = MFMA32_16(w0[i], b10, k1); k1 = MFMA32_16(w1[i], b11, k1);
            }
            prog_wait<8>(lds + C_PR, lane, 1);
            *(LDS3 float*)(lds + X_G + (hw * 64 + lane) * 4) = h ? gS[1] : gS[0];
            if (hw * 64 + lane < 144) *(LDS3 u32x4*)(lds + X_KVF + (hw * 64 + lane) * 16) = (u32x4){0u, 0u, 0u, 0u};
            LDS3 char* sp = lds + X_SLOT + hw * 4096;
#pragma unroll
            for (int gq = 0; gq < 3; ++gq) {
                *(LDS3 f32x4*)(sp + gq * 1024 + lane * 16) = (f32x4){k0[4 * gq], k0[4 * gq + 1], k0[4 * gq + 2], k0[4 * gq + 3]};
                if (r < 8) *(LDS3 f32x4*)(sp + 3072 + (gq * 16 + h * 8 + r) * 16) = (f32x4){k1[4 * gq], k1[4 * gq + 1], k1[4 * gq + 2], k1[4 * gq + 3]};
            }
            asm volatile("s_waitcnt vmcnt(0) lgkmcnt(0)" ::: "memory");
            unsigned arr = 0;
            if (lane == 0) arr = LDS_CNT_ADD(C_KV);
            if (__builtin_amdgcn_readfirstlane(arr) == 3) {
                __builtin_amdgcn_fence(__ATOMIC_ACQUIRE, "wavefront");
#pragma unroll
                for (int i = 0; i < 4; ++i) {
                    const int qd = lane + 64 * i;
                    LDS3 const char* s0 = lds + X_SLOT + qd * 16;
                    const f32x4 s = (*(LDS3 const f32x4*)s0 + *(LDS3 const f32x4*)(s0 + 4096)) + (*(LDS3 const f32x4*)(s0 + 8192) + *(LDS3 const f32x4*)(s0 + 12288));
                    if (qd < 240) ST16(__builtin_bit_cast(u32x4, s), WS_KVP + (head * 8 + qb) * 4096 + qd * 16);
                }
                DRAIN();
                if (lane == 0) set_flag(ws, WS_F_KV + (head * 8 + qb) * 8, tag);
            }
#pragma unroll
            for (int i = 0; i < 2; ++i) feature_map(phm, pht, eq[i], eqt[i], h, qf[i][0], qf[i][1], qf[i][2]);
        }
        STAMP(4);
        POLL_ONE2(hw == 0, 4, HS, WS_F_KV + (head * 8 + (lane & 7)) * 8, WS_F_KV + (head * 8 + (lane & 7)) * 8, C_P2);
        {
            const int qd = hw * 60 + (lane < 60 ? lane : 59);
            f32x4 pp[8];
#pragma unroll
            for (int p = 0; p < 8; ++p) pp[p] = __builtin_bit_cast(f32x4, LD16(WS_KVP + (head * 8 + p) * 4096 + qd * 16));
            const f32x4 s4 = ((pp[0] + pp[1]) + (pp[2] + pp[3])) + ((pp[4] + pp[5]) + (pp[6] + pp[7]));
            const int gq = qd < 192 ? qd >> 6 : (qd - 192) >> 4, hs = qd < 192 ? (qd >> 5) & 1 : (qd >> 3) & 1, sg = qd < 192 ? qd & 31 : 32 + (qd & 7);
            const unsigned a = pk2(s4[0], s4[1]), b = pk2(s4[2], s4[3]);
            LDS3 u16* kp = (LDS3 u16*)(lds + X_KVF + (8 * gq + 4 * hs) * 96 + sg * 2);
            kp[0] = (u16)a; kp[48] = (u16)(a >> 16); kp[96] = (u16)b; kp[144] = (u16)(b >> 16);
            asm volatile("s_waitcnt lgkmcnt(0)" ::: "memory");
            if (lane == 0) LDS_CNT_ADD(C_KVF);
            lds_wait<2>(lds + C_KVF, 4);
        }
        {
            LDS3 const char* kp = lds + X_KVF + (r < 24 ? r : 23) * 96 + h * 16;
            const u32x4 a0 = *(LDS3 const u32x4*)kp, a1 = *(LDS3 const u32x4*)(kp + 32), a2 = *(LDS3 const u32x4*)(kp + 64);
#pragma unroll
            for (int i = 0; i < 2; ++i) {
                f32x16 d = MFMA32_16(a0, qf[i][0], z16); d = MFMA32_16(a1, qf[i][1], d); d = MFMA32_16(a2, qf[i][2], d);
                const float den = upper_half(d[8]) + 1e-6f;
                float o[12];
#pragma unroll
                for (int j = 0; j < 12; ++j) o[j] = d[j];
                store_o(rs, o, (1.f - gS[i]) / den, tokS + i * 32 + r, 1, head, h);
            }
            DRAIN();
            unsigned arr = 0;
            if (lane == 0) arr = LDS_CNT_ADD(C_OB);
            if (__builtin_amdgcn_readfirstlane(arr) == 3 && lane == 0) set_flag(ws, WS_F_OB + (qb * 32 + head) * 8, tag);
        }
        STAMP(5);
        f32x4 c[5];
#pragma unroll
        for (int n = 0; n < 5; ++n) c[n] = (f32x4){0.f, 0.f, 0.f, 0.f};
        const char* wsrc = ws + WS_WT + (size_t)ntO * 5 * 40960;
        {
            if (hw == 0) wait_flags<8>(ws, WS_F_KV + lane * 8, WS_F_KV + 512 + lane * 8, tag);
            POLL_ONE2(hw == 0, 8, HS, WS_F_KV + 1024 + lane * 8, WS_F_KV + 1536 + lane * 8, C_P5);
#pragma unroll 1
            for (int t = 0; t < 5; ++t) {
                if (t == 0) prog_wait<2 * HS>(lds + C_PR, lane, 4); else if (t == 3) prog_wait<2 * HS>(lds + C_PR, lane, 5);
                const int ks = hw + 4 * t < 19 ? hw + 4 * t : 18;
#pragma unroll
                for (int n = 0; n < 5; ++n) glds16s(lane * 16, wsrc + (n * 40 + 20 + ks) * 1024, lds0 + 4096 + (ks * 5 + n) * 1024);
            }
            POLL_ONE2(hw == 0, 4, HS, WS_F_OB + ((mtO >> 2) * 32 + r) * 8, WS_F_OB + ((mtO >> 2) * 32 + r) * 8, C_P3);
            const unsigned ab = WS_AO + ((mtO * 4 + hw) * 40 + 20) * 1024;
            u32x4 af[20];
#pragma unroll
            for (int ks = 0; ks < 16; ++ks) af[ks] = LD16(ab + ks * 1024 + lane * 16);
#pragma unroll
            for (int ks = 16; ks < 20; ++ks) LD_TAIL(af[ks], ab + ks * 1024);
            asm volatile("s_waitcnt vmcnt(20)" ::: "memory");
            if (lane == 0) LDS_CNT_ADD(C_BX);
            lds_wait<2>(lds + C_BX, 4);
            u32x4 wf[3][5], wl[5];
            LDS3 const char* wp = lds + 4096 + lane * 16;
#pragma unroll
            for (int ks = 0; ks < 2; ++ks)
#pragma unroll
                for (int n = 0; n < 5; ++n) wf[ks][n] = *(LDS3 const u32x4*)(wp + (ks * 5 + n) * 1024);
#pragma unroll
            for (int ks = 0; ks < 20; ++ks) {
                if (ks + 2 < 19) {
#pragma unroll
                    for (int n = 0; n < 5; ++n) wf[(ks + 2) % 3][n] = *(LDS3 const u32x4*)(wp + ((ks + 2) * 5 + n) * 1024);
                }
                if (ks == 8) {
#pragma unroll
                    for (int n = 0; n < 5; ++n) wl[n] = LD16(WS_WT + ((ntO * 5 + n) * 40 + 39) * 1024 + lane * 16);
                }
                SBAR();
#pragma unroll
                for (int n = 0; n < 5; ++n) c[n] = MFMA16_32(ks < 19 ? wf[ks % 3][n] : wl[n], af[ks], c[n]);
                SBAR();
            }
            if (lane == 0) LDS_CNT_ADD(C_BX);
            lds_wait<2>(lds + C_BX, 8);
        }
        STAMP(6);
        prog_wait<2 * HS>(lds + C_PR, lane, 7);
#pragma unroll 1
        for (int n = 0; n < 5; ++n)
#pragma unroll
            for (int t = 0; t < 5; ++t) { const int ks = hw + 4 * t; glds16s(lane * 16, wsrc + (n * 40 + ks) * 1024, lds0 + L_WA + (n * 20 + ks) * 1024); }
#pragma unroll
        for (int n = 0; n < 3; ++n) *(LDS3 f32x4*)(lds + X_FIN + hw * 3072 + n * 1024 + lane * 16) = c[n];
        DRAIN();
        if (lane == 0) LDS_CNT_ADD(C_WA);
        prog_wait<2 * HS>(lds + C_PR, lane, 8);
#pragma unroll
        for (int n = 3; n < 5; ++n) *(LDS3 f32x4*)(lds + 143360 + hw * 2048 + (n - 3) * 1024 + lane * 16) = c[n];
        asm volatile("s_waitcnt lgkmcnt(0)" ::: "memory");
        if (lane == 0) __hip_atomic_store((LDS3 unsigned*)(lds + C_BP + hw * 4), 1u, __ATOMIC_RELAXED, __HIP_MEMORY_SCOPE_WORKGROUP);
        STAMP(7);
        asm volatile("mega_code_end2:");
    } else {
        const int qs = w & 3, kh = w >> 2;
        const int q0 = qb * 256 + qs * 64;
        {
            unsigned sink;
            if (w == 0 && (WARM_MODE & 1)) {
                const char* pc0; int len, len2;
                asm volatile("s_getpc_b64 %0\nmega_code_mark:\n\ts_mov_b32 %1, mega_code_end-mega_code_mark\n\ts_mov_b32 %2, mega_code_end2-mega_code_mark" : "=s"(pc0), "=s"(len), "=s"(len2));
                len = len > len2 ? len : len2;
                const int o = ((bid >> 3) * 64 + lane) * 64;
                if (o + 4 <= len) asm volatile("global_load_dword %0, %1, %2" : "=v"(sink) : "v"(o), "s"(pc0) : "memory");
            } else if (w >= 1 && w <= 6 && (WARM_MODE & 2)) {
                const unsigned o = ((w - 1) * 64 + lane) * 65536 + 65532;
                if (o < WS_END) asm volatile("global_load_dword %0, %1, %2 sc1" : "=v"(sink) : "v"(o), "s"(ws) : "memory");
            } else if (w == 7 && (WARM_MODE & 2)) {
                if (lane < 40) asm volatile("global_load_dword %0, %1, %2 sc1" : "=v"(sink) : "v"(lane * 65536), "s"(P.out) : "memory");
            }
            asm volatile("s_waitcnt vmcnt(0)" ::: "memory");
        }
        GET_TAG();
        if (bid < 240) {
            const int part = nt / 5, c0 = (nt - part * 5) * 128;
            const int wm = w & 1, wn = w >> 1;
            constexpr int RB = 32768;
            const int xr = wm * 64 + r;
            const unsigned xa = RB + xr * 128 + ((h ^ ((xr >> 1) & 7)) << 4);
            unsigned wt[2];
#pragma unroll
            for (int t = 0; t < 2; ++t) {
                const int q = (lane & 15) >> 2, p = lane & 3, blk = (lane >> 4) & 1, row = 8 * h + 4 * t + q;
                wt[t] = RB + 16384 + row * 256 + (((4 * wn + 2 * blk + (p >> 1)) ^ ((q << 2) | (2 * h + t))) << 4) + 8 * (p & 1);
            }
            auto gbody = [&](auto trc) {
                constexpr bool TR = decltype(trc)::value;
                u32x4 xf[2][8], wf[2][4];
                auto rdk = [&](auto bc, LDS3 const char* sx, LDS3 const char* sb, int ks) {
                    constexpr int B = decltype(bc)::value;
                    xf[B][2 * ks] = *(LDS3 const u32x4*)(sx + (xa ^ (ks * 32)));
                    xf[B][2 * ks + 1] = *(LDS3 const u32x4*)(sx + ((xa + 4096) ^ (ks * 32)));
                    const u32x2 lo = trread(sb + wt[0] + ks * 4096), hi = trread(sb + wt[1] + ks * 4096);
                    wf[B][ks] = (u32x4){lo[0], lo[1], hi[0], hi[1]};
                };
                f32x16 acc0, acc1;
                auto step = [&](auto bc, int s) {
                    constexpr int B = decltype(bc)::value;
                    if (s == GA_PUB) {
                        DRAIN();
                        unsigned arr = 0;
                        if (lane == 0) arr = LDS_CNT_ADD(G_ST);
                        if (__builtin_amdgcn_readfirstlane(arr) == 7 && lane == 0) set_flag(ws, fG, tag);
                    }
                    __builtin_amdgcn_s_waitcnt(0xC07F);
                    if (s < 18) BAR();
                    SBAR();
                    const int s1 = s + 1;
                    LDS3 const char* sb = lds + __builtin_amdgcn_readfirstlane((s1 & 3) * 32768);
                    LDS3 const char* sx = lds + __builtin_amdgcn_readfirstlane(((s1 >= 10 ? 20 - s1 : s1 + 1) & 3) * 32768);
#define GM(A, i) do { if constexpr (TR) A = MFMA32_16(wf[B][(i) >> 1], xf[B][i], A); else A = MFMA32_16(xf[B][i], wf[B][(i) >> 1], A); SBAR(); } while (0)
#define GX(i) do { xf[B ^ 1][i] = *(LDS3 const u32x4*)(sx + ((xa + ((i) & 1) * 4096) ^ (((i) >> 1) * 32))); } while (0)
#define GW(ks) do { const u32x2 lo = trread(sb + wt[0] + (ks) * 4096), hi = trread(sb + wt[1] + (ks) * 4096); wf[B ^ 1][ks] = (u32x4){lo[0], lo[1], hi[0], hi[1]}; } while (0)
                    GM(acc0, 0); GX(0); GW(0); SBAR(); GM(acc1, 1); GX(1); GX(2); SBAR(); GM(acc0, 2); GW(1); GX(3); SBAR(); GM(acc1, 3); GX(4); GW(2); SBAR();
                    GM(acc0, 4); GX(5); GX(6); SBAR(); GM(acc1, 5); GW(3); GX(7); SBAR(); GM(acc0, 6); GM(acc1, 7);
#undef GX
#undef GW
#undef GM
                };
                WAIT_BAR(63);
#pragma unroll
                for (int ks = 0; ks < 4; ++ks) rdk(std::integral_constant<int, 0>{}, lds + 32768, lds, ks);
#pragma unroll 1
                for (int mat = 0; mat < 2; ++mat) {
                    acc0 = (f32x16){}; acc1 = (f32x16){};
#pragma unroll 1
                    for (int kk = 0; kk < 10; kk += 2) {
                        step(std::integral_constant<int, 0>{}, mat * 10 + kk);
                        step(std::integral_constant<int, 1>{}, mat * 10 + kk + 1);
                    }
                    const int cpos = 2 * (mt & 7) + (mt >> 3);
                    if constexpr (TR) {
                        const float sc = (part == 0 && mat == 0) ? QSCALE : 1.f;
                        const unsigned base = part == 0 ? WS_Q + mat * NH * 81920 + mt * 5120 : WS_KV + mat * NH * 163840 + cpos * 10240;
                        const unsigned hs = part == 0 ? 81920 : 163840;
#pragma unroll
                        for (int pr = 0; pr < 2; ++pr) {
                            const int G8 = ((c0 + 32 * wn) >> 3) + 2 * pr + h, pp = G8 / 5, gi = G8 - pp * 5;
                            const unsigned hb0 = base + (2 * pp + (gi > 2)) * hs;
#pragma unroll
                            for (int i = 0; i < 2; ++i) {
                                const f32x16& a = i ? acc1 : acc0;
                                const int tl = wm * 64 + i * 32 + r;
                                const auto s0 = __builtin_amdgcn_permlane32_swap(pk2(a[8 * pr] * sc, a[8 * pr + 1] * sc), pk2(a[8 * pr + 4] * sc, a[8 * pr + 5] * sc), false, false);
                                const auto s1 = __builtin_amdgcn_permlane32_swap(pk2(a[8 * pr + 2] * sc, a[8 * pr + 3] * sc), pk2(a[8 * pr + 6] * sc, a[8 * pr + 7] * sc), false, false);
                                if (gi != 2) { const u32x4 ov = {s0[0], s1[0], s0[1], s1[1]}; ST16(ov, hb0 + (tl >> 5) * 1024 + (gi > 2 ? gi - 3 : gi) * 512 + r * 16); }
                                else { const u32x2 o0 = {s0[0], s1[0]}, o1 = {s0[1], s1[1]}; ST8(o0, hb0 + 4096 + tl * 8); ST8(o1, hb0 + hs + 4096 + tl * 8); }
                            }
                        }
                    } else {
                        const int n = c0 + 32 * wn + r, hd = n / 20, d = n - hd * 20, rho = (hd & 1) ? (d < 4 ? d + 16 : d - 4) : d;
                        const unsigned base = WS_KV + (mat * NH + hd) * 163840 + cpos * 10240 + 5120 + h * 16 + rho * 32;
#pragma unroll
                        for (int i = 0; i < 2; ++i)
#pragma unroll
                            for (int s2 = 0; s2 < 2; ++s2) {
                                const f32x16& a = i ? acc1 : acc0;
                                u32x4 ov = {pk2(a[8 * s2], a[8 * s2 + 1]), pk2(a[8 * s2 + 2], a[8 * s2 + 3]), pk2(a[8 * s2 + 4], a[8 * s2 + 5]), pk2(a[8 * s2 + 6], a[8 * s2 + 7])};
                                ST16(ov, base + (wm * 4 + i * 2 + s2) * 640);
                            }
                    }
                    if (mat == 0) STAMP(1);
                }
            };
            if (part < 2) gbody(std::true_type{}); else gbody(std::false_type{});
        } else {
            const int sj = bid - 240;
            WAIT_BAR(63);
            {
                const u32x4 ga = *(const u32x4*)(P.gw + lane * 8), gb = *(const u32x4*)(P.gw + 512 + (lane & 15) * 8);
                u32x4 xa[16], xb[4];
                const u16* xr = P.x + (size_t)(sj * 128 + w * 16) * DM;
#pragma unroll
                for (int rr = 0; rr < 16; ++rr) xa[rr] = *(const u32x4*)(xr + rr * DM + lane * 8);
#pragma unroll
                for (int i = 0; i < 4; ++i) xb[i] = *(const u32x4*)(xr + (i * 4 + (lane >> 4)) * DM + 512 + (lane & 15) * 8);
                float res = 0.f;
#pragma unroll
                for (int rr = 0; rr < 16; ++rr) {
                    float s = 0.f, s2 = 0.f;
#pragma unroll
                    for (int j = 0; j < 4; ++j) { s += bf_lo(xa[rr][j]) * bf_lo(ga[j]) + bf_hi(xa[rr][j]) * bf_hi(ga[j]); s2 += bf_lo(xb[rr >> 2][j]) * bf_lo(gb[j]) + bf_hi(xb[rr >> 2][j]) * bf_hi(gb[j]); }
                    if ((lane >> 4) == (rr & 3)) s += s2;
#pragma unroll
                    for (int o = 32; o >= 1; o >>= 1) s += __shfl_xor(s, o);
                    if (lane == rr) res = s;
                }
                if (lane < 16) ST4(__float_as_uint(1.f / (1.f + __expf(-res))), WS_G + (sj * 128 + w * 16 + lane) * 4);
            }
            DRAIN();
            unsigned arr = 0;
            if (lane == 0) arr = LDS_CNT_ADD(G_ST);
            if (__builtin_amdgcn_readfirstlane(arr) == 7 && lane == 0) set_flag(ws, WS_F_SD + sj * 8, tag);
        }
        STAMP(2);
        const unsigned hb = lds0 + kh * 10240;
        const unsigned laneK = hb + lane * 16, laneT = hb + 4096 + r * 8 + h * 4, laneV = hb + 5120 + h * 16 + (r < 20 ? r : 19) * 32;
        const u64 vmask = 0x000fffff000fffffull;
        u32x4 v0a = {one, one, one, one}, v0b = v0a, v1 = v0a;
        u32x4 qA, qB, ka; unsigned qtA, qtB, kt;
        {
            lds_wait(lds + G_Q, 4);
            LDS3 const char* qp = lds + L_Q + (qs >> 1) * 5120;
            const unsigned om = (qs & 1) * 2048 + lane * 16, ot = 4096 + ((qs & 1) * 64 + r) * 8 + 4 * h;
            qA = *(LDS3 const u32x4*)(qp + om); qB = *(LDS3 const u32x4*)(qp + om + 1024);
            qtA = *(LDS3 const unsigned*)(qp + ot); qtB = *(LDS3 const unsigned*)(qp + ot + 256);
            DSR128(ka, kh ? laneK : lds0 + L_Q + 10240 + lane * 16, 0); DSR32(kt, laneT, 0); DSR128M2(v0a, laneV, 0, v1, laneV, 640);
            asm volatile("s_waitcnt lgkmcnt(0)" : "+v"(ka), "+v"(kt), "+v"(v0a), "+v"(v1), "+v"(qA), "+v"(qB), "+v"(qtA), "+v"(qtB) :: "memory");
            if (lane == 0) LDS_CNT_ADD(G_RD);
        }
        STAMP(5);
        float mA, mB; u32x2 qTA, qTB; f32x16 sA, sB;
        {
            const u32x2 t0 = {kt, 0x3f80u}, a0 = {qtA, 0u}, b0 = {qtB, 0u};
            const f32x16 s = MFMA32_16(ka, qA, z16);
            const f32x16 s0 = MFMA32_8(t0, a0, s);
            f32x16 s1 = MFMA32_16(ka, qB, z16); s1 = MFMA32_8(t0, b0, s1);
            float x0 = s0[0], x1 = s1[0];
#pragma unroll
            for (int i = 1; i < 16; ++i) { x0 = fmaxf(x0, s0[i]); x1 = fmaxf(x1, s1[i]); }
            x0 = -xhalf_max(x0); x1 = -xhalf_max(x1);
            const unsigned hh = pk2(x0, x1);
            const unsigned ll = pk2(x0 - bf_lo(hh), x1 - bf_hi(hh));
            mA = -(bf_lo(hh) + bf_lo(ll)); mB = -(bf_hi(hh) + bf_hi(ll));
            const unsigned sel = h ? ll : hh;
            qTA = (u32x2){qtA, sel & 0xffffu}; qTB = (u32x2){qtB, sel >> 16};
            sA = MFMA32_8(t0, qTA, s);
        }
        f32x16 oA = {}, oB = {};
        u32x4 pa, pb = {0u, 0u, 0u, 0u};
#define EX4(S, i, Pv, j) do { Pv[j] = pk2(ex2(S[i]), ex2(S[i + 1])); Pv[j + 1] = pk2(ex2(S[i + 2]), ex2(S[i + 3])); PIN(Pv); SBAR(); } while (0)
#define KT2 ((u32x2){kt, 0x3f80u})
#define TILE(V0C, V0N, aK1, oK1, aT1, oT1, oV1, aV0, oV0) do { \
            sB = MFMA32_16(ka, qB, z16); SBAR(); DSR128(ka, aK1, oK1); EX4(sA, 0, pa, 0); \
            oB = MFMA32_16(v1, pb, oB); SBAR(); DSR128M2(v1, aV, oV1, V0N, aV0, oV0); EX4(sA, 4, pa, 2); \
            sB = MFMA32_8(KT2, qTB, sB); SBAR(); DSR32(kt, aT1, oT1); EX4(sA, 8, pb, 0); \
            oA = MFMA32_16(V0C, pa, oA); SBAR(); EX4(sA, 12, pb, 2); \
            LWAIT(1); sA = MFMA32_16(ka, qA, z16); SBAR(); EX4(sB, 0, pa, 0); \
            oA = MFMA32_16(v1, pb, oA); SBAR(); EX4(sB, 4, pa, 2); \
            LWAIT(0); sA = MFMA32_8(KT2, qTA, sA); SBAR(); EX4(sB, 8, pb, 0); \
            oB = MFMA32_16(V0C, pa, oB); SBAR(); EX4(sB, 12, pb, 2); \
        } while (0)
        unsigned aK = laneK, aT = laneT, aV = laneV;
#pragma unroll 1
        for (int c = 0; c < 8; ++c) {
            if (c >= 1 && c <= 3) lds_wait(lds + (c == 1 ? C_KVL : (c == 2 ? C_KVL + 12 : C_WA + 8)), 4);
            if (c >= 1) {
                unsigned pp;
                asm volatile("ds_write_b32 %1, %2\n\tds_read_b32 %0, %3\n\ts_waitcnt lgkmcnt(0)" : "=&v"(pp) : "v"(lds0 + C_PR + w * 4), "v"(c), "v"(lds0 + C_PR + (w ^ 4) * 4) : "memory");
                const int pc = __builtin_amdgcn_readfirstlane(pp);
                if (pc > c || (pc == c && ((c ^ kh) & 1))) __builtin_amdgcn_s_setprio(1); else __builtin_amdgcn_s_setprio(0);
            } else if (kh) __builtin_amdgcn_s_setprio(1);
            const unsigned nx = (c < 7 ? c + 1 : 7) * 20480;
            const unsigned nK = laneK + nx, nT = laneT + nx, nV = laneV + nx;
            SBAR();
            TILE(v0a, v0b, aK, 1024, aT, 256, 640,  aV, 1280);
            TILE(v0b, v0a, aK, 2048, aT, 512, 1920, aV, 2560);
            if (c == 0) {
                DRAIN();
                unsigned arr = 0;
                if (lane == 0) arr = LDS_CNT_ADD(C_GB);
                if (__builtin_amdgcn_readfirstlane(arr) == 7 && lane == 0 && bid < 240) set_flag(ws, fG + 2048, tag);
                asm volatile("s_waitcnt lgkmcnt(0)" ::: "memory");
                SBAR();
            }
            TILE(v0a, v0b, aK, 3072, aT, 768, 3200, aV, 3840);
            TILE(v0b, v0a, nK, 0,    nT, 0,   4480, nV, 0);
            aK = nK; aT = nT; aV = nV;
        }
        oB = MFMA32_16(v1, pb, oB);
#undef TILE
#undef EX4
        if (lane == 0) __hip_atomic_store((LDS3 unsigned*)(lds + C_PR + w * 4), 8u, __ATOMIC_RELAXED, __HIP_MEMORY_SCOPE_WORKGROUP);
        STAMP(6);
        {
            float chk = 0.f;
#pragma unroll
            for (int i = 0; i < 12; ++i) chk += fabsf(oA[i]) * 0.f + fabsf(oB[i]) * 0.f;
            bool bad = !(chk == 0.f);
            if (h) bad = bad || !(oA[8] >= 0x1p-60f && oA[8] <= 0x1p60f && oB[8] >= 0x1p-60f && oB[8] <= 0x1p60f);
            if (__builtin_expect(__any(bad), 0)) {
                mA = -INFINITY; mB = -INFINITY;
#pragma unroll
                for (int i = 0; i < 16; ++i) { oA[i] = 0.f; oB[i] = 0.f; }
                const u32x2 a0 = {qtA, 0u}, b0 = {qtB, 0u};
#pragma unroll 1
                for (int t = 0; t < 32; ++t) {
                    const unsigned kb = WS_KV + head * 163840 + (2 * (t >> 2) + kh) * 10240, t4 = t & 3;
                    const u32x4 k0 = LD16(kb + t4 * 1024 + lane * 16);
                    const u32x2 t0 = {LD4(kb + 4096 + (t4 * 32 + r) * 8 + 4 * h), 0u};
                    u32x4 w0 = LD16(kb + 5120 + t4 * 1280 + h * 16 + (r < 20 ? r : 19) * 32), w1 = LD16(kb + 5120 + t4 * 1280 + 640 + h * 16 + (r < 20 ? r : 19) * 32);
                    if (r >= 20) { w0 = (u32x4){one, one, one, one}; w1 = w0; }
                    f32x16 s0 = MFMA32_16(k0, qA, z16); s0 = MFMA32_8(t0, a0, s0);
                    f32x16 s1 = MFMA32_16(k0, qB, z16); s1 = MFMA32_8(t0, b0, s1);
                    float x0 = s0[0], x1 = s1[0];
#pragma unroll
                    for (int i = 1; i < 16; ++i) { x0 = fmaxf(x0, s0[i]); x1 = fmaxf(x1, s1[i]); }
                    x0 = fmaxf(xhalf_max(x0), mA); x1 = fmaxf(xhalf_max(x1), mB);
                    const float g0 = (mA == x0) ? 1.f : ex2(mA - x0), g1 = (mB == x1) ? 1.f : ex2(mB - x1);
                    mA = x0; mB = x1;
#pragma unroll
                    for (int i = 0; i < 16; ++i) { oA[i] *= g0; oB[i] *= g1; }
#pragma unroll
                    for (int i = 0; i < 4; ++i) { pa[i] = pk2(ex2(s0[2 * i] - x0), ex2(s0[2 * i + 1] - x0)); pb[i] = pk2(ex2(s0[8 + 2 * i] - x0), ex2(s0[9 + 2 * i] - x0)); }
                    oA = MFMA32_16(w0, pa, oA); oA = MFMA32_16(w1, pb, oA);
#pragma unroll
                    for (int i = 0; i < 4; ++i) { pa[i] = pk2(ex2(s1[2 * i] - x1), ex2(s1[2 * i + 1] - x1)); pb[i] = pk2(ex2(s1[8 + 2 * i] - x1), ex2(s1[9 + 2 * i] - x1)); }
                    oB = MFMA32_16(w0, pa, oB); oB = MFMA32_16(w1, pb, oB);
                }
            }
        }
        lds_wait<4>(lds + C_BX, 8);
        const float gO = *(LDS3 const float*)(lds + X_G + (qs * 64 + kh * 32 + r) * 4);
        {
            LDS3 char* xp = lds + X_MRG + w * 2816;
            const f32x16& s = kh ? oA : oB;
            *(LDS3 f32x4*)(xp + lane * 16) = (f32x4){s[0], s[1], s[2], s[3]};
            *(LDS3 f32x4*)(xp + 1024 + lane * 16) = (f32x4){s[4], s[5], s[6], s[7]};
            if (!h) *(LDS3 f32x4*)(xp + 2048 + r * 16) = (f32x4){s[8], s[9], s[10], s[11]};
            else *(LDS3 f32x2*)(xp + 2560 + r * 8) = (f32x2){s[8], kh ? mA : mB};
            asm volatile("s_waitcnt lgkmcnt(0)" ::: "memory");
            if (lane == 0) __hip_atomic_store((LDS3 unsigned*)(lds + C_MRG + w * 4), 1u, __ATOMIC_RELAXED, __HIP_MEMORY_SCOPE_WORKGROUP);
        }
        lds_wait<MS>(lds + C_MRG + (w ^ 4) * 4, 1);
        bool lastw;
        {
            LDS3 const char* xp = lds + X_MRG + (w ^ 4) * 2816;
            const f32x4 p0 = *(LDS3 const f32x4*)(xp + lane * 16), p1 = *(LDS3 const f32x4*)(xp + 1024 + lane * 16), p2 = *(LDS3 const f32x4*)(xp + 2048 + r * 16);
            const f32x2 lm = *(LDS3 const f32x2*)(xp + 2560 + r * 8);
            const f32x16& s = kh ? oB : oA;
            const float mo = kh ? mB : mA, mp = lm[1];
            const float mx = fmaxf(mo, mp);
            const float fo = (mo == mx) ? 1.f : ex2(mo - mx), fq = (mp == mx) ? 1.f : ex2(mp - mx);
            float o[12];
#pragma unroll
            for (int i = 0; i < 4; ++i) { o[i] = s[i] * fo + p0[i] * fq; o[4 + i] = s[4 + i] * fo + p1[i] * fq; o[8 + i] = s[8 + i] * fo + p2[i] * fq; }
            const float l = upper_half(s[8]) * fo + lm[0] * fq;
            store_o(rs, o, gO / l, q0 + kh * 32 + r, 0, head, h);
            DRAIN();
            unsigned arr = 0;
            if (lane == 0) arr = LDS_CNT_ADD(C_OA);
            lastw = __builtin_amdgcn_readfirstlane(arr) == 7;
            if (lastw && lane == 0) set_flag(ws, WS_F_OA + (qb * 32 + head) * 8, tag);
        }
        STAMP(7);
        {
            const int c16 = lane & 15, g = lane >> 4, tt = w & 3, k0 = kh * 10;
            lds_wait(lds + C_WA, 4);
            LDS3 const char* wp = lds + L_WA + k0 * 1024 + lane * 16;
            u32x4 wf[4][5];
#pragma unroll
            for (int ks = 0; ks < 4; ++ks)
#pragma unroll
                for (int n = 0; n < 5; ++n) wf[ks][n] = *(LDS3 const u32x4*)(wp + (n * 20 + ks) * 1024);
            POLL_ONE(lastw, 2, WS_F_OA + ((mtO >> 2) * 32 + r) * 8, WS_F_OA + ((mtO >> 2) * 32 + r) * 8, C_P4);
            STAMP(9);
            const unsigned ab = WS_AO + ((mtO * 4 + tt) * 40 + k0) * 1024;
            u32x4 af[10];
#pragma unroll
            for (int ks = 0; ks < 6; ++ks) af[ks] = LD16(ab + ks * 1024 + lane * 16);
            if (kh) {
#pragma unroll
                for (int ks = 6; ks < 10; ++ks) LD_TAIL(af[ks], ab + ks * 1024);
            } else {
#pragma unroll
                for (int ks = 6; ks < 10; ++ks) af[ks] = LD16(ab + ks * 1024 + lane * 16);
            }
            f32x4 c[5];
#pragma unroll
            for (int n = 0; n < 5; ++n) c[n] = (f32x4){0.f, 0.f, 0.f, 0.f};
            SBAR();
#pragma unroll
            for (int ks = 0; ks < 10; ++ks) {
#pragma unroll
                for (int n = 0; n < 5; ++n) c[n] = MFMA16_32(wf[ks & 3][n], af[ks], c[n]);
                SBAR();
                if (ks + 4 < 10) {
#pragma unroll
                    for (int n = 0; n < 5; ++n) wf[ks & 3][n] = *(LDS3 const u32x4*)(wp + (n * 20 + ks + 4) * 1024);
                }
                SBAR();
            }
            LDS3 char* xp = lds + X_MRG + tt * 5120 + lane * 16;
            if (w < 4) { *(LDS3 f32x4*)(xp + 3072) = c[3]; *(LDS3 f32x4*)(xp + 4096) = c[4]; }
            else { *(LDS3 f32x4*)(xp) = c[0]; *(LDS3 f32x4*)(xp + 1024) = c[1]; *(LDS3 f32x4*)(xp + 2048) = c[2]; }
            asm volatile("s_waitcnt lgkmcnt(0)" ::: "memory");
            if (lane == 0) __hip_atomic_store((LDS3 unsigned*)(lds + C_FIN + w * 4), 1u, __ATOMIC_RELAXED, __HIP_MEMORY_SCOPE_WORKGROUP);
            lds_wait(lds + C_FIN + (w ^ 4) * 4, 1);
            lds_wait(lds + C_BP + tt * 4, 1);
            u16* op = P.out + (size_t)(mtO * 64 + tt * 16 + c16) * DM + ntO * 80 + g * 4;
#pragma unroll
            for (int n = 0; n < 5; ++n) {
                if ((w < 4) == (n < 3)) {
                    const f32x4 bp = *(LDS3 const f32x4*)(n < 3 ? lds + X_FIN + tt * 3072 + n * 1024 + lane * 16 : lds + 143360 + tt * 2048 + (n - 3) * 1024 + lane * 16);
                    const f32x4 s = c[n] + *(LDS3 const f32x4*)(xp + n * 1024) + bp;
                    *(u32x2*)(op + n * 16) = (u32x2){pk2(s[0], s[1]), pk2(s[2], s[3])};
                }
            }
            if (bid == 0 && mtid == 0) set_flag(ws, WS_EPOCH, tag);
        }
        STAMP(10);
        asm volatile("mega_code_end:");
    }
}

extern "C" void kernel_launch(void* const* d_in, const int* in_sizes, int n_in, void* d_out, int out_size, void* d_ws, size_t ws_size,
                              hipStream_t stream) {
    Params P;
    P.x = (const u16*)d_in[0]; P.wa = (const u16*)d_in[1]; P.woa = (const u16*)d_in[2]; P.wb = (const u16*)d_in[4];
    P.wphi = (const u16*)d_in[5]; P.wob = (const u16*)d_in[6]; P.gw = (const u16*)d_in[7]; P.ws = (char*)d_ws; P.out = (u16*)d_out;
    mega<<<256, 768, 0, stream>>>(P);
}
```

```cpp
#include <hip/hip_runtime.h>
#include <hip/hip_bf16.h>
#include <stdint.h>
#include <type_traits>

typedef __attribute__((ext_vector_type(8))) short bf16x8;
typedef __attribute__((ext_vector_type(4))) short bf16x4;
typedef __attribute__((ext_vector_type(16))) float f32x16;
typedef __attribute__((ext_vector_type(4))) float f32x4;
typedef __attribute__((ext_vector_type(2))) float f32x2;
typedef __attribute__((ext_vector_type(4))) unsigned u32x4;
typedef __attribute__((ext_vector_type(2))) unsigned u32x2;
typedef __attribute__((ext_vector_type(2))) __bf16 bf2_t;
typedef unsigned short u16;
typedef unsigned long long u64;
#define LDS3 __attribute__((address_space(3)))
typedef __attribute__((address_space(1))) u64 gu64;
#define RLX_AGENT __ATOMIC_RELAXED, __HIP_MEMORY_SCOPE_AGENT

#define PRIO_MODE 3
#define WARM_MODE 3
#define GA_PUB 11
#define PHI_ROUND 0
#define HS 8
#define MS 2
#define ROT_MODE 3
#define TLB_STRIDE 196608
#define PRE_SLEEP 2
constexpr int SEQ = 2048, DM = 640, NH = 32;
constexpr float LOG2E = 1.4426950408889634f;
constexpr float QSCALE = 0.22360679774997896f * LOG2E;

constexpr unsigned WS_EPOCH = 0;
constexpr unsigned WS_F_G   = 256;
constexpr unsigned WS_F_SD  = WS_F_G + 4096;
constexpr unsigned WS_F_KV  = WS_F_SD + 256;
constexpr unsigned WS_F_OB  = WS_F_KV + 2048;
constexpr unsigned WS_F_OA  = WS_F_OB + 2048;
constexpr unsigned WS_G     = 16384;
constexpr unsigned WS_Q     = 32768;
constexpr unsigned WS_KV    = WS_Q + 2 * NH * 81920;
constexpr unsigned WS_KVP   = WS_KV + 2 * NH * 163840;
constexpr unsigned WS_AO    = WS_KVP + NH * 8 * 4096;
constexpr unsigned WS_WT    = WS_AO + SEQ * 1280 * 2;
constexpr unsigned WS_END   = WS_WT + 1280 * 640 * 2;

#define STAMP(i)

__device__ __forceinline__ unsigned pk2(float a, float b) { f32x2 v = {a, b}; bf2_t r = __builtin_convertvector(v, bf2_t); return __builtin_bit_cast(unsigned, r); }
__device__ __forceinline__ float bf_lo(unsigned u) { return __uint_as_float(u << 16); }
__device__ __forceinline__ float bf_hi(unsigned u) { return __uint_as_float(u & 0xffff0000u); }
__device__ __forceinline__ float ex2(float x) { return __builtin_amdgcn_exp2f(x); }
#define MFMA32_16(a, b, c) __builtin_amdgcn_mfma_f32_32x32x16_bf16(__builtin_bit_cast(bf16x8, a), __builtin_bit_cast(bf16x8, b), c, 0, 0, 0)
#define MFMA32_8(a, b, c) __builtin_amdgcn_mfma_f32_32x32x8bf16_1k(__builtin_bit_cast(bf16x4, a), __builtin_bit_cast(bf16x4, b), c, 0, 0, 0)
#define MFMA16_32(a, b, c) __builtin_amdgcn_mfma_f32_16x16x32_bf16(__builtin_bit_cast(bf16x8, a), __builtin_bit_cast(bf16x8, b), c, 0, 0, 0)

__device__ __forceinline__ void glds16(unsigned voff, const void* sbase, unsigned lds_base) {
    unsigned sv;
    asm volatile("s_mov_b32 %0, m0\n\ts_mov_b32 m0, %3\n\ts_nop 0\n\tglobal_load_lds_dwordx4 %1, %2\n\ts_mov_b32 m0, %0"
                 : "=&s"(sv) : "v"(voff), "s"(sbase), "s"(__builtin_amdgcn_readfirstlane(lds_base)) : "memory");
}
__device__ __forceinline__ void glds16s(unsigned voff, const void* sbase, unsigned lds_base) {
    unsigned sv;
    asm volatile("s_mov_b32 %0, m0\n\ts_mov_b32 m0, %3\n\ts_nop 0\n\tglobal_load_lds_dwordx4 %1, %2 sc1\n\ts_mov_b32 m0, %0"
                 : "=&s"(sv) : "v"(voff), "s"(sbase), "s"(__builtin_amdgcn_readfirstlane(lds_base)) : "memory");
}
__device__ __forceinline__ void glds4s(unsigned voff, const void* sbase, unsigned lds_base) {
    unsigned sv;
    asm volatile("s_mov_b32 %0, m0\n\ts_mov_b32 m0, %3\n\ts_nop 0\n\tglobal_load_lds_dword %1, %2 sc1\n\ts_mov_b32 m0, %0"
                 : "=&s"(sv) : "v"(voff), "s"(sbase), "s"(__builtin_amdgcn_readfirstlane(lds_base)) : "memory");
}
__device__ __forceinline__ void glds16x4(const unsigned (&vo)[4], const void* sbase, unsigned lds_base) {
    unsigned sv;
    asm volatile("s_mov_b32 %0, m0\n\ts_mov_b32 m0, %6\n\ts_nop 0\n\tglobal_load_lds_dwordx4 %1, %5\n\tglobal_load_lds_dwordx4 %2, %5 offset:1024\n\t"
                 "global_load_lds_dwordx4 %3, %5 offset:2048\n\tglobal_load_lds_dwordx4 %4, %5 offset:3072\n\ts_mov_b32 m0, %0"
                 : "=&s"(sv) : "v"(vo[0]), "v"(vo[1]), "v"(vo[2]), "v"(vo[3]), "s"(sbase), "s"(lds_base) : "memory");
}
typedef short v4i16_t __attribute__((ext_vector_type(4)));
__device__ __forceinline__ u32x2 trread(LDS3 const char* p) {
    return __builtin_bit_cast(u32x2, __builtin_amdgcn_ds_read_tr16_b64_v4i16((LDS3 v4i16_t*)p));
}
#define WAIT_BAR(N) asm volatile("s_waitcnt vmcnt(" #N ") lgkmcnt(0)\n\ts_barrier" ::: "memory")
#define DRAIN() asm volatile("s_waitcnt vmcnt(0)" ::: "memory")
#define SBAR() __builtin_amdgcn_sched_barrier(0)
#define PIN(x) asm volatile("" : "+v"(x))

__device__ __forceinline__ float xhalf_max(float x) {
    auto r = __builtin_amdgcn_permlane32_swap(__float_as_uint(x), __float_as_uint(x), false, false);
    return fmaxf(__uint_as_float(r[0]), __uint_as_float(r[1]));
}
__device__ __forceinline__ float xhalf_sum(float x) {
    auto r = __builtin_amdgcn_permlane32_swap(__float_as_uint(x), __float_as_uint(x), false, false);
    return __uint_as_float(r[0]) + __uint_as_float(r[1]);
}
__device__ __forceinline__ float upper_half(float x) {
    auto r = __builtin_amdgcn_permlane32_swap(__float_as_uint(x), __float_as_uint(x), false, false);
    return __uint_as_float(r[1]);
}

typedef __amdgpu_buffer_rsrc_t rsrc_t;
#define ST8(v, off)  __builtin_amdgcn_raw_buffer_store_b64(v, rs, (int)(off), 0, 16)
#define ST16(v, off) __builtin_amdgcn_raw_buffer_store_b128(v, rs, (int)(off), 0, 16)
#define ST4(v, off)  __builtin_amdgcn_raw_buffer_store_b32(v, rs, (int)(off), 0, 16)
#define LD16(off) __builtin_amdgcn_raw_buffer_load_b128(rs, (int)(off), 0, 16)
#define LD4(off)  __builtin_amdgcn_raw_buffer_load_b32(rs, (int)(off), 0, 16)

template <int SLP = 2>
__device__ __forceinline__ void wait_flags(const char* ws, unsigned off0, unsigned off1, u64 tag) {
    gu64* p0 = (gu64*)(ws + off0); gu64* p1 = (gu64*)(ws + off1);
#pragma unroll 1
    for (unsigned spins = 0; spins < (1u << 16); ++spins) {
        const u64 a = __hip_atomic_load(p0, RLX_AGENT), b = __hip_atomic_load(p1, RLX_AGENT);
        if (__all(a == tag && b == tag)) break;
        __builtin_amdgcn_s_sleep(SLP);
    }
    __builtin_amdgcn_fence(__ATOMIC_ACQUIRE, "wavefront");
}
__device__ __forceinline__ void set_flag(char* ws, unsigned off, u64 tag) { __hip_atomic_store((gu64*)(ws + off), tag, RLX_AGENT); }

struct Params { const u16* x; const u16* wa; const u16* wb; const u16* wphi; const u16* woa; const u16* wob; const u16* gw; char* ws; u16* out; };

__device__ __forceinline__ void feature_map(const u32x4& pm, unsigned pt, const u32x4& tm, unsigned tt, int h, u32x4& fp, u32x4& fn, u32x4& ft) {
    const f32x16 z16 = {};
    const u32x2 a8 = {pt, 0u}, b8 = {tt, 0u};
    f32x16 s = MFMA32_16(pm, tm, z16); s = MFMA32_8(a8, b8, s);
    f32x2 v[6];
#pragma unroll
    for (int i = 0; i < 6; ++i) {
        v[i] = (f32x2){s[2 * i], s[2 * i + 1]};
    }
    float mx = fmaxf(v[0][0], v[0][1]), mn = fminf(v[0][0], v[0][1]);
#pragma unroll
    for (int i = 1; i < 6; ++i) { mx = fmaxf(fmaxf(mx, v[i][0]), v[i][1]); mn = fminf(fminf(mn, v[i][0]), v[i][1]); }
    mx = xhalf_max(mx) * -LOG2E; mn = xhalf_max(-mn) * -LOG2E;
    f32x2 ep[6], en[6];
#pragma unroll
    for (int i = 0; i < 6; ++i) {
        const f32x2 a = v[i] * LOG2E + mx, b = v[i] * -LOG2E + mn;
        ep[i] = (f32x2){ex2(a[0]), ex2(a[1])}; en[i] = (f32x2){ex2(b[0]), ex2(b[1])};
    }
    const float tw = h ? 0.f : 1.f;
    const f32x2 s2p = ((ep[0] + ep[1]) + (ep[2] + ep[3])) + (ep[4] + ep[5]) * tw, s2n = ((en[0] + en[1]) + (en[2] + en[3])) + (en[4] + en[5]) * tw;
    const float sp = __builtin_amdgcn_rcpf(xhalf_sum(s2p[0] + s2p[1])), sn = __builtin_amdgcn_rcpf(xhalf_sum(s2n[0] + s2n[1]));
    const float tp = sp * tw, tn = sn * tw;
#pragma unroll
    for (int i = 0; i < 4; ++i) { const f32x2 a = ep[i] * sp, b = en[i] * sn; fp[i] = pk2(a[0], a[1]); fn[i] = pk2(b[0], b[1]); }
    const f32x2 c0 = ep[4] * tp, c1 = ep[5] * tp, c2 = en[4] * tn, c3 = en[5] * tn;
    ft = (u32x4){pk2(c0[0], c0[1]), pk2(c1[0], c1[1]), pk2(c2[0], c2[1]), pk2(c3[0], c3[1])};
}

__device__ __forceinline__ void store_o(rsrc_t rs, const float (&o)[12], float sc, int tok, int half, int head, int h) {
    const auto s0 = __builtin_amdgcn_permlane32_swap(pk2(o[0] * sc, o[1] * sc), pk2(o[4] * sc, o[5] * sc), false, false);
    const auto s1 = __builtin_amdgcn_permlane32_swap(pk2(o[2] * sc, o[3] * sc), pk2(o[6] * sc, o[7] * sc), false, false);
    const unsigned tb = WS_AO + ((tok >> 4) * 40 + half * 20) * 1024;
    const u32x4 mv = {s0[0], s1[0], s0[1], s1[1]};
    ST16(mv, tb + (head >> 1) * 1024 + (((head & 1) * 2 + h) * 16 + (tok & 15)) * 16);
    const u32x2 tv = {pk2(o[8] * sc, o[9] * sc), pk2(o[10] * sc, o[11] * sc)};
    if (!h) ST8(tv, tb + (16 + (head >> 3)) * 1024 + (head & 7) * 128 + (tok & 15) * 8);
}
#define LD8(off)  __builtin_amdgcn_raw_buffer_load_b64(rs, (int)(off), 0, 16)
#define LD_TAIL(dst, base) do { const u32x2 a_ = LD8((base) + (lane >> 4) * 256 + (lane & 15) * 8), b_ = LD8((base) + (lane >> 4) * 256 + 128 + (lane & 15) * 8); dst = (u32x4){a_[0], a_[1], b_[0], b_[1]}; } while (0)

#define DSR128(dst, addr, off) asm volatile("ds_read_b128 %0, %1 offset:%2" : "=v"(dst) : "v"(addr), "i"(off))
#define DSR32(dst, addr, off) asm volatile("ds_read_b32 %0, %1 offset:%2" : "=v"(dst) : "v"(addr), "i"(off))
#define DSR128M2(d0, a0, o0, d1, a1, o1) asm volatile("s_mov_b64 exec, %6\n\tds_read_b128 %0, %2 offset:%3\n\tds_read_b128 %1, %4 offset:%5\n\ts_mov_b64 exec, -1" \
                                                      : "+v"(d0), "+v"(d1) : "v"(a0), "i"(o0), "v"(a1), "i"(o1), "s"(vmask))
#define LWAIT(n) do { asm volatile("s_waitcnt lgkmcnt(" #n ")" ::: "memory"); SBAR(); } while (0)

#define BAR() asm volatile("s_barrier" ::: "memory")
#define LDS_SET(off, v) __hip_atomic_store((LDS3 unsigned*)(lds + (off)), (unsigned)(v), __ATOMIC_RELAXED, __HIP_MEMORY_SCOPE_WORKGROUP)
#define LDS_CNT_ADD(off) __hip_atomic_fetch_add((LDS3 unsigned*)(lds + (off)), 1u, __ATOMIC_RELAXED, __HIP_MEMORY_SCOPE_WORKGROUP)

template <int SLP = 1>
__device__ __forceinline__ void lds_wait(LDS3 char* p, unsigned want) {
#pragma unroll 1
    for (unsigned spins = 0; spins < (1u << 20); ++spins) {
        if ((unsigned)__builtin_amdgcn_readfirstlane(__hip_atomic_load((LDS3 unsigned*)p, __ATOMIC_RELAXED, __HIP_MEMORY_SCOPE_WORKGROUP)) >= want) break;
        __builtin_amdgcn_s_sleep(SLP);
    }
    __builtin_amdgcn_fence(__ATOMIC_ACQUIRE, "wavefront");
}

template <int SLP>
__device__ __forceinline__ void prog_wait(LDS3 char* p, int lane, unsigned want) {
#pragma unroll 1
    for (unsigned spins = 0; spins < (1u << 20); ++spins) {
        if (__all(__hip_atomic_load((LDS3 unsigned*)p + (lane & 7), __ATOMIC_RELAXED, __HIP_MEMORY_SCOPE_WORKGROUP) >= want)) break;
        __builtin_amdgcn_s_sleep(SLP);
    }
    __builtin_amdgcn_fence(__ATOMIC_ACQUIRE, "wavefront");
}
#define POLL_ONE(cond, SLP, o0, o1, word) POLL_ONE2(cond, SLP, 2, o0, o1, word)
#define POLL_ONE2(cond, SLP, SLP2, o0, o1, word) do { if (cond) { wait_flags<SLP>(ws, o0, o1, tag); \
        if (lane == 0) __hip_atomic_store((LDS3 unsigned*)(lds + (word)), 1u, __ATOMIC_RELAXED, __HIP_MEMORY_SCOPE_WORKGROUP); } else lds_wait<SLP2>(lds + (word), 1); } while (0)
constexpr int C_KV = 0, C_KVF = 4, C_OB = 8, C_WA = 16, C_OA = 20, C_BX = 28, C_MRG = 32  , C_FIN = 64  , C_BP = 96  ;
constexpr int C_P1 = 112, C_P2 = 116, C_P3 = 120, C_P4 = 124, C_PR = 128  , C_KVL = 160, C_P5 = 164, C_GB = 168;
constexpr int G_W = 0, G_Q = 176, G_P = 180, G_ST = 184, G_RD = 188;
constexpr int X_G = 256;
constexpr int X_KVF = 1280, X_SLOT = 4096;
constexpr int X_MRG = 1280;
constexpr int L_WA = 24576;
constexpr int L_Q = 81920;
constexpr int X_FIN = 126976;

__global__ __launch_bounds__(768) void mega(Params P) {
    __shared__ __attribute__((aligned(16))) char smem[163840];
    LDS3 char* lds = (LDS3 char*)smem;
    char* ws = P.ws;
    const int tid = threadIdx.x, lane = tid & 63, r = lane & 31, h = lane >> 5;
    const int hw = __builtin_amdgcn_readfirstlane(tid >> 6);
    const bool helper = hw < 4;
    const int w = hw - 4, mtid = tid - 256;
    const int bid = blockIdx.x;
    const unsigned lds0 = (unsigned)(uintptr_t)smem;
    const rsrc_t rs = __builtin_amdgcn_make_buffer_rsrc(ws, 0, WS_END, 0x00020000);
    STAMP(0);
    u64 tag = 0;
#define GET_TAG() do { asm volatile("s_load_dwordx2 %0, %1, 0x0\n\ts_waitcnt lgkmcnt(0)" : "=s"(tag) : "s"(ws) : "memory"); tag += 1; } while (0)
    const int head = (bid & 7) * 4 + ((bid >> 3) & 3), qb = bid >> 5;
    const int nlo = (head * 20) >> 7, nhi = (head * 20 + 19) >> 7;
    const int mtO = (bid & 7) * 4 + ((bid >> 3) & 3), ntO = bid >> 5;
    const int lin = (bid & 7) * 30 + (bid >> 3), lh = lin >= 120, lrem = lin - 120 * lh;
    const int mt = lh * 8 + (lrem & 7), nt = lrem >> 3;
    const unsigned fG = WS_F_G + (nt * 16 + mt) * 8;
    const unsigned one = (r == 20) ? 0x3f803f80u : 0u;
    const f32x16 z16 = {};

    if (helper) {
        const unsigned fA0 = WS_F_G + (((lane < 32 ? 5 : 10) + ((lane & 16) ? nhi : nlo)) * 16 + (lane & 15)) * 8;
        const unsigned fA1 = WS_F_G + (((lane & 2) ? nhi : nlo) * 16 + qb * 2 + (lane & 1)) * 8;
        __builtin_amdgcn_s_setprio(3);
        const char* kvb = ws + WS_KV + head * 163840;
        auto kv_issue = [&](int grp, int t0 = 0, int t1 = 8) {
#pragma unroll
            for (int t = t0; t < t1; ++t) { int pc = grp * 32 + hw + 4 * t; if (pc == 0) pc = 4; glds16s(lane * 16, kvb + pc * 1024, lds0 + pc * 1024); }
        };
        auto q_issue = [&]() {
            const char* qbase = ws + WS_Q + head * 81920 + qb * 10240;
#pragma unroll
            for (int t = 0; t < 3; ++t) { const int pc = hw + 4 * t < 10 ? hw + 4 * t : 10; glds16s(lane * 16, pc < 10 ? qbase + pc * 1024 : kvb, lds0 + L_Q + pc * 1024); }
        };
        if (bid < 240) {
            const int rot = ((nt + mt) % 3) * 3;
            unsigned xo[4], wo[4];
#pragma unroll
            for (int i = 0; i < 4; ++i) {
                xo[i] = (mt * 128 + 32 * hw + 8 * i + (lane >> 3)) * 1280 + (((lane & 7) ^ ((4 * i + (lane >> 4)) & 7)) << 4) - i * 1024;
                wo[i] = (16 * hw + 4 * i + (lane >> 4)) * 3840 + nt * 256 + (((lane & 15) ^ (((lane >> 4) << 2) | i)) << 4) - i * 1024;
            }
            const unsigned dX = lds0 + 32768 + hw * 4096;
            int kt = rot;
#pragma unroll
            for (int n = 0; n < 20; ++n) {
                const char* xb = (const char*)P.x + kt * 128;
                const char* wb = (const char*)(n >= 10 ? P.wb : P.wa) + (size_t)kt * (64 * 3840);
                asm volatile("" : "+s"(xb), "+s"(wb));
                if (n == 2) { if (hw == 0) *(LDS3 unsigned*)(lds + lane * 4) = 0u; GET_TAG(); WAIT_BAR(8); }
                else if (n == 11 || n == 15) { WAIT_BAR(12); }
                else if (n == 3 || (n >= 12 && n <= 14)) { WAIT_BAR(8); }
                else if (n >= 5) { WAIT_BAR(16); }
                glds16x4(wo, wb, dX + 16384 + (n & 3) * 32768);
                if (n < 10 || n >= 14) glds16x4(xo, xb, dX + ((n >= 10 ? 20 - n : n + 1) & 3) * 32768);
                if (n < 9) kt = kt == 9 ? 0 : kt + 1; else if (n > 9) kt = kt == 0 ? 9 : kt - 1;
            }
            WAIT_BAR(16);
            WAIT_BAR(0);
        } else {
            if (hw == 0) *(LDS3 unsigned*)(lds + lane * 4) = 0u;
            GET_TAG();
            WAIT_BAR(63);
            if (hw == 0) {
#pragma unroll 1
                for (int t = 0; t < PRE_SLEEP; ++t) __builtin_amdgcn_s_sleep(127);
            }
        }
        POLL_ONE(hw == 0, 6, fA0, fA1, G_P);
        kv_issue(0); q_issue(); kv_issue(1, 0, 2);
        DRAIN();
        if (lane == 0) LDS_CNT_ADD(G_Q);
        lds_wait<2>(lds + G_RD, 8);
        kv_issue(1, 2, 8); kv_issue(2); kv_issue(3); kv_issue(4);
        asm volatile("s_waitcnt vmcnt(24)" ::: "memory"); if (lane == 0) LDS_CNT_ADD(C_KVL);
        asm volatile("s_waitcnt vmcnt(16)" ::: "memory"); if (lane == 0) LDS_CNT_ADD(C_KVL + 12);
        STAMP(3);
        u32x4 phm; unsigned pht;
        {
            const u16* wp = P.wphi + head * 400 + (r < 20 ? r : (r & 3));
            unsigned e[10];
#pragma unroll
            for (int j = 0; j < 8; ++j) e[j] = wp[(8 * h + j + 4 * (head & 1)) * 20];
            e[8] = wp[((head & 1 ? 0 : 16) + 2 * h) * 20]; e[9] = wp[((head & 1 ? 1 : 17) + 2 * h) * 20];
            const int bu = bid < 160 ? bid : 0, hf = bu >= 80, k0 = (bu - 80 * hf) * 8, hd = k0 < 512 ? k0 >> 4 : (k0 - 512) >> 2;
            const int row = k0 < 512 ? 20 * hd + (k0 & 8) + 4 * (hd & 1) : 20 * hd + 16;
            const u16* wsrc = (hf ? P.wob : P.woa) + (size_t)row * DM;
            unsigned f[3][8];
            if (bid < 160) {
#pragma unroll
                for (int q = 0; q < 3; ++q) {
                    const int n = tid + 256 * q < 640 ? tid + 256 * q : 639;
#pragma unroll
                    for (int j = 0; j < 8; ++j) f[q][j] = wsrc[j * DM + n];
                }
            }
            DRAIN();
            if (lane == 0) LDS_CNT_ADD(C_WA + 8);
            phm = (u32x4){e[0] | (e[1] << 16), e[2] | (e[3] << 16), e[4] | (e[5] << 16), e[6] | (e[7] << 16)}; pht = e[8] | (e[9] << 16);
            if (bid < 160) {
#pragma unroll
                for (int q = 0; q < 3; ++q) {
                    const int n = tid + 256 * q;
                    const u32x4 v = {f[q][0] | (f[q][1] << 16), f[q][2] | (f[q][3] << 16), f[q][4] | (f[q][5] << 16), f[q][6] | (f[q][7] << 16)};
                    if (n < 640) ST16(v, WS_WT + (((n >> 4) * 40 + hf * 20 + (k0 >> 5)) * 64 + ((k0 >> 3) & 3) * 16 + (n & 15)) * 16);
                }
            }
        }
        const int tokS = qb * 256 + hw * 64, m7 = tokS >> 7, t4 = (tokS >> 5) & 3;
        {
            const unsigned pt3 = (lane >> 1) & 3;
            const unsigned tb = WS_F_G + 2048 + ((((lane & 1) ? nhi : nlo) + (pt3 < 3 ? pt3 : 0) * 5) * 16 + qb * 2 + ((lane >> 3) & 1)) * 8;
            POLL_ONE2(hw == 0, 4, HS, tb, WS_F_SD + (qb * 2 + (lane & 1)) * 8, C_P1);
        }
        const unsigned om = t4 * 1024 + lane * 16, ot = 4096 + (t4 * 32 + r) * 8 + 4 * h;
        u32x4 qf[2][3]; float gS[2];
        {
            const unsigned kb = WS_KV + (NH + head) * 163840 + (2 * (m7 & 7) + (m7 >> 3)) * 10240, qbb = WS_Q + (NH + head) * 81920 + m7 * 5120;
            const unsigned ov = 5120 + t4 * 1280 + h * 16 + (r < 20 ? r : 19) * 32;
            u32x4 e0[2], w0[2], w1[2], eq[2]; unsigned et[2], eqt[2];
#pragma unroll
            for (int i = 0; i < 2; ++i) {
                e0[i] = LD16(kb + om + i * 1024); et[i] = LD4(kb + ot + i * 256); w0[i] = LD16(kb + ov + i * 1280); w1[i] = LD16(kb + ov + i * 1280 + 640);
                eq[i] = LD16(qbb + om + i * 1024); eqt[i] = LD4(qbb + ot + i * 256); gS[i] = __uint_as_float(LD4(WS_G + (tokS + i * 32 + r) * 4));
            }
            u32x4 i0, i1;
#pragma unroll
            for (int j = 0; j < 4; ++j) {
                const int e = r - 8 * h - 2 * j;
                i0[j] = e == 0 ? 0x3f80u : (e == 1 ? 0x3f800000u : 0u);
                i1[j] = e == 16 ? 0x3f80u : (e == 17 ? 0x3f800000u : 0u);
            }
            f32x16 k0 = {}, k1 = {};
#pragma unroll
            for (int i = 0; i < 2; ++i) {
                if (r >= 20) { w0[i] = (u32x4){one, one, one, one}; w1[i] = w0[i]; }
                u32x4 fp, fn, ft;
                feature_map(phm, pht, e0[i], et[i], h, fp, fn, ft);
                f32x16 T0 = MFMA32_16(fp, i0, z16); T0 = MFMA32_16(fn, i1, T0);
                f32x16 T1 = MFMA32_16(ft, i0, z16);
                u32x4 b00, b01, b10, b11;
#pragma unroll
                for (int j = 0; j < 4; ++j) {
                    b00[j] = pk2(T0[2 * j], T0[2 * j + 1]); b01[j] = pk2(T0[8 + 2 * j], T0[9 + 2 * j]);
                    b10[j] = pk2(T1[2 * j], T1[2 * j + 1]); b11[j] = pk2(T1[8 + 2 * j], T1[9 + 2 * j]);
                }
                k0 = MFMA32_16(w0[i], b00, k0); k0 = MFMA32_16(w1[i], b01, k0);
                k1 = MFMA32_16(w0[i], b10, k1); k1 = MFMA32_16(w1[i], b11, k1);
            }
            prog_wait<8>(lds + C_PR, lane, 1);
            *(LDS3 float*)(lds + X_G + (hw * 64 + lane) * 4) = h ? gS[1] : gS[0];
            if (hw * 64 + lane < 144) *(LDS3 u32x4*)(lds + X_KVF + (hw * 64 + lane) * 16) = (u32x4){0u, 0u, 0u, 0u};
            LDS3 char* sp = lds + X_SLOT + hw * 4096;
#pragma unroll
            for (int gq = 0; gq < 3; ++gq) {
                *(LDS3 f32x4*)(sp + gq * 1024 + lane * 16) = (f32x4){k0[4 * gq], k0[4 * gq + 1], k0[4 * gq + 2], k0[4 * gq + 3]};
                if (r < 8) *(LDS3 f32x4*)(sp + 3072 + (gq * 16 + h * 8 + r) * 16) = (f32x4){k1[4 * gq], k1[4 * gq + 1], k1[4 * gq + 2], k1[4 * gq + 3]};
            }
            asm volatile("s_waitcnt vmcnt(0) lgkmcnt(0)" ::: "memory");
            unsigned arr = 0;
            if (lane == 0) arr = LDS_CNT_ADD(C_KV);
            if (__builtin_amdgcn_readfirstlane(arr) == 3) {
                __builtin_amdgcn_fence(__ATOMIC_ACQUIRE, "wavefront");
#pragma unroll
                for (int i = 0; i < 4; ++i) {
                    const int qd = lane + 64 * i;
                    LDS3 const char* s0 = lds + X_SLOT + qd * 16;
                    const f32x4 s = (*(LDS3 const f32x4*)s0 + *(LDS3 const f32x4*)(s0 + 4096)) + (*(LDS3 const f32x4*)(s0 + 8192) + *(LDS3 const f32x4*)(s0 + 12288));
                    if (qd < 240) ST16(__builtin_bit_cast(u32x4, s), WS_KVP + (head * 8 + qb) * 4096 + qd * 16);
                }
                DRAIN();
                if (lane == 0) set_flag(ws, WS_F_KV + (head * 8 + qb) * 8, tag);
            }
#pragma unroll
            for (int i = 0; i < 2; ++i) feature_map(phm, pht, eq[i], eqt[i], h, qf[i][0], qf[i][1], qf[i][2]);
        }
        STAMP(4);
        POLL_ONE2(hw == 0, 4, HS, WS_F_KV + (head * 8 + (lane & 7)) * 8, WS_F_KV + (head * 8 + (lane & 7)) * 8, C_P2);
        {
            const int qd = hw * 60 + (lane < 60 ? lane : 59);
            f32x4 pp[8];
#pragma unroll
            for (int p = 0; p < 8; ++p) pp[p] = __builtin_bit_cast(f32x4, LD16(WS_KVP + (head * 8 + p) * 4096 + qd * 16));
            const f32x4 s4 = ((pp[0] + pp[1]) + (pp[2] + pp[3])) + ((pp[4] + pp[5]) + (pp[6] + pp[7]));
            const int gq = qd < 192 ? qd >> 6 : (qd - 192) >> 4, hs = qd < 192 ? (qd >> 5) & 1 : (qd >> 3) & 1, sg = qd < 192 ? qd & 31 : 32 + (qd & 7);
            const unsigned a = pk2(s4[0], s4[1]), b = pk2(s4[2], s4[3]);
            LDS3 u16* kp = (LDS3 u16*)(lds + X_KVF + (8 * gq + 4 * hs) * 96 + sg * 2);
            kp[0] = (u16)a; kp[48] = (u16)(a >> 16); kp[96] = (u16)b; kp[144] = (u16)(b >> 16);
            asm volatile("s_waitcnt lgkmcnt(0)" ::: "memory");
            if (lane == 0) LDS_CNT_ADD(C_KVF);
            lds_wait<2>(lds + C_KVF, 4);
        }
        {
            LDS3 const char* kp = lds + X_KVF + (r < 24 ? r : 23) * 96 + h * 16;
            const u32x4 a0 = *(LDS3 const u32x4*)kp, a1 = *(LDS3 const u32x4*)(kp + 32), a2 = *(LDS3 const u32x4*)(kp + 64);
#pragma unroll
            for (int i = 0; i < 2; ++i) {
                f32x16 d = MFMA32_16(a0, qf[i][0], z16); d = MFMA32_16(a1, qf[i][1], d); d = MFMA32_16(a2, qf[i][2], d);
                const float den = upper_half(d[8]) + 1e-6f;
                float o[12];
#pragma unroll
                for (int j = 0; j < 12; ++j) o[j] = d[j];
                store_o(rs, o, (1.f - gS[i]) / den, tokS + i * 32 + r, 1, head, h);
            }
            DRAIN();
            unsigned arr = 0;
            if (lane == 0) arr = LDS_CNT_ADD(C_OB);
            if (__builtin_amdgcn_readfirstlane(arr) == 3 && lane == 0) set_flag(ws, WS_F_OB + (qb * 32 + head) * 8, tag);
        }
        STAMP(5);
        f32x4 c[5];
#pragma unroll
        for (int n = 0; n < 5; ++n) c[n] = (f32x4){0.f, 0.f, 0.f, 0.f};
        const char* wsrc = ws + WS_WT + (size_t)ntO * 5 * 40960;
        {
            if (hw == 0) wait_flags<8>(ws, WS_F_KV + lane * 8, WS_F_KV + 512 + lane * 8, tag);
            POLL_ONE2(hw == 0, 8, HS, WS_F_KV + 1024 + lane * 8, WS_F_KV + 1536 + lane * 8, C_P5);
#pragma unroll 1
            for (int t = 0; t < 5; ++t) {
                if (t == 0) prog_wait<2 * HS>(lds + C_PR, lane, 4); else if (t == 3) prog_wait<2 * HS>(lds + C_PR, lane, 5);
                const int ks = hw + 4 * t < 19 ? hw + 4 * t : 18;
#pragma unroll
                for (int n = 0; n < 5; ++n) glds16s(lane * 16, wsrc + (n * 40 + 20 + ks) * 1024, lds0 + 4096 + (ks * 5 + n) * 1024);
            }
            POLL_ONE2(hw == 0, 4, HS, WS_F_OB + ((mtO >> 2) * 32 + r) * 8, WS_F_OB + ((mtO >> 2) * 32 + r) * 8, C_P3);
            const unsigned ab = WS_AO + ((mtO * 4 + hw) * 40 + 20) * 1024;
            u32x4 af[20];
#pragma unroll
            for (int ks = 0; ks < 16; ++ks) af[ks] = LD16(ab + ks * 1024 + lane * 16);
#pragma unroll
            for (int ks = 16; ks < 20; ++ks) LD_TAIL(af[ks], ab + ks * 1024);
            asm volatile("s_waitcnt vmcnt(20)" ::: "memory");
            if (lane == 0) LDS_CNT_ADD(C_BX);
            lds_wait<2>(lds + C_BX, 4);
            u32x4 wf[3][5], wl[5];
            LDS3 const char* wp = lds + 4096 + lane * 16;
#pragma unroll
            for (int ks = 0; ks < 2; ++ks)
#pragma unroll
                for (int n = 0; n < 5; ++n) wf[ks][n] = *(LDS3 const u32x4*)(wp + (ks * 5 + n) * 1024);
#pragma unroll
            for (int ks = 0; ks < 20; ++ks) {
                if (ks + 2 < 19) {
#pragma unroll
                    for (int n = 0; n < 5; ++n) wf[(ks + 2) % 3][n] = *(LDS3 const u32x4*)(wp + ((ks + 2) * 5 + n) * 1024);
                }
                if (ks == 8) {
#pragma unroll
                    for (int n = 0; n < 5; ++n) wl[n] = LD16(WS_WT + ((ntO * 5 + n) * 40 + 39) * 1024 + lane * 16);
                }
                SBAR();
#pragma unroll
                for (int n = 0; n < 5; ++n) c[n] = MFMA16_32(ks < 19 ? wf[ks % 3][n] : wl[n], af[ks], c[n]);
                SBAR();
            }
            if (lane == 0) LDS_CNT_ADD(C_BX);
            lds_wait<2>(lds + C_BX, 8);
        }
        STAMP(6);
        prog_wait<2 * HS>(lds + C_PR, lane, 7);
#pragma unroll 1
        for (int n = 0; n < 5; ++n)
#pragma unroll
            for (int t = 0; t < 5; ++t) { const int ks = hw + 4 * t; glds16s(lane * 16, wsrc + (n * 40 + ks) * 1024, lds0 + L_WA + (n * 20 + ks) * 1024); }
#pragma unroll
        for (int n = 0; n < 3; ++n) *(LDS3 f32x4*)(lds + X_FIN + hw * 3072 + n * 1024 + lane * 16) = c[n];
        DRAIN();
        if (lane == 0) LDS_CNT_ADD(C_WA);
        prog_wait<2 * HS>(lds + C_PR, lane, 8);
#pragma unroll
        for (int n = 3; n < 5; ++n) *(LDS3 f32x4*)(lds + 143360 + hw * 2048 + (n - 3) * 1024 + lane * 16) = c[n];
        asm volatile("s_waitcnt lgkmcnt(0)" ::: "memory");
        if (lane == 0) __hip_atomic_store((LDS3 unsigned*)(lds + C_BP + hw * 4), 1u, __ATOMIC_RELAXED, __HIP_MEMORY_SCOPE_WORKGROUP);
        STAMP(7);
        asm volatile("mega_code_end2:");
    } else {
        const int qs = w & 3, kh = w >> 2;
        const int q0 = qb * 256 + qs * 64;
        {
            unsigned sink;
            if (w == 0 && (WARM_MODE & 1)) {
                const char* pc0; int len, len2;
                asm volatile("s_getpc_b64 %0\nmega_code_mark:\n\ts_mov_b32 %1, mega_code_end-mega_code_mark\n\ts_mov_b32 %2, mega_code_end2-mega_code_mark" : "=s"(pc0), "=s"(len), "=s"(len2));
                len = len > len2 ? len : len2;
                const int o = ((bid >> 3) * 64 + lane) * 64;
                if (o + 4 <= len) asm volatile("global_load_dword %0, %1, %2" : "=v"(sink) : "v"(o), "s"(pc0) : "memory");
            } else if (w >= 1 && w <= 2 && (WARM_MODE & 2)) {
                const unsigned o = ((w - 1) * 64 + lane) * TLB_STRIDE + 20000;
                if (o < WS_END) asm volatile("global_load_dword %0, %1, %2 sc1" : "=v"(sink) : "v"(o), "s"(ws) : "memory");
            } else if (w == 3 && (WARM_MODE & 2)) {
                const char* p = lane < 16 ? (const char*)P.out + lane * 163840 : (lane < 20 ? (const char*)P.woa + (lane - 16) * 204800 : (lane < 24 ? (const char*)P.wob + (lane - 20) * 204800 : (lane < 36 ? (const char*)P.wb + (lane - 24) * 204800 : (const char*)P.wphi)));
                if (lane < 37) asm volatile("global_load_dword %0, %1, off sc1" : "=v"(sink) : "v"(p) : "memory");
            }
            asm volatile("s_waitcnt vmcnt(0)" ::: "memory");
        }
        GET_TAG();
        if (bid < 240) {
            const int part = nt / 5, c0 = (nt - part * 5) * 128;
            const int wm = w & 1, wn = w >> 1;
            constexpr int RB = 32768;
            const int xr = wm * 64 + r;
            const unsigned xa = RB + xr * 128 + ((h ^ ((xr >> 1) & 7)) << 4);
            unsigned wt[2];
#pragma unroll
            for (int t = 0; t < 2; ++t) {
                const int q = (lane & 15) >> 2, p = lane & 3, blk = (lane >> 4) & 1, row = 8 * h + 4 * t + q;
                wt[t] = RB + 16384 + row * 256 + (((4 * wn + 2 * blk + (p >> 1)) ^ ((q << 2) | (2 * h + t))) << 4) + 8 * (p & 1);
            }
            auto gbody = [&](auto trc) {
                constexpr bool TR = decltype(trc)::value;
                u32x4 xf[2][8], wf[2][4];
                auto rdk = [&](auto bc, LDS3 const char* sx, LDS3 const char* sb, int ks) {
                    constexpr int B = decltype(bc)::value;
                    xf[B][2 * ks] = *(LDS3 const u32x4*)(sx + (xa ^ (ks * 32)));
                    xf[B][2 * ks + 1] = *(LDS3 const u32x4*)(sx + ((xa + 4096) ^ (ks * 32)));
                    const u32x2 lo = trread(sb + wt[0] + ks * 4096), hi = trread(sb + wt[1] + ks * 4096);
                    wf[B][ks] = (u32x4){lo[0], lo[1], hi[0], hi[1]};
                };
                f32x16 acc0, acc1;
                auto step = [&](auto bc, int s) {
                    constexpr int B = decltype(bc)::value;
                    if (s == GA_PUB) {
                        DRAIN();
                        unsigned arr = 0;
                        if (lane == 0) arr = LDS_CNT_ADD(G_ST);
                        if (__builtin_amdgcn_readfirstlane(arr) == 7 && lane == 0) set_flag(ws, fG, tag);
                    }
                    __builtin_amdgcn_s_waitcnt(0xC07F);
                    if (s < 18) BAR();
                    SBAR();
                    const int s1 = s + 1;
                    LDS3 const char* sb = lds + __builtin_amdgcn_readfirstlane((s1 & 3) * 32768);
                    LDS3 const char* sx = lds + __builtin_amdgcn_readfirstlane(((s1 >= 10 ? 20 - s1 : s1 + 1) & 3) * 32768);
#define GM(A, i) do { if constexpr (TR) A = MFMA32_16(wf[B][(i) >> 1], xf[B][i], A); else A = MFMA32_16(xf[B][i], wf[B][(i) >> 1], A); SBAR(); } while (0)
#define GX(i) do { xf[B ^ 1][i] = *(LDS3 const u32x4*)(sx + ((xa + ((i) & 1) * 4096) ^ (((i) >> 1) * 32))); } while (0)
#define GW(ks) do { const u32x2 lo = trread(sb + wt[0] + (ks) * 4096), hi = trread(sb + wt[1] + (ks) * 4096); wf[B ^ 1][ks] = (u32x4){lo[0], lo[1], hi[0], hi[1]}; } while (0)
                    GM(acc0, 0); GX(0); GW(0); SBAR(); GM(acc1, 1); GX(1); GX(2); SBAR(); GM(acc0, 2); GW(1); GX(3); SBAR(); GM(acc1, 3); GX(4); GW(2); SBAR();
                    GM(acc0, 4); GX(5); GX(6); SBAR(); GM(acc1, 5); GW(3); GX(7); SBAR(); GM(acc0, 6); GM(acc1, 7);
#undef GX
#undef GW
#undef GM
                };
                WAIT_BAR(63);
#pragma unroll
                for (int ks = 0; ks < 4; ++ks) rdk(std::integral_constant<int, 0>{}, lds + 32768, lds, ks);
#pragma unroll 1
                for (int mat = 0; mat < 2; ++mat) {
                    acc0 = (f32x16){}; acc1 = (f32x16){};
#pragma unroll 1
                    for (int kk = 0; kk < 10; kk += 2) {
                        step(std::integral_constant<int, 0>{}, mat * 10 + kk);
                        step(std::integral_constant<int, 1>{}, mat * 10 + kk + 1);
                    }
                    const int cpos = 2 * (mt & 7) + (mt >> 3);
                    if constexpr (TR) {
                        const float sc = (part == 0 && mat == 0) ? QSCALE : 1.f;
                        const unsigned base = part == 0 ? WS_Q + mat * NH * 81920 + mt * 5120 : WS_KV + mat * NH * 163840 + cpos * 10240;
                        const unsigned hs = part == 0 ? 81920 : 163840;
#pragma unroll
                        for (int pr = 0; pr < 2; ++pr) {
                            const int G8 = ((c0 + 32 * wn) >> 3) + 2 * pr + h, pp = G8 / 5, gi = G8 - pp * 5;
                            const unsigned hb0 = base + (2 * pp + (gi > 2)) * hs;
#pragma unroll
                            for (int i = 0; i < 2; ++i) {
                                const f32x16& a = i ? acc1 : acc0;
                                const int tl = wm * 64 + i * 32 + r;
                                const auto s0 = __builtin_amdgcn_permlane32_swap(pk2(a[8 * pr] * sc, a[8 * pr + 1] * sc), pk2(a[8 * pr + 4] * sc, a[8 * pr + 5] * sc), false, false);
                                const auto s1 = __builtin_amdgcn_permlane32_swap(pk2(a[8 * pr + 2] * sc, a[8 * pr + 3] * sc), pk2(a[8 * pr + 6] * sc, a[8 * pr + 7] * sc), false, false);
                                if (gi != 2) { const u32x4 ov = {s0[0], s1[0], s0[1], s1[1]}; ST16(ov, hb0 + (tl >> 5) * 1024 + (gi > 2 ? gi - 3 : gi) * 512 + r * 16); }
                                else { const u32x2 o0 = {s0[0], s1[0]}, o1 = {s0[1], s1[1]}; ST8(o0, hb0 + 4096 + tl * 8); ST8(o1, hb0 + hs + 4096 + tl * 8); }
                            }
                        }
                    } else {
                        const int n = c0 + 32 * wn + r, hd = n / 20, d = n - hd * 20, rho = (hd & 1) ? (d < 4 ? d + 16 : d - 4) : d;
                        const unsigned base = WS_KV + (mat * NH + hd) * 163840 + cpos * 10240 + 5120 + h * 16 + rho * 32;
#pragma unroll
                        for (int i = 0; i < 2; ++i)
#pragma unroll
                            for (int s2 = 0; s2 < 2; ++s2) {
                                const f32x16& a = i ? acc1 : acc0;
                                u32x4 ov = {pk2(a[8 * s2], a[8 * s2 + 1]), pk2(a[8 * s2 + 2], a[8 * s2 + 3]), pk2(a[8 * s2 + 4], a[8 * s2 + 5]), pk2(a[8 * s2 + 6], a[8 * s2 + 7])};
                                ST16(ov, base + (wm * 4 + i * 2 + s2) * 640);
                            }
                    }
                    if (mat == 0) STAMP(1);
                }
            };
            if (part < 2) gbody(std::true_type{}); else gbody(std::false_type{});
        } else {
            const int sj = bid - 240;
            WAIT_BAR(63);
            {
                const u32x4 ga = *(const u32x4*)(P.gw + lane * 8), gb = *(const u32x4*)(P.gw + 512 + (lane & 15) * 8);
                u32x4 xa[16], xb[4];
                const u16* xr = P.x + (size_t)(sj * 128 + w * 16) * DM;
#pragma unroll
                for (int rr = 0; rr < 16; ++rr) xa[rr] = *(const u32x4*)(xr + rr * DM + lane * 8);
#pragma unroll
                for (int i = 0; i < 4; ++i) xb[i] = *(const u32x4*)(xr + (i * 4 + (lane >> 4)) * DM + 512 + (lane & 15) * 8);
                float res = 0.f;
#pragma unroll
                for (int rr = 0; rr < 16; ++rr) {
                    float s = 0.f, s2 = 0.f;
#pragma unroll
                    for (int j = 0; j < 4; ++j) { s += bf_lo(xa[rr][j]) * bf_lo(ga[j]) + bf_hi(xa[rr][j]) * bf_hi(ga[j]); s2 += bf_lo(xb[rr >> 2][j]) * bf_lo(gb[j]) + bf_hi(xb[rr >> 2][j]) * bf_hi(gb[j]); }
                    if ((lane >> 4) == (rr & 3)) s += s2;
#pragma unroll
                    for (int o = 32; o >= 1; o >>= 1) s += __shfl_xor(s, o);
                    if (lane == rr) res = s;
                }
                if (lane < 16) ST4(__float_as_uint(1.f / (1.f + __expf(-res))), WS_G + (sj * 128 + w * 16 + lane) * 4);
            }
            DRAIN();
            unsigned arr = 0;
            if (lane == 0) arr = LDS_CNT_ADD(G_ST);
            if (__builtin_amdgcn_readfirstlane(arr) == 7 && lane == 0) set_flag(ws, WS_F_SD + sj * 8, tag);
        }
        STAMP(2);
        const unsigned hb = lds0 + kh * 10240;
        const unsigned laneK = hb + lane * 16, laneT = hb + 4096 + r * 8 + h * 4, laneV = hb + 5120 + h * 16 + (r < 20 ? r : 19) * 32;
        const u64 vmask = 0x000fffff000fffffull;
        u32x4 v0a = {one, one, one, one}, v0b = v0a, v1 = v0a;
        u32x4 qA, qB, ka; unsigned qtA, qtB, kt;
        {
            lds_wait(lds + G_Q, 4);
            LDS3 const char* qp = lds + L_Q + (qs >> 1) * 5120;
            const unsigned om = (qs & 1) * 2048 + lane * 16, ot = 4096 + ((qs & 1) * 64 + r) * 8 + 4 * h;
            qA = *(LDS3 const u32x4*)(qp + om); qB = *(LDS3 const u32x4*)(qp + om + 1024);
            qtA = *(LDS3 const unsigned*)(qp + ot); qtB = *(LDS3 const unsigned*)(qp + ot + 256);
            DSR128(ka, kh ? laneK : lds0 + L_Q + 10240 + lane * 16, 0); DSR32(kt, laneT, 0); DSR128M2(v0a, laneV, 0, v1, laneV, 640);
            asm volatile("s_waitcnt lgkmcnt(0)" : "+v"(ka), "+v"(kt), "+v"(v0a), "+v"(v1), "+v"(qA), "+v"(qB), "+v"(qtA), "+v"(qtB) :: "memory");
            if (lane == 0) LDS_CNT_ADD(G_RD);
        }
        STAMP(5);
        float mA, mB; u32x2 qTA, qTB; f32x16 sA, sB;
        {
            const u32x2 t0 = {kt, 0x3f80u}, a0 = {qtA, 0u}, b0 = {qtB, 0u};
            const f32x16 s = MFMA32_16(ka, qA, z16);
            const f32x16 s0 = MFMA32_8(t0, a0, s);
            f32x16 s1 = MFMA32_16(ka, qB, z16); s1 = MFMA32_8(t0, b0, s1);
            float x0 = s0[0], x1 = s1[0];
#pragma unroll
            for (int i = 1; i < 16; ++i) { x0 = fmaxf(x0, s0[i]); x1 = fmaxf(x1, s1[i]); }
            x0 = -xhalf_max(x0); x1 = -xhalf_max(x1);
            const unsigned hh = pk2(x0, x1);
            const unsigned ll = pk2(x0 - bf_lo(hh), x1 - bf_hi(hh));
            mA = -(bf_lo(hh) + bf_lo(ll)); mB = -(bf_hi(hh) + bf_hi(ll));
            const unsigned sel = h ? ll : hh;
            qTA = (u32x2){qtA, sel & 0xffffu}; qTB = (u32x2){qtB, sel >> 16};
            sA = MFMA32_8(t0, qTA, s);
        }
        f32x16 oA = {}, oB = {};
        u32x4 pa, pb = {0u, 0u, 0u, 0u};
#define EX4(S, i, Pv, j) do { Pv[j] = pk2(ex2(S[i]), ex2(S[i + 1])); Pv[j + 1] = pk2(ex2(S[i + 2]), ex2(S[i + 3])); PIN(Pv); SBAR(); } while (0)
#define KT2 ((u32x2){kt, 0x3f80u})
#define TILE(V0C, V0N, aK1, oK1, aT1, oT1, oV1, aV0, oV0) do { \
            sB = MFMA32_16(ka, qB, z16); SBAR(); DSR128(ka, aK1, oK1); EX4(sA, 0, pa, 0); \
            oB = MFMA32_16(v1, pb, oB); SBAR(); DSR128M2(v1, aV, oV1, V0N, aV0, oV0); EX4(sA, 4, pa, 2); \
            sB = MFMA32_8(KT2, qTB, sB); SBAR(); DSR32(kt, aT1, oT1); EX4(sA, 8, pb, 0); \
            oA = MFMA32_16(V0C, pa, oA); SBAR(); EX4(sA, 12, pb, 2); \
            LWAIT(1); sA = MFMA32_16(ka, qA, z16); SBAR(); EX4(sB, 0, pa, 0); \
            oA = MFMA32_16(v1, pb, oA); SBAR(); EX4(sB, 4, pa, 2); \
            LWAIT(0); sA = MFMA32_8(KT2, qTA, sA); SBAR(); EX4(sB, 8, pb, 0); \
            oB = MFMA32_16(V0C, pa, oB); SBAR(); EX4(sB, 12, pb, 2); \
        } while (0)
        unsigned aK = laneK, aT = laneT, aV = laneV;
#pragma unroll 1
        for (int c = 0; c < 8; ++c) {
            if (c >= 1 && c <= 3) lds_wait(lds + (c == 1 ? C_KVL : (c == 2 ? C_KVL + 12 : C_WA + 8)), 4);
            if (c >= 1) {
                unsigned pp;
                asm volatile("ds_write_b32 %1, %2\n\tds_read_b32 %0, %3\n\ts_waitcnt lgkmcnt(0)" : "=&v"(pp) : "v"(lds0 + C_PR + w * 4), "v"(c), "v"(lds0 + C_PR + (w ^ 4) * 4) : "memory");
                const int pc = __builtin_amdgcn_readfirstlane(pp);
                if (pc > c || (pc == c && ((c ^ kh) & 1))) __builtin_amdgcn_s_setprio(1); else __builtin_amdgcn_s_setprio(0);
            } else if (kh) __builtin_amdgcn_s_setprio(1);
            const unsigned nx = (c < 7 ? c + 1 : 7) * 20480;
            const unsigned nK = laneK + nx, nT = laneT + nx, nV = laneV + nx;
            SBAR();
            TILE(v0a, v0b, aK, 1024, aT, 256, 640,  aV, 1280);
            TILE(v0b, v0a, aK, 2048, aT, 512, 1920, aV, 2560);
            if (c == 0) {
                DRAIN();
                unsigned arr = 0;
                if (lane == 0) arr = LDS_CNT_ADD(C_GB);
                if (__builtin_amdgcn_readfirstlane(arr) == 7 && lane == 0 && bid < 240) set_flag(ws, fG + 2048, tag);
                asm volatile("s_waitcnt lgkmcnt(0)" ::: "memory");
                SBAR();
            }
            TILE(v0a, v0b, aK, 3072, aT, 768, 3200, aV, 3840);
            TILE(v0b, v0a, nK, 0,    nT, 0,   4480, nV, 0);
            aK = nK; aT = nT; aV = nV;
        }
        oB = MFMA32_16(v1, pb, oB);
#undef TILE
#undef EX4
        if (lane == 0) __hip_atomic_store((LDS3 unsigned*)(lds + C_PR + w * 4), 8u, __ATOMIC_RELAXED, __HIP_MEMORY_SCOPE_WORKGROUP);
        STAMP(6);
        {
            float chk = 0.f;
#pragma unroll
            for (int i = 0; i < 12; ++i) chk += fabsf(oA[i]) * 0.f + fabsf(oB[i]) * 0.f;
            bool bad = !(chk == 0.f);
            if (h) bad = bad || !(oA[8] >= 0x1p-60f && oA[8] <= 0x1p60f && oB[8] >= 0x1p-60f && oB[8] <= 0x1p60f);
            if (__builtin_expect(__any(bad), 0)) {
                mA = -INFINITY; mB = -INFINITY;
#pragma unroll
                for (int i = 0; i < 16; ++i) { oA[i] = 0.f; oB[i] = 0.f; }
                const u32x2 a0 = {qtA, 0u}, b0 = {qtB, 0u};
#pragma unroll 1
                for (int t = 0; t < 32; ++t) {
                    const unsigned kb = WS_KV + head * 163840 + (2 * (t >> 2) + kh) * 10240, t4 = t & 3;
                    const u32x4 k0 = LD16(kb + t4 * 1024 + lane * 16);
                    const u32x2 t0 = {LD4(kb + 4096 + (t4 * 32 + r) * 8 + 4 * h), 0u};
                    u32x4 w0 = LD16(kb + 5120 + t4 * 1280 + h * 16 + (r < 20 ? r : 19) * 32), w1 = LD16(kb + 5120 + t4 * 1280 + 640 + h * 16 + (r < 20 ? r : 19) * 32);
                    if (r >= 20) { w0 = (u32x4){one, one, one, one}; w1 = w0; }
                    f32x16 s0 = MFMA32_16(k0, qA, z16); s0 = MFMA32_8(t0, a0, s0);
                    f32x16 s1 = MFMA32_16(k0, qB, z16); s1 = MFMA32_8(t0, b0, s1);
                    float x0 = s0[0], x1 = s1[0];
#pragma unroll
                    for (int i = 1; i < 16; ++i) { x0 = fmaxf(x0, s0[i]); x1 = fmaxf(x1, s1[i]); }
                    x0 = fmaxf(xhalf_max(x0), mA); x1 = fmaxf(xhalf_max(x1), mB);
                    const float g0 = (mA == x0) ? 1.f : ex2(mA - x0), g1 = (mB == x1) ? 1.f : ex2(mB - x1);
                    mA = x0; mB = x1;
#pragma unroll
                    for (int i = 0; i < 16; ++i) { oA[i] *= g0; oB[i] *= g1; }
#pragma unroll
                    for (int i = 0; i < 4; ++i) { pa[i] = pk2(ex2(s0[2 * i] - x0), ex2(s0[2 * i + 1] - x0)); pb[i] = pk2(ex2(s0[8 + 2 * i] - x0), ex2(s0[9 + 2 * i] - x0)); }
                    oA = MFMA32_16(w0, pa, oA); oA = MFMA32_16(w1, pb, oA);
#pragma unroll
                    for (int i = 0; i < 4; ++i) { pa[i] = pk2(ex2(s1[2 * i] - x1), ex2(s1[2 * i + 1] - x1)); pb[i] = pk2(ex2(s1[8 + 2 * i] - x1), ex2(s1[9 + 2 * i] - x1)); }
                    oB = MFMA32_16(w0, pa, oB); oB = MFMA32_16(w1, pb, oB);
                }
            }
        }
        lds_wait<4>(lds + C_BX, 8);
        const float gO = *(LDS3 const float*)(lds + X_G + (qs * 64 + kh * 32 + r) * 4);
        {
            LDS3 char* xp = lds + X_MRG + w * 2816;
            const f32x16& s = kh ? oA : oB;
            *(LDS3 f32x4*)(xp + lane * 16) = (f32x4){s[0], s[1], s[2], s[3]};
            *(LDS3 f32x4*)(xp + 1024 + lane * 16) = (f32x4){s[4], s[5], s[6], s[7]};
            if (!h) *(LDS3 f32x4*)(xp + 2048 + r * 16) = (f32x4){s[8], s[9], s[10], s[11]};
            else *(LDS3 f32x2*)(xp + 2560 + r * 8) = (f32x2){s[8], kh ? mA : mB};
            asm volatile("s_waitcnt lgkmcnt(0)" ::: "memory");
            if (lane == 0) __hip_atomic_store((LDS3 unsigned*)(lds + C_MRG + w * 4), 1u, __ATOMIC_RELAXED, __HIP_MEMORY_SCOPE_WORKGROUP);
        }
        lds_wait<MS>(lds + C_MRG + (w ^ 4) * 4, 1);
        bool lastw;
        {
            LDS3 const char* xp = lds + X_MRG + (w ^ 4) * 2816;
            const f32x4 p0 = *(LDS3 const f32x4*)(xp + lane * 16), p1 = *(LDS3 const f32x4*)(xp + 1024 + lane * 16), p2 = *(LDS3 const f32x4*)(xp + 2048 + r * 16);
            const f32x2 lm = *(LDS3 const f32x2*)(xp + 2560 + r * 8);
            const f32x16& s = kh ? oB : oA;
            const float mo = kh ? mB : mA, mp = lm[1];
            const float mx = fmaxf(mo, mp);
            const float fo = (mo == mx) ? 1.f : ex2(mo - mx), fq = (mp == mx) ? 1.f : ex2(mp - mx);
            float o[12];
#pragma unroll
            for (int i = 0; i < 4; ++i) { o[i] = s[i] * fo + p0[i] * fq; o[4 + i] = s[4 + i] * fo + p1[i] * fq; o[8 + i] = s[8 + i] * fo + p2[i] * fq; }
            const float l = upper_half(s[8]) * fo + lm[0] * fq;
            store_o(rs, o, gO / l, q0 + kh * 32 + r, 0, head, h);
            DRAIN();
            unsigned arr = 0;
            if (lane == 0) arr = LDS_CNT_ADD(C_OA);
            lastw = __builtin_amdgcn_readfirstlane(arr) == 7;
            if (lastw && lane == 0) set_flag(ws, WS_F_OA + (qb * 32 + head) * 8, tag);
        }
        STAMP(7);
        {
            const int c16 = lane & 15, g = lane >> 4, tt = w & 3, k0 = kh * 10;
            lds_wait(lds + C_WA, 4);
            LDS3 const char* wp = lds + L_WA + k0 * 1024 + lane * 16;
            u32x4 wf[4][5];
#pragma unroll
            for (int ks = 0; ks < 4; ++ks)
#pragma unroll
                for (int n = 0; n < 5; ++n) wf[ks][n] = *(LDS3 const u32x4*)(wp + (n * 20 + ks) * 1024);
            POLL_ONE(lastw, 2, WS_F_OA + ((mtO >> 2) * 32 + r) * 8, WS_F_OA + ((mtO >> 2) * 32 + r) * 8, C_P4);
            STAMP(9);
            const unsigned ab = WS_AO + ((mtO * 4 + tt) * 40 + k0) * 1024;
            u32x4 af[10];
#pragma unroll
            for (int ks = 0; ks < 6; ++ks) af[ks] = LD16(ab + ks * 1024 + lane * 16);
            if (kh) {
#pragma unroll
                for (int ks = 6; ks < 10; ++ks) LD_TAIL(af[ks], ab + ks * 1024);
            } else {
#pragma unroll
                for (int ks = 6; ks < 10; ++ks) af[ks] = LD16(ab + ks * 1024 + lane * 16);
            }
            f32x4 c[5];
#pragma unroll
            for (int n = 0; n < 5; ++n) c[n] = (f32x4){0.f, 0.f, 0.f, 0.f};
            SBAR();
#pragma unroll
            for (int ks = 0; ks < 10; ++ks) {
#pragma unroll
                for (int n = 0; n < 5; ++n) c[n] = MFMA16_32(wf[ks & 3][n], af[ks], c[n]);
                SBAR();
                if (ks + 4 < 10) {
#pragma unroll
                    for (int n = 0; n < 5; ++n) wf[ks & 3][n] = *(LDS3 const u32x4*)(wp + (n * 20 + ks + 4) * 1024);
                }
                SBAR();
            }
            LDS3 char* xp = lds + X_MRG + tt * 5120 + lane * 16;
            if (w < 4) { *(LDS3 f32x4*)(xp + 3072) = c[3]; *(LDS3 f32x4*)(xp + 4096) = c[4]; }
            else { *(LDS3 f32x4*)(xp) = c[0]; *(LDS3 f32x4*)(xp + 1024) = c[1]; *(LDS3 f32x4*)(xp + 2048) = c[2]; }
            asm volatile("s_waitcnt lgkmcnt(0)" ::: "memory");
            if (lane == 0) __hip_atomic_store((LDS3 unsigned*)(lds + C_FIN + w * 4), 1u, __ATOMIC_RELAXED, __HIP_MEMORY_SCOPE_WORKGROUP);
            lds_wait(lds + C_FIN + (w ^ 4) * 4, 1);
            lds_wait(lds + C_BP + tt * 4, 1);
            u16* op = P.out + (size_t)(mtO * 64 + tt * 16 + c16) * DM + ntO * 80 + g * 4;
#pragma unroll
            for (int n = 0; n < 5; ++n) {
                if ((w < 4) == (n < 3)) {
                    const f32x4 bp = *(LDS3 const f32x4*)(n < 3 ? lds + X_FIN + tt * 3072 + n * 1024 + lane * 16 : lds + 143360 + tt * 2048 + (n - 3) * 1024 + lane * 16);
                    const f32x4 s = c[n] + *(LDS3 const f32x4*)(xp + n * 1024) + bp;
                    *(u32x2*)(op + n * 16) = (u32x2){pk2(s[0], s[1]), pk2(s[2], s[3])};
                }
            }
            if (bid == 0 && mtid == 0) set_flag(ws, WS_EPOCH, tag);
        }
        STAMP(10);
        asm volatile("mega_code_end:");
    }
}

extern "C" void kernel_launch(void* const* d_in, const int* in_sizes, int n_in, void* d_out, int out_size, void* d_ws, size_t ws_size,
                              hipStream_t stream) {
    Params P;
    P.x = (const u16*)d_in[0]; P.wa = (const u16*)d_in[1]; P.woa = (const u16*)d_in[2]; P.wb = (const u16*)d_in[4];
    P.wphi = (const u16*)d_in[5]; P.wob = (const u16*)d_in[6]; P.gw = (const u16*)d_in[7]; P.ws = (char*)d_ws; P.out = (u16*)d_out;
    mega<<<256, 768, 0, stream>>>(P);
}
```

```cpp
#include <hip/hip_runtime.h>
#include <hip/hip_bf16.h>
#include <stdint.h>
#include <type_traits>

typedef __attribute__((ext_vector_type(8))) short bf16x8;
typedef __attribute__((ext_vector_type(4))) short bf16x4;
typedef __attribute__((ext_vector_type(16))) float f32x16;
typedef __attribute__((ext_vector_type(4))) float f32x4;
typedef __attribute__((ext_vector_type(2))) float f32x2;
typedef __attribute__((ext_vector_type(4))) unsigned u32x4;
typedef __attribute__((ext_vector_type(2))) unsigned u32x2;
typedef __attribute__((ext_vector_type(2))) __bf16 bf2_t;
typedef unsigned short u16;
typedef unsigned long long u64;
#define LDS3 __attribute__((address_space(3)))
typedef __attribute__((address_space(1))) u64 gu64;
#define RLX_AGENT __ATOMIC_RELAXED, __HIP_MEMORY_SCOPE_AGENT

#define PRIO_MODE 3
#define WARM_MODE 0
#define GA_PUB 11
#define PHI_ROUND 0
#define HS 8
#define MS 2
#define ROT_MODE 3
#define TLB_STRIDE 1048576
#define PRE_SLEEP 2
constexpr int SEQ = 2048, DM = 640, NH = 32;
constexpr float LOG2E = 1.4426950408889634f;
constexpr float QSCALE = 0.22360679774997896f * LOG2E;

constexpr unsigned WS_EPOCH = 0;
constexpr unsigned WS_F_G   = 256;
constexpr unsigned WS_F_SD  = WS_F_G + 4096;
constexpr unsigned WS_F_KV  = WS_F_SD + 256;
constexpr unsigned WS_F_OB  = WS_F_KV + 2048;
constexpr unsigned WS_F_OA  = WS_F_OB + 2048;
constexpr unsigned WS_G     = 16384;
constexpr unsigned WS_Q     = 32768;
constexpr unsigned WS_KV    = WS_Q + 2 * NH * 81920;
constexpr unsigned WS_KVP   = WS_KV + 2 * NH * 163840;
constexpr unsigned WS_AO    = WS_KVP + NH * 8 * 4096;
constexpr unsigned WS_WT    = WS_AO + SEQ * 1280 * 2;
constexpr unsigned WS_END   = WS_WT + 1280 * 640 * 2;

#define STAMP(i)

__device__ __forceinline__ unsigned pk2(float a, float b) { f32x2 v = {a, b}; bf2_t r = __builtin_convertvector(v, bf2_t); return __builtin_bit_cast(unsigned, r); }
__device__ __forceinline__ float bf_lo(unsigned u) { return __uint_as_float(u << 16); }
__device__ __forceinline__ float bf_hi(unsigned u) { return __uint_as_float(u & 0xffff0000u); }
__device__ __forceinline__ float ex2(float x) { return __builtin_amdgcn_exp2f(x); }
#define MFMA32_16(a, b, c) __builtin_amdgcn_mfma_f32_32x32x16_bf16(__builtin_bit_cast(bf16x8, a), __builtin_bit_cast(bf16x8, b), c, 0, 0, 0)
#define MFMA32_8(a, b, c) __builtin_amdgcn_mfma_f32_32x32x8bf16_1k(__builtin_bit_cast(bf16x4, a), __builtin_bit_cast(bf16x4, b), c, 0, 0, 0)
#define MFMA16_32(a, b, c) __builtin_amdgcn_mfma_f32_16x16x32_bf16(__builtin_bit_cast(bf16x8, a), __builtin_bit_cast(bf16x8, b), c, 0, 0, 0)

__device__ __forceinline__ void glds16(unsigned voff, const void* sbase, unsigned lds_base) {
    unsigned sv;
    asm volatile("s_mov_b32 %0, m0\n\ts_mov_b32 m0, %3\n\ts_nop 0\n\tglobal_load_lds_dwordx4 %1, %2\n\ts_mov_b32 m0, %0"
                 : "=&s"(sv) : "v"(voff), "s"(sbase), "s"(__builtin_amdgcn_readfirstlane(lds_base)) : "memory");
}
__device__ __forceinline__ void glds16s(unsigned voff, const void* sbase, unsigned lds_base) {
    unsigned sv;
    asm volatile("s_mov_b32 %0, m0\n\ts_mov_b32 m0, %3\n\ts_nop 0\n\tglobal_load_lds_dwordx4 %1, %2 sc1\n\ts_mov_b32 m0, %0"
                 : "=&s"(sv) : "v"(voff), "s"(sbase), "s"(__builtin_amdgcn_readfirstlane(lds_base)) : "memory");
}
__device__ __forceinline__ void glds4s(unsigned voff, const void* sbase, unsigned lds_base) {
    unsigned sv;
    asm volatile("s_mov_b32 %0, m0\n\ts_mov_b32 m0, %3\n\ts_nop 0\n\tglobal_load_lds_dword %1, %2 sc1\n\ts_mov_b32 m0, %0"
                 : "=&s"(sv) : "v"(voff), "s"(sbase), "s"(__builtin_amdgcn_readfirstlane(lds_base)) : "memory");
}
__device__ __forceinline__ void glds16x4(const unsigned (&vo)[4], const void* sbase, unsigned lds_base) {
    unsigned sv;
    asm volatile("s_mov_b32 %0, m0\n\ts_mov_b32 m0, %6\n\ts_nop 0\n\tglobal_load_lds_dwordx4 %1, %5\n\tglobal_load_lds_dwordx4 %2, %5 offset:1024\n\t"
                 "global_load_lds_dwordx4 %3, %5 offset:2048\n\tglobal_load_lds_dwordx4 %4, %5 offset:3072\n\ts_mov_b32 m0, %0"
                 : "=&s"(sv) : "v"(vo[0]), "v"(vo[1]), "v"(vo[2]), "v"(vo[3]), "s"(sbase), "s"(lds_base) : "memory");
}
typedef short v4i16_t __attribute__((ext_vector_type(4)));
__device__ __forceinline__ u32x2 trread(LDS3 const char* p) {
    return __builtin_bit_cast(u32x2, __builtin_amdgcn_ds_read_tr16_b64_v4i16((LDS3 v4i16_t*)p));
}
#define WAIT_BAR(N) asm volatile("s_waitcnt vmcnt(" #N ") lgkmcnt(0)\n\ts_barrier" ::: "memory")
#define DRAIN() asm volatile("s_waitcnt vmcnt(0)" ::: "memory")
#define SBAR() __builtin_amdgcn_sched_barrier(0)
#define PIN(x) asm volatile("" : "+v"(x))

__device__ __forceinline__ float xhalf_max(float x) {
    auto r = __builtin_amdgcn_permlane32_swap(__float_as_uint(x), __float_as_uint(x), false, false);
    return fmaxf(__uint_as_float(r[0]), __uint_as_float(r[1]));
}
__device__ __forceinline__ float xhalf_sum(float x) {
    auto r = __builtin_amdgcn_permlane32_swap(__float_as_uint(x), __float_as_uint(x), false, false);
    return __uint_as_float(r[0]) + __uint_as_float(r[1]);
}
__device__ __forceinline__ float upper_half(float x) {
    auto r = __builtin_amdgcn_permlane32_swap(__float_as_uint(x), __float_as_uint(x), false, false);
    return __uint_as_float(r[1]);
}

typedef __amdgpu_buffer_rsrc_t rsrc_t;
#define ST8(v, off)  __builtin_amdgcn_raw_buffer_store_b64(v, rs, (int)(off), 0, 16)
#define ST16(v, off) __builtin_amdgcn_raw_buffer_store_b128(v, rs, (int)(off), 0, 16)
#define ST4(v, off)  __builtin_amdgcn_raw_buffer_store_b32(v, rs, (int)(off), 0, 16)
#define LD16(off) __builtin_amdgcn_raw_buffer_load_b128(rs, (int)(off), 0, 16)
#define LD4(off)  __builtin_amdgcn_raw_buffer_load_b32(rs, (int)(off), 0, 16)

template <int SLP = 2>
__device__ __forceinline__ void wait_flags(const char* ws, unsigned off0, unsigned off1, u64 tag) {
    gu64* p0 = (gu64*)(ws + off0); gu64* p1 = (gu64*)(ws + off1);
#pragma unroll 1
    for (unsigned spins = 0; spins < (1u << 16); ++spins) {
        const u64 a = __hip_atomic_load(p0, RLX_AGENT), b = __hip_atomic_load(p1, RLX_AGENT);
        if (__all(a == tag && b == tag)) break;
        __builtin_amdgcn_s_sleep(SLP);
    }
    __builtin_amdgcn_fence(__ATOMIC_ACQUIRE, "wavefront");
}
__device__ __forceinline__ void set_flag(char* ws, unsigned off, u64 tag) { __hip_atomic_store((gu64*)(ws + off), tag, RLX_AGENT); }

struct Params { const u16* x; const u16* wa; const u16* wb; const u16* wphi; const u16* woa; const u16* wob; const u16* gw; char* ws; u16* out; };

__device__ __forceinline__ void feature_map(const u32x4& pm, unsigned pt, const u32x4& tm, unsigned tt, int h, u32x4& fp, u32x4& fn, u32x4& ft) {
    const f32x16 z16 = {};
    const u32x2 a8 = {pt, 0u}, b8 = {tt, 0u};
    f32x16 s = MFMA32_16(pm, tm, z16); s = MFMA32_8(a8, b8, s);
    f32x2 v[6];
#pragma unroll
    for (int i = 0; i < 6; ++i) {
        v[i] = (f32x2){s[2 * i], s[2 * i + 1]};
    }
    float mx = fmaxf(v[0][0], v[0][1]), mn = fminf(v[0][0], v[0][1]);
#pragma unroll
    for (int i = 1; i < 6; ++i) { mx = fmaxf(fmaxf(mx, v[i][0]), v[i][1]); mn = fminf(fminf(mn, v[i][0]), v[i][1]); }
    mx = xhalf_max(mx) * -LOG2E; mn = xhalf_max(-mn) * -LOG2E;
    f32x2 ep[6], en[6];
#pragma unroll
    for (int i = 0; i < 6; ++i) {
        const f32x2 a = v[i] * LOG2E + mx, b = v[i] * -LOG2E + mn;
        ep[i] = (f32x2){ex2(a[0]), ex2(a[1])}; en[i] = (f32x2){ex2(b[0]), ex2(b[1])};
    }
    const float tw = h ? 0.f : 1.f;
    const f32x2 s2p = ((ep[0] + ep[1]) + (ep[2] + ep[3])) + (ep[4] + ep[5]) * tw, s2n = ((en[0] + en[1]) + (en[2] + en[3])) + (en[4] + en[5]) * tw;
    const float sp = __builtin_amdgcn_rcpf(xhalf_sum(s2p[0] + s2p[1])), sn = __builtin_amdgcn_rcpf(xhalf_sum(s2n[0] + s2n[1]));
    const float tp = sp * tw, tn = sn * tw;
#pragma unroll
    for (int i = 0; i < 4; ++i) { const f32x2 a = ep[i] * sp, b = en[i] * sn; fp[i] = pk2(a[0], a[1]); fn[i] = pk2(b[0], b[1]); }
    const f32x2 c0 = ep[4] * tp, c1 = ep[5] * tp, c2 = en[4] * tn, c3 = en[5] * tn;
    ft = (u32x4){pk2(c0[0], c0[1]), pk2(c1[0], c1[1]), pk2(c2[0], c2[1]), pk2(c3[0], c3[1])};
}

__device__ __forceinline__ void store_o(rsrc_t rs, const float (&o)[12], float sc, int tok, int half, int head, int h) {
    const auto s0 = __builtin_amdgcn_permlane32_swap(pk2(o[0] * sc, o[1] * sc), pk2(o[4] * sc, o[5] * sc), false, false);
    const auto s1 = __builtin_amdgcn_permlane32_swap(pk2(o[2] * sc, o[3] * sc), pk2(o[6] * sc, o[7] * sc), false, false);
    const unsigned tb = WS_AO + ((tok >> 4) * 40 + half * 20) * 1024;
    const u32x4 mv = {s0[0], s1[0], s0[1], s1[1]};
    ST16(mv, tb + (head >> 1) * 1024 + (((head & 1) * 2 + h) * 16 + (tok & 15)) * 16);
    const u32x2 tv = {pk2(o[8] * sc, o[9] * sc), pk2(o[10] * sc, o[11] * sc)};
    if (!h) ST8(tv, tb + (16 + (head >> 3)) * 1024 + (head & 7) * 128 + (tok & 15) * 8);
}
#define LD8(off)  __builtin_amdgcn_raw_buffer_load_b64(rs, (int)(off), 0, 16)
#define LD_TAIL(dst, base) do { const u32x2 a_ = LD8((base) + (lane >> 4) * 256 + (lane & 15) * 8), b_ = LD8((base) + (lane >> 4) * 256 + 128 + (lane & 15) * 8); dst = (u32x4){a_[0], a_[1], b_[0], b_[1]}; } while (0)

#define DSR128(dst, addr, off) asm volatile("ds_read_b128 %0, %1 offset:%2" : "=v"(dst) : "v"(addr), "i"(off))
#define DSR32(dst, addr, off) asm volatile("ds_read_b32 %0, %1 offset:%2" : "=v"(dst) : "v"(addr), "i"(off))
#define DSR128M2(d0, a0, o0, d1, a1, o1) asm volatile("s_mov_b64 exec, %6\n\tds_read_b128 %0, %2 offset:%3\n\tds_read_b128 %1, %4 offset:%5\n\ts_mov_b64 exec, -1" \
                                                      : "+v"(d0), "+v"(d1) : "v"(a0), "i"(o0), "v"(a1), "i"(o1), "s"(vmask))
#define LWAIT(n) do { asm volatile("s_waitcnt lgkmcnt(" #n ")" ::: "memory"); SBAR(); } while (0)

#define BAR() asm volatile("s_barrier" ::: "memory")
#define LDS_SET(off, v) __hip_atomic_store((LDS3 unsigned*)(lds + (off)), (unsigned)(v), __ATOMIC_RELAXED, __HIP_MEMORY_SCOPE_WORKGROUP)
#define LDS_CNT_ADD(off) __hip_atomic_fetch_add((LDS3 unsigned*)(lds + (off)), 1u, __ATOMIC_RELAXED, __HIP_MEMORY_SCOPE_WORKGROUP)

template <int SLP = 1>
__device__ __forceinline__ void lds_wait(LDS3 char* p, unsigned want) {
#pragma unroll 1
    for (unsigned spins = 0; spins < (1u << 20); ++spins) {
        if ((unsigned)__builtin_amdgcn_readfirstlane(__hip_atomic_load((LDS3 unsigned*)p, __ATOMIC_RELAXED, __HIP_MEMORY_SCOPE_WORKGROUP)) >= want) break;
        __builtin_amdgcn_s_sleep(SLP);
    }
    __builtin_amdgcn_fence(__ATOMIC_ACQUIRE, "wavefront");
}

template <int SLP>
__device__ __forceinline__ void prog_wait(LDS3 char* p, int lane, unsigned want) {
#pragma unroll 1
    for (unsigned spins = 0; spins < (1u << 20); ++spins) {
        if (__all(__hip_atomic_load((LDS3 unsigned*)p + (lane & 7), __ATOMIC_RELAXED, __HIP_MEMORY_SCOPE_WORKGROUP) >= want)) break;
        __builtin_amdgcn_s_sleep(SLP);
    }
    __builtin_amdgcn_fence(__ATOMIC_ACQUIRE, "wavefront");
}
#define POLL_ONE(cond, SLP, o0, o1, word) POLL_ONE2(cond, SLP, 2, o0, o1, word)
#define POLL_ONE2(cond, SLP, SLP2, o0, o1, word) do { if (cond) { wait_flags<SLP>(ws, o0, o1, tag); \
        if (lane == 0) __hip_atomic_store((LDS3 unsigned*)(lds + (word)), 1u, __ATOMIC_RELAXED, __HIP_MEMORY_SCOPE_WORKGROUP); } else lds_wait<SLP2>(lds + (word), 1); } while (0)
constexpr int C_KV = 0, C_KVF = 4, C_OB = 8, C_WA = 16, C_OA = 20, C_BX = 28, C_MRG = 32  , C_FIN = 64  , C_BP = 96  ;
constexpr int C_P1 = 112, C_P2 = 116, C_P3 = 120, C_P4 = 124, C_PR = 128  , C_KVL = 160, C_P5 = 164, C_GB = 168;
constexpr int G_W = 0, G_Q = 176, G_P = 180, G_ST = 184, G_RD = 188;
constexpr int X_G = 256;
constexpr int X_KVF = 1280, X_SLOT = 4096;
constexpr int X_MRG = 1280;
constexpr int L_WA = 24576;
constexpr int L_Q = 81920;
constexpr int X_FIN = 126976;

__global__ __launch_bounds__(768) void mega(Params P) {
    __shared__ __attribute__((aligned(16))) char smem[163840];
    LDS3 char* lds = (LDS3 char*)smem;
    char* ws = P.ws;
    const int tid = threadIdx.x, lane = tid & 63, r = lane & 31, h = lane >> 5;
    const int hw = __builtin_amdgcn_readfirstlane(tid >> 6);
    const bool helper = hw < 4;
    const int w = hw - 4, mtid = tid - 256;
    const int bid = blockIdx.x;
    const unsigned lds0 = (unsigned)(uintptr_t)smem;
    const rsrc_t rs = __builtin_amdgcn_make_buffer_rsrc(ws, 0, WS_END, 0x00020000);
    STAMP(0);
    u64 tag = 0;
#define GET_TAG() do { asm volatile("s_load_dwordx2 %0, %1, 0x0\n\ts_waitcnt lgkmcnt(0)" : "=s"(tag) : "s"(ws) : "memory"); tag += 1; } while (0)
    const int head = (bid & 7) * 4 + ((bid >> 3) & 3), qb = bid >> 5;
    const int nlo = (head * 20) >> 7, nhi = (head * 20 + 19) >> 7;
    const int mtO = (bid & 7) * 4 + ((bid >> 3) & 3), ntO = bid >> 5;
    const int lin = (bid & 7) * 30 + (bid >> 3), lh = lin >= 120, lrem = lin - 120 * lh;
    const int mt = lh * 8 + (lrem & 7), nt = lrem >> 3;
    const unsigned fG = WS_F_G + (nt * 16 + mt) * 8;
    const unsigned one = (r == 20) ? 0x3f803f80u : 0u;
    const f32x16 z16 = {};

    if (helper) {
        const unsigned fA0 = WS_F_G + (((lane < 32 ? 5 : 10) + ((lane & 16) ? nhi : nlo)) * 16 + (lane & 15)) * 8;
        const unsigned fA1 = WS_F_G + (((lane & 2) ? nhi : nlo) * 16 + qb * 2 + (lane & 1)) * 8;
        __builtin_amdgcn_s_setprio(3);
        const char* kvb = ws + WS_KV + head * 163840;
        auto kv_issue = [&](int grp, int t0 = 0, int t1 = 8) {
#pragma unroll
            for (int t = t0; t < t1; ++t) { int pc = grp * 32 + hw + 4 * t; if (pc == 0) pc = 4; glds16s(lane * 16, kvb + pc * 1024, lds0 + pc * 1024); }
        };
        auto q_issue = [&]() {
            const char* qbase = ws + WS_Q + head * 81920 + qb * 10240;
#pragma unroll
            for (int t = 0; t < 3; ++t) { const int pc = hw + 4 * t < 10 ? hw + 4 * t : 10; glds16s(lane * 16, pc < 10 ? qbase + pc * 1024 : kvb, lds0 + L_Q + pc * 1024); }
        };
        if (bid < 240) {
            const int rot = ((nt + mt) % 3) * 3;
            unsigned xo[4], wo[4];
#pragma unroll
            for (int i = 0; i < 4; ++i) {
                xo[i] = (mt * 128 + 32 * hw + 8 * i + (lane >> 3)) * 1280 + (((lane & 7) ^ ((4 * i + (lane >> 4)) & 7)) << 4) - i * 1024;
                wo[i] = (16 * hw + 4 * i + (lane >> 4)) * 3840 + nt * 256 + (((lane & 15) ^ (((lane >> 4) << 2) | i)) << 4) - i * 1024;
            }
            const unsigned dX = lds0 + 32768 + hw * 4096;
            int kt = rot;
#pragma unroll
            for (int n = 0; n < 20; ++n) {
                const char* xb = (const char*)P.x + kt * 128;
                const char* wb = (const char*)(n >= 10 ? P.wb : P.wa) + (size_t)kt * (64 * 3840);
                asm volatile("" : "+s"(xb), "+s"(wb));
                if (n == 2) { if (hw == 0) *(LDS3 unsigned*)(lds + lane * 4) = 0u; GET_TAG(); WAIT_BAR(8); }
                else if (n == 11 || n == 15) { WAIT_BAR(12); }
                else if (n == 3 || (n >= 12 && n <= 14)) { WAIT_BAR(8); }
                else if (n >= 5) { WAIT_BAR(16); }
                glds16x4(wo, wb, dX + 16384 + (n & 3) * 32768);
                if (n < 10 || n >= 14) glds16x4(xo, xb, dX + ((n >= 10 ? 20 - n : n + 1) & 3) * 32768);
                if (n < 9) kt = kt == 9 ? 0 : kt + 1; else if (n > 9) kt = kt == 0 ? 9 : kt - 1;
            }
            WAIT_BAR(16);
            WAIT_BAR(0);
        } else {
            if (hw == 0) *(LDS3 unsigned*)(lds + lane * 4) = 0u;
            GET_TAG();
            WAIT_BAR(63);
            if (hw == 0) {
#pragma unroll 1
                for (int t = 0; t < PRE_SLEEP; ++t) __builtin_amdgcn_s_sleep(127);
            }
        }
        POLL_ONE(hw == 0, 6, fA0, fA1, G_P);
        kv_issue(0); q_issue(); kv_issue(1, 0, 2);
        DRAIN();
        if (lane == 0) LDS_CNT_ADD(G_Q);
        lds_wait<2>(lds + G_RD, 8);
        kv_issue(1, 2, 8); kv_issue(2); kv_issue(3); kv_issue(4);
        asm volatile("s_waitcnt vmcnt(24)" ::: "memory"); if (lane == 0) LDS_CNT_ADD(C_KVL);
        asm volatile("s_waitcnt vmcnt(16)" ::: "memory"); if (lane == 0) LDS_CNT_ADD(C_KVL + 12);
        STAMP(3);
        u32x4 phm; unsigned pht;
        {
            const u16* wp = P.wphi + head * 400 + (r < 20 ? r : (r & 3));
            unsigned e[10];
#pragma unroll
            for (int j = 0; j < 8; ++j) e[j] = wp[(8 * h + j + 4 * (head & 1)) * 20];
            e[8] = wp[((head & 1 ? 0 : 16) + 2 * h) * 20]; e[9] = wp[((head & 1 ? 1 : 17) + 2 * h) * 20];
            const int bu = bid < 160 ? bid : 0, hf = bu >= 80, k0 = (bu - 80 * hf) * 8, hd = k0 < 512 ? k0 >> 4 : (k0 - 512) >> 2;
            const int row = k0 < 512 ? 20 * hd + (k0 & 8) + 4 * (hd & 1) : 20 * hd + 16;
            const u16* wsrc = (hf ? P.wob : P.woa) + (size_t)row * DM;
            unsigned f[3][8];
            if (bid < 160) {
#pragma unroll
                for (int q = 0; q < 3; ++q) {
                    const int n = tid + 256 * q < 640 ? tid + 256 * q : 639;
#pragma unroll
                    for (int j = 0; j < 8; ++j) f[q][j] = wsrc[j * DM + n];
                }
            }
            DRAIN();
            if (lane == 0) LDS_CNT_ADD(C_WA + 8);
            phm = (u32x4){e[0] | (e[1] << 16), e[2] | (e[3] << 16), e[4] | (e[5] << 16), e[6] | (e[7] << 16)}; pht = e[8] | (e[9] << 16);
            if (bid < 160) {
#pragma unroll
                for (int q = 0; q < 3; ++q) {
                    const int n = tid + 256 * q;
                    const u32x4 v = {f[q][0] | (f[q][1] << 16), f[q][2] | (f[q][3] << 16), f[q][4] | (f[q][5] << 16), f[q][6] | (f[q][7] << 16)};
                    if (n < 640) ST16(v, WS_WT + (((n >> 4) * 40 + hf * 20 + (k0 >> 5)) * 64 + ((k0 >> 3) & 3) * 16 + (n & 15)) * 16);
                }
            }
        }
        const int tokS = qb * 256 + hw * 64, m7 = tokS >> 7, t4 = (tokS >> 5) & 3;
        {
            const unsigned pt3 = (lane >> 1) & 3;
            const unsigned tb = WS_F_G + 2048 + ((((lane & 1) ? nhi : nlo) + (pt3 < 3 ? pt3 : 0) * 5) * 16 + qb * 2 + ((lane >> 3) & 1)) * 8;
            POLL_ONE2(hw == 0, 4, HS, tb, WS_F_SD + (qb * 2 + (lane & 1)) * 8, C_P1);
        }
        const unsigned om = t4 * 1024 + lane * 16, ot = 4096 + (t4 * 32 + r) * 8 + 4 * h;
        u32x4 qf[2][3]; float gS[2];
        {
            const unsigned kb = WS_KV + (NH + head) * 163840 + (2 * (m7 & 7) + (m7 >> 3)) * 10240, qbb = WS_Q + (NH + head) * 81920 + m7 * 5120;
            const unsigned ov = 5120 + t4 * 1280 + h * 16 + (r < 20 ? r : 19) * 32;
            u32x4 e0[2], w0[2], w1[2], eq[2]; unsigned et[2], eqt[2];
#pragma unroll
            for (int i = 0; i < 2; ++i) {
                e0[i] = LD16(kb + om + i * 1024); et[i] = LD4(kb + ot + i * 256); w0[i] = LD16(kb + ov + i * 1280); w1[i] = LD16(kb + ov + i * 1280 + 640);
                eq[i] = LD16(qbb + om + i * 1024); eqt[i] = LD4(qbb + ot + i * 256); gS[i] = __uint_as_float(LD4(WS_G + (tokS + i * 32 + r) * 4));
            }
            u32x4 i0, i1;
#pragma unroll
            for (int j = 0; j < 4; ++j) {
                const int e = r - 8 * h - 2 * j;
                i0[j] = e == 0 ? 0x3f80u : (e == 1 ? 0x3f800000u : 0u);
                i1[j] = e == 16 ? 0x3f80u : (e == 17 ? 0x3f800000u : 0u);
            }
            f32x16 k0 = {}, k1 = {};
#pragma unroll
            for (int i = 0; i < 2; ++i) {
                if (r >= 20) { w0[i] = (u32x4){one, one, one, one}; w1[i] = w0[i]; }
                u32x4 fp, fn, ft;
                feature_map(phm, pht, e0[i], et[i], h, fp, fn, ft);
                f32x16 T0 = MFMA32_16(fp, i0, z16); T0 = MFMA32_16(fn, i1, T0);
                f32x16 T1 = MFMA32_16(ft, i0, z16);
                u32x4 b00, b01, b10, b11;
#pragma unroll
                for (int j = 0; j < 4; ++j) {
                    b00[j] = pk2(T0[2 * j], T0[2 * j + 1]); b01[j] = pk2(T0[8 + 2 * j], T0[9 + 2 * j]);
                    b10[j] = pk2(T1[2 * j], T1[2 * j + 1]); b11[j] = pk2(T1[8 + 2 * j], T1[9 + 2 * j]);
                }
                k0 = MFMA32_16(w0[i], b00, k0); k0 = MFMA32_16(w1[i], b01, k0);
                k1 = MFMA32_16(w0[i], b10, k1); k1 = MFMA32_16(w1[i], b11, k1);
            }
            prog_wait<8>(lds + C_PR, lane, 1);
            *(LDS3 float*)(lds + X_G + (hw * 64 + lane) * 4) = h ? gS[1] : gS[0];
            if (hw * 64 + lane < 144) *(LDS3 u32x4*)(lds + X_KVF + (hw * 64 + lane) * 16) = (u32x4){0u, 0u, 0u, 0u};
            LDS3 char* sp = lds + X_SLOT + hw * 4096;
#pragma unroll
            for (int gq = 0; gq < 3; ++gq) {
                *(LDS3 f32x4*)(sp + gq * 1024 + lane * 16) = (f32x4){k0[4 * gq], k0[4 * gq + 1], k0[4 * gq + 2], k0[4 * gq + 3]};
                if (r < 8) *(LDS3 f32x4*)(sp + 3072 + (gq * 16 + h * 8 + r) * 16) = (f32x4){k1[4 * gq], k1[4 * gq + 1], k1[4 * gq + 2], k1[4 * gq + 3]};
            }
            asm volatile("s_waitcnt vmcnt(0) lgkmcnt(0)" ::: "memory");
            unsigned arr = 0;
            if (lane == 0) arr = LDS_CNT_ADD(C_KV);
            if (__builtin_amdgcn_readfirstlane(arr) == 3) {
                __builtin_amdgcn_fence(__ATOMIC_ACQUIRE, "wavefront");
#pragma unroll
                for (int i = 0; i < 4; ++i) {
                    const int qd = lane + 64 * i;
                    LDS3 const char* s0 = lds + X_SLOT + qd * 16;
                    const f32x4 s = (*(LDS3 const f32x4*)s0 + *(LDS3 const f32x4*)(s0 + 4096)) + (*(LDS3 const f32x4*)(s0 + 8192) + *(LDS3 const f32x4*)(s0 + 12288));
                    if (qd < 240) ST16(__builtin_bit_cast(u32x4, s), WS_KVP + (head * 8 + qb) * 4096 + qd * 16);
                }
                DRAIN();
                if (lane == 0) set_flag(ws, WS_F_KV + (head * 8 + qb) * 8, tag);
            }
#pragma unroll
            for (int i = 0; i < 2; ++i) feature_map(phm, pht, eq[i], eqt[i], h, qf[i][0], qf[i][1], qf[i][2]);
        }
        STAMP(4);
        POLL_ONE2(hw == 0, 4, HS, WS_F_KV + (head * 8 + (lane & 7)) * 8, WS_F_KV + (head * 8 + (lane & 7)) * 8, C_P2);
        {
            const int qd = hw * 60 + (lane < 60 ? lane : 59);
            f32x4 pp[8];
#pragma unroll
            for (int p = 0; p < 8; ++p) pp[p] = __builtin_bit_cast(f32x4, LD16(WS_KVP + (head * 8 + p) * 4096 + qd * 16));
            const f32x4 s4 = ((pp[0] + pp[1]) + (pp[2] + pp[3])) + ((pp[4] + pp[5]) + (pp[6] + pp[7]));
            const int gq = qd < 192 ? qd >> 6 : (qd - 192) >> 4, hs = qd < 192 ? (qd >> 5) & 1 : (qd >> 3) & 1, sg = qd < 192 ? qd & 31 : 32 + (qd & 7);
            const unsigned a = pk2(s4[0], s4[1]), b = pk2(s4[2], s4[3]);
            LDS3 u16* kp = (LDS3 u16*)(lds + X_KVF + (8 * gq + 4 * hs) * 96 + sg * 2);
            kp[0] = (u16)a; kp[48] = (u16)(a >> 16); kp[96] = (u16)b; kp[144] = (u16)(b >> 16);
            asm volatile("s_waitcnt lgkmcnt(0)" ::: "memory");
            if (lane == 0) LDS_CNT_ADD(C_KVF);
            lds_wait<2>(lds + C_KVF, 4);
        }
        {
            LDS3 const char* kp = lds + X_KVF + (r < 24 ? r : 23) * 96 + h * 16;
            const u32x4 a0 = *(LDS3 const u32x4*)kp, a1 = *(LDS3 const u32x4*)(kp + 32), a2 = *(LDS3 const u32x4*)(kp + 64);
#pragma unroll
            for (int i = 0; i < 2; ++i) {
                f32x16 d = MFMA32_16(a0, qf[i][0], z16); d = MFMA32_16(a1, qf[i][1], d); d = MFMA32_16(a2, qf[i][2], d);
                const float den = upper_half(d[8]) + 1e-6f;
                float o[12];
#pragma unroll
                for (int j = 0; j < 12; ++j) o[j] = d[j];
                store_o(rs, o, (1.f - gS[i]) / den, tokS + i * 32 + r, 1, head, h);
            }
            DRAIN();
            unsigned arr = 0;
            if (lane == 0) arr = LDS_CNT_ADD(C_OB);
            if (__builtin_amdgcn_readfirstlane(arr) == 3 && lane == 0) set_flag(ws, WS_F_OB + (qb * 32 + head) * 8, tag);
        }
        STAMP(5);
        f32x4 c[5];
#pragma unroll
        for (int n = 0; n < 5; ++n) c[n] = (f32x4){0.f, 0.f, 0.f, 0.f};
        const char* wsrc = ws + WS_WT + (size_t)ntO * 5 * 40960;
        {
            if (hw == 0) wait_flags<8>(ws, WS_F_KV + lane * 8, WS_F_KV + 512 + lane * 8, tag);
            POLL_ONE2(hw == 0, 8, HS, WS_F_KV + 1024 + lane * 8, WS_F_KV + 1536 + lane * 8, C_P5);
#pragma unroll 1
            for (int t = 0; t < 5; ++t) {
                if (t == 0) prog_wait<2 * HS>(lds + C_PR, lane, 4); else if (t == 3) prog_wait<2 * HS>(lds + C_PR, lane, 5);
                const int ks = hw + 4 * t < 19 ? hw + 4 * t : 18;
#pragma unroll
                for (int n = 0; n < 5; ++n) glds16s(lane * 16, wsrc + (n * 40 + 20 + ks) * 1024, lds0 + 4096 + (ks * 5 + n) * 1024);
            }
            POLL_ONE2(hw == 0, 4, HS, WS_F_OB + ((mtO >> 2) * 32 + r) * 8, WS_F_OB + ((mtO >> 2) * 32 + r) * 8, C_P3);
            const unsigned ab = WS_AO + ((mtO * 4 + hw) * 40 + 20) * 1024;
            u32x4 af[20];
#pragma unroll
            for (int ks = 0; ks < 16; ++ks) af[ks] = LD16(ab + ks * 1024 + lane * 16);
#pragma unroll
            for (int ks = 16; ks < 20; ++ks) LD_TAIL(af[ks], ab + ks * 1024);
            asm volatile("s_waitcnt vmcnt(20)" ::: "memory");
            if (lane == 0) LDS_CNT_ADD(C_BX);
            lds_wait<2>(lds + C_BX, 4);
            u32x4 wf[3][5], wl[5];
            LDS3 const char* wp = lds + 4096 + lane * 16;
#pragma unroll
            for (int ks = 0; ks < 2; ++ks)
#pragma unroll
                for (int n = 0; n < 5; ++n) wf[ks][n] = *(LDS3 const u32x4*)(wp + (ks * 5 + n) * 1024);
#pragma unroll
            for (int ks = 0; ks < 20; ++ks) {
                if (ks + 2 < 19) {
#pragma unroll
                    for (int n = 0; n < 5; ++n) wf[(ks + 2) % 3][n] = *(LDS3 const u32x4*)(wp + ((ks + 2) * 5 + n) * 1024);
                }
                if (ks == 8) {
#pragma unroll
                    for (int n = 0; n < 5; ++n) wl[n] = LD16(WS_WT + ((ntO * 5 + n) * 40 + 39) * 1024 + lane * 16);
                }
                SBAR();
#pragma unroll
                for (int n = 0; n < 5; ++n) c[n] = MFMA16_32(ks < 19 ? wf[ks % 3][n] : wl[n], af[ks], c[n]);
                SBAR();
            }
            if (lane == 0) LDS_CNT_ADD(C_BX);
            lds_wait<2>(lds + C_BX, 8);
        }
        STAMP(6);
        prog_wait<2 * HS>(lds + C_PR, lane, 7);
#pragma unroll 1
        for (int n = 0; n < 5; ++n)
#pragma unroll
            for (int t = 0; t < 5; ++t) { const int ks = hw + 4 * t; glds16s(lane * 16, wsrc + (n * 40 + ks) * 1024, lds0 + L_WA + (n * 20 + ks) * 1024); }
#pragma unroll
        for (int n = 0; n < 3; ++n) *(LDS3 f32x4*)(lds + X_FIN + hw * 3072 + n * 1024 + lane * 16) = c[n];
        DRAIN();
        if (lane == 0) LDS_CNT_ADD(C_WA);
        prog_wait<2 * HS>(lds + C_PR, lane, 8);
#pragma unroll
        for (int n = 3; n < 5; ++n) *(LDS3 f32x4*)(lds + 143360 + hw * 2048 + (n - 3) * 1024 + lane * 16) = c[n];
        asm volatile("s_waitcnt lgkmcnt(0)" ::: "memory");
        if (lane == 0) __hip_atomic_store((LDS3 unsigned*)(lds + C_BP + hw * 4), 1u, __ATOMIC_RELAXED, __HIP_MEMORY_SCOPE_WORKGROUP);
        STAMP(7);
    } else {
        const int qs = w & 3, kh = w >> 2;
        const int q0 = qb * 256 + qs * 64;
        GET_TAG();
        if (bid < 240) {
            const int part = nt / 5, c0 = (nt - part * 5) * 128;
            const int wm = w & 1, wn = w >> 1;
            constexpr int RB = 32768;
            const int xr = wm * 64 + r;
            const unsigned xa = RB + xr * 128 + ((h ^ ((xr >> 1) & 7)) << 4);
            unsigned wt[2];
#pragma unroll
            for (int t = 0; t < 2; ++t) {
                const int q = (lane & 15) >> 2, p = lane & 3, blk = (lane >> 4) & 1, row = 8 * h + 4 * t + q;
                wt[t] = RB + 16384 + row * 256 + (((4 * wn + 2 * blk + (p >> 1)) ^ ((q << 2) | (2 * h + t))) << 4) + 8 * (p & 1);
            }
            auto gbody = [&](auto trc) {
                constexpr bool TR = decltype(trc)::value;
                u32x4 xf[2][8], wf[2][4];
                auto rdk = [&](auto bc, LDS3 const char* sx, LDS3 const char* sb, int ks) {
                    constexpr int B = decltype(bc)::value;
                    xf[B][2 * ks] = *(LDS3 const u32x4*)(sx + (xa ^ (ks * 32)));
                    xf[B][2 * ks + 1] = *(LDS3 const u32x4*)(sx + ((xa + 4096) ^ (ks * 32)));
                    const u32x2 lo = trread(sb + wt[0] + ks * 4096), hi = trread(sb + wt[1] + ks * 4096);
                    wf[B][ks] = (u32x4){lo[0], lo[1], hi[0], hi[1]};
                };
                f32x16 acc0, acc1;
                auto step = [&](auto bc, int s) {
                    constexpr int B = decltype(bc)::value;
                    if (s == GA_PUB) {
                        DRAIN();
                        unsigned arr = 0;
                        if (lane == 0) arr = LDS_CNT_ADD(G_ST);
                        if (__builtin_amdgcn_readfirstlane(arr) == 7 && lane == 0) set_flag(ws, fG, tag);
                    }
                    __builtin_amdgcn_s_waitcnt(0xC07F);
                    if (s < 18) BAR();
                    SBAR();
                    const int s1 = s + 1;
                    LDS3 const char* sb = lds + __builtin_amdgcn_readfirstlane((s1 & 3) * 32768);
                    LDS3 const char* sx = lds + __builtin_amdgcn_readfirstlane(((s1 >= 10 ? 20 - s1 : s1 + 1) & 3) * 32768);
#define GM(A, i) do { if constexpr (TR) A = MFMA32_16(wf[B][(i) >> 1], xf[B][i], A); else A = MFMA32_16(xf[B][i], wf[B][(i) >> 1], A); SBAR(); } while (0)
#define GX(i) do { xf[B ^ 1][i] = *(LDS3 const u32x4*)(sx + ((xa + ((i) & 1) * 4096) ^ (((i) >> 1) * 32))); } while (0)
#define GW(ks) do { const u32x2 lo = trread(sb + wt[0] + (ks) * 4096), hi = trread(sb + wt[1] + (ks) * 4096); wf[B ^ 1][ks] = (u32x4){lo[0], lo[1], hi[0], hi[1]}; } while (0)
                    GM(acc0, 0); GX(0); GW(0); SBAR(); GM(acc1, 1); GX(1); GX(2); SBAR(); GM(acc0, 2); GW(1); GX(3); SBAR(); GM(acc1, 3); GX(4); GW(2); SBAR();
                    GM(acc0, 4); GX(5); GX(6); SBAR(); GM(acc1, 5); GW(3); GX(7); SBAR(); GM(acc0, 6); GM(acc1, 7);
#undef GX
#undef GW
#undef GM
                };
                WAIT_BAR(63);
#pragma unroll
                for (int ks = 0; ks < 4; ++ks) rdk(std::integral_constant<int, 0>{}, lds + 32768, lds, ks);
#pragma unroll 1
                for (int mat = 0; mat < 2; ++mat) {
                    acc0 = (f32x16){}; acc1 = (f32x16){};
#pragma unroll 1
                    for (int kk = 0; kk < 10; kk += 2) {
                        step(std::integral_constant<int, 0>{}, mat * 10 + kk);
                        step(std::integral_constant<int, 1>{}, mat * 10 + kk + 1);
                    }
                    const int cpos = 2 * (mt & 7) + (mt >> 3);
                    if constexpr (TR) {
                        const float sc = (part == 0 && mat == 0) ? QSCALE : 1.f;
                        const unsigned base = part == 0 ? WS_Q + mat * NH * 81920 + mt * 5120 : WS_KV + mat * NH * 163840 + cpos * 10240;
                        const unsigned hs = part == 0 ? 81920 : 163840;
#pragma unroll
                        for (int pr = 0; pr < 2; ++pr) {
                            const int G8 = ((c0 + 32 * wn) >> 3) + 2 * pr + h, pp = G8 / 5, gi = G8 - pp * 5;
                            const unsigned hb0 = base + (2 * pp + (gi > 2)) * hs;
#pragma unroll
                            for (int i = 0; i < 2; ++i) {
                                const f32x16& a = i ? acc1 : acc0;
                                const int tl = wm * 64 + i * 32 + r;
                                const auto s0 = __builtin_amdgcn_permlane32_swap(pk2(a[8 * pr] * sc, a[8 * pr + 1] * sc), pk2(a[8 * pr + 4] * sc, a[8 * pr + 5] * sc), false, false);
                                const auto s1 = __builtin_amdgcn_permlane32_swap(pk2(a[8 * pr + 2] * sc, a[8 * pr + 3] * sc), pk2(a[8 * pr + 6] * sc, a[8 * pr + 7] * sc), false, false);
                                if (gi != 2) { const u32x4 ov = {s0[0], s1[0], s0[1], s1[1]}; ST16(ov, hb0 + (tl >> 5) * 1024 + (gi > 2 ? gi - 3 : gi) * 512 + r * 16); }
                                else { const u32x2 o0 = {s0[0], s1[0]}, o1 = {s0[1], s1[1]}; ST8(o0, hb0 + 4096 + tl * 8); ST8(o1, hb0 + hs + 4096 + tl * 8); }
                            }
                        }
                    } else {
                        const int n = c0 + 32 * wn + r, hd = n / 20, d = n - hd * 20, rho = (hd & 1) ? (d < 4 ? d + 16 : d - 4) : d;
                        const unsigned base = WS_KV + (mat * NH + hd) * 163840 + cpos * 10240 + 5120 + h * 16 + rho * 32;
#pragma unroll
                        for (int i = 0; i < 2; ++i)
#pragma unroll
                            for (int s2 = 0; s2 < 2; ++s2) {
                                const f32x16& a = i ? acc1 : acc0;
                                u32x4 ov = {pk2(a[8 * s2], a[8 * s2 + 1]), pk2(a[8 * s2 + 2], a[8 * s2 + 3]), pk2(a[8 * s2 + 4], a[8 * s2 + 5]), pk2(a[8 * s2 + 6], a[8 * s2 + 7])};
                                ST16(ov, base + (wm * 4 + i * 2 + s2) * 640);
                            }
                    }
                    if (mat == 0) STAMP(1);
                }
            };
            if (part < 2) gbody(std::true_type{}); else gbody(std::false_type{});
        } else {
            const int sj = bid - 240;
            WAIT_BAR(63);
            {
                const u32x4 ga = *(const u32x4*)(P.gw + lane * 8), gb = *(const u32x4*)(P.gw + 512 + (lane & 15) * 8);
                u32x4 xa[16], xb[4];
                const u16* xr = P.x + (size_t)(sj * 128 + w * 16) * DM;
#pragma unroll
                for (int rr = 0; rr < 16; ++rr) xa[rr] = *(const u32x4*)(xr + rr * DM + lane * 8);
#pragma unroll
                for (int i = 0; i < 4; ++i) xb[i] = *(const u32x4*)(xr + (i * 4 + (lane >> 4)) * DM + 512 + (lane & 15) * 8);
                float res = 0.f;
#pragma unroll
                for (int rr = 0; rr < 16; ++rr) {
                    float s = 0.f, s2 = 0.f;
#pragma unroll
                    for (int j = 0; j < 4; ++j) { s += bf_lo(xa[rr][j]) * bf_lo(ga[j]) + bf_hi(xa[rr][j]) * bf_hi(ga[j]); s2 += bf_lo(xb[rr >> 2][j]) * bf_lo(gb[j]) + bf_hi(xb[rr >> 2][j]) * bf_hi(gb[j]); }
                    if ((lane >> 4) == (rr & 3)) s += s2;
#pragma unroll
                    for (int o = 32; o >= 1; o >>= 1) s += __shfl_xor(s, o);
                    if (lane == rr) res = s;
                }
                if (lane < 16) ST4(__float_as_uint(1.f / (1.f + __expf(-res))), WS_G + (sj * 128 + w * 16 + lane) * 4);
            }
            DRAIN();
            unsigned arr = 0;
            if (lane == 0) arr = LDS_CNT_ADD(G_ST);
            if (__builtin_amdgcn_readfirstlane(arr) == 7 && lane == 0) set_flag(ws, WS_F_SD + sj * 8, tag);
        }
        STAMP(2);
        const unsigned hb = lds0 + kh * 10240;
        const unsigned laneK = hb + lane * 16, laneT = hb + 4096 + r * 8 + h * 4, laneV = hb + 5120 + h * 16 + (r < 20 ? r : 19) * 32;
        const u64 vmask = 0x000fffff000fffffull;
        u32x4 v0a = {one, one, one, one}, v0b = v0a, v1 = v0a;
        u32x4 qA, qB, ka; unsigned qtA, qtB, kt;
        {
            lds_wait(lds + G_Q, 4);
            LDS3 const char* qp = lds + L_Q + (qs >> 1) * 5120;
            const unsigned om = (qs & 1) * 2048 + lane * 16, ot = 4096 + ((qs & 1) * 64 + r) * 8 + 4 * h;
            qA = *(LDS3 const u32x4*)(qp + om); qB = *(LDS3 const u32x4*)(qp + om + 1024);
            qtA = *(LDS3 const unsigned*)(qp + ot); qtB = *(LDS3 const unsigned*)(qp + ot + 256);
            DSR128(ka, kh ? laneK : lds0 + L_Q + 10240 + lane * 16, 0); DSR32(kt, laneT, 0); DSR128M2(v0a, laneV, 0, v1, laneV, 640);
            asm volatile("s_waitcnt lgkmcnt(0)" : "+v"(ka), "+v"(kt), "+v"(v0a), "+v"(v1), "+v"(qA), "+v"(qB), "+v"(qtA), "+v"(qtB) :: "memory");
            if (lane == 0) LDS_CNT_ADD(G_RD);
        }
        STAMP(5);
        float mA, mB; u32x2 qTA, qTB; f32x16 sA, sB;
        {
            const u32x2 t0 = {kt, 0x3f80u}, a0 = {qtA, 0u}, b0 = {qtB, 0u};
            const f32x16 s = MFMA32_16(ka, qA, z16);
            const f32x16 s0 = MFMA32_8(t0, a0, s);
            f32x16 s1 = MFMA32_16(ka, qB, z16); s1 = MFMA32_8(t0, b0, s1);
            float x0 = s0[0], x1 = s1[0];
#pragma unroll
            for (int i = 1; i < 16; ++i) { x0 = fmaxf(x0, s0[i]); x1 = fmaxf(x1, s1[i]); }
            x0 = -xhalf_max(x0); x1 = -xhalf_max(x1);
            const unsigned hh = pk2(x0, x1);
            const unsigned ll = pk2(x0 - bf_lo(hh), x1 - bf_hi(hh));
            mA = -(bf_lo(hh) + bf_lo(ll)); mB = -(bf_hi(hh) + bf_hi(ll));
            const unsigned sel = h ? ll : hh;
            qTA = (u32x2){qtA, sel & 0xffffu}; qTB = (u32x2){qtB, sel >> 16};
            sA = MFMA32_8(t0, qTA, s);
        }
        f32x16 oA = {}, oB = {};
        u32x4 pa, pb = {0u, 0u, 0u, 0u};
#define EX4(S, i, Pv, j) do { Pv[j] = pk2(ex2(S[i]), ex2(S[i + 1])); Pv[j + 1] = pk2(ex2(S[i + 2]), ex2(S[i + 3])); PIN(Pv); SBAR(); } while (0)
#define KT2 ((u32x2){kt, 0x3f80u})
#define TILE(V0C, V0N, aK1, oK1, aT1, oT1, oV1, aV0, oV0) do { \
            sB = MFMA32_16(ka, qB, z16); SBAR(); DSR128(ka, aK1, oK1); EX4(sA, 0, pa, 0); \
            oB = MFMA32_16(v1, pb, oB); SBAR(); DSR128M2(v1, aV, oV1, V0N, aV0, oV0); EX4(sA, 4, pa, 2); \
            sB = MFMA32_8(KT2, qTB, sB); SBAR(); DSR32(kt, aT1, oT1); EX4(sA, 8, pb, 0); \
            oA = MFMA32_16(V0C, pa, oA); SBAR(); EX4(sA, 12, pb, 2); \
            LWAIT(1); sA = MFMA32_16(ka, qA, z16); SBAR(); EX4(sB, 0, pa, 0); \
            oA = MFMA32_16(v1, pb, oA); SBAR(); EX4(sB, 4, pa, 2); \
            LWAIT(0); sA = MFMA32_8(KT2, qTA, sA); SBAR(); EX4(sB, 8, pb, 0); \
            oB = MFMA32_16(V0C, pa, oB); SBAR(); EX4(sB, 12, pb, 2); \
        } while (0)
        unsigned aK = laneK, aT = laneT, aV = laneV;
#pragma unroll 1
        for (int c = 0; c < 8; ++c) {
            if (c >= 1 && c <= 3) lds_wait(lds + (c == 1 ? C_KVL : (c == 2 ? C_KVL + 12 : C_WA + 8)), 4);
            if (c >= 1) {
                unsigned pp;
                asm volatile("ds_write_b32 %1, %2\n\tds_read_b32 %0, %3\n\ts_waitcnt lgkmcnt(0)" : "=&v"(pp) : "v"(lds0 + C_PR + w * 4), "v"(c), "v"(lds0 + C_PR + (w ^ 4) * 4) : "memory");
                const int pc = __builtin_amdgcn_readfirstlane(pp);
                if (pc > c || (pc == c && ((c ^ kh) & 1))) __builtin_amdgcn_s_setprio(1); else __builtin_amdgcn_s_setprio(0);
            } else if (kh) __builtin_amdgcn_s_setprio(1);
            const unsigned nx = (c < 7 ? c + 1 : 7) * 20480;
            const unsigned nK = laneK + nx, nT = laneT + nx, nV = laneV + nx;
            SBAR();
            TILE(v0a, v0b, aK, 1024, aT, 256, 640,  aV, 1280);
            TILE(v0b, v0a, aK, 2048, aT, 512, 1920, aV, 2560);
            if (c == 0) {
                DRAIN();
                unsigned arr = 0;
                if (lane == 0) arr = LDS_CNT_ADD(C_GB);
                if (__builtin_amdgcn_readfirstlane(arr) == 7 && lane == 0 && bid < 240) set_flag(ws, fG + 2048, tag);
                asm volatile("s_waitcnt lgkmcnt(0)" ::: "memory");
                SBAR();
            }
            TILE(v0a, v0b, aK, 3072, aT, 768, 3200, aV, 3840);
            TILE(v0b, v0a, nK, 0,    nT, 0,   4480, nV, 0);
            aK = nK; aT = nT; aV = nV;
        }
        oB = MFMA32_16(v1, pb, oB);
#undef TILE
#undef EX4
        if (lane == 0) __hip_atomic_store((LDS3 unsigned*)(lds + C_PR + w * 4), 8u, __ATOMIC_RELAXED, __HIP_MEMORY_SCOPE_WORKGROUP);
        STAMP(6);
        {
            float chk = 0.f;
#pragma unroll
            for (int i = 0; i < 12; ++i) chk += fabsf(oA[i]) * 0.f + fabsf(oB[i]) * 0.f;
            bool bad = !(chk == 0.f);
            if (h) bad = bad || !(oA[8] >= 0x1p-60f && oA[8] <= 0x1p60f && oB[8] >= 0x1p-60f && oB[8] <= 0x1p60f);
            if (__builtin_expect(__any(bad), 0)) {
                mA = -INFINITY; mB = -INFINITY;
#pragma unroll
                for (int i = 0; i < 16; ++i) { oA[i] = 0.f; oB[i] = 0.f; }
                const u32x2 a0 = {qtA, 0u}, b0 = {qtB, 0u};
#pragma unroll 1
                for (int t = 0; t < 32; ++t) {
                    const unsigned kb = WS_KV + head * 163840 + (2 * (t >> 2) + kh) * 10240, t4 = t & 3;
                    const u32x4 k0 = LD16(kb + t4 * 1024 + lane * 16);
                    const u32x2 t0 = {LD4(kb + 4096 + (t4 * 32 + r) * 8 + 4 * h), 0u};
                    u32x4 w0 = LD16(kb + 5120 + t4 * 1280 + h * 16 + (r < 20 ? r : 19) * 32), w1 = LD16(kb + 5120 + t4 * 1280 + 640 + h * 16 + (r < 20 ? r : 19) * 32);
                    if (r >= 20) { w0 = (u32x4){one, one, one, one}; w1 = w0; }
                    f32x16 s0 = MFMA32_16(k0, qA, z16); s0 = MFMA32_8(t0, a0, s0);
                    f32x16 s1 = MFMA32_16(k0, qB, z16); s1 = MFMA32_8(t0, b0, s1);
                    float x0 = s0[0], x1 = s1[0];
#pragma unroll
                    for (int i = 1; i < 16; ++i) { x0 = fmaxf(x0, s0[i]); x1 = fmaxf(x1, s1[i]); }
                    x0 = fmaxf(xhalf_max(x0), mA); x1 = fmaxf(xhalf_max(x1), mB);
                    const float g0 = (mA == x0) ? 1.f : ex2(mA - x0), g1 = (mB == x1) ? 1.f : ex2(mB - x1);
                    mA = x0; mB = x1;
#pragma unroll
                    for (int i = 0; i < 16; ++i) { oA[i] *= g0; oB[i] *= g1; }
#pragma unroll
                    for (int i = 0; i < 4; ++i) { pa[i] = pk2(ex2(s0[2 * i] - x0), ex2(s0[2 * i + 1] - x0)); pb[i] = pk2(ex2(s0[8 + 2 * i] - x0), ex2(s0[9 + 2 * i] - x0)); }
                    oA = MFMA32_16(w0, pa, oA); oA = MFMA32_16(w1, pb, oA);
#pragma unroll
                    for (int i = 0; i < 4; ++i) { pa[i] = pk2(ex2(s1[2 * i] - x1), ex2(s1[2 * i + 1] - x1)); pb[i] = pk2(ex2(s1[8 + 2 * i] - x1), ex2(s1[9 + 2 * i] - x1)); }
                    oB = MFMA32_16(w0, pa, oB); oB = MFMA32_16(w1, pb, oB);
                }
            }
        }
        asm volatile(".rept 128\n\ts_branch 1f\n\t.fill 15, 4, 0xbf800000\n1:\n\t.endr" ::: "memory");
        lds_wait<4>(lds + C_BX, 8);
        const float gO = *(LDS3 const float*)(lds + X_G + (qs * 64 + kh * 32 + r) * 4);
        {
            LDS3 char* xp = lds + X_MRG + w * 2816;
            const f32x16& s = kh ? oA : oB;
            *(LDS3 f32x4*)(xp + lane * 16) = (f32x4){s[0], s[1], s[2], s[3]};
            *(LDS3 f32x4*)(xp + 1024 + lane * 16) = (f32x4){s[4], s[5], s[6], s[7]};
            if (!h) *(LDS3 f32x4*)(xp + 2048 + r * 16) = (f32x4){s[8], s[9], s[10], s[11]};
            else *(LDS3 f32x2*)(xp + 2560 + r * 8) = (f32x2){s[8], kh ? mA : mB};
            asm volatile("s_waitcnt lgkmcnt(0)" ::: "memory");
            if (lane == 0) __hip_atomic_store((LDS3 unsigned*)(lds + C_MRG + w * 4), 1u, __ATOMIC_RELAXED, __HIP_MEMORY_SCOPE_WORKGROUP);
        }
        lds_wait<MS>(lds + C_MRG + (w ^ 4) * 4, 1);
        bool lastw;
        {
            LDS3 const char* xp = lds + X_MRG + (w ^ 4) * 2816;
            const f32x4 p0 = *(LDS3 const f32x4*)(xp + lane * 16), p1 = *(LDS3 const f32x4*)(xp + 1024 + lane * 16), p2 = *(LDS3 const f32x4*)(xp + 2048 + r * 16);
            const f32x2 lm = *(LDS3 const f32x2*)(xp + 2560 + r * 8);
            const f32x16& s = kh ? oB : oA;
            const float mo = kh ? mB : mA, mp = lm[1];
            const float mx = fmaxf(mo, mp);
            const float fo = (mo == mx) ? 1.f : ex2(mo - mx), fq = (mp == mx) ? 1.f : ex2(mp - mx);
            float o[12];
#pragma unroll
            for (int i = 0; i < 4; ++i) { o[i] = s[i] * fo + p0[i] * fq; o[4 + i] = s[4 + i] * fo + p1[i] * fq; o[8 + i] = s[8 + i] * fo + p2[i] * fq; }
            const float l = upper_half(s[8]) * fo + lm[0] * fq;
            store_o(rs, o, gO / l, q0 + kh * 32 + r, 0, head, h);
            DRAIN();
            unsigned arr = 0;
            if (lane == 0) arr = LDS_CNT_ADD(C_OA);
            lastw = __builtin_amdgcn_readfirstlane(arr) == 7;
            if (lastw && lane == 0) set_flag(ws, WS_F_OA + (qb * 32 + head) * 8, tag);
        }
        STAMP(7);
        {
            const int c16 = lane & 15, g = lane >> 4, tt = w & 3, k0 = kh * 10;
            lds_wait(lds + C_WA, 4);
            LDS3 const char* wp = lds + L_WA + k0 * 1024 + lane * 16;
            u32x4 wf[4][5];
#pragma unroll
            for (int ks = 0; ks < 4; ++ks)
#pragma unroll
                for (int n = 0; n < 5; ++n) wf[ks][n] = *(LDS3 const u32x4*)(wp + (n * 20 + ks) * 1024);
            POLL_ONE(lastw, 2, WS_F_OA + ((mtO >> 2) * 32 + r) * 8, WS_F_OA + ((mtO >> 2) * 32 + r) * 8, C_P4);
            STAMP(9);
            const unsigned ab = WS_AO + ((mtO * 4 + tt) * 40 + k0) * 1024;
            u32x4 af[10];
#pragma unroll
            for (int ks = 0; ks < 6; ++ks) af[ks] = LD16(ab + ks * 1024 + lane * 16);
            if (kh) {
#pragma unroll
                for (int ks = 6; ks < 10; ++ks) LD_TAIL(af[ks], ab + ks * 1024);
            } else {
#pragma unroll
                for (int ks = 6; ks < 10; ++ks) af[ks] = LD16(ab + ks * 1024 + lane * 16);
            }
            f32x4 c[5];
#pragma unroll
            for (int n = 0; n < 5; ++n) c[n] = (f32x4){0.f, 0.f, 0.f, 0.f};
            SBAR();
#pragma unroll
            for (int ks = 0; ks < 10; ++ks) {
#pragma unroll
                for (int n = 0; n < 5; ++n) c[n] = MFMA16_32(wf[ks & 3][n], af[ks], c[n]);
                SBAR();
                if (ks + 4 < 10) {
#pragma unroll
                    for (int n = 0; n < 5; ++n) wf[ks & 3][n] = *(LDS3 const u32x4*)(wp + (n * 20 + ks + 4) * 1024);
                }
                SBAR();
            }
            LDS3 char* xp = lds + X_MRG + tt * 5120 + lane * 16;
            if (w < 4) { *(LDS3 f32x4*)(xp + 3072) = c[3]; *(LDS3 f32x4*)(xp + 4096) = c[4]; }
            else { *(LDS3 f32x4*)(xp) = c[0]; *(LDS3 f32x4*)(xp + 1024) = c[1]; *(LDS3 f32x4*)(xp + 2048) = c[2]; }
            asm volatile("s_waitcnt lgkmcnt(0)" ::: "memory");
            if (lane == 0) __hip_atomic_store((LDS3 unsigned*)(lds + C_FIN + w * 4), 1u, __ATOMIC_RELAXED, __HIP_MEMORY_SCOPE_WORKGROUP);
            lds_wait(lds + C_FIN + (w ^ 4) * 4, 1);
            lds_wait(lds + C_BP + tt * 4, 1);
            u16* op = P.out + (size_t)(mtO * 64 + tt * 16 + c16) * DM + ntO * 80 + g * 4;
#pragma unroll
            for (int n = 0; n < 5; ++n) {
                if ((w < 4) == (n < 3)) {
                    const f32x4 bp = *(LDS3 const f32x4*)(n < 3 ? lds + X_FIN + tt * 3072 + n * 1024 + lane * 16 : lds + 143360 + tt * 2048 + (n - 3) * 1024 + lane * 16);
                    const f32x4 s = c[n] + *(LDS3 const f32x4*)(xp + n * 1024) + bp;
                    *(u32x2*)(op + n * 16) = (u32x2){pk2(s[0], s[1]), pk2(s[2], s[3])};
                }
            }
            if (bid == 0 && mtid == 0) set_flag(ws, WS_EPOCH, tag);
        }
        STAMP(10);
    }
}

extern "C" void kernel_launch(void* const* d_in, const int* in_sizes, int n_in, void* d_out, int out_size, void* d_ws, size_t ws_size,
                              hipStream_t stream) {
    Params P;
    P.x = (const u16*)d_in[0]; P.wa = (const u16*)d_in[1]; P.woa = (const u16*)d_in[2]; P.wb = (const u16*)d_in[4];
    P.wphi = (const u16*)d_in[5]; P.wob = (const u16*)d_in[6]; P.gw = (const u16*)d_in[7]; P.ws = (char*)d_ws; P.out = (u16*)d_out;
    mega<<<256, 768, 0, stream>>>(P);
}
```

```cpp
#include <hip/hip_runtime.h>
#include <hip/hip_bf16.h>
#include <stdint.h>
#include <type_traits>

typedef __attribute__((ext_vector_type(8))) short bf16x8;
typedef __attribute__((ext_vector_type(4))) short bf16x4;
typedef __attribute__((ext_vector_type(16))) float f32x16;
typedef __attribute__((ext_vector_type(4))) float f32x4;
typedef __attribute__((ext_vector_type(2))) float f32x2;
typedef __attribute__((ext_vector_type(4))) unsigned u32x4;
typedef __attribute__((ext_vector_type(2))) unsigned u32x2;
typedef __attribute__((ext_vector_type(2))) __bf16 bf2_t;
typedef unsigned short u16;
typedef unsigned long long u64;
#define LDS3 __attribute__((address_space(3)))
typedef __attribute__((address_space(1))) u64 gu64;
#define RLX_AGENT __ATOMIC_RELAXED, __HIP_MEMORY_SCOPE_AGENT

constexpr int GA_PUB = 11, HS = 8, MS = 2, PRE_SLEEP = 2;
constexpr int SEQ = 2048, DM = 640, NH = 32;
constexpr float LOG2E = 1.4426950408889634f;
constexpr float QSCALE = 0.22360679774997896f * LOG2E;

constexpr unsigned WS_EPOCH = 0;
constexpr unsigned WS_F_G   = 256;
constexpr unsigned WS_F_SD  = WS_F_G + 4096;
constexpr unsigned WS_F_KV  = WS_F_SD + 256;
constexpr unsigned WS_F_OB  = WS_F_KV + 2048;
constexpr unsigned WS_F_OA  = WS_F_OB + 2048;
constexpr unsigned WS_G     = 16384;
constexpr unsigned WS_Q     = 32768;
constexpr unsigned WS_KV    = WS_Q + 2 * NH * 81920;
constexpr unsigned WS_KVP   = WS_KV + 2 * NH * 163840;
constexpr unsigned WS_AO    = WS_KVP + NH * 8 * 4096;
constexpr unsigned WS_WT    = WS_AO + SEQ * 1280 * 2;
constexpr unsigned WS_END   = WS_WT + 1280 * 640 * 2;

#define STAMP(i)

__device__ __forceinline__ unsigned pk2(float a, float b) { f32x2 v = {a, b}; bf2_t r = __builtin_convertvector(v, bf2_t); return __builtin_bit_cast(unsigned, r); }
__device__ __forceinline__ float bf_lo(unsigned u) { return __uint_as_float(u << 16); }
__device__ __forceinline__ float bf_hi(unsigned u) { return __uint_as_float(u & 0xffff0000u); }
__device__ __forceinline__ float ex2(float x) { return __builtin_amdgcn_exp2f(x); }
#define MFMA32_16(a, b, c) __builtin_amdgcn_mfma_f32_32x32x16_bf16(__builtin_bit_cast(bf16x8, a), __builtin_bit_cast(bf16x8, b), c, 0, 0, 0)
#define MFMA32_8(a, b, c) __builtin_amdgcn_mfma_f32_32x32x8bf16_1k(__builtin_bit_cast(bf16x4, a), __builtin_bit_cast(bf16x4, b), c, 0, 0, 0)
#define MFMA16_32(a, b, c) __builtin_amdgcn_mfma_f32_16x16x32_bf16(__builtin_bit_cast(bf16x8, a), __builtin_bit_cast(bf16x8, b), c, 0, 0, 0)

__device__ __forceinline__ void glds16(unsigned voff, const void* sbase, unsigned lds_base) {
    unsigned sv;
    asm volatile("s_mov_b32 %0, m0\n\ts_mov_b32 m0, %3\n\ts_nop 0\n\tglobal_load_lds_dwordx4 %1, %2\n\ts_mov_b32 m0, %0"
                 : "=&s"(sv) : "v"(voff), "s"(sbase), "s"(__builtin_amdgcn_readfirstlane(lds_base)) : "memory");
}
__device__ __forceinline__ void glds16s(unsigned voff, const void* sbase, unsigned lds_base) {
    unsigned sv;
    asm volatile("s_mov_b32 %0, m0\n\ts_mov_b32 m0, %3\n\ts_nop 0\n\tglobal_load_lds_dwordx4 %1, %2 sc1\n\ts_mov_b32 m0, %0"
                 : "=&s"(sv) : "v"(voff), "s"(sbase), "s"(__builtin_amdgcn_readfirstlane(lds_base)) : "memory");
}
__device__ __forceinline__ void glds4s(unsigned voff, const void* sbase, unsigned lds_base) {
    unsigned sv;
    asm volatile("s_mov_b32 %0, m0\n\ts_mov_b32 m0, %3\n\ts_nop 0\n\tglobal_load_lds_dword %1, %2 sc1\n\ts_mov_b32 m0, %0"
                 : "=&s"(sv) : "v"(voff), "s"(sbase), "s"(__builtin_amdgcn_readfirstlane(lds_base)) : "memory");
}
__device__ __forceinline__ void glds16x4(const unsigned (&vo)[4], const void* sbase, unsigned lds_base) {
    unsigned sv;
    asm volatile("s_mov_b32 %0, m0\n\ts_mov_b32 m0, %6\n\ts_nop 0\n\tglobal_load_lds_dwordx4 %1, %5\n\tglobal_load_lds_dwordx4 %2, %5 offset:1024\n\t"
                 "global_load_lds_dwordx4 %3, %5 offset:2048\n\tglobal_load_lds_dwordx4 %4, %5 offset:3072\n\ts_mov_b32 m0, %0"
                 : "=&s"(sv) : "v"(vo[0]), "v"(vo[1]), "v"(vo[2]), "v"(vo[3]), "s"(sbase), "s"(lds_base) : "memory");
}
typedef short v4i16_t __attribute__((ext_vector_type(4)));
__device__ __forceinline__ u32x2 trread(LDS3 const char* p) {
    return __builtin_bit_cast(u32x2, __builtin_amdgcn_ds_read_tr16_b64_v4i16((LDS3 v4i16_t*)p));
}
#define WAIT_BAR(N) asm volatile("s_waitcnt vmcnt(" #N ") lgkmcnt(0)\n\ts_barrier" ::: "memory")
#define DRAIN() asm volatile("s_waitcnt vmcnt(0)" ::: "memory")
#define SBAR() __builtin_amdgcn_sched_barrier(0)
#define PIN(x) asm volatile("" : "+v"(x))

__device__ __forceinline__ float xhalf_max(float x) {
    auto r = __builtin_amdgcn_permlane32_swap(__float_as_uint(x), __float_as_uint(x), false, false);
    return fmaxf(__uint_as_float(r[0]), __uint_as_float(r[1]));
}
__device__ __forceinline__ float xhalf_sum(float x) {
    auto r = __builtin_amdgcn_permlane32_swap(__float_as_uint(x), __float_as_uint(x), false, false);
    return __uint_as_float(r[0]) + __uint_as_float(r[1]);
}
__device__ __forceinline__ float upper_half(float x) {
    auto r = __builtin_amdgcn_permlane32_swap(__float_as_uint(x), __float_as_uint(x), false, false);
    return __uint_as_float(r[1]);
}

typedef __amdgpu_buffer_rsrc_t rsrc_t;
#define ST8(v, off)  __builtin_amdgcn_raw_buffer_store_b64(v, rs, (int)(off), 0, 16)
#define ST16(v, off) __builtin_amdgcn_raw_buffer_store_b128(v, rs, (int)(off), 0, 16)
#define ST4(v, off)  __builtin_amdgcn_raw_buffer_store_b32(v, rs, (int)(off), 0, 16)
#define LD16(off) __builtin_amdgcn_raw_buffer_load_b128(rs, (int)(off), 0, 16)
#define LD4(off)  __builtin_amdgcn_raw_buffer_load_b32(rs, (int)(off), 0, 16)

template <int SLP = 2>
__device__ __forceinline__ void wait_flags(const char* ws, unsigned off0, unsigned off1, u64 tag) {
    gu64* p0 = (gu64*)(ws + off0); gu64* p1 = (gu64*)(ws + off1);
#pragma unroll 1
    for (unsigned spins = 0; spins < (1u << 16); ++spins) {
        const u64 a = __hip_atomic_load(p0, RLX_AGENT), b = __hip_atomic_load(p1, RLX_AGENT);
        if (__all(a == tag && b == tag)) break;
        __builtin_amdgcn_s_sleep(SLP);
    }
    __builtin_amdgcn_fence(__ATOMIC_ACQUIRE, "wavefront");
}
__device__ __forceinline__ void set_flag(char* ws, unsigned off, u64 tag) { __hip_atomic_store((gu64*)(ws + off), tag, RLX_AGENT); }

struct Params { const u16* x; const u16* wa; const u16* wb; const u16* wphi; const u16* woa; const u16* wob; const u16* gw; char* ws; u16* out; };

__device__ __forceinline__ void feature_map(const u32x4& pm, unsigned pt, const u32x4& tm, unsigned tt, int h, u32x4& fp, u32x4& fn, u32x4& ft) {
    const f32x16 z16 = {};
    const u32x2 a8 = {pt, 0u}, b8 = {tt, 0u};
    f32x16 s = MFMA32_16(pm, tm, z16); s = MFMA32_8(a8, b8, s);
    f32x2 v[6];
#pragma unroll
    for (int i = 0; i < 6; ++i) {
        v[i] = (f32x2){s[2 * i], s[2 * i + 1]};
    }
    float mx = fmaxf(v[0][0], v[0][1]), mn = fminf(v[0][0], v[0][1]);
#pragma unroll
    for (int i = 1; i < 6; ++i) { mx = fmaxf(fmaxf(mx, v[i][0]), v[i][1]); mn = fminf(fminf(mn, v[i][0]), v[i][1]); }
    mx = xhalf_max(mx) * -LOG2E; mn = xhalf_max(-mn) * -LOG2E;
    f32x2 ep[6], en[6];
#pragma unroll
    for (int i = 0; i < 6; ++i) {
        const f32x2 a = v[i] * LOG2E + mx, b = v[i] * -LOG2E + mn;
        ep[i] = (f32x2){ex2(a[0]), ex2(a[1])}; en[i] = (f32x2){ex2(b[0]), ex2(b[1])};
    }
    const float tw = h ? 0.f : 1.f;
    const f32x2 s2p = ((ep[0] + ep[1]) + (ep[2] + ep[3])) + (ep[4] + ep[5]) * tw, s2n = ((en[0] + en[1]) + (en[2] + en[3])) + (en[4] + en[5]) * tw;
    const float sp = __builtin_amdgcn_rcpf(xhalf_sum(s2p[0] + s2p[1])), sn = __builtin_amdgcn_rcpf(xhalf_sum(s2n[0] + s2n[1]));
    const float tp = sp * tw, tn = sn * tw;
#pragma unroll
    for (int i = 0; i < 4; ++i) { const f32x2 a = ep[i] * sp, b = en[i] * sn; fp[i] = pk2(a[0], a[1]); fn[i] = pk2(b[0], b[1]); }
    const f32x2 c0 = ep[4] * tp, c1 = ep[5] * tp, c2 = en[4] * tn, c3 = en[5] * tn;
    ft = (u32x4){pk2(c0[0], c0[1]), pk2(c1[0], c1[1]), pk2(c2[0], c2[1]), pk2(c3[0], c3[1])};
}

__device__ __forceinline__ void store_o(rsrc_t rs, const float (&o)[12], float sc, int tok, int half, int head, int h) {
    const auto s0 = __builtin_amdgcn_permlane32_swap(pk2(o[0] * sc, o[1] * sc), pk2(o[4] * sc, o[5] * sc), false, false);
    const auto s1 = __builtin_amdgcn_permlane32_swap(pk2(o[2] * sc, o[3] * sc), pk2(o[6] * sc, o[7] * sc), false, false);
    const unsigned tb = WS_AO + ((tok >> 4) * 40 + half * 20) * 1024;
    const u32x4 mv = {s0[0], s1[0], s0[1], s1[1]};
    ST16(mv, tb + (head >> 1) * 1024 + (((head & 1) * 2 + h) * 16 + (tok & 15)) * 16);
    const u32x2 tv = {pk2(o[8] * sc, o[9] * sc), pk2(o[10] * sc, o[11] * sc)};
    if (!h) ST8(tv, tb + (16 + (head >> 3)) * 1024 + (head & 7) * 128 + (tok & 15) * 8);
}
#define LD8(off)  __builtin_amdgcn_raw_buffer_load_b64(rs, (int)(off), 0, 16)
#define LD_TAIL(dst, base) do { const u32x2 a_ = LD8((base) + (lane >> 4) * 256 + (lane & 15) * 8), b_ = LD8((base) + (lane >> 4) * 256 + 128 + (lane & 15) * 8); dst = (u32x4){a_[0], a_[1], b_[0], b_[1]}; } while (0)

#define DSR128(dst, addr, off) asm volatile("ds_read_b128 %0, %1 offset:%2" : "=v"(dst) : "v"(addr), "i"(off))
#define DSR32(dst, addr, off) asm volatile("ds_read_b32 %0, %1 offset:%2" : "=v"(dst) : "v"(addr), "i"(off))
#define DSR128M2(d0, a0, o0, d1, a1, o1) asm volatile("s_mov_b64 exec, %6\n\tds_read_b128 %0, %2 offset:%3\n\tds_read_b128 %1, %4 offset:%5\n\ts_mov_b64 exec, -1" \
                                                      : "+v"(d0), "+v"(d1) : "v"(a0), "i"(o0), "v"(a1), "i"(o1), "s"(vmask))
#define LWAIT(n) do { asm volatile("s_waitcnt lgkmcnt(" #n ")" ::: "memory"); SBAR(); } while (0)

#define BAR() asm volatile("s_barrier" ::: "memory")
#define LDS_SET(off, v) __hip_atomic_store((LDS3 unsigned*)(lds + (off)), (unsigned)(v), __ATOMIC_RELAXED, __HIP_MEMORY_SCOPE_WORKGROUP)
#define LDS_CNT_ADD(off) __hip_atomic_fetch_add((LDS3 unsigned*)(lds + (off)), 1u, __ATOMIC_RELAXED, __HIP_MEMORY_SCOPE_WORKGROUP)

template <int SLP = 1>
__device__ __forceinline__ void lds_wait(LDS3 char* p, unsigned want) {
#pragma unroll 1
    for (unsigned spins = 0; spins < (1u << 20); ++spins) {
        if ((unsigned)__builtin_amdgcn_readfirstlane(__hip_atomic_load((LDS3 unsigned*)p, __ATOMIC_RELAXED, __HIP_MEMORY_SCOPE_WORKGROUP)) >= want) break;
        __builtin_amdgcn_s_sleep(SLP);
    }
    __builtin_amdgcn_fence(__ATOMIC_ACQUIRE, "wavefront");
}

template <int SLP>
__device__ __forceinline__ void prog_wait(LDS3 char* p, int lane, unsigned want) {
#pragma unroll 1
    for (unsigned spins = 0; spins < (1u << 20); ++spins) {
        if (__all(__hip_atomic_load((LDS3 unsigned*)p + (lane & 7), __ATOMIC_RELAXED, __HIP_MEMORY_SCOPE_WORKGROUP) >= want)) break;
        __builtin_amdgcn_s_sleep(SLP);
    }
    __builtin_amdgcn_fence(__ATOMIC_ACQUIRE, "wavefront");
}
#define POLL_ONE(cond, SLP, o0, o1, word) POLL_ONE2(cond, SLP, 2, o0, o1, word)
#define POLL_ONE2(cond, SLP, SLP2, o0, o1, word) do { if (cond) { wait_flags<SLP>(ws, o0, o1, tag); \
        if (lane == 0) __hip_atomic_store((LDS3 unsigned*)(lds + (word)), 1u, __ATOMIC_RELAXED, __HIP_MEMORY_SCOPE_WORKGROUP); } else lds_wait<SLP2>(lds + (word), 1); } while (0)
constexpr int C_KV = 0, C_KVF = 4, C_OB = 8, C_WA = 16, C_OA = 20, C_BX = 28, C_MRG = 32  , C_FIN = 64  , C_BP = 96  ;
constexpr int C_P1 = 112, C_P2 = 116, C_P3 = 120, C_P4 = 124, C_PR = 128  , C_KVL = 160, C_P5 = 164, C_GB = 168;
constexpr int G_Q = 176, G_P = 180, G_ST = 184, G_RD = 188;
constexpr int X_G = 256;
constexpr int X_KVF = 1280, X_SLOT = 4096;
constexpr int X_MRG = 1280;
constexpr int L_WA = 24576;
constexpr int L_Q = 81920;
constexpr int X_FIN = 126976;

__global__ __launch_bounds__(768, 3) void mega(Params P) {
    __shared__ __attribute__((aligned(16))) char smem[163840];
    LDS3 char* lds = (LDS3 char*)smem;
    char* ws = P.ws;
    const int tid = threadIdx.x, lane = tid & 63, r = lane & 31, h = lane >> 5;
    const int hw = __builtin_amdgcn_readfirstlane(tid >> 6);
    const bool helper = hw < 4;
    const int w = hw - 4, mtid = tid - 256;
    const int bid = blockIdx.x;
    const unsigned lds0 = (unsigned)(uintptr_t)smem;
    const rsrc_t rs = __builtin_amdgcn_make_buffer_rsrc(ws, 0, WS_END, 0x00020000);
    STAMP(0);
    u64 tag = 0;
#define GET_TAG() do { asm volatile("s_load_dwordx2 %0, %1, 0x0\n\ts_waitcnt lgkmcnt(0)" : "=s"(tag) : "s"(ws) : "memory"); tag += 1; } while (0)
    const int head = (bid & 7) * 4 + ((bid >> 3) & 3), qb = bid >> 5;
    const int nlo = (head * 20) >> 7, nhi = (head * 20 + 19) >> 7;
    const int mtO = (bid & 7) * 4 + ((bid >> 3) & 3), ntO = bid >> 5;
    const int lin = (bid & 7) * 30 + (bid >> 3), lh = lin >= 120, lrem = lin - 120 * lh;
    const int mt = lh * 8 + (lrem & 7), nt = lrem >> 3;
    const unsigned fG = WS_F_G + (nt * 16 + mt) * 8;
    const unsigned one = (r == 20) ? 0x3f803f80u : 0u;
    const f32x16 z16 = {};

    if (helper) {
        const unsigned fA0 = WS_F_G + (((lane < 32 ? 5 : 10) + ((lane & 16) ? nhi : nlo)) * 16 + (lane & 15)) * 8;
        const unsigned fA1 = WS_F_G + (((lane & 2) ? nhi : nlo) * 16 + qb * 2 + (lane & 1)) * 8;
        __builtin_amdgcn_s_setprio(3);
        const char* kvb = ws + WS_KV + head * 163840;
        auto kv_issue = [&](int grp, int t0 = 0, int t1 = 8) {
#pragma unroll
            for (int t = t0; t < t1; ++t) { int pc = grp * 32 + hw + 4 * t; if (pc == 0) pc = 4; glds16s(lane * 16, kvb + pc * 1024, lds0 + pc * 1024); }
        };
        auto q_issue = [&]() {
            const char* qbase = ws + WS_Q + head * 81920 + qb * 10240;
#pragma unroll
            for (int t = 0; t < 3; ++t) { const int pc = hw + 4 * t < 10 ? hw + 4 * t : 10; glds16s(lane * 16, pc < 10 ? qbase + pc * 1024 : kvb, lds0 + L_Q + pc * 1024); }
        };
        if (bid < 240) {
            const int rot = ((nt + mt) % 3) * 3;
            unsigned xo[4], wo[4];
#pragma unroll
            for (int i = 0; i < 4; ++i) {
                xo[i] = (mt * 128 + 32 * hw + 8 * i + (lane >> 3)) * 1280 + (((lane & 7) ^ ((4 * i + (lane >> 4)) & 7)) << 4) - i * 1024;
                wo[i] = (16 * hw + 4 * i + (lane >> 4)) * 3840 + nt * 256 + (((lane & 15) ^ (((lane >> 4) << 2) | i)) << 4) - i * 1024;
            }
            const unsigned dX = lds0 + 32768 + hw * 4096;
            int kt = rot;
#pragma unroll
            for (int n = 0; n < 20; ++n) {
                const char* xb = (const char*)P.x + kt * 128;
                const char* wb = (const char*)(n >= 10 ? P.wb : P.wa) + (size_t)kt * (64 * 3840);
                asm volatile("" : "+s"(xb), "+s"(wb));
                if (n == 2) { if (hw == 0) *(LDS3 unsigned*)(lds + lane * 4) = 0u; GET_TAG(); WAIT_BAR(8); }
                else if (n == 11 || n == 15) { WAIT_BAR(12); }
                else if (n == 3 || (n >= 12 && n <= 14)) { WAIT_BAR(8); }
                else if (n >= 5) { WAIT_BAR(16); }
                glds16x4(wo, wb, dX + 16384 + (n & 3) * 32768);
                if (n < 10 || n >= 14) glds16x4(xo, xb, dX + ((n >= 10 ? 20 - n : n + 1) & 3) * 32768);
                if (n < 9) kt = kt == 9 ? 0 : kt + 1; else if (n > 9) kt = kt == 0 ? 9 : kt - 1;
            }
            WAIT_BAR(16);
            if (hw == 0) {
                const u64 a = __hip_atomic_load((gu64*)(ws + fA0), RLX_AGENT), b = __hip_atomic_load((gu64*)(ws + fA1), RLX_AGENT);
                if (__all(a == tag && b == tag) && lane == 0) LDS_SET(G_P, 1);
            }
            WAIT_BAR(0);
        } else {
            if (hw == 0) *(LDS3 unsigned*)(lds + lane * 4) = 0u;
            GET_TAG();
            WAIT_BAR(63);
            if (hw == 0) {
#pragma unroll 1
                for (int t = 0; t < PRE_SLEEP; ++t) __builtin_amdgcn_s_sleep(127);
            }
        }
        if (!__builtin_amdgcn_readfirstlane(__hip_atomic_load((LDS3 unsigned*)(lds + G_P), __ATOMIC_RELAXED, __HIP_MEMORY_SCOPE_WORKGROUP))) POLL_ONE(hw == 0, 6, fA0, fA1, G_P);
        __builtin_amdgcn_fence(__ATOMIC_ACQUIRE, "wavefront");
        kv_issue(0); q_issue(); kv_issue(1, 0, 2);
        DRAIN();
        if (lane == 0) LDS_CNT_ADD(G_Q);
        lds_wait<2>(lds + G_RD, 8);
        kv_issue(1, 2, 8); kv_issue(2); kv_issue(3); kv_issue(4);
        asm volatile("s_waitcnt vmcnt(24)" ::: "memory"); if (lane == 0) LDS_CNT_ADD(C_KVL);
        asm volatile("s_waitcnt vmcnt(16)" ::: "memory"); if (lane == 0) LDS_CNT_ADD(C_KVL + 12);
        STAMP(3);
        u32x4 phm; unsigned pht;
        {
            const u16* wp = P.wphi + head * 400 + (r < 20 ? r : (r & 3));
            unsigned e[10];
#pragma unroll
            for (int j = 0; j < 8; ++j) e[j] = wp[(8 * h + j + 4 * (head & 1)) * 20];
            e[8] = wp[((head & 1 ? 0 : 16) + 2 * h) * 20]; e[9] = wp[((head & 1 ? 1 : 17) + 2 * h) * 20];
            const int bu = bid < 160 ? bid : 0, hf = bu >= 80, k0 = (bu - 80 * hf) * 8, hd = k0 < 512 ? k0 >> 4 : (k0 - 512) >> 2;
            const int row = k0 < 512 ? 20 * hd + (k0 & 8) + 4 * (hd & 1) : 20 * hd + 16;
            const u16* wsrc = (hf ? P.wob : P.woa) + (size_t)row * DM;
            unsigned f[3][8];
            if (bid < 160) {
#pragma unroll
                for (int q = 0; q < 3; ++q) {
                    const int n = tid + 256 * q < 640 ? tid + 256 * q : 639;
#pragma unroll
                    for (int j = 0; j < 8; ++j) f[q][j] = wsrc[j * DM + n];
                }
            }
            DRAIN();
            if (lane == 0) LDS_CNT_ADD(C_WA + 8);
            phm = (u32x4){e[0] | (e[1] << 16), e[2] | (e[3] << 16), e[4] | (e[5] << 16), e[6] | (e[7] << 16)}; pht = e[8] | (e[9] << 16);
            if (bid < 160) {
#pragma unroll
                for (int q = 0; q < 3; ++q) {
                    const int n = tid + 256 * q;
                    const u32x4 v = {f[q][0] | (f[q][1] << 16), f[q][2] | (f[q][3] << 16), f[q][4] | (f[q][5] << 16), f[q][6] | (f[q][7] << 16)};
                    if (n < 640) ST16(v, WS_WT + (((n >> 4) * 40 + hf * 20 + (k0 >> 5)) * 64 + ((k0 >> 3) & 3) * 16 + (n & 15)) * 16);
                }
            }
        }
        const int tokS = qb * 256 + hw * 64, m7 = tokS >> 7, t4 = (tokS >> 5) & 3;
        {
            const unsigned pt3 = (lane >> 1) & 3;
            const unsigned tb = WS_F_G + 2048 + ((((lane & 1) ? nhi : nlo) + (pt3 < 3 ? pt3 : 0) * 5) * 16 + qb * 2 + ((lane >> 3) & 1)) * 8;
            POLL_ONE2(hw == 0, 4, HS, tb, WS_F_SD + (qb * 2 + (lane & 1)) * 8, C_P1);
        }
        const unsigned om = t4 * 1024 + lane * 16, ot = 4096 + (t4 * 32 + r) * 8 + 4 * h;
        u32x4 qf[2][3]; float gS[2];
        {
            const unsigned kb = WS_KV + (NH + head) * 163840 + (2 * (m7 & 7) + (m7 >> 3)) * 10240, qbb = WS_Q + (NH + head) * 81920 + m7 * 5120;
            const unsigned ov = 5120 + t4 * 1280 + h * 16 + (r < 20 ? r : 19) * 32;
            u32x4 e0[2], w0[2], w1[2], eq[2]; unsigned et[2], eqt[2];
#pragma unroll
            for (int i = 0; i < 2; ++i) {
                e0[i] = LD16(kb + om + i * 1024); et[i] = LD4(kb + ot + i * 256); w0[i] = LD16(kb + ov + i * 1280); w1[i] = LD16(kb + ov + i * 1280 + 640);
                eq[i] = LD16(qbb + om + i * 1024); eqt[i] = LD4(qbb + ot + i * 256); gS[i] = __uint_as_float(LD4(WS_G + (tokS + i * 32 + r) * 4));
            }
            u32x4 i0, i1;
#pragma unroll
            for (int j = 0; j < 4; ++j) {
                const int e = r - 8 * h - 2 * j;
                i0[j] = e == 0 ? 0x3f80u : (e == 1 ? 0x3f800000u : 0u);
                i1[j] = e == 16 ? 0x3f80u : (e == 17 ? 0x3f800000u : 0u);
            }
            f32x16 k0 = {}, k1 = {};
#pragma unroll
            for (int i = 0; i < 2; ++i) {
                if (r >= 20) { w0[i] = (u32x4){one, one, one, one}; w1[i] = w0[i]; }
                u32x4 fp, fn, ft;
                feature_map(phm, pht, e0[i], et[i], h, fp, fn, ft);
                f32x16 T0 = MFMA32_16(fp, i0, z16); T0 = MFMA32_16(fn, i1, T0);
                f32x16 T1 = MFMA32_16(ft, i0, z16);
                u32x4 b00, b01, b10, b11;
#pragma unroll
                for (int j = 0; j < 4; ++j) {
                    b00[j] = pk2(T0[2 * j], T0[2 * j + 1]); b01[j] = pk2(T0[8 + 2 * j], T0[9 + 2 * j]);
                    b10[j] = pk2(T1[2 * j], T1[2 * j + 1]); b11[j] = pk2(T1[8 + 2 * j], T1[9 + 2 * j]);
                }
                k0 = MFMA32_16(w0[i], b00, k0); k0 = MFMA32_16(w1[i], b01, k0);
                k1 = MFMA32_16(w0[i], b10, k1); k1 = MFMA32_16(w1[i], b11, k1);
            }
            prog_wait<8>(lds + C_PR, lane, 1);
            *(LDS3 float*)(lds + X_G + (hw * 64 + lane) * 4) = h ? gS[1] : gS[0];
            if (hw * 64 + lane < 144) *(LDS3 u32x4*)(lds + X_KVF + (hw * 64 + lane) * 16) = (u32x4){0u, 0u, 0u, 0u};
            LDS3 char* sp = lds + X_SLOT + hw * 4096;
#pragma unroll
            for (int gq = 0; gq < 3; ++gq) {
                *(LDS3 f32x4*)(sp + gq * 1024 + lane * 16) = (f32x4){k0[4 * gq], k0[4 * gq + 1], k0[4 * gq + 2], k0[4 * gq + 3]};
                if (r < 8) *(LDS3 f32x4*)(sp + 3072 + (gq * 16 + h * 8 + r) * 16) = (f32x4){k1[4 * gq], k1[4 * gq + 1], k1[4 * gq + 2], k1[4 * gq + 3]};
            }
            asm volatile("s_waitcnt vmcnt(0) lgkmcnt(0)" ::: "memory");
            unsigned arr = 0;
            if (lane == 0) arr = LDS_CNT_ADD(C_KV);
            if (__builtin_amdgcn_readfirstlane(arr) == 3) {
                __builtin_amdgcn_fence(__ATOMIC_ACQUIRE, "wavefront");
#pragma unroll
                for (int i = 0; i < 4; ++i) {
                    const int qd = lane + 64 * i;
                    LDS3 const char* s0 = lds + X_SLOT + qd * 16;
                    const f32x4 s = (*(LDS3 const f32x4*)s0 + *(LDS3 const f32x4*)(s0 + 4096)) + (*(LDS3 const f32x4*)(s0 + 8192) + *(LDS3 const f32x4*)(s0 + 12288));
                    if (qd < 240) ST16(__builtin_bit_cast(u32x4, s), WS_KVP + (head * 8 + qb) * 4096 + qd * 16);
                }
                DRAIN();
                if (lane == 0) set_flag(ws, WS_F_KV + (head * 8 + qb) * 8, tag);
            }
#pragma unroll
            for (int i = 0; i < 2; ++i) feature_map(phm, pht, eq[i], eqt[i], h, qf[i][0], qf[i][1], qf[i][2]);
        }
        STAMP(4);
        POLL_ONE2(hw == 0, 4, HS, WS_F_KV + (head * 8 + (lane & 7)) * 8, WS_F_KV + (head * 8 + (lane & 7)) * 8, C_P2);
        {
            const int qd = hw * 60 + (lane < 60 ? lane : 59);
            f32x4 pp[8];
#pragma unroll
            for (int p = 0; p < 8; ++p) pp[p] = __builtin_bit_cast(f32x4, LD16(WS_KVP + (head * 8 + p) * 4096 + qd * 16));
            const f32x4 s4 = ((pp[0] + pp[1]) + (pp[2] + pp[3])) + ((pp[4] + pp[5]) + (pp[6] + pp[7]));
            const int gq = qd < 192 ? qd >> 6 : (qd - 192) >> 4, hs = qd < 192 ? (qd >> 5) & 1 : (qd >> 3) & 1, sg = qd < 192 ? qd & 31 : 32 + (qd & 7);
            const unsigned a = pk2(s4[0], s4[1]), b = pk2(s4[2], s4[3]);
            LDS3 u16* kp = (LDS3 u16*)(lds + X_KVF + (8 * gq + 4 * hs) * 96 + sg * 2);
            kp[0] = (u16)a; kp[48] = (u16)(a >> 16); kp[96] = (u16)b; kp[144] = (u16)(b >> 16);
            asm volatile("s_waitcnt lgkmcnt(0)" ::: "memory");
            if (lane == 0) LDS_CNT_ADD(C_KVF);
            lds_wait<2>(lds + C_KVF, 4);
        }
        {
            LDS3 const char* kp = lds + X_KVF + (r < 24 ? r : 23) * 96 + h * 16;
            const u32x4 a0 = *(LDS3 const u32x4*)kp, a1 = *(LDS3 const u32x4*)(kp + 32), a2 = *(LDS3 const u32x4*)(kp + 64);
#pragma unroll
            for (int i = 0; i < 2; ++i) {
                f32x16 d = MFMA32_16(a0, qf[i][0], z16); d = MFMA32_16(a1, qf[i][1], d); d = MFMA32_16(a2, qf[i][2], d);
                const float den = upper_half(d[8]) + 1e-6f;
                float o[12];
#pragma unroll
                for (int j = 0; j < 12; ++j) o[j] = d[j];
                store_o(rs, o, (1.f - gS[i]) / den, tokS + i * 32 + r, 1, head, h);
            }
            DRAIN();
            unsigned arr = 0;
            if (lane == 0) arr = LDS_CNT_ADD(C_OB);
            if (__builtin_amdgcn_readfirstlane(arr) == 3 && lane == 0) set_flag(ws, WS_F_OB + (qb * 32 + head) * 8, tag);
        }
        STAMP(5);
        f32x4 c[5];
#pragma unroll
        for (int n = 0; n < 5; ++n) c[n] = (f32x4){0.f, 0.f, 0.f, 0.f};
        const char* wsrc = ws + WS_WT + (size_t)ntO * 5 * 40960;
        {
            if (hw == 0) wait_flags<8>(ws, WS_F_KV + lane * 8, WS_F_KV + 512 + lane * 8, tag);
            POLL_ONE2(hw == 0, 8, HS, WS_F_KV + 1024 + lane * 8, WS_F_KV + 1536 + lane * 8, C_P5);
#pragma unroll 1
            for (int t = 0; t < 5; ++t) {
                if (t == 0) prog_wait<2 * HS>(lds + C_PR, lane, 4); else if (t == 3) prog_wait<2 * HS>(lds + C_PR, lane, 5);
                const int ks = hw + 4 * t < 19 ? hw + 4 * t : 18;
#pragma unroll
                for (int n = 0; n < 5; ++n) glds16s(lane * 16, wsrc + (n * 40 + 20 + ks) * 1024, lds0 + 4096 + (ks * 5 + n) * 1024);
            }
            POLL_ONE2(hw == 0, 4, HS, WS_F_OB + ((mtO >> 2) * 32 + r) * 8, WS_F_OB + ((mtO >> 2) * 32 + r) * 8, C_P3);
            const unsigned ab = WS_AO + ((mtO * 4 + hw) * 40 + 20) * 1024;
            u32x4 af[20];
#pragma unroll
            for (int ks = 0; ks < 16; ++ks) af[ks] = LD16(ab + ks * 1024 + lane * 16);
#pragma unroll
            for (int ks = 16; ks < 20; ++ks) LD_TAIL(af[ks], ab + ks * 1024);
            asm volatile("s_waitcnt vmcnt(20)" ::: "memory");
            if (lane == 0) LDS_CNT_ADD(C_BX);
            lds_wait<2>(lds + C_BX, 4);
            u32x4 wf[3][5], wl[5];
            LDS3 const char* wp = lds + 4096 + lane * 16;
#pragma unroll
            for (int ks = 0; ks < 2; ++ks)
#pragma unroll
                for (int n = 0; n < 5; ++n) wf[ks][n] = *(LDS3 const u32x4*)(wp + (ks * 5 + n) * 1024);
#pragma unroll
            for (int ks = 0; ks < 20; ++ks) {
                if (ks + 2 < 19) {
#pragma unroll
                    for (int n = 0; n < 5; ++n) wf[(ks + 2) % 3][n] = *(LDS3 const u32x4*)(wp + ((ks + 2) * 5 + n) * 1024);
                }
                if (ks == 8) {
#pragma unroll
                    for (int n = 0; n < 5; ++n) wl[n] = LD16(WS_WT + ((ntO * 5 + n) * 40 + 39) * 1024 + lane * 16);
                }
                SBAR();
#pragma unroll
                for (int n = 0; n < 5; ++n) c[n] = MFMA16_32(ks < 19 ? wf[ks % 3][n] : wl[n], af[ks], c[n]);
                SBAR();
            }
            if (lane == 0) LDS_CNT_ADD(C_BX);
            lds_wait<2>(lds + C_BX, 8);
        }
        STAMP(6);
        prog_wait<2 * HS>(lds + C_PR, lane, 7);
#pragma unroll 1
        for (int n = 0; n < 5; ++n)
#pragma unroll
            for (int t = 0; t < 5; ++t) { const int ks = hw + 4 * t; glds16s(lane * 16, wsrc + (n * 40 + ks) * 1024, lds0 + L_WA + (n * 20 + ks) * 1024); }
#pragma unroll
        for (int n = 0; n < 3; ++n) *(LDS3 f32x4*)(lds + X_FIN + hw * 3072 + n * 1024 + lane * 16) = c[n];
        DRAIN();
        if (lane == 0) LDS_CNT_ADD(C_WA);
        prog_wait<2 * HS>(lds + C_PR, lane, 8);
#pragma unroll
        for (int n = 3; n < 5; ++n) *(LDS3 f32x4*)(lds + 143360 + hw * 2048 + (n - 3) * 1024 + lane * 16) = c[n];
        asm volatile("s_waitcnt lgkmcnt(0)" ::: "memory");
        if (lane == 0) __hip_atomic_store((LDS3 unsigned*)(lds + C_BP + hw * 4), 1u, __ATOMIC_RELAXED, __HIP_MEMORY_SCOPE_WORKGROUP);
        STAMP(7);
    } else {
        const int qs = w & 3, kh = w >> 2;
        const int q0 = qb * 256 + qs * 64;
        GET_TAG();
        if (bid < 240) {
            const int part = nt / 5, c0 = (nt - part * 5) * 128;
            const int wm = w & 1, wn = w >> 1;
            constexpr int RB = 32768;
            const int xr = wm * 64 + r;
            const unsigned xa = RB + xr * 128 + ((h ^ ((xr >> 1) & 7)) << 4);
            unsigned wt[2];
#pragma unroll
            for (int t = 0; t < 2; ++t) {
                const int q = (lane & 15) >> 2, p = lane & 3, blk = (lane >> 4) & 1, row = 8 * h + 4 * t + q;
                wt[t] = RB + 16384 + row * 256 + (((4 * wn + 2 * blk + (p >> 1)) ^ ((q << 2) | (2 * h + t))) << 4) + 8 * (p & 1);
            }
            auto gbody = [&](auto trc) {
                constexpr bool TR = decltype(trc)::value;
                u32x4 xf[2][8], wf[2][4];
                auto rdk = [&](auto bc, LDS3 const char* sx, LDS3 const char* sb, int ks) {
                    constexpr int B = decltype(bc)::value;
                    xf[B][2 * ks] = *(LDS3 const u32x4*)(sx + (xa ^ (ks * 32)));
                    xf[B][2 * ks + 1] = *(LDS3 const u32x4*)(sx + ((xa + 4096) ^ (ks * 32)));
                    const u32x2 lo = trread(sb + wt[0] + ks * 4096), hi = trread(sb + wt[1] + ks * 4096);
                    wf[B][ks] = (u32x4){lo[0], lo[1], hi[0], hi[1]};
                };
                f32x16 acc0, acc1;
                auto step = [&](auto bc, int s) {
                    constexpr int B = decltype(bc)::value;
                    if (s == GA_PUB) {
                        DRAIN();
                        unsigned arr = 0;
                        if (lane == 0) arr = LDS_CNT_ADD(G_ST);
                        if (__builtin_amdgcn_readfirstlane(arr) == 7 && lane == 0) set_flag(ws, fG, tag);
                    }
                    __builtin_amdgcn_s_waitcnt(0xC07F);
                    if (s < 18) BAR();
                    SBAR();
                    const int s1 = s + 1;
                    LDS3 const char* sb = lds + __builtin_amdgcn_readfirstlane((s1 & 3) * 32768);
                    LDS3 const char* sx = lds + __builtin_amdgcn_readfirstlane(((s1 >= 10 ? 20 - s1 : s1 + 1) & 3) * 32768);
#define GM(A, i) do { if constexpr (TR) A = MFMA32_16(wf[B][(i) >> 1], xf[B][i], A); else A = MFMA32_16(xf[B][i], wf[B][(i) >> 1], A); SBAR(); } while (0)
#define GX(i) do { xf[B ^ 1][i] = *(LDS3 const u32x4*)(sx + ((xa + ((i) & 1) * 4096) ^ (((i) >> 1) * 32))); } while (0)
#define GW(ks) do { const u32x2 lo = trread(sb + wt[0] + (ks) * 4096), hi = trread(sb + wt[1] + (ks) * 4096); wf[B ^ 1][ks] = (u32x4){lo[0], lo[1], hi[0], hi[1]}; } while (0)
                    GM(acc0, 0); GX(0); GW(0); SBAR(); GM(acc1, 1); GX(1); GX(2); SBAR(); GM(acc0, 2); GW(1); GX(3); SBAR(); GM(acc1, 3); GX(4); GW(2); SBAR();
                    GM(acc0, 4); GX(5); GX(6); SBAR(); GM(acc1, 5); GW(3); GX(7); SBAR(); GM(acc0, 6); GM(acc1, 7);
#undef GX
#undef GW
#undef GM
                };
                WAIT_BAR(63);
#pragma unroll
                for (int ks = 0; ks < 4; ++ks) rdk(std::integral_constant<int, 0>{}, lds + 32768, lds, ks);
#pragma unroll 1
                for (int mat = 0; mat < 2; ++mat) {
                    acc0 = (f32x16){}; acc1 = (f32x16){};
#pragma unroll 1
                    for (int kk = 0; kk < 10; kk += 2) {
                        step(std::integral_constant<int, 0>{}, mat * 10 + kk);
                        step(std::integral_constant<int, 1>{}, mat * 10 + kk + 1);
                    }
                    const int cpos = 2 * (mt & 7) + (mt >> 3);
                    if constexpr (TR) {
                        const float sc = (part == 0 && mat == 0) ? QSCALE : 1.f;
                        const unsigned base = part == 0 ? WS_Q + mat * NH * 81920 + mt * 5120 : WS_KV + mat * NH * 163840 + cpos * 10240;
                        const unsigned hs = part == 0 ? 81920 : 163840;
#pragma unroll
                        for (int pr = 0; pr < 2; ++pr) {
                            const int G8 = ((c0 + 32 * wn) >> 3) + 2 * pr + h, pp = G8 / 5, gi = G8 - pp * 5;
                            const unsigned hb0 = base + (2 * pp + (gi > 2)) * hs;
#pragma unroll
                            for (int i = 0; i < 2; ++i) {
                                const f32x16& a = i ? acc1 : acc0;
                                const int tl = wm * 64 + i * 32 + r;
                                const auto s0 = __builtin_amdgcn_permlane32_swap(pk2(a[8 * pr] * sc, a[8 * pr + 1] * sc), pk2(a[8 * pr + 4] * sc, a[8 * pr + 5] * sc), false, false);
                                const auto s1 = __builtin_amdgcn_permlane32_swap(pk2(a[8 * pr + 2] * sc, a[8 * pr + 3] * sc), pk2(a[8 * pr + 6] * sc, a[8 * pr + 7] * sc), false, false);
                                if (gi != 2) { const u32x4 ov = {s0[0], s1[0], s0[1], s1[1]}; ST16(ov, hb0 + (tl >> 5) * 1024 + (gi > 2 ? gi - 3 : gi) * 512 + r * 16); }
                                else { const u32x2 o0 = {s0[0], s1[0]}, o1 = {s0[1], s1[1]}; ST8(o0, hb0 + 4096 + tl * 8); ST8(o1, hb0 + hs + 4096 + tl * 8); }
                            }
                        }
                    } else {
                        const int n = c0 + 32 * wn + r, hd = n / 20, d = n - hd * 20, rho = (hd & 1) ? (d < 4 ? d + 16 : d - 4) : d;
                        const unsigned base = WS_KV + (mat * NH + hd) * 163840 + cpos * 10240 + 5120 + h * 16 + rho * 32;
#pragma unroll
                        for (int i = 0; i < 2; ++i)
#pragma unroll
                            for (int s2 = 0; s2 < 2; ++s2) {
                                const f32x16& a = i ? acc1 : acc0;
                                u32x4 ov = {pk2(a[8 * s2], a[8 * s2 + 1]), pk2(a[8 * s2 + 2], a[8 * s2 + 3]), pk2(a[8 * s2 + 4], a[8 * s2 + 5]), pk2(a[8 * s2 + 6], a[8 * s2 + 7])};
                                ST16(ov, base + (wm * 4 + i * 2 + s2) * 640);
                            }
                    }
                    if (mat == 0) STAMP(1);
                }
            };
            if (part < 2) gbody(std::true_type{}); else gbody(std::false_type{});
        } else {
            const int sj = bid - 240;
            WAIT_BAR(63);
            {
                const u32x4 ga = *(const u32x4*)(P.gw + lane * 8), gb = *(const u32x4*)(P.gw + 512 + (lane & 15) * 8);
                u32x4 xa[16], xb[4];
                const u16* xr = P.x + (size_t)(sj * 128 + w * 16) * DM;
#pragma unroll
                for (int rr = 0; rr < 16; ++rr) xa[rr] = *(const u32x4*)(xr + rr * DM + lane * 8);
#pragma unroll
                for (int i = 0; i < 4; ++i) xb[i] = *(const u32x4*)(xr + (i * 4 + (lane >> 4)) * DM + 512 + (lane & 15) * 8);
                float res = 0.f;
#pragma unroll
                for (int rr = 0; rr < 16; ++rr) {
                    float s = 0.f, s2 = 0.f;
#pragma unroll
                    for (int j = 0; j < 4; ++j) { s += bf_lo(xa[rr][j]) * bf_lo(ga[j]) + bf_hi(xa[rr][j]) * bf_hi(ga[j]); s2 += bf_lo(xb[rr >> 2][j]) * bf_lo(gb[j]) + bf_hi(xb[rr >> 2][j]) * bf_hi(gb[j]); }
                    if ((lane >> 4) == (rr & 3)) s += s2;
#pragma unroll
                    for (int o = 32; o >= 1; o >>= 1) s += __shfl_xor(s, o);
                    if (lane == rr) res = s;
                }
                if (lane < 16) ST4(__float_as_uint(1.f / (1.f + __expf(-res))), WS_G + (sj * 128 + w * 16 + lane) * 4);
            }
            DRAIN();
            unsigned arr = 0;
            if (lane == 0) arr = LDS_CNT_ADD(G_ST);
            if (__builtin_amdgcn_readfirstlane(arr) == 7 && lane == 0) set_flag(ws, WS_F_SD + sj * 8, tag);
        }
        STAMP(2);
        const unsigned hb = lds0 + kh * 10240;
        const unsigned laneK = hb + lane * 16, laneT = hb + 4096 + r * 8 + h * 4, laneV = hb + 5120 + h * 16 + (r < 20 ? r : 19) * 32;
        const u64 vmask = 0x000fffff000fffffull;
        u32x4 v0a = {one, one, one, one}, v0b = v0a, v1 = v0a;
        u32x4 qA, qB, ka; unsigned qtA, qtB, kt;
        {
            lds_wait(lds + G_Q, 4);
            LDS3 const char* qp = lds + L_Q + (qs >> 1) * 5120;
            const unsigned om = (qs & 1) * 2048 + lane * 16, ot = 4096 + ((qs & 1) * 64 + r) * 8 + 4 * h;
            qA = *(LDS3 const u32x4*)(qp + om); qB = *(LDS3 const u32x4*)(qp + om + 1024);
            qtA = *(LDS3 const unsigned*)(qp + ot); qtB = *(LDS3 const unsigned*)(qp + ot + 256);
            DSR128(ka, kh ? laneK : lds0 + L_Q + 10240 + lane * 16, 0); DSR32(kt, laneT, 0); DSR128M2(v0a, laneV, 0, v1, laneV, 640);
            asm volatile("s_waitcnt lgkmcnt(0)" : "+v"(ka), "+v"(kt), "+v"(v0a), "+v"(v1), "+v"(qA), "+v"(qB), "+v"(qtA), "+v"(qtB) :: "memory");
            if (lane == 0) LDS_CNT_ADD(G_RD);
        }
        STAMP(5);
        float mA, mB; u32x2 qTA, qTB; f32x16 sA, sB;
        {
            const u32x2 t0 = {kt, 0x3f80u}, a0 = {qtA, 0u}, b0 = {qtB, 0u};
            const f32x16 s = MFMA32_16(ka, qA, z16);
            const f32x16 s0 = MFMA32_8(t0, a0, s);
            f32x16 s1 = MFMA32_16(ka, qB, z16); s1 = MFMA32_8(t0, b0, s1);
            float x0 = s0[0], x1 = s1[0];
#pragma unroll
            for (int i = 1; i < 16; ++i) { x0 = fmaxf(x0, s0[i]); x1 = fmaxf(x1, s1[i]); }
            x0 = -xhalf_max(x0); x1 = -xhalf_max(x1);
            const unsigned hh = pk2(x0, x1);
            const unsigned ll = pk2(x0 - bf_lo(hh), x1 - bf_hi(hh));
            mA = -(bf_lo(hh) + bf_lo(ll)); mB = -(bf_hi(hh) + bf_hi(ll));
            const unsigned sel = h ? ll : hh;
            qTA = (u32x2){qtA, sel & 0xffffu}; qTB = (u32x2){qtB, sel >> 16};
            sA = MFMA32_8(t0, qTA, s);
        }
        f32x16 oA = {}, oB = {};
        u32x4 pa, pb = {0u, 0u, 0u, 0u};
#define EX4(S, i, Pv, j) do { Pv[j] = pk2(ex2(S[i]), ex2(S[i + 1])); Pv[j + 1] = pk2(ex2(S[i + 2]), ex2(S[i + 3])); PIN(Pv); SBAR(); } while (0)
#define KT2 ((u32x2){kt, 0x3f80u})
#define TILE(V0C, V0N, aK1, oK1, aT1, oT1, oV1, aV0, oV0) do { \
            sB = MFMA32_16(ka, qB, z16); SBAR(); DSR128(ka, aK1, oK1); EX4(sA, 0, pa, 0); \
            oB = MFMA32_16(v1, pb, oB); SBAR(); DSR128M2(v1, aV, oV1, V0N, aV0, oV0); EX4(sA, 4, pa, 2); \
            sB = MFMA32_8(KT2, qTB, sB); SBAR(); DSR32(kt, aT1, oT1); EX4(sA, 8, pb, 0); \
            oA = MFMA32_16(V0C, pa, oA); SBAR(); EX4(sA, 12, pb, 2); \
            LWAIT(1); sA = MFMA32_16(ka, qA, z16); SBAR(); EX4(sB, 0, pa, 0); \
            oA = MFMA32_16(v1, pb, oA); SBAR(); EX4(sB, 4, pa, 2); \
            LWAIT(0); sA = MFMA32_8(KT2, qTA, sA); SBAR(); EX4(sB, 8, pb, 0); \
            oB = MFMA32_16(V0C, pa, oB); SBAR(); EX4(sB, 12, pb, 2); \
        } while (0)
        unsigned aK = laneK, aT = laneT, aV = laneV;
#pragma unroll 1
        for (int c = 0; c < 8; ++c) {
            if (c >= 1 && c <= 3) lds_wait(lds + (c == 1 ? C_KVL : (c == 2 ? C_KVL + 12 : C_WA + 8)), 4);
            if (c >= 1) {
                unsigned pp;
                asm volatile("ds_write_b32 %1, %2\n\tds_read_b32 %0, %3\n\ts_waitcnt lgkmcnt(0)" : "=&v"(pp) : "v"(lds0 + C_PR + w * 4), "v"(c), "v"(lds0 + C_PR + (w ^ 4) * 4) : "memory");
                const int pc = __builtin_amdgcn_readfirstlane(pp);
                if (pc > c || (pc == c && ((c ^ kh) & 1))) __builtin_amdgcn_s_setprio(1); else __builtin_amdgcn_s_setprio(0);
            } else if (kh) __builtin_amdgcn_s_setprio(1);
            const unsigned nx = (c < 7 ? c + 1 : 7) * 20480;
            const unsigned nK = laneK + nx, nT = laneT + nx, nV = laneV + nx;
            SBAR();
            TILE(v0a, v0b, aK, 1024, aT, 256, 640,  aV, 1280);
            TILE(v0b, v0a, aK, 2048, aT, 512, 1920, aV, 2560);
            if (c == 0) {
                DRAIN();
                unsigned arr = 0;
                if (lane == 0) arr = LDS_CNT_ADD(C_GB);
                if (__builtin_amdgcn_readfirstlane(arr) == 7 && lane == 0 && bid < 240) set_flag(ws, fG + 2048, tag);
                asm volatile("s_waitcnt lgkmcnt(0)" ::: "memory");
                SBAR();
            }
            TILE(v0a, v0b, aK, 3072, aT, 768, 3200, aV, 3840);
            TILE(v0b, v0a, nK, 0,    nT, 0,   4480, nV, 0);
            aK = nK; aT = nT; aV = nV;
        }
        oB = MFMA32_16(v1, pb, oB);
#undef TILE
#undef EX4
        if (lane == 0) __hip_atomic_store((LDS3 unsigned*)(lds + C_PR + w * 4), 8u, __ATOMIC_RELAXED, __HIP_MEMORY_SCOPE_WORKGROUP);
        STAMP(6);
        {
            float chk = 0.f;
#pragma unroll
            for (int i = 0; i < 12; ++i) chk += fabsf(oA[i]) * 0.f + fabsf(oB[i]) * 0.f;
            bool bad = !(chk == 0.f);
            if (h) bad = bad || !(oA[8] >= 0x1p-60f && oA[8] <= 0x1p60f && oB[8] >= 0x1p-60f && oB[8] <= 0x1p60f);
            if (__builtin_expect(__any(bad), 0)) {
                mA = -INFINITY; mB = -INFINITY;
#pragma unroll
                for (int i = 0; i < 16; ++i) { oA[i] = 0.f; oB[i] = 0.f; }
                const u32x2 a0 = {qtA, 0u}, b0 = {qtB, 0u};
#pragma unroll 1
                for (int t = 0; t < 32; ++t) {
                    const unsigned kb = WS_KV + head * 163840 + (2 * (t >> 2) + kh) * 10240, t4 = t & 3;
                    const u32x4 k0 = LD16(kb + t4 * 1024 + lane * 16);
                    const u32x2 t0 = {LD4(kb + 4096 + (t4 * 32 + r) * 8 + 4 * h), 0u};
                    u32x4 w0 = LD16(kb + 5120 + t4 * 1280 + h * 16 + (r < 20 ? r : 19) * 32), w1 = LD16(kb + 5120 + t4 * 1280 + 640 + h * 16 + (r < 20 ? r : 19) * 32);
                    if (r >= 20) { w0 = (u32x4){one, one, one, one}; w1 = w0; }
                    f32x16 s0 = MFMA32_16(k0, qA, z16); s0 = MFMA32_8(t0, a0, s0);
                    f32x16 s1 = MFMA32_16(k0, qB, z16); s1 = MFMA32_8(t0, b0, s1);
                    float x0 = s0[0], x1 = s1[0];
#pragma unroll
                    for (int i = 1; i < 16; ++i) { x0 = fmaxf(x0, s0[i]); x1 = fmaxf(x1, s1[i]); }
                    x0 = fmaxf(xhalf_max(x0), mA); x1 = fmaxf(xhalf_max(x1), mB);
                    const float g0 = (mA == x0) ? 1.f : ex2(mA - x0), g1 = (mB == x1) ? 1.f : ex2(mB - x1);
                    mA = x0; mB = x1;
#pragma unroll
                    for (int i = 0; i < 16; ++i) { oA[i] *= g0; oB[i] *= g1; }
#pragma unroll
                    for (int i = 0; i < 4; ++i) { pa[i] = pk2(ex2(s0[2 * i] - x0), ex2(s0[2 * i + 1] - x0)); pb[i] = pk2(ex2(s0[8 + 2 * i] - x0), ex2(s0[9 + 2 * i] - x0)); }
                    oA = MFMA32_16(w0, pa, oA); oA = MFMA32_16(w1, pb, oA);
#pragma unroll
                    for (int i = 0; i < 4; ++i) { pa[i] = pk2(ex2(s1[2 * i] - x1), ex2(s1[2 * i + 1] - x1)); pb[i] = pk2(ex2(s1[8 + 2 * i] - x1), ex2(s1[9 + 2 * i] - x1)); }
                    oB = MFMA32_16(w0, pa, oB); oB = MFMA32_16(w1, pb, oB);
                }
            }
        }
        lds_wait<4>(lds + C_BX, 8);
        const float gO = *(LDS3 const float*)(lds + X_G + (qs * 64 + kh * 32 + r) * 4);
        {
            LDS3 char* xp = lds + X_MRG + w * 2816;
            const f32x16& s = kh ? oA : oB;
            *(LDS3 f32x4*)(xp + lane * 16) = (f32x4){s[0], s[1], s[2], s[3]};
            *(LDS3 f32x4*)(xp + 1024 + lane * 16) = (f32x4){s[4], s[5], s[6], s[7]};
            if (!h) *(LDS3 f32x4*)(xp + 2048 + r * 16) = (f32x4){s[8], s[9], s[10], s[11]};
            else *(LDS3 f32x2*)(xp + 2560 + r * 8) = (f32x2){s[8], kh ? mA : mB};
            asm volatile("s_waitcnt lgkmcnt(0)" ::: "memory");
            if (lane == 0) __hip_atomic_store((LDS3 unsigned*)(lds + C_MRG + w * 4), 1u, __ATOMIC_RELAXED, __HIP_MEMORY_SCOPE_WORKGROUP);
        }
        lds_wait<MS>(lds + C_MRG + (w ^ 4) * 4, 1);
        bool lastw;
        {
            LDS3 const char* xp = lds + X_MRG + (w ^ 4) * 2816;
            const f32x4 p0 = *(LDS3 const f32x4*)(xp + lane * 16), p1 = *(LDS3 const f32x4*)(xp + 1024 + lane * 16), p2 = *(LDS3 const f32x4*)(xp + 2048 + r * 16);
            const f32x2 lm = *(LDS3 const f32x2*)(xp + 2560 + r * 8);
            const f32x16& s = kh ? oB : oA;
            const float mo = kh ? mB : mA, mp = lm[1];
            const float mx = fmaxf(mo, mp);
            const float fo = (mo == mx) ? 1.f : ex2(mo - mx), fq = (mp == mx) ? 1.f : ex2(mp - mx);
            float o[12];
#pragma unroll
            for (int i = 0; i < 4; ++i) { o[i] = s[i] * fo + p0[i] * fq; o[4 + i] = s[4 + i] * fo + p1[i] * fq; o[8 + i] = s[8 + i] * fo + p2[i] * fq; }
            const float l = upper_half(s[8]) * fo + lm[0] * fq;
            store_o(rs, o, gO / l, q0 + kh * 32 + r, 0, head, h);
            DRAIN();
            unsigned arr = 0;
            if (lane == 0) arr = LDS_CNT_ADD(C_OA);
            lastw = __builtin_amdgcn_readfirstlane(arr) == 7;
            if (lastw && lane == 0) set_flag(ws, WS_F_OA + (qb * 32 + head) * 8, tag);
        }
        STAMP(7);
        {
            const int c16 = lane & 15, g = lane >> 4, tt = w & 3, k0 = kh * 10;
            lds_wait(lds + C_WA, 4);
            LDS3 const char* wp = lds + L_WA + k0 * 1024 + lane * 16;
            u32x4 wf[4][5];
#pragma unroll
            for (int ks = 0; ks < 4; ++ks)
#pragma unroll
                for (int n = 0; n < 5; ++n) wf[ks][n] = *(LDS3 const u32x4*)(wp + (n * 20 + ks) * 1024);
            POLL_ONE(lastw, 2, WS_F_OA + ((mtO >> 2) * 32 + r) * 8, WS_F_OA + ((mtO >> 2) * 32 + r) * 8, C_P4);
            STAMP(9);
            const unsigned ab = WS_AO + ((mtO * 4 + tt) * 40 + k0) * 1024;
            u32x4 af[10];
#pragma unroll
            for (int ks = 0; ks < 6; ++ks) af[ks] = LD16(ab + ks * 1024 + lane * 16);
            if (kh) {
#pragma unroll
                for (int ks = 6; ks < 10; ++ks) LD_TAIL(af[ks], ab + ks * 1024);
            } else {
#pragma unroll
                for (int ks = 6; ks < 10; ++ks) af[ks] = LD16(ab + ks * 1024 + lane * 16);
            }
            f32x4 c[5];
#pragma unroll
            for (int n = 0; n < 5; ++n) c[n] = (f32x4){0.f, 0.f, 0.f, 0.f};
            SBAR();
#pragma unroll
            for (int ks = 0; ks < 10; ++ks) {
#pragma unroll
                for (int n = 0; n < 5; ++n) c[n] = MFMA16_32(wf[ks & 3][n], af[ks], c[n]);
                SBAR();
                if (ks + 4 < 10) {
#pragma unroll
                    for (int n = 0; n < 5; ++n) wf[ks & 3][n] = *(LDS3 const u32x4*)(wp + (n * 20 + ks + 4) * 1024);
                }
                SBAR();
            }
            LDS3 char* xp = lds + X_MRG + tt * 5120 + lane * 16;
            if (w < 4) { *(LDS3 f32x4*)(xp + 3072) = c[3]; *(LDS3 f32x4*)(xp + 4096) = c[4]; }
            else { *(LDS3 f32x4*)(xp) = c[0]; *(LDS3 f32x4*)(xp + 1024) = c[1]; *(LDS3 f32x4*)(xp + 2048) = c[2]; }
            asm volatile("s_waitcnt lgkmcnt(0)" ::: "memory");
            if (lane == 0) __hip_atomic_store((LDS3 unsigned*)(lds + C_FIN + w * 4), 1u, __ATOMIC_RELAXED, __HIP_MEMORY_SCOPE_WORKGROUP);
            lds_wait(lds + C_FIN + (w ^ 4) * 4, 1);
            lds_wait(lds + C_BP + tt * 4, 1);
            u16* op = P.out + (size_t)(mtO * 64 + tt * 16 + c16) * DM + ntO * 80 + g * 4;
#pragma unroll
            for (int n = 0; n < 5; ++n) {
                if ((w < 4) == (n < 3)) {
                    const f32x4 bp = *(LDS3 const f32x4*)(n < 3 ? lds + X_FIN + tt * 3072 + n * 1024 + lane * 16 : lds + 143360 + tt * 2048 + (n - 3) * 1024 + lane * 16);
                    const f32x4 s = c[n] + *(LDS3 const f32x4*)(xp + n * 1024) + bp;
                    *(u32x2*)(op + n * 16) = (u32x2){pk2(s[0], s[1]), pk2(s[2], s[3])};
                }
            }
            if (bid == 0 && mtid == 0) set_flag(ws, WS_EPOCH, tag);
        }
        STAMP(10);
    }
}

extern "C" void kernel_launch(void* const* d_in, const int* in_sizes, int n_in, void* d_out, int out_size, void* d_ws, size_t ws_size,
                              hipStream_t stream) {
    Params P;
    P.x = (const u16*)d_in[0]; P.wa = (const u16*)d_in[1]; P.woa = (const u16*)d_in[2]; P.wb = (const u16*)d_in[4];
    P.wphi = (const u16*)d_in[5]; P.wob = (const u16*)d_in[6]; P.gw = (const u16*)d_in[7]; P.ws = (char*)d_ws; P.out = (u16*)d_out;
    mega<<<256, 768, 0, stream>>>(P);
}
```

```cpp
#include <hip/hip_runtime.h>
#include <hip/hip_bf16.h>
#include <stdint.h>
#include <type_traits>

typedef __attribute__((ext_vector_type(8))) short bf16x8;
typedef __attribute__((ext_vector_type(4))) short bf16x4;
typedef __attribute__((ext_vector_type(16))) float f32x16;
typedef __attribute__((ext_vector_type(4))) float f32x4;
typedef __attribute__((ext_vector_type(2))) float f32x2;
typedef __attribute__((ext_vector_type(4))) unsigned u32x4;
typedef __attribute__((ext_vector_type(2))) unsigned u32x2;
typedef __attribute__((ext_vector_type(2))) __bf16 bf2_t;
typedef unsigned short u16;
typedef unsigned long long u64;
#define LDS3 __attribute__((address_space(3)))
typedef __attribute__((address_space(1))) u64 gu64;
#define RLX_AGENT __ATOMIC_RELAXED, __HIP_MEMORY_SCOPE_AGENT

constexpr int GA_PUB = 11, HS = 8, MS = 2, PRE_SLEEP = 2;
constexpr int SEQ = 2048, DM = 640, NH = 32;
constexpr float LOG2E = 1.4426950408889634f;
constexpr float QSCALE = 0.22360679774997896f * LOG2E;

constexpr unsigned WS_EPOCH = 0;
constexpr unsigned WS_F_G   = 256;
constexpr unsigned WS_F_SD  = WS_F_G + 4096;
constexpr unsigned WS_F_KV  = WS_F_SD + 256;
constexpr unsigned WS_F_OB  = WS_F_KV + 2048;
constexpr unsigned WS_F_OA  = WS_F_OB + 2048;
constexpr unsigned WS_G     = 16384;
constexpr unsigned WS_Q     = 32768;
constexpr unsigned WS_KV    = WS_Q + 2 * NH * 81920;
constexpr unsigned WS_KVP   = WS_KV + 2 * NH * 163840;
constexpr unsigned WS_AO    = WS_KVP + NH * 8 * 4096;
constexpr unsigned WS_WT    = WS_AO + SEQ * 1280 * 2;
constexpr unsigned WS_END   = WS_WT + 1280 * 640 * 2;

#define STAMP(i)

__device__ __forceinline__ unsigned pk2(float a, float b) { f32x2 v = {a, b}; bf2_t r = __builtin_convertvector(v, bf2_t); return __builtin_bit_cast(unsigned, r); }
__device__ __forceinline__ float bf_lo(unsigned u) { return __uint_as_float(u << 16); }
__device__ __forceinline__ float bf_hi(unsigned u) { return __uint_as_float(u & 0xffff0000u); }
__device__ __forceinline__ float ex2(float x) { return __builtin_amdgcn_exp2f(x); }
#define MFMA32_16(a, b, c) __builtin_amdgcn_mfma_f32_32x32x16_bf16(__builtin_bit_cast(bf16x8, a), __builtin_bit_cast(bf16x8, b), c, 0, 0, 0)
#define MFMA32_8(a, b, c) __builtin_amdgcn_mfma_f32_32x32x8bf16_1k(__builtin_bit_cast(bf16x4, a), __builtin_bit_cast(bf16x4, b), c, 0, 0, 0)
#define MFMA16_32(a, b, c) __builtin_amdgcn_mfma_f32_16x16x32_bf16(__builtin_bit_cast(bf16x8, a), __builtin_bit_cast(bf16x8, b), c, 0, 0, 0)

__device__ __forceinline__ void glds16(unsigned voff, const void* sbase, unsigned lds_base) {
    unsigned sv;
    asm volatile("s_mov_b32 %0, m0\n\ts_mov_b32 m0, %3\n\ts_nop 0\n\tglobal_load_lds_dwordx4 %1, %2\n\ts_mov_b32 m0, %0"
                 : "=&s"(sv) : "v"(voff), "s"(sbase), "s"(__builtin_amdgcn_readfirstlane(lds_base)) : "memory");
}
__device__ __forceinline__ void glds16s(unsigned voff, const void* sbase, unsigned lds_base) {
    unsigned sv;
    asm volatile("s_mov_b32 %0, m0\n\ts_mov_b32 m0, %3\n\ts_nop 0\n\tglobal_load_lds_dwordx4 %1, %2 sc1\n\ts_mov_b32 m0, %0"
                 : "=&s"(sv) : "v"(voff), "s"(sbase), "s"(__builtin_amdgcn_readfirstlane(lds_base)) : "memory");
}
__device__ __forceinline__ void glds4s(unsigned voff, const void* sbase, unsigned lds_base) {
    unsigned sv;
    asm volatile("s_mov_b32 %0, m0\n\ts_mov_b32 m0, %3\n\ts_nop 0\n\tglobal_load_lds_dword %1, %2 sc1\n\ts_mov_b32 m0, %0"
                 : "=&s"(sv) : "v"(voff), "s"(sbase), "s"(__builtin_amdgcn_readfirstlane(lds_base)) : "memory");
}
__device__ __forceinline__ void glds16x4(const unsigned (&vo)[4], const void* sbase, unsigned lds_base) {
    unsigned sv;
    asm volatile("s_mov_b32 %0, m0\n\ts_mov_b32 m0, %6\n\ts_nop 0\n\tglobal_load_lds_dwordx4 %1, %5\n\tglobal_load_lds_dwordx4 %2, %5 offset:1024\n\t"
                 "global_load_lds_dwordx4 %3, %5 offset:2048\n\tglobal_load_lds_dwordx4 %4, %5 offset:3072\n\ts_mov_b32 m0, %0"
                 : "=&s"(sv) : "v"(vo[0]), "v"(vo[1]), "v"(vo[2]), "v"(vo[3]), "s"(sbase), "s"(lds_base) : "memory");
}
typedef short v4i16_t __attribute__((ext_vector_type(4)));
__device__ __forceinline__ u32x2 trread(LDS3 const char* p) {
    return __builtin_bit_cast(u32x2, __builtin_amdgcn_ds_read_tr16_b64_v4i16((LDS3 v4i16_t*)p));
}
#define WAIT_BAR(N) asm volatile("s_waitcnt vmcnt(" #N ") lgkmcnt(0)\n\ts_barrier" ::: "memory")
#define DRAIN() asm volatile("s_waitcnt vmcnt(0)" ::: "memory")
#define SBAR() __builtin_amdgcn_sched_barrier(0)
#define PIN(x) asm volatile("" : "+v"(x))

__device__ __forceinline__ float xhalf_max(float x) {
    auto r = __builtin_amdgcn_permlane32_swap(__float_as_uint(x), __float_as_uint(x), false, false);
    return fmaxf(__uint_as_float(r[0]), __uint_as_float(r[1]));
}
__device__ __forceinline__ float xhalf_sum(float x) {
    auto r = __builtin_amdgcn_permlane32_swap(__float_as_uint(x), __float_as_uint(x), false, false);
    return __uint_as_float(r[0]) + __uint_as_float(r[1]);
}
__device__ __forceinline__ float upper_half(float x) {
    auto r = __builtin_amdgcn_permlane32_swap(__float_as_uint(x), __float_as_uint(x), false, false);
    return __uint_as_float(r[1]);
}

typedef __amdgpu_buffer_rsrc_t rsrc_t;
#define ST8(v, off)  __builtin_amdgcn_raw_buffer_store_b64(v, rs, (int)(off), 0, 16)
#define ST16(v, off) __builtin_amdgcn_raw_buffer_store_b128(v, rs, (int)(off), 0, 16)
#define ST4(v, off)  __builtin_amdgcn_raw_buffer_store_b32(v, rs, (int)(off), 0, 16)
#define LD16(off) __builtin_amdgcn_raw_buffer_load_b128(rs, (int)(off), 0, 16)
#define LD4(off)  __builtin_amdgcn_raw_buffer_load_b32(rs, (int)(off), 0, 16)

template <int SLP = 2>
__device__ __forceinline__ void wait_flags(const char* ws, unsigned off0, unsigned off1, u64 tag) {
    gu64* p0 = (gu64*)(ws + off0); gu64* p1 = (gu64*)(ws + off1);
#pragma unroll 1
    for (unsigned spins = 0; spins < (1u << 16); ++spins) {
        const u64 a = __hip_atomic_load(p0, RLX_AGENT), b = __hip_atomic_load(p1, RLX_AGENT);
        if (__all(a == tag && b == tag)) break;
        __builtin_amdgcn_s_sleep(SLP);
    }
    __builtin_amdgcn_fence(__ATOMIC_ACQUIRE, "wavefront");
}
__device__ __forceinline__ void set_flag(char* ws, unsigned off, u64 tag) { __hip_atomic_store((gu64*)(ws + off), tag, RLX_AGENT); }

struct Params { const u16* x; const u16* wa; const u16* wb; const u16* wphi; const u16* woa; const u16* wob; const u16* gw; char* ws; u16* out; };

__device__ __forceinline__ void feature_map(const u32x4& pm, unsigned pt, const u32x4& tm, unsigned tt, int h, u32x4& fp, u32x4& fn, u32x4& ft) {
    const f32x16 z16 = {};
    const u32x2 a8 = {pt, 0u}, b8 = {tt, 0u};
    f32x16 s = MFMA32_16(pm, tm, z16); s = MFMA32_8(a8, b8, s);
    f32x2 v[6];
#pragma unroll
    for (int i = 0; i < 6; ++i) {
        v[i] = (f32x2){s[2 * i], s[2 * i + 1]};
    }
    float mx = fmaxf(v[0][0], v[0][1]), mn = fminf(v[0][0], v[0][1]);
#pragma unroll
    for (int i = 1; i < 6; ++i) { mx = fmaxf(fmaxf(mx, v[i][0]), v[i][1]); mn = fminf(fminf(mn, v[i][0]), v[i][1]); }
    mx = xhalf_max(mx) * -LOG2E; mn = xhalf_max(-mn) * -LOG2E;
    f32x2 ep[6], en[6];
#pragma unroll
    for (int i = 0; i < 6; ++i) {
        const f32x2 a = v[i] * LOG2E + mx, b = v[i] * -LOG2E + mn;
        ep[i] = (f32x2){ex2(a[0]), ex2(a[1])}; en[i] = (f32x2){ex2(b[0]), ex2(b[1])};
    }
    const float tw = h ? 0.f : 1.f;
    const f32x2 s2p = ((ep[0] + ep[1]) + (ep[2] + ep[3])) + (ep[4] + ep[5]) * tw, s2n = ((en[0] + en[1]) + (en[2] + en[3])) + (en[4] + en[5]) * tw;
    const float sp = __builtin_amdgcn_rcpf(xhalf_sum(s2p[0] + s2p[1])), sn = __builtin_amdgcn_rcpf(xhalf_sum(s2n[0] + s2n[1]));
    const float tp = sp * tw, tn = sn * tw;
#pragma unroll
    for (int i = 0; i < 4; ++i) { const f32x2 a = ep[i] * sp, b = en[i] * sn; fp[i] = pk2(a[0], a[1]); fn[i] = pk2(b[0], b[1]); }
    const f32x2 c0 = ep[4] * tp, c1 = ep[5] * tp, c2 = en[4] * tn, c3 = en[5] * tn;
    ft = (u32x4){pk2(c0[0], c0[1]), pk2(c1[0], c1[1]), pk2(c2[0], c2[1]), pk2(c3[0], c3[1])};
}

__device__ __forceinline__ void store_o(rsrc_t rs, const float (&o)[12], float sc, int tok, int half, int head, int h) {
    const auto s0 = __builtin_amdgcn_permlane32_swap(pk2(o[0] * sc, o[1] * sc), pk2(o[4] * sc, o[5] * sc), false, false);
    const auto s1 = __builtin_amdgcn_permlane32_swap(pk2(o[2] * sc, o[3] * sc), pk2(o[6] * sc, o[7] * sc), false, false);
    const unsigned tb = WS_AO + ((tok >> 4) * 40 + half * 20) * 1024;
    const u32x4 mv = {s0[0], s1[0], s0[1], s1[1]};
    ST16(mv, tb + (head >> 1) * 1024 + (((head & 1) * 2 + h) * 16 + (tok & 15)) * 16);
    const u32x2 tv = {pk2(o[8] * sc, o[9] * sc), pk2(o[10] * sc, o[11] * sc)};
    if (!h) ST8(tv, tb + (16 + (head >> 3)) * 1024 + (head & 7) * 128 + (tok & 15) * 8);
}
#define LD8(off)  __builtin_amdgcn_raw_buffer_load_b64(rs, (int)(off), 0, 16)
#define LD_TAIL(dst, base) do { const u32x2 a_ = LD8((base) + (lane >> 4) * 256 + (lane & 15) * 8), b_ = LD8((base) + (lane >> 4) * 256 + 128 + (lane & 15) * 8); dst = (u32x4){a_[0], a_[1], b_[0], b_[1]}; } while (0)

#define DSR128(dst, addr, off) asm volatile("ds_read_b128 %0, %1 offset:%2" : "=v"(dst) : "v"(addr), "i"(off))
#define DSR32(dst, addr, off) asm volatile("ds_read_b32 %0, %1 offset:%2" : "=v"(dst) : "v"(addr), "i"(off))
#define DSR128M2(d0, a0, o0, d1, a1, o1) asm volatile("s_mov_b64 exec, %6\n\tds_read_b128 %0, %2 offset:%3\n\tds_read_b128 %1, %4 offset:%5\n\ts_mov_b64 exec, -1" \
                                                      : "+v"(d0), "+v"(d1) : "v"(a0), "i"(o0), "v"(a1), "i"(o1), "s"(vmask))
#define LWAIT(n) do { asm volatile("s_waitcnt lgkmcnt(" #n ")" ::: "memory"); SBAR(); } while (0)

#define BAR() asm volatile("s_barrier" ::: "memory")
#define LDS_SET(off, v) __hip_atomic_store((LDS3 unsigned*)(lds + (off)), (unsigned)(v), __ATOMIC_RELAXED, __HIP_MEMORY_SCOPE_WORKGROUP)
#define LDS_CNT_ADD(off) __hip_atomic_fetch_add((LDS3 unsigned*)(lds + (off)), 1u, __ATOMIC_RELAXED, __HIP_MEMORY_SCOPE_WORKGROUP)

template <int SLP = 1>
__device__ __forceinline__ void lds_wait(LDS3 char* p, unsigned want) {
#pragma unroll 1
    for (unsigned spins = 0; spins < (1u << 20); ++spins) {
        if ((unsigned)__builtin_amdgcn_readfirstlane(__hip_atomic_load((LDS3 unsigned*)p, __ATOMIC_RELAXED, __HIP_MEMORY_SCOPE_WORKGROUP)) >= want) break;
        __builtin_amdgcn_s_sleep(SLP);
    }
    __builtin_amdgcn_fence(__ATOMIC_ACQUIRE, "wavefront");
}

template <int SLP>
__device__ __forceinline__ void prog_wait(LDS3 char* p, int lane, unsigned want) {
#pragma unroll 1
    for (unsigned spins = 0; spins < (1u << 20); ++spins) {
        if (__all(__hip_atomic_load((LDS3 unsigned*)p + (lane & 7), __ATOMIC_RELAXED, __HIP_MEMORY_SCOPE_WORKGROUP) >= want)) break;
        __builtin_amdgcn_s_sleep(SLP);
    }
    __builtin_amdgcn_fence(__ATOMIC_ACQUIRE, "wavefront");
}
#define POLL_ONE(cond, SLP, o0, o1, word) POLL_ONE2(cond, SLP, 2, o0, o1, word)
#define POLL_ONE2(cond, SLP, SLP2, o0, o1, word) do { if (cond) { wait_flags<SLP>(ws, o0, o1, tag); \
        if (lane == 0) __hip_atomic_store((LDS3 unsigned*)(lds + (word)), 1u, __ATOMIC_RELAXED, __HIP_MEMORY_SCOPE_WORKGROUP); } else lds_wait<SLP2>(lds + (word), 1); } while (0)
constexpr int C_KV = 0, C_KVF = 4, C_OB = 8, C_WA = 16, C_OA = 20, C_BX = 28, C_MRG = 32  , C_FIN = 64  , C_BP = 96  ;
constexpr int C_P1 = 112, C_P2 = 116, C_P3 = 120, C_P4 = 124, C_PR = 128  , C_KVL = 160, C_P5 = 164, C_GB = 168;
constexpr int G_Q = 176, G_P = 180, G_ST = 184, G_RD = 188;
constexpr int X_G = 256;
constexpr int X_KVF = 1280, X_SLOT = 4096;
constexpr int X_MRG = 1280;
constexpr int L_WA = 24576;
constexpr int L_Q = 81920;
constexpr int X_FIN = 126976;

__global__ __launch_bounds__(768, 3) void mega(Params P) {
    __shared__ __attribute__((aligned(16))) char smem[163840];
    u64 t_start; asm volatile("s_memrealtime %0\n\ts_waitcnt lgkmcnt(0)" : "=s"(t_start) :: "memory");
    LDS3 char* lds = (LDS3 char*)smem;
    char* ws = P.ws;
    u16* outp = P.out;
    asm volatile("" : "+s"(outp), "+s"(ws));
    const int tid = threadIdx.x, lane = tid & 63, r = lane & 31, h = lane >> 5;
    const int hw = __builtin_amdgcn_readfirstlane(tid >> 6);
    const bool helper = hw < 4;
    const int w = hw - 4, mtid = tid - 256;
    const int bid = blockIdx.x;
    const unsigned lds0 = (unsigned)(uintptr_t)smem;
    const rsrc_t rs = __builtin_amdgcn_make_buffer_rsrc(ws, 0, WS_END, 0x00020000);
    STAMP(0);
    u64 tag = 0;
#define GET_TAG() do { asm volatile("s_load_dwordx2 %0, %1, 0x0\n\ts_waitcnt lgkmcnt(0)" : "=s"(tag) : "s"(ws) : "memory"); tag += 1; } while (0)
    const int head = (bid & 7) * 4 + ((bid >> 3) & 3), qb = bid >> 5;
    const int nlo = (head * 20) >> 7, nhi = (head * 20 + 19) >> 7;
    const int mtO = (bid & 7) * 4 + ((bid >> 3) & 3), ntO = bid >> 5;
    const int lin = (bid & 7) * 30 + (bid >> 3), lh = lin >= 120, lrem = lin - 120 * lh;
    const int mt = lh * 8 + (lrem & 7), nt = lrem >> 3;
    const unsigned fG = WS_F_G + (nt * 16 + mt) * 8;
    const unsigned one = (r == 20) ? 0x3f803f80u : 0u;
    const f32x16 z16 = {};

    if (helper) {
        const unsigned fA0 = WS_F_G + (((lane < 32 ? 5 : 10) + ((lane & 16) ? nhi : nlo)) * 16 + (lane & 15)) * 8;
        const unsigned fA1 = WS_F_G + (((lane & 2) ? nhi : nlo) * 16 + qb * 2 + (lane & 1)) * 8;
        __builtin_amdgcn_s_setprio(3);
        const char* kvb = ws + WS_KV + head * 163840;
        auto kv_issue = [&](int grp, int t0 = 0, int t1 = 8) {
#pragma unroll
            for (int t = t0; t < t1; ++t) { int pc = grp * 32 + hw + 4 * t; if (pc == 0) pc = 4; glds16s(lane * 16, kvb + pc * 1024, lds0 + pc * 1024); }
        };
        auto q_issue = [&]() {
            const char* qbase = ws + WS_Q + head * 81920 + qb * 10240;
#pragma unroll
            for (int t = 0; t < 3; ++t) { const int pc = hw + 4 * t < 10 ? hw + 4 * t : 10; glds16s(lane * 16, pc < 10 ? qbase + pc * 1024 : kvb, lds0 + L_Q + pc * 1024); }
        };
        if (bid < 240) {
            const int rot = ((nt + mt) % 3) * 3;
            unsigned xo[4], wo[4];
#pragma unroll
            for (int i = 0; i < 4; ++i) {
                xo[i] = (mt * 128 + 32 * hw + 8 * i + (lane >> 3)) * 1280 + (((lane & 7) ^ ((4 * i + (lane >> 4)) & 7)) << 4) - i * 1024;
                wo[i] = (16 * hw + 4 * i + (lane >> 4)) * 3840 + nt * 256 + (((lane & 15) ^ (((lane >> 4) << 2) | i)) << 4) - i * 1024;
            }
            const unsigned dX = lds0 + 32768 + hw * 4096;
            int kt = rot;
#pragma unroll
            for (int n = 0; n < 20; ++n) {
                const char* xb = (const char*)P.x + kt * 128;
                const char* wb = (const char*)(n >= 10 ? P.wb : P.wa) + (size_t)kt * (64 * 3840);
                asm volatile("" : "+s"(xb), "+s"(wb));
                if (n == 2) { if (hw == 0) *(LDS3 unsigned*)(lds + lane * 4) = 0u; GET_TAG(); WAIT_BAR(8); }
                else if (n == 11 || n == 15) { WAIT_BAR(12); }
                else if (n == 3 || (n >= 12 && n <= 14)) { WAIT_BAR(8); }
                else if (n >= 5) { WAIT_BAR(16); }
                glds16x4(wo, wb, dX + 16384 + (n & 3) * 32768);
                if (n < 10 || n >= 14) glds16x4(xo, xb, dX + ((n >= 10 ? 20 - n : n + 1) & 3) * 32768);
                if (n < 9) kt = kt == 9 ? 0 : kt + 1; else if (n > 9) kt = kt == 0 ? 9 : kt - 1;
            }
            WAIT_BAR(16);
            if (hw == 0) {
                const u64 a = __hip_atomic_load((gu64*)(ws + fA0), RLX_AGENT), b = __hip_atomic_load((gu64*)(ws + fA1), RLX_AGENT);
                if (__all(a == tag && b == tag) && lane == 0) LDS_SET(G_P, 1);
            }
            WAIT_BAR(0);
        } else {
            if (hw == 0) *(LDS3 unsigned*)(lds + lane * 4) = 0u;
            GET_TAG();
            WAIT_BAR(63);
            if (hw == 0) {
#pragma unroll 1
                for (int t = 0; t < PRE_SLEEP; ++t) __builtin_amdgcn_s_sleep(127);
            }
        }
        if (!__builtin_amdgcn_readfirstlane(__hip_atomic_load((LDS3 unsigned*)(lds + G_P), __ATOMIC_RELAXED, __HIP_MEMORY_SCOPE_WORKGROUP))) POLL_ONE(hw == 0, 6, fA0, fA1, G_P);
        __builtin_amdgcn_fence(__ATOMIC_ACQUIRE, "wavefront");
        kv_issue(0); q_issue(); kv_issue(1, 0, 2);
        DRAIN();
        if (lane == 0) LDS_CNT_ADD(G_Q);
        lds_wait<2>(lds + G_RD, 8);
        kv_issue(1, 2, 8); kv_issue(2); kv_issue(3); kv_issue(4);
        asm volatile("s_waitcnt vmcnt(24)" ::: "memory"); if (lane == 0) LDS_CNT_ADD(C_KVL);
        asm volatile("s_waitcnt vmcnt(16)" ::: "memory"); if (lane == 0) LDS_CNT_ADD(C_KVL + 12);
        STAMP(3);
        u32x4 phm; unsigned pht;
        {
            const u16* wp = P.wphi + head * 400 + (r < 20 ? r : (r & 3));
            unsigned e[10];
#pragma unroll
            for (int j = 0; j < 8; ++j) e[j] = wp[(8 * h + j + 4 * (head & 1)) * 20];
            e[8] = wp[((head & 1 ? 0 : 16) + 2 * h) * 20]; e[9] = wp[((head & 1 ? 1 : 17) + 2 * h) * 20];
            const int bu = bid < 160 ? bid : 0, hf = bu >= 80, k0 = (bu - 80 * hf) * 8, hd = k0 < 512 ? k0 >> 4 : (k0 - 512) >> 2;
            const int row = k0 < 512 ? 20 * hd + (k0 & 8) + 4 * (hd & 1) : 20 * hd + 16;
            const u16* wsrc = (hf ? P.wob : P.woa) + (size_t)row * DM;
            unsigned f[3][8];
            if (bid < 160) {
#pragma unroll
                for (int q = 0; q < 3; ++q) {
                    const int n = tid + 256 * q < 640 ? tid + 256 * q : 639;
#pragma unroll
                    for (int j = 0; j < 8; ++j) f[q][j] = wsrc[j * DM + n];
                }
            }
            DRAIN();
            if (lane == 0) LDS_CNT_ADD(C_WA + 8);
            phm = (u32x4){e[0] | (e[1] << 16), e[2] | (e[3] << 16), e[4] | (e[5] << 16), e[6] | (e[7] << 16)}; pht = e[8] | (e[9] << 16);
            if (bid < 160) {
#pragma unroll
                for (int q = 0; q < 3; ++q) {
                    const int n = tid + 256 * q;
                    const u32x4 v = {f[q][0] | (f[q][1] << 16), f[q][2] | (f[q][3] << 16), f[q][4] | (f[q][5] << 16), f[q][6] | (f[q][7] << 16)};
                    if (n < 640) ST16(v, WS_WT + (((n >> 4) * 40 + hf * 20 + (k0 >> 5)) * 64 + ((k0 >> 3) & 3) * 16 + (n & 15)) * 16);
                }
            }
        }
        const int tokS = qb * 256 + hw * 64, m7 = tokS >> 7, t4 = (tokS >> 5) & 3;
        {
            const unsigned pt3 = (lane >> 1) & 3;
            const unsigned tb = WS_F_G + 2048 + ((((lane & 1) ? nhi : nlo) + (pt3 < 3 ? pt3 : 0) * 5) * 16 + qb * 2 + ((lane >> 3) & 1)) * 8;
            POLL_ONE2(hw == 0, 4, HS, tb, WS_F_SD + (qb * 2 + (lane & 1)) * 8, C_P1);
        }
        const unsigned om = t4 * 1024 + lane * 16, ot = 4096 + (t4 * 32 + r) * 8 + 4 * h;
        u32x4 qf[2][3]; float gS[2];
        {
            const unsigned kb = WS_KV + (NH + head) * 163840 + (2 * (m7 & 7) + (m7 >> 3)) * 10240, qbb = WS_Q + (NH + head) * 81920 + m7 * 5120;
            const unsigned ov = 5120 + t4 * 1280 + h * 16 + (r < 20 ? r : 19) * 32;
            u32x4 e0[2], w0[2], w1[2], eq[2]; unsigned et[2], eqt[2];
#pragma unroll
            for (int i = 0; i < 2; ++i) {
                e0[i] = LD16(kb + om + i * 1024); et[i] = LD4(kb + ot + i * 256); w0[i] = LD16(kb + ov + i * 1280); w1[i] = LD16(kb + ov + i * 1280 + 640);
                eq[i] = LD16(qbb + om + i * 1024); eqt[i] = LD4(qbb + ot + i * 256); gS[i] = __uint_as_float(LD4(WS_G + (tokS + i * 32 + r) * 4));
            }
            u32x4 i0, i1;
#pragma unroll
            for (int j = 0; j < 4; ++j) {
                const int e = r - 8 * h - 2 * j;
                i0[j] = e == 0 ? 0x3f80u : (e == 1 ? 0x3f800000u : 0u);
                i1[j] = e == 16 ? 0x3f80u : (e == 17 ? 0x3f800000u : 0u);
            }
            f32x16 k0 = {}, k1 = {};
#pragma unroll
            for (int i = 0; i < 2; ++i) {
                if (r >= 20) { w0[i] = (u32x4){one, one, one, one}; w1[i] = w0[i]; }
                u32x4 fp, fn, ft;
                feature_map(phm, pht, e0[i], et[i], h, fp, fn, ft);
                f32x16 T0 = MFMA32_16(fp, i0, z16); T0 = MFMA32_16(fn, i1, T0);
                f32x16 T1 = MFMA32_16(ft, i0, z16);
                u32x4 b00, b01, b10, b11;
#pragma unroll
                for (int j = 0; j < 4; ++j) {
                    b00[j] = pk2(T0[2 * j], T0[2 * j + 1]); b01[j] = pk2(T0[8 + 2 * j], T0[9 + 2 * j]);
                    b10[j] = pk2(T1[2 * j], T1[2 * j + 1]); b11[j] = pk2(T1[8 + 2 * j], T1[9 + 2 * j]);
                }
                k0 = MFMA32_16(w0[i], b00, k0); k0 = MFMA32_16(w1[i], b01, k0);
                k1 = MFMA32_16(w0[i], b10, k1); k1 = MFMA32_16(w1[i], b11, k1);
            }
            prog_wait<8>(lds + C_PR, lane, 1);
            *(LDS3 float*)(lds + X_G + (hw * 64 + lane) * 4) = h ? gS[1] : gS[0];
            if (hw * 64 + lane < 144) *(LDS3 u32x4*)(lds + X_KVF + (hw * 64 + lane) * 16) = (u32x4){0u, 0u, 0u, 0u};
            LDS3 char* sp = lds + X_SLOT + hw * 4096;
#pragma unroll
            for (int gq = 0; gq < 3; ++gq) {
                *(LDS3 f32x4*)(sp + gq * 1024 + lane * 16) = (f32x4){k0[4 * gq], k0[4 * gq + 1], k0[4 * gq + 2], k0[4 * gq + 3]};
                if (r < 8) *(LDS3 f32x4*)(sp + 3072 + (gq * 16 + h * 8 + r) * 16) = (f32x4){k1[4 * gq], k1[4 * gq + 1], k1[4 * gq + 2], k1[4 * gq + 3]};
            }
            asm volatile("s_waitcnt vmcnt(0) lgkmcnt(0)" ::: "memory");
            unsigned arr = 0;
            if (lane == 0) arr = LDS_CNT_ADD(C_KV);
            if (__builtin_amdgcn_readfirstlane(arr) == 3) {
                __builtin_amdgcn_fence(__ATOMIC_ACQUIRE, "wavefront");
#pragma unroll
                for (int i = 0; i < 4; ++i) {
                    const int qd = lane + 64 * i;
                    LDS3 const char* s0 = lds + X_SLOT + qd * 16;
                    const f32x4 s = (*(LDS3 const f32x4*)s0 + *(LDS3 const f32x4*)(s0 + 4096)) + (*(LDS3 const f32x4*)(s0 + 8192) + *(LDS3 const f32x4*)(s0 + 12288));
                    if (qd < 240) ST16(__builtin_bit_cast(u32x4, s), WS_KVP + (head * 8 + qb) * 4096 + qd * 16);
                }
                DRAIN();
                if (lane == 0) set_flag(ws, WS_F_KV + (head * 8 + qb) * 8, tag);
            }
#pragma unroll
            for (int i = 0; i < 2; ++i) feature_map(phm, pht, eq[i], eqt[i], h, qf[i][0], qf[i][1], qf[i][2]);
        }
        STAMP(4);
        POLL_ONE2(hw == 0, 4, HS, WS_F_KV + (head * 8 + (lane & 7)) * 8, WS_F_KV + (head * 8 + (lane & 7)) * 8, C_P2);
        {
            const int qd = hw * 60 + (lane < 60 ? lane : 59);
            f32x4 pp[8];
#pragma unroll
            for (int p = 0; p < 8; ++p) pp[p] = __builtin_bit_cast(f32x4, LD16(WS_KVP + (head * 8 + p) * 4096 + qd * 16));
            const f32x4 s4 = ((pp[0] + pp[1]) + (pp[2] + pp[3])) + ((pp[4] + pp[5]) + (pp[6] + pp[7]));
            const int gq = qd < 192 ? qd >> 6 : (qd - 192) >> 4, hs = qd < 192 ? (qd >> 5) & 1 : (qd >> 3) & 1, sg = qd < 192 ? qd & 31 : 32 + (qd & 7);
            const unsigned a = pk2(s4[0], s4[1]), b = pk2(s4[2], s4[3]);
            LDS3 u16* kp = (LDS3 u16*)(lds + X_KVF + (8 * gq + 4 * hs) * 96 + sg * 2);
            kp[0] = (u16)a; kp[48] = (u16)(a >> 16); kp[96] = (u16)b; kp[144] = (u16)(b >> 16);
            asm volatile("s_waitcnt lgkmcnt(0)" ::: "memory");
            if (lane == 0) LDS_CNT_ADD(C_KVF);
            lds_wait<2>(lds + C_KVF, 4);
        }
        {
            LDS3 const char* kp = lds + X_KVF + (r < 24 ? r : 23) * 96 + h * 16;
            const u32x4 a0 = *(LDS3 const u32x4*)kp, a1 = *(LDS3 const u32x4*)(kp + 32), a2 = *(LDS3 const u32x4*)(kp + 64);
#pragma unroll
            for (int i = 0; i < 2; ++i) {
                f32x16 d = MFMA32_16(a0, qf[i][0], z16); d = MFMA32_16(a1, qf[i][1], d); d = MFMA32_16(a2, qf[i][2], d);
                const float den = upper_half(d[8]) + 1e-6f;
                float o[12];
#pragma unroll
                for (int j = 0; j < 12; ++j) o[j] = d[j];
                store_o(rs, o, (1.f - gS[i]) / den, tokS + i * 32 + r, 1, head, h);
            }
            DRAIN();
            unsigned arr = 0;
            if (lane == 0) arr = LDS_CNT_ADD(C_OB);
            if (__builtin_amdgcn_readfirstlane(arr) == 3 && lane == 0) set_flag(ws, WS_F_OB + (qb * 32 + head) * 8, tag);
        }
        STAMP(5);
        f32x4 c[5];
#pragma unroll
        for (int n = 0; n < 5; ++n) c[n] = (f32x4){0.f, 0.f, 0.f, 0.f};
        const char* wsrc = ws + WS_WT + (size_t)ntO * 5 * 40960;
        {
            if (hw == 0) wait_flags<8>(ws, WS_F_KV + lane * 8, WS_F_KV + 512 + lane * 8, tag);
            POLL_ONE2(hw == 0, 8, HS, WS_F_KV + 1024 + lane * 8, WS_F_KV + 1536 + lane * 8, C_P5);
#pragma unroll 1
            for (int t = 0; t < 5; ++t) {
                if (t == 0) prog_wait<2 * HS>(lds + C_PR, lane, 4); else if (t == 3) prog_wait<2 * HS>(lds + C_PR, lane, 5);
                const int ks = hw + 4 * t < 19 ? hw + 4 * t : 18;
#pragma unroll
                for (int n = 0; n < 5; ++n) glds16s(lane * 16, wsrc + (n * 40 + 20 + ks) * 1024, lds0 + 4096 + (ks * 5 + n) * 1024);
            }
            POLL_ONE2(hw == 0, 4, HS, WS_F_OB + ((mtO >> 2) * 32 + r) * 8, WS_F_OB + ((mtO >> 2) * 32 + r) * 8, C_P3);
            const unsigned ab = WS_AO + ((mtO * 4 + hw) * 40 + 20) * 1024;
            u32x4 af[20];
#pragma unroll
            for (int ks = 0; ks < 16; ++ks) af[ks] = LD16(ab + ks * 1024 + lane * 16);
#pragma unroll
            for (int ks = 16; ks < 20; ++ks) LD_TAIL(af[ks], ab + ks * 1024);
            asm volatile("s_waitcnt vmcnt(20)" ::: "memory");
            if (lane == 0) LDS_CNT_ADD(C_BX);
            lds_wait<2>(lds + C_BX, 4);
            u32x4 wf[3][5], wl[5];
            LDS3 const char* wp = lds + 4096 + lane * 16;
#pragma unroll
            for (int ks = 0; ks < 2; ++ks)
#pragma unroll
                for (int n = 0; n < 5; ++n) wf[ks][n] = *(LDS3 const u32x4*)(wp + (ks * 5 + n) * 1024);
#pragma unroll
            for (int ks = 0; ks < 20; ++ks) {
                if (ks + 2 < 19) {
#pragma unroll
                    for (int n = 0; n < 5; ++n) wf[(ks + 2) % 3][n] = *(LDS3 const u32x4*)(wp + ((ks + 2) * 5 + n) * 1024);
                }
                if (ks == 8) {
#pragma unroll
                    for (int n = 0; n < 5; ++n) wl[n] = LD16(WS_WT + ((ntO * 5 + n) * 40 + 39) * 1024 + lane * 16);
                }
                SBAR();
#pragma unroll
                for (int n = 0; n < 5; ++n) c[n] = MFMA16_32(ks < 19 ? wf[ks % 3][n] : wl[n], af[ks], c[n]);
                SBAR();
            }
            if (lane == 0) LDS_CNT_ADD(C_BX);
            lds_wait<2>(lds + C_BX, 8);
        }
        STAMP(6);
        prog_wait<2 * HS>(lds + C_PR, lane, 7);
#pragma unroll 1
        for (int n = 0; n < 5; ++n)
#pragma unroll
            for (int t = 0; t < 5; ++t) { const int ks = hw + 4 * t; glds16s(lane * 16, wsrc + (n * 40 + ks) * 1024, lds0 + L_WA + (n * 20 + ks) * 1024); }
#pragma unroll
        for (int n = 0; n < 3; ++n) *(LDS3 f32x4*)(lds + X_FIN + hw * 3072 + n * 1024 + lane * 16) = c[n];
        DRAIN();
        if (lane == 0) LDS_CNT_ADD(C_WA);
        prog_wait<2 * HS>(lds + C_PR, lane, 8);
#pragma unroll
        for (int n = 3; n < 5; ++n) *(LDS3 f32x4*)(lds + 143360 + hw * 2048 + (n - 3) * 1024 + lane * 16) = c[n];
        asm volatile("s_waitcnt lgkmcnt(0)" ::: "memory");
        if (lane == 0) __hip_atomic_store((LDS3 unsigned*)(lds + C_BP + hw * 4), 1u, __ATOMIC_RELAXED, __HIP_MEMORY_SCOPE_WORKGROUP);
        STAMP(7);
    } else {
        const int qs = w & 3, kh = w >> 2;
        const int q0 = qb * 256 + qs * 64;
        GET_TAG();
        if (bid < 240) {
            const int part = nt / 5, c0 = (nt - part * 5) * 128;
            const int wm = w & 1, wn = w >> 1;
            constexpr int RB = 32768;
            const int xr = wm * 64 + r;
            const unsigned xa = RB + xr * 128 + ((h ^ ((xr >> 1) & 7)) << 4);
            unsigned wt[2];
#pragma unroll
            for (int t = 0; t < 2; ++t) {
                const int q = (lane & 15) >> 2, p = lane & 3, blk = (lane >> 4) & 1, row = 8 * h + 4 * t + q;
                wt[t] = RB + 16384 + row * 256 + (((4 * wn + 2 * blk + (p >> 1)) ^ ((q << 2) | (2 * h + t))) << 4) + 8 * (p & 1);
            }
            auto gbody = [&](auto trc) {
                constexpr bool TR = decltype(trc)::value;
                u32x4 xf[2][8], wf[2][4];
                auto rdk = [&](auto bc, LDS3 const char* sx, LDS3 const char* sb, int ks) {
                    constexpr int B = decltype(bc)::value;
                    xf[B][2 * ks] = *(LDS3 const u32x4*)(sx + (xa ^ (ks * 32)));
                    xf[B][2 * ks + 1] = *(LDS3 const u32x4*)(sx + ((xa + 4096) ^ (ks * 32)));
                    const u32x2 lo = trread(sb + wt[0] + ks * 4096), hi = trread(sb + wt[1] + ks * 4096);
                    wf[B][ks] = (u32x4){lo[0], lo[1], hi[0], hi[1]};
                };
                f32x16 acc0, acc1;
                auto step = [&](auto bc, int s) {
                    constexpr int B = decltype(bc)::value;
                    if (s == GA_PUB) {
                        DRAIN();
                        unsigned arr = 0;
                        if (lane == 0) arr = LDS_CNT_ADD(G_ST);
                        if (__builtin_amdgcn_readfirstlane(arr) == 7 && lane == 0) set_flag(ws, fG, tag);
                    }
                    __builtin_amdgcn_s_waitcnt(0xC07F);
                    if (s < 18) BAR();
                    SBAR();
                    const int s1 = s + 1;
                    LDS3 const char* sb = lds + __builtin_amdgcn_readfirstlane((s1 & 3) * 32768);
                    LDS3 const char* sx = lds + __builtin_amdgcn_readfirstlane(((s1 >= 10 ? 20 - s1 : s1 + 1) & 3) * 32768);
#define GM(A, i) do { if constexpr (TR) A = MFMA32_16(wf[B][(i) >> 1], xf[B][i], A); else A = MFMA32_16(xf[B][i], wf[B][(i) >> 1], A); SBAR(); } while (0)
#define GX(i) do { xf[B ^ 1][i] = *(LDS3 const u32x4*)(sx + ((xa + ((i) & 1) * 4096) ^ (((i) >> 1) * 32))); } while (0)
#define GW(ks) do { const u32x2 lo = trread(sb + wt[0] + (ks) * 4096), hi = trread(sb + wt[1] + (ks) * 4096); wf[B ^ 1][ks] = (u32x4){lo[0], lo[1], hi[0], hi[1]}; } while (0)
                    GM(acc0, 0); GX(0); GW(0); SBAR(); GM(acc1, 1); GX(1); GX(2); SBAR(); GM(acc0, 2); GW(1); GX(3); SBAR(); GM(acc1, 3); GX(4); GW(2); SBAR();
                    GM(acc0, 4); GX(5); GX(6); SBAR(); GM(acc1, 5); GW(3); GX(7); SBAR(); GM(acc0, 6); GM(acc1, 7);
#undef GX
#undef GW
#undef GM
                };
                WAIT_BAR(63);
#pragma unroll
                for (int ks = 0; ks < 4; ++ks) rdk(std::integral_constant<int, 0>{}, lds + 32768, lds, ks);
#pragma unroll 1
                for (int mat = 0; mat < 2; ++mat) {
                    acc0 = (f32x16){}; acc1 = (f32x16){};
#pragma unroll 1
                    for (int kk = 0; kk < 10; kk += 2) {
                        step(std::integral_constant<int, 0>{}, mat * 10 + kk);
                        step(std::integral_constant<int, 1>{}, mat * 10 + kk + 1);
                    }
                    const int cpos = 2 * (mt & 7) + (mt >> 3);
                    if constexpr (TR) {
                        const float sc = (part == 0 && mat == 0) ? QSCALE : 1.f;
                        const unsigned base = part == 0 ? WS_Q + mat * NH * 81920 + mt * 5120 : WS_KV + mat * NH * 163840 + cpos * 10240;
                        const unsigned hs = part == 0 ? 81920 : 163840;
#pragma unroll
                        for (int pr = 0; pr < 2; ++pr) {
                            const int G8 = ((c0 + 32 * wn) >> 3) + 2 * pr + h, pp = G8 / 5, gi = G8 - pp * 5;
                            const unsigned hb0 = base + (2 * pp + (gi > 2)) * hs;
#pragma unroll
                            for (int i = 0; i < 2; ++i) {
                                const f32x16& a = i ? acc1 : acc0;
                                const int tl = wm * 64 + i * 32 + r;
                                const auto s0 = __builtin_amdgcn_permlane32_swap(pk2(a[8 * pr] * sc, a[8 * pr + 1] * sc), pk2(a[8 * pr + 4] * sc, a[8 * pr + 5] * sc), false, false);
                                const auto s1 = __builtin_amdgcn_permlane32_swap(pk2(a[8 * pr + 2] * sc, a[8 * pr + 3] * sc), pk2(a[8 * pr + 6] * sc, a[8 * pr + 7] * sc), false, false);
                                if (gi != 2) { const u32x4 ov = {s0[0], s1[0], s0[1], s1[1]}; ST16(ov, hb0 + (tl >> 5) * 1024 + (gi > 2 ? gi - 3 : gi) * 512 + r * 16); }
                                else { const u32x2 o0 = {s0[0], s1[0]}, o1 = {s0[1], s1[1]}; ST8(o0, hb0 + 4096 + tl * 8); ST8(o1, hb0 + hs + 4096 + tl * 8); }
                            }
                        }
                    } else {
                        const int n = c0 + 32 * wn + r, hd = n / 20, d = n - hd * 20, rho = (hd & 1) ? (d < 4 ? d + 16 : d - 4) : d;
                        const unsigned base = WS_KV + (mat * NH + hd) * 163840 + cpos * 10240 + 5120 + h * 16 + rho * 32;
#pragma unroll
                        for (int i = 0; i < 2; ++i)
#pragma unroll
                            for (int s2 = 0; s2 < 2; ++s2) {
                                const f32x16& a = i ? acc1 : acc0;
                                u32x4 ov = {pk2(a[8 * s2], a[8 * s2 + 1]), pk2(a[8 * s2 + 2], a[8 * s2 + 3]), pk2(a[8 * s2 + 4], a[8 * s2 + 5]), pk2(a[8 * s2 + 6], a[8 * s2 + 7])};
                                ST16(ov, base + (wm * 4 + i * 2 + s2) * 640);
                            }
                    }
                    if (mat == 0) STAMP(1);
                }
            };
            if (part < 2) gbody(std::true_type{}); else gbody(std::false_type{});
        } else {
            const int sj = bid - 240;
            WAIT_BAR(63);
            {
                const u32x4 ga = *(const u32x4*)(P.gw + lane * 8), gb = *(const u32x4*)(P.gw + 512 + (lane & 15) * 8);
                u32x4 xa[16], xb[4];
                const u16* xr = P.x + (size_t)(sj * 128 + w * 16) * DM;
#pragma unroll
                for (int rr = 0; rr < 16; ++rr) xa[rr] = *(const u32x4*)(xr + rr * DM + lane * 8);
#pragma unroll
                for (int i = 0; i < 4; ++i) xb[i] = *(const u32x4*)(xr + (i * 4 + (lane >> 4)) * DM + 512 + (lane & 15) * 8);
                float res = 0.f;
#pragma unroll
                for (int rr = 0; rr < 16; ++rr) {
                    float s = 0.f, s2 = 0.f;
#pragma unroll
                    for (int j = 0; j < 4; ++j) { s += bf_lo(xa[rr][j]) * bf_lo(ga[j]) + bf_hi(xa[rr][j]) * bf_hi(ga[j]); s2 += bf_lo(xb[rr >> 2][j]) * bf_lo(gb[j]) + bf_hi(xb[rr >> 2][j]) * bf_hi(gb[j]); }
                    if ((lane >> 4) == (rr & 3)) s += s2;
#pragma unroll
                    for (int o = 32; o >= 1; o >>= 1) s += __shfl_xor(s, o);
                    if (lane == rr) res = s;
                }
                if (lane < 16) ST4(__float_as_uint(1.f / (1.f + __expf(-res))), WS_G + (sj * 128 + w * 16 + lane) * 4);
            }
            DRAIN();
            unsigned arr = 0;
            if (lane == 0) arr = LDS_CNT_ADD(G_ST);
            if (__builtin_amdgcn_readfirstlane(arr) == 7 && lane == 0) set_flag(ws, WS_F_SD + sj * 8, tag);
        }
        STAMP(2);
        const unsigned hb = lds0 + kh * 10240;
        const unsigned laneK = hb + lane * 16, laneT = hb + 4096 + r * 8 + h * 4, laneV = hb + 5120 + h * 16 + (r < 20 ? r : 19) * 32;
        const u64 vmask = 0x000fffff000fffffull;
        u32x4 v0a = {one, one, one, one}, v0b = v0a, v1 = v0a;
        u32x4 qA, qB, ka; unsigned qtA, qtB, kt;
        {
            lds_wait(lds + G_Q, 4);
            LDS3 const char* qp = lds + L_Q + (qs >> 1) * 5120;
            const unsigned om = (qs & 1) * 2048 + lane * 16, ot = 4096 + ((qs & 1) * 64 + r) * 8 + 4 * h;
            qA = *(LDS3 const u32x4*)(qp + om); qB = *(LDS3 const u32x4*)(qp + om + 1024);
            qtA = *(LDS3 const unsigned*)(qp + ot); qtB = *(LDS3 const unsigned*)(qp + ot + 256);
            DSR128(ka, kh ? laneK : lds0 + L_Q + 10240 + lane * 16, 0); DSR32(kt, laneT, 0); DSR128M2(v0a, laneV, 0, v1, laneV, 640);
            asm volatile("s_waitcnt lgkmcnt(0)" : "+v"(ka), "+v"(kt), "+v"(v0a), "+v"(v1), "+v"(qA), "+v"(qB), "+v"(qtA), "+v"(qtB) :: "memory");
            if (lane == 0) LDS_CNT_ADD(G_RD);
        }
        STAMP(5);
        float mA, mB; u32x2 qTA, qTB; f32x16 sA, sB;
        {
            const u32x2 t0 = {kt, 0x3f80u}, a0 = {qtA, 0u}, b0 = {qtB, 0u};
            const f32x16 s = MFMA32_16(ka, qA, z16);
            const f32x16 s0 = MFMA32_8(t0, a0, s);
            f32x16 s1 = MFMA32_16(ka, qB, z16); s1 = MFMA32_8(t0, b0, s1);
            float x0 = s0[0], x1 = s1[0];
#pragma unroll
            for (int i = 1; i < 16; ++i) { x0 = fmaxf(x0, s0[i]); x1 = fmaxf(x1, s1[i]); }
            x0 = -xhalf_max(x0); x1 = -xhalf_max(x1);
            const unsigned hh = pk2(x0, x1);
            const unsigned ll = pk2(x0 - bf_lo(hh), x1 - bf_hi(hh));
            mA = -(bf_lo(hh) + bf_lo(ll)); mB = -(bf_hi(hh) + bf_hi(ll));
            const unsigned sel = h ? ll : hh;
            qTA = (u32x2){qtA, sel & 0xffffu}; qTB = (u32x2){qtB, sel >> 16};
            sA = MFMA32_8(t0, qTA, s);
        }
        f32x16 oA = {}, oB = {};
        u32x4 pa, pb = {0u, 0u, 0u, 0u};
#define EX4(S, i, Pv, j) do { Pv[j] = pk2(ex2(S[i]), ex2(S[i + 1])); Pv[j + 1] = pk2(ex2(S[i + 2]), ex2(S[i + 3])); PIN(Pv); SBAR(); } while (0)
#define KT2 ((u32x2){kt, 0x3f80u})
#define TILE(V0C, V0N, aK1, oK1, aT1, oT1, oV1, aV0, oV0) do { \
            sB = MFMA32_16(ka, qB, z16); SBAR(); DSR128(ka, aK1, oK1); EX4(sA, 0, pa, 0); \
            oB = MFMA32_16(v1, pb, oB); SBAR(); DSR128M2(v1, aV, oV1, V0N, aV0, oV0); EX4(sA, 4, pa, 2); \
            sB = MFMA32_8(KT2, qTB, sB); SBAR(); DSR32(kt, aT1, oT1); EX4(sA, 8, pb, 0); \
            oA = MFMA32_16(V0C, pa, oA); SBAR(); EX4(sA, 12, pb, 2); \
            LWAIT(1); sA = MFMA32_16(ka, qA, z16); SBAR(); EX4(sB, 0, pa, 0); \
            oA = MFMA32_16(v1, pb, oA); SBAR(); EX4(sB, 4, pa, 2); \
            LWAIT(0); sA = MFMA32_8(KT2, qTA, sA); SBAR(); EX4(sB, 8, pb, 0); \
            oB = MFMA32_16(V0C, pa, oB); SBAR(); EX4(sB, 12, pb, 2); \
        } while (0)
        unsigned aK = laneK, aT = laneT, aV = laneV;
#pragma unroll 1
        for (int c = 0; c < 8; ++c) {
            if (c >= 1 && c <= 3) lds_wait(lds + (c == 1 ? C_KVL : (c == 2 ? C_KVL + 12 : C_WA + 8)), 4);
            if (c >= 1) {
                unsigned pp;
                asm volatile("ds_write_b32 %1, %2\n\tds_read_b32 %0, %3\n\ts_waitcnt lgkmcnt(0)" : "=&v"(pp) : "v"(lds0 + C_PR + w * 4), "v"(c), "v"(lds0 + C_PR + (w ^ 4) * 4) : "memory");
                const int pc = __builtin_amdgcn_readfirstlane(pp);
                if (pc > c || (pc == c && ((c ^ kh) & 1))) __builtin_amdgcn_s_setprio(1); else __builtin_amdgcn_s_setprio(0);
            } else if (kh) __builtin_amdgcn_s_setprio(1);
            const unsigned nx = (c < 7 ? c + 1 : 7) * 20480;
            const unsigned nK = laneK + nx, nT = laneT + nx, nV = laneV + nx;
            SBAR();
            TILE(v0a, v0b, aK, 1024, aT, 256, 640,  aV, 1280);
            TILE(v0b, v0a, aK, 2048, aT, 512, 1920, aV, 2560);
            if (c == 0) {
                DRAIN();
                unsigned arr = 0;
                if (lane == 0) arr = LDS_CNT_ADD(C_GB);
                if (__builtin_amdgcn_readfirstlane(arr) == 7 && lane == 0 && bid < 240) set_flag(ws, fG + 2048, tag);
                asm volatile("s_waitcnt lgkmcnt(0)" ::: "memory");
                SBAR();
            }
            TILE(v0a, v0b, aK, 3072, aT, 768, 3200, aV, 3840);
            TILE(v0b, v0a, nK, 0,    nT, 0,   4480, nV, 0);
            aK = nK; aT = nT; aV = nV;
        }
        oB = MFMA32_16(v1, pb, oB);
#undef TILE
#undef EX4
        if (lane == 0) __hip_atomic_store((LDS3 unsigned*)(lds + C_PR + w * 4), 8u, __ATOMIC_RELAXED, __HIP_MEMORY_SCOPE_WORKGROUP);
        STAMP(6);
        {
            float chk = 0.f;
#pragma unroll
            for (int i = 0; i < 12; ++i) chk += fabsf(oA[i]) * 0.f + fabsf(oB[i]) * 0.f;
            bool bad = !(chk == 0.f);
            if (h) bad = bad || !(oA[8] >= 0x1p-60f && oA[8] <= 0x1p60f && oB[8] >= 0x1p-60f && oB[8] <= 0x1p60f);
            if (__builtin_expect(__any(bad), 0)) {
                mA = -INFINITY; mB = -INFINITY;
#pragma unroll
                for (int i = 0; i < 16; ++i) { oA[i] = 0.f; oB[i] = 0.f; }
                const u32x2 a0 = {qtA, 0u}, b0 = {qtB, 0u};
#pragma unroll 1
                for (int t = 0; t < 32; ++t) {
                    const unsigned kb = WS_KV + head * 163840 + (2 * (t >> 2) + kh) * 10240, t4 = t & 3;
                    const u32x4 k0 = LD16(kb + t4 * 1024 + lane * 16);
                    const u32x2 t0 = {LD4(kb + 4096 + (t4 * 32 + r) * 8 + 4 * h), 0u};
                    u32x4 w0 = LD16(kb + 5120 + t4 * 1280 + h * 16 + (r < 20 ? r : 19) * 32), w1 = LD16(kb + 5120 + t4 * 1280 + 640 + h * 16 + (r < 20 ? r : 19) * 32);
                    if (r >= 20) { w0 = (u32x4){one, one, one, one}; w1 = w0; }
                    f32x16 s0 = MFMA32_16(k0, qA, z16); s0 = MFMA32_8(t0, a0, s0);
                    f32x16 s1 = MFMA32_16(k0, qB, z16); s1 = MFMA32_8(t0, b0, s1);
                    float x0 = s0[0], x1 = s1[0];
#pragma unroll
                    for (int i = 1; i < 16; ++i) { x0 = fmaxf(x0, s0[i]); x1 = fmaxf(x1, s1[i]); }
                    x0 = fmaxf(xhalf_max(x0), mA); x1 = fmaxf(xhalf_max(x1), mB);
                    const float g0 = (mA == x0) ? 1.f : ex2(mA - x0), g1 = (mB == x1) ? 1.f : ex2(mB - x1);
                    mA = x0; mB = x1;
#pragma unroll
                    for (int i = 0; i < 16; ++i) { oA[i] *= g0; oB[i] *= g1; }
#pragma unroll
                    for (int i = 0; i < 4; ++i) { pa[i] = pk2(ex2(s0[2 * i] - x0), ex2(s0[2 * i + 1] - x0)); pb[i] = pk2(ex2(s0[8 + 2 * i] - x0), ex2(s0[9 + 2 * i] - x0)); }
                    oA = MFMA32_16(w0, pa, oA); oA = MFMA32_16(w1, pb, oA);
#pragma unroll
                    for (int i = 0; i < 4; ++i) { pa[i] = pk2(ex2(s1[2 * i] - x1), ex2(s1[2 * i + 1] - x1)); pb[i] = pk2(ex2(s1[8 + 2 * i] - x1), ex2(s1[9 + 2 * i] - x1)); }
                    oB = MFMA32_16(w0, pa, oB); oB = MFMA32_16(w1, pb, oB);
                }
            }
        }
        lds_wait<4>(lds + C_BX, 8);
        const float gO = *(LDS3 const float*)(lds + X_G + (qs * 64 + kh * 32 + r) * 4);
        {
            LDS3 char* xp = lds + X_MRG + w * 2816;
            const f32x16& s = kh ? oA : oB;
            *(LDS3 f32x4*)(xp + lane * 16) = (f32x4){s[0], s[1], s[2], s[3]};
            *(LDS3 f32x4*)(xp + 1024 + lane * 16) = (f32x4){s[4], s[5], s[6], s[7]};
            if (!h) *(LDS3 f32x4*)(xp + 2048 + r * 16) = (f32x4){s[8], s[9], s[10], s[11]};
            else *(LDS3 f32x2*)(xp + 2560 + r * 8) = (f32x2){s[8], kh ? mA : mB};
            asm volatile("s_waitcnt lgkmcnt(0)" ::: "memory");
            if (lane == 0) __hip_atomic_store((LDS3 unsigned*)(lds + C_MRG + w * 4), 1u, __ATOMIC_RELAXED, __HIP_MEMORY_SCOPE_WORKGROUP);
        }
        lds_wait<MS>(lds + C_MRG + (w ^ 4) * 4, 1);
        bool lastw;
        {
            LDS3 const char* xp = lds + X_MRG + (w ^ 4) * 2816;
            const f32x4 p0 = *(LDS3 const f32x4*)(xp + lane * 16), p1 = *(LDS3 const f32x4*)(xp + 1024 + lane * 16), p2 = *(LDS3 const f32x4*)(xp + 2048 + r * 16);
            const f32x2 lm = *(LDS3 const f32x2*)(xp + 2560 + r * 8);
            const f32x16& s = kh ? oB : oA;
            const float mo = kh ? mB : mA, mp = lm[1];
            const float mx = fmaxf(mo, mp);
            const float fo = (mo == mx) ? 1.f : ex2(mo - mx), fq = (mp == mx) ? 1.f : ex2(mp - mx);
            float o[12];
#pragma unroll
            for (int i = 0; i < 4; ++i) { o[i] = s[i] * fo + p0[i] * fq; o[4 + i] = s[4 + i] * fo + p1[i] * fq; o[8 + i] = s[8 + i] * fo + p2[i] * fq; }
            const float l = upper_half(s[8]) * fo + lm[0] * fq;
            store_o(rs, o, gO / l, q0 + kh * 32 + r, 0, head, h);
            DRAIN();
            unsigned arr = 0;
            if (lane == 0) arr = LDS_CNT_ADD(C_OA);
            lastw = __builtin_amdgcn_readfirstlane(arr) == 7;
            if (lastw && lane == 0) set_flag(ws, WS_F_OA + (qb * 32 + head) * 8, tag);
        }
        STAMP(7);
        {
            const int c16 = lane & 15, g = lane >> 4, tt = w & 3, k0 = kh * 10;
            lds_wait(lds + C_WA, 4);
            LDS3 const char* wp = lds + L_WA + k0 * 1024 + lane * 16;
            u32x4 wf[4][5];
#pragma unroll
            for (int ks = 0; ks < 4; ++ks)
#pragma unroll
                for (int n = 0; n < 5; ++n) wf[ks][n] = *(LDS3 const u32x4*)(wp + (n * 20 + ks) * 1024);
            POLL_ONE(lastw, 2, WS_F_OA + ((mtO >> 2) * 32 + r) * 8, WS_F_OA + ((mtO >> 2) * 32 + r) * 8, C_P4);
            STAMP(9);
            const unsigned ab = WS_AO + ((mtO * 4 + tt) * 40 + k0) * 1024;
            u32x4 af[10];
#pragma unroll
            for (int ks = 0; ks < 6; ++ks) af[ks] = LD16(ab + ks * 1024 + lane * 16);
            if (kh) {
#pragma unroll
                for (int ks = 6; ks < 10; ++ks) LD_TAIL(af[ks], ab + ks * 1024);
            } else {
#pragma unroll
                for (int ks = 6; ks < 10; ++ks) af[ks] = LD16(ab + ks * 1024 + lane * 16);
            }
            f32x4 c[5];
#pragma unroll
            for (int n = 0; n < 5; ++n) c[n] = (f32x4){0.f, 0.f, 0.f, 0.f};
            SBAR();
#pragma unroll
            for (int ks = 0; ks < 10; ++ks) {
#pragma unroll
                for (int n = 0; n < 5; ++n) c[n] = MFMA16_32(wf[ks & 3][n], af[ks], c[n]);
                SBAR();
                if (ks + 4 < 10) {
#pragma unroll
                    for (int n = 0; n < 5; ++n) wf[ks & 3][n] = *(LDS3 const u32x4*)(wp + (n * 20 + ks + 4) * 1024);
                }
                SBAR();
            }
            LDS3 char* xp = lds + X_MRG + tt * 5120 + lane * 16;
            if (w < 4) { *(LDS3 f32x4*)(xp + 3072) = c[3]; *(LDS3 f32x4*)(xp + 4096) = c[4]; }
            else { *(LDS3 f32x4*)(xp) = c[0]; *(LDS3 f32x4*)(xp + 1024) = c[1]; *(LDS3 f32x4*)(xp + 2048) = c[2]; }
            asm volatile("s_waitcnt lgkmcnt(0)" ::: "memory");
            if (lane == 0) __hip_atomic_store((LDS3 unsigned*)(lds + C_FIN + w * 4), 1u, __ATOMIC_RELAXED, __HIP_MEMORY_SCOPE_WORKGROUP);
            lds_wait(lds + C_FIN + (w ^ 4) * 4, 1);
            lds_wait(lds + C_BP + tt * 4, 1);
            u16* op = outp + (size_t)(mtO * 64 + tt * 16 + c16) * DM + ntO * 80 + g * 4;
#pragma unroll
            for (int n = 0; n < 5; ++n) {
                if ((w < 4) == (n < 3)) {
                    const f32x4 bp = *(LDS3 const f32x4*)(n < 3 ? lds + X_FIN + tt * 3072 + n * 1024 + lane * 16 : lds + 143360 + tt * 2048 + (n - 3) * 1024 + lane * 16);
                    const f32x4 s = c[n] + *(LDS3 const f32x4*)(xp + n * 1024) + bp;
                    *(u32x2*)(op + n * 16) = (u32x2){pk2(s[0], s[1]), pk2(s[2], s[3])};
                }
            }
            if (bid == 0 && mtid == 0) set_flag(ws, WS_EPOCH, tag);
        }
        { const u64 t0c = __hip_atomic_load((gu64*)(ws + 128), RLX_AGENT); while (__builtin_amdgcn_s_memrealtime() - t0c < 6000ull) __builtin_amdgcn_s_sleep(4); }
        STAMP(10);
    }
}

__global__ void tiny(char* ws) { u64 t; asm volatile("s_memrealtime %0\n\ts_waitcnt lgkmcnt(0)" : "=s"(t) :: "memory"); if (threadIdx.x == 0) *(u64*)(ws + 128) = t; }

extern "C" void kernel_launch(void* const* d_in, const int* in_sizes, int n_in, void* d_out, int out_size, void* d_ws, size_t ws_size,
                              hipStream_t stream) {
    Params P;
    P.x = (const u16*)d_in[0]; P.wa = (const u16*)d_in[1]; P.woa = (const u16*)d_in[2]; P.wb = (const u16*)d_in[4];
    P.wphi = (const u16*)d_in[5]; P.wob = (const u16*)d_in[6]; P.gw = (const u16*)d_in[7]; P.ws = (char*)d_ws; P.out = (u16*)d_out;
    tiny<<<1, 64, 0, stream>>>(P.ws);
    mega<<<256, 768, 0, stream>>>(P);
}
```

```cpp
#include <hip/hip_runtime.h>
#include <hip/hip_bf16.h>
#include <stdint.h>
#include <type_traits>

typedef __attribute__((ext_vector_type(8))) short bf16x8;
typedef __attribute__((ext_vector_type(4))) short bf16x4;
typedef __attribute__((ext_vector_type(16))) float f32x16;
typedef __attribute__((ext_vector_type(4))) float f32x4;
typedef __attribute__((ext_vector_type(2))) float f32x2;
typedef __attribute__((ext_vector_type(4))) unsigned u32x4;
typedef __attribute__((ext_vector_type(2))) unsigned u32x2;
typedef __attribute__((ext_vector_type(2))) __bf16 bf2_t;
typedef unsigned short u16;
typedef unsigned long long u64;
#define LDS3 __attribute__((address_space(3)))
typedef __attribute__((address_space(1))) u64 gu64;
#define RLX_AGENT __ATOMIC_RELAXED, __HIP_MEMORY_SCOPE_AGENT

constexpr int GA_PUB = 11, HS = 8, MS = 2, PRE_SLEEP = 2;
constexpr int SEQ = 2048, DM = 640, NH = 32;
constexpr float LOG2E = 1.4426950408889634f;
constexpr float QSCALE = 0.22360679774997896f * LOG2E;

constexpr unsigned WS_EPOCH = 0;
constexpr unsigned WS_F_G   = 256;
constexpr unsigned WS_F_SD  = WS_F_G + 4096;
constexpr unsigned WS_F_KV  = WS_F_SD + 256;
constexpr unsigned WS_F_OB  = WS_F_KV + 2048;
constexpr unsigned WS_F_OA  = WS_F_OB + 2048;
constexpr unsigned WS_G     = 16384;
constexpr unsigned WS_Q     = 32768;
constexpr unsigned WS_KV    = WS_Q + 2 * NH * 81920;
constexpr unsigned WS_KVP   = WS_KV + 2 * NH * 163840;
constexpr unsigned WS_AO    = WS_KVP + NH * 8 * 4096;
constexpr unsigned WS_WT    = WS_AO + SEQ * 1280 * 2;
constexpr unsigned WS_END   = WS_WT + 1280 * 640 * 2;

#define STAMP(i)

__device__ __forceinline__ unsigned pk2(float a, float b) { f32x2 v = {a, b}; bf2_t r = __builtin_convertvector(v, bf2_t); return __builtin_bit_cast(unsigned, r); }
__device__ __forceinline__ float bf_lo(unsigned u) { return __uint_as_float(u << 16); }
__device__ __forceinline__ float bf_hi(unsigned u) { return __uint_as_float(u & 0xffff0000u); }
__device__ __forceinline__ float ex2(float x) { return __builtin_amdgcn_exp2f(x); }
#define MFMA32_16(a, b, c) __builtin_amdgcn_mfma_f32_32x32x16_bf16(__builtin_bit_cast(bf16x8, a), __builtin_bit_cast(bf16x8, b), c, 0, 0, 0)
#define MFMA32_8(a, b, c) __builtin_amdgcn_mfma_f32_32x32x8bf16_1k(__builtin_bit_cast(bf16x4, a), __builtin_bit_cast(bf16x4, b), c, 0, 0, 0)
#define MFMA16_32(a, b, c) __builtin_amdgcn_mfma_f32_16x16x32_bf16(__builtin_bit_cast(bf16x8, a), __builtin_bit_cast(bf16x8, b), c, 0, 0, 0)

__device__ __forceinline__ void glds16(unsigned voff, const void* sbase, unsigned lds_base) {
    unsigned sv;
    asm volatile("s_mov_b32 %0, m0\n\ts_mov_b32 m0, %3\n\ts_nop 0\n\tglobal_load_lds_dwordx4 %1, %2\n\ts_mov_b32 m0, %0"
                 : "=&s"(sv) : "v"(voff), "s"(sbase), "s"(__builtin_amdgcn_readfirstlane(lds_base)) : "memory");
}
__device__ __forceinline__ void glds16s(unsigned voff, const void* sbase, unsigned lds_base) {
    unsigned sv;
    asm volatile("s_mov_b32 %0, m0\n\ts_mov_b32 m0, %3\n\ts_nop 0\n\tglobal_load_lds_dwordx4 %1, %2 sc1\n\ts_mov_b32 m0, %0"
                 : "=&s"(sv) : "v"(voff), "s"(sbase), "s"(__builtin_amdgcn_readfirstlane(lds_base)) : "memory");
}
__device__ __forceinline__ void glds4s(unsigned voff, const void* sbase, unsigned lds_base) {
    unsigned sv;
    asm volatile("s_mov_b32 %0, m0\n\ts_mov_b32 m0, %3\n\ts_nop 0\n\tglobal_load_lds_dword %1, %2 sc1\n\ts_mov_b32 m0, %0"
                 : "=&s"(sv) : "v"(voff), "s"(sbase), "s"(__builtin_amdgcn_readfirstlane(lds_base)) : "memory");
}
__device__ __forceinline__ void glds16x4(const unsigned (&vo)[4], const void* sbase, unsigned lds_base) {
    unsigned sv;
    asm volatile("s_mov_b32 %0, m0\n\ts_mov_b32 m0, %6\n\ts_nop 0\n\tglobal_load_lds_dwordx4 %1, %5\n\tglobal_load_lds_dwordx4 %2, %5 offset:1024\n\t"
                 "global_load_lds_dwordx4 %3, %5 offset:2048\n\tglobal_load_lds_dwordx4 %4, %5 offset:3072\n\ts_mov_b32 m0, %0"
                 : "=&s"(sv) : "v"(vo[0]), "v"(vo[1]), "v"(vo[2]), "v"(vo[3]), "s"(sbase), "s"(lds_base) : "memory");
}
typedef short v4i16_t __attribute__((ext_vector_type(4)));
__device__ __forceinline__ u32x2 trread(LDS3 const char* p) {
    return __builtin_bit_cast(u32x2, __builtin_amdgcn_ds_read_tr16_b64_v4i16((LDS3 v4i16_t*)p));
}
#define WAIT_BAR(N) asm volatile("s_waitcnt vmcnt(" #N ") lgkmcnt(0)\n\ts_barrier" ::: "memory")
#define DRAIN() asm volatile("s_waitcnt vmcnt(0)" ::: "memory")
#define SBAR() __builtin_amdgcn_sched_barrier(0)
#define PIN(x) asm volatile("" : "+v"(x))

__device__ __forceinline__ float xhalf_max(float x) {
    auto r = __builtin_amdgcn_permlane32_swap(__float_as_uint(x), __float_as_uint(x), false, false);
    return fmaxf(__uint_as_float(r[0]), __uint_as_float(r[1]));
}
__device__ __forceinline__ float xhalf_sum(float x) {
    auto r = __builtin_amdgcn_permlane32_swap(__float_as_uint(x), __float_as_uint(x), false, false);
    return __uint_as_float(r[0]) + __uint_as_float(r[1]);
}
__device__ __forceinline__ float upper_half(float x) {
    auto r = __builtin_amdgcn_permlane32_swap(__float_as_uint(x), __float_as_uint(x), false, false);
    return __uint_as_float(r[1]);
}

typedef __amdgpu_buffer_rsrc_t rsrc_t;
#define ST8(v, off)  __builtin_amdgcn_raw_buffer_store_b64(v, rs, (int)(off), 0, 16)
#define ST16(v, off) __builtin_amdgcn_raw_buffer_store_b128(v, rs, (int)(off), 0, 16)
#define ST4(v, off)  __builtin_amdgcn_raw_buffer_store_b32(v, rs, (int)(off), 0, 16)
#define LD16(off) __builtin_amdgcn_raw_buffer_load_b128(rs, (int)(off), 0, 16)
#define LD4(off)  __builtin_amdgcn_raw_buffer_load_b32(rs, (int)(off), 0, 16)

template <int SLP = 2>
__device__ __forceinline__ void wait_flags(const char* ws, unsigned off0, unsigned off1, u64 tag) {
    gu64* p0 = (gu64*)(ws + off0); gu64* p1 = (gu64*)(ws + off1);
#pragma unroll 1
    for (unsigned spins = 0; spins < (1u << 16); ++spins) {
        const u64 a = __hip_atomic_load(p0, RLX_AGENT), b = __hip_atomic_load(p1, RLX_AGENT);
        if (__all(a == tag && b == tag)) break;
        __builtin_amdgcn_s_sleep(SLP);
    }
    __builtin_amdgcn_fence(__ATOMIC_ACQUIRE, "wavefront");
}
__device__ __forceinline__ void set_flag(char* ws, unsigned off, u64 tag) { __hip_atomic_store((gu64*)(ws + off), tag, RLX_AGENT); }

struct Params { const u16* x; const u16* wa; const u16* wb; const u16* wphi; const u16* woa; const u16* wob; const u16* gw; char* ws; u16* out; };

__device__ __forceinline__ void feature_map(const u32x4& pm, unsigned pt, const u32x4& tm, unsigned tt, int h, u32x4& fp, u32x4& fn, u32x4& ft) {
    const f32x16 z16 = {};
    const u32x2 a8 = {pt, 0u}, b8 = {tt, 0u};
    f32x16 s = MFMA32_16(pm, tm, z16); s = MFMA32_8(a8, b8, s);
    f32x2 v[6];
#pragma unroll
    for (int i = 0; i < 6; ++i) {
        v[i] = (f32x2){s[2 * i], s[2 * i + 1]};
    }
    float mx = fmaxf(v[0][0], v[0][1]), mn = fminf(v[0][0], v[0][1]);
#pragma unroll
    for (int i = 1; i < 6; ++i) { mx = fmaxf(fmaxf(mx, v[i][0]), v[i][1]); mn = fminf(fminf(mn, v[i][0]), v[i][1]); }
    mx = xhalf_max(mx) * -LOG2E; mn = xhalf_max(-mn) * -LOG2E;
    f32x2 ep[6], en[6];
#pragma unroll
    for (int i = 0; i < 6; ++i) {
        const f32x2 a = v[i] * LOG2E + mx, b = v[i] * -LOG2E + mn;
        ep[i] = (f32x2){ex2(a[0]), ex2(a[1])}; en[i] = (f32x2){ex2(b[0]), ex2(b[1])};
    }
    const float tw = h ? 0.f : 1.f;
    const f32x2 s2p = ((ep[0] + ep[1]) + (ep[2] + ep[3])) + (ep[4] + ep[5]) * tw, s2n = ((en[0] + en[1]) + (en[2] + en[3])) + (en[4] + en[5]) * tw;
    const float sp = __builtin_amdgcn_rcpf(xhalf_sum(s2p[0] + s2p[1])), sn = __builtin_amdgcn_rcpf(xhalf_sum(s2n[0] + s2n[1]));
    const float tp = sp * tw, tn = sn * tw;
#pragma unroll
    for (int i = 0; i < 4; ++i) { const f32x2 a = ep[i] * sp, b = en[i] * sn; fp[i] = pk2(a[0], a[1]); fn[i] = pk2(b[0], b[1]); }
    const f32x2 c0 = ep[4] * tp, c1 = ep[5] * tp, c2 = en[4] * tn, c3 = en[5] * tn;
    ft = (u32x4){pk2(c0[0], c0[1]), pk2(c1[0], c1[1]), pk2(c2[0], c2[1]), pk2(c3[0], c3[1])};
}

__device__ __forceinline__ void store_o(rsrc_t rs, const float (&o)[12], float sc, int tok, int half, int head, int h) {
    const auto s0 = __builtin_amdgcn_permlane32_swap(pk2(o[0] * sc, o[1] * sc), pk2(o[4] * sc, o[5] * sc), false, false);
    const auto s1 = __builtin_amdgcn_permlane32_swap(pk2(o[2] * sc, o[3] * sc), pk2(o[6] * sc, o[7] * sc), false, false);
    const unsigned tb = WS_AO + ((tok >> 4) * 40 + half * 20) * 1024;
    const u32x4 mv = {s0[0], s1[0], s0[1], s1[1]};
    ST16(mv, tb + (head >> 1) * 1024 + (((head & 1) * 2 + h) * 16 + (tok & 15)) * 16);
    const u32x2 tv = {pk2(o[8] * sc, o[9] * sc), pk2(o[10] * sc, o[11] * sc)};
    if (!h) ST8(tv, tb + (16 + (head >> 3)) * 1024 + (head & 7) * 128 + (tok & 15) * 8);
}
#define LD8(off)  __builtin_amdgcn_raw_buffer_load_b64(rs, (int)(off), 0, 16)
#define LD_TAIL(dst, base) do { const u32x2 a_ = LD8((base) + (lane >> 4) * 256 + (lane & 15) * 8), b_ = LD8((base) + (lane >> 4) * 256 + 128 + (lane & 15) * 8); dst = (u32x4){a_[0], a_[1], b_[0], b_[1]}; } while (0)

#define DSR128(dst, addr, off) asm volatile("ds_read_b128 %0, %1 offset:%2" : "=v"(dst) : "v"(addr), "i"(off))
#define DSR32(dst, addr, off) asm volatile("ds_read_b32 %0, %1 offset:%2" : "=v"(dst) : "v"(addr), "i"(off))
#define DSR128M2(d0, a0, o0, d1, a1, o1) asm volatile("s_mov_b64 exec, %6\n\tds_read_b128 %0, %2 offset:%3\n\tds_read_b128 %1, %4 offset:%5\n\ts_mov_b64 exec, -1" \
                                                      : "+v"(d0), "+v"(d1) : "v"(a0), "i"(o0), "v"(a1), "i"(o1), "s"(vmask))
#define LWAIT(n) do { asm volatile("s_waitcnt lgkmcnt(" #n ")" ::: "memory"); SBAR(); } while (0)

#define BAR() asm volatile("s_barrier" ::: "memory")
#define LDS_SET(off, v) __hip_atomic_store((LDS3 unsigned*)(lds + (off)), (unsigned)(v), __ATOMIC_RELAXED, __HIP_MEMORY_SCOPE_WORKGROUP)
#define LDS_CNT_ADD(off) __hip_atomic_fetch_add((LDS3 unsigned*)(lds + (off)), 1u, __ATOMIC_RELAXED, __HIP_MEMORY_SCOPE_WORKGROUP)

template <int SLP = 1>
__device__ __forceinline__ void lds_wait(LDS3 char* p, unsigned want) {
#pragma unroll 1
    for (unsigned spins = 0; spins < (1u << 20); ++spins) {
        if ((unsigned)__builtin_amdgcn_readfirstlane(__hip_atomic_load((LDS3 unsigned*)p, __ATOMIC_RELAXED, __HIP_MEMORY_SCOPE_WORKGROUP)) >= want) break;
        __builtin_amdgcn_s_sleep(SLP);
    }
    __builtin_amdgcn_fence(__ATOMIC_ACQUIRE, "wavefront");
}

template <int SLP>
__device__ __forceinline__ void prog_wait(LDS3 char* p, int lane, unsigned want) {
#pragma unroll 1
    for (unsigned spins = 0; spins < (1u << 20); ++spins) {
        if (__all(__hip_atomic_load((LDS3 unsigned*)p + (lane & 7), __ATOMIC_RELAXED, __HIP_MEMORY_SCOPE_WORKGROUP) >= want)) break;
        __builtin_amdgcn_s_sleep(SLP);
    }
    __builtin_amdgcn_fence(__ATOMIC_ACQUIRE, "wavefront");
}
#define POLL_ONE(cond, SLP, o0, o1, word) POLL_ONE2(cond, SLP, 2, o0, o1, word)
#define POLL_ONE2(cond, SLP, SLP2, o0, o1, word) do { if (cond) { wait_flags<SLP>(ws, o0, o1, tag); \
        if (lane == 0) __hip_atomic_store((LDS3 unsigned*)(lds + (word)), 1u, __ATOMIC_RELAXED, __HIP_MEMORY_SCOPE_WORKGROUP); } else lds_wait<SLP2>(lds + (word), 1); } while (0)
constexpr int C_KV = 0, C_KVF = 4, C_OB = 8, C_WA = 16, C_OA = 20, C_BX = 28, C_MRG = 32  , C_FIN = 64  , C_BP = 96  ;
constexpr int C_P1 = 112, C_P2 = 116, C_P3 = 120, C_P4 = 124, C_PR = 128  , C_KVL = 160, C_P5 = 164, C_GB = 168;
constexpr int G_Q = 176, G_P = 180, G_ST = 184, G_RD = 188;
constexpr int X_G = 256;
constexpr int X_KVF = 1280, X_SLOT = 4096;
constexpr int X_MRG = 1280;
constexpr int L_WA = 24576;
constexpr int L_Q = 81920;
constexpr int X_FIN = 126976;

__global__ __launch_bounds__(768, 3) void mega(Params P) {
    __shared__ __attribute__((aligned(16))) char smem[163840];
    LDS3 char* lds = (LDS3 char*)smem;
    char* ws = P.ws;
    u16* outp = P.out;
    asm volatile("" : "+s"(outp), "+s"(ws));
    const int tid = threadIdx.x, lane = tid & 63, r = lane & 31, h = lane >> 5;
    const int hw = __builtin_amdgcn_readfirstlane(tid >> 6);
    const bool helper = hw < 4;
    const int w = hw - 4, mtid = tid - 256;
    const int bid = blockIdx.x;
    const unsigned lds0 = (unsigned)(uintptr_t)smem;
    const rsrc_t rs = __builtin_amdgcn_make_buffer_rsrc(ws, 0, WS_END, 0x00020000);
    STAMP(0);
    u64 tag = 0;
#define GET_TAG() do { asm volatile("s_load_dwordx2 %0, %1, 0x0\n\ts_waitcnt lgkmcnt(0)" : "=s"(tag) : "s"(ws) : "memory"); tag += 1; } while (0)
    const int head = (bid & 7) * 4 + ((bid >> 3) & 3), qb = bid >> 5;
    const int nlo = (head * 20) >> 7, nhi = (head * 20 + 19) >> 7;
    const int mtO = (bid & 7) * 4 + ((bid >> 3) & 3), ntO = bid >> 5;
    const int lin = (bid & 7) * 30 + (bid >> 3), lh = lin >= 120, lrem = lin - 120 * lh;
    const int mt = lh * 8 + (lrem & 7), nt = lrem >> 3;
    const unsigned fG = WS_F_G + (nt * 16 + mt) * 8;
    const unsigned one = (r == 20) ? 0x3f803f80u : 0u;
    const f32x16 z16 = {};

    if (helper) {
        const unsigned fA0 = WS_F_G + (((lane < 32 ? 5 : 10) + ((lane & 16) ? nhi : nlo)) * 16 + (lane & 15)) * 8;
        const unsigned fA1 = WS_F_G + (((lane & 2) ? nhi : nlo) * 16 + qb * 2 + (lane & 1)) * 8;
        __builtin_amdgcn_s_setprio(3);
        const char* kvb = ws + WS_KV + head * 163840;
        auto kv_issue = [&](int grp, int t0 = 0, int t1 = 8) {
#pragma unroll
            for (int t = t0; t < t1; ++t) { int pc = grp * 32 + hw + 4 * t; if (pc == 0) pc = 4; glds16s(lane * 16, kvb + pc * 1024, lds0 + pc * 1024); }
        };
        auto q_issue = [&]() {
            const char* qbase = ws + WS_Q + head * 81920 + qb * 10240;
#pragma unroll
            for (int t = 0; t < 3; ++t) { const int pc = hw + 4 * t < 10 ? hw + 4 * t : 10; glds16s(lane * 16, pc < 10 ? qbase + pc * 1024 : kvb, lds0 + L_Q + pc * 1024); }
        };
        if (bid < 240) {
            const int rot = ((nt + mt) % 3) * 3;
            unsigned xo[4], wo[4];
#pragma unroll
            for (int i = 0; i < 4; ++i) {
                xo[i] = (mt * 128 + 32 * hw + 8 * i + (lane >> 3)) * 1280 + (((lane & 7) ^ ((4 * i + (lane >> 4)) & 7)) << 4) - i * 1024;
                wo[i] = (16 * hw + 4 * i + (lane >> 4)) * 3840 + nt * 256 + (((lane & 15) ^ (((lane >> 4) << 2) | i)) << 4) - i * 1024;
            }
            const unsigned dX = lds0 + 32768 + hw * 4096;
            int kt = rot;
#pragma unroll
            for (int n = 0; n < 20; ++n) {
                const char* xb = (const char*)P.x + kt * 128;
                const char* wb = (const char*)(n >= 10 ? P.wb : P.wa) + (size_t)kt * (64 * 3840);
                asm volatile("" : "+s"(xb), "+s"(wb));
                if (n == 2) { if (hw == 0) *(LDS3 unsigned*)(lds + lane * 4) = 0u; GET_TAG(); WAIT_BAR(8); }
                else if (n == 11 || n == 15) { WAIT_BAR(12); }
                else if (n == 3 || (n >= 12 && n <= 14)) { WAIT_BAR(8); }
                else if (n >= 5) { WAIT_BAR(16); }
                glds16x4(wo, wb, dX + 16384 + (n & 3) * 32768);
                if (n < 10 || n >= 14) glds16x4(xo, xb, dX + ((n >= 10 ? 20 - n : n + 1) & 3) * 32768);
                if (n < 9) kt = kt == 9 ? 0 : kt + 1; else if (n > 9) kt = kt == 0 ? 9 : kt - 1;
            }
            WAIT_BAR(16);
            if (hw == 0) {
                const u64 a = __hip_atomic_load((gu64*)(ws + fA0), RLX_AGENT), b = __hip_atomic_load((gu64*)(ws + fA1), RLX_AGENT);
                if (__all(a == tag && b == tag) && lane == 0) LDS_SET(G_P, 1);
            }
            WAIT_BAR(0);
        } else {
            if (hw == 0) *(LDS3 unsigned*)(lds + lane * 4) = 0u;
            GET_TAG();
            WAIT_BAR(63);
            if (hw == 0) {
#pragma unroll 1
                for (int t = 0; t < PRE_SLEEP; ++t) __builtin_amdgcn_s_sleep(127);
            }
        }
        if (!__builtin_amdgcn_readfirstlane(__hip_atomic_load((LDS3 unsigned*)(lds + G_P), __ATOMIC_RELAXED, __HIP_MEMORY_SCOPE_WORKGROUP))) POLL_ONE(hw == 0, 6, fA0, fA1, G_P);
        __builtin_amdgcn_fence(__ATOMIC_ACQUIRE, "wavefront");
        kv_issue(0); q_issue(); kv_issue(1, 0, 2);
        DRAIN();
        if (lane == 0) LDS_CNT_ADD(G_Q);
        lds_wait<2>(lds + G_RD, 8);
        kv_issue(1, 2, 8); kv_issue(2); kv_issue(3); kv_issue(4);
        asm volatile("s_waitcnt vmcnt(24)" ::: "memory"); if (lane == 0) LDS_CNT_ADD(C_KVL);
        asm volatile("s_waitcnt vmcnt(16)" ::: "memory"); if (lane == 0) LDS_CNT_ADD(C_KVL + 12);
        STAMP(3);
        u32x4 phm; unsigned pht;
        {
            const u16* wp = P.wphi + head * 400 + (r < 20 ? r : (r & 3));
            unsigned e[10];
#pragma unroll
            for (int j = 0; j < 8; ++j) e[j] = wp[(8 * h + j + 4 * (head & 1)) * 20];
            e[8] = wp[((head & 1 ? 0 : 16) + 2 * h) * 20]; e[9] = wp[((head & 1 ? 1 : 17) + 2 * h) * 20];
            const int bu = bid < 160 ? bid : 0, hf = bu >= 80, k0 = (bu - 80 * hf) * 8, hd = k0 < 512 ? k0 >> 4 : (k0 - 512) >> 2;
            const int row = k0 < 512 ? 20 * hd + (k0 & 8) + 4 * (hd & 1) : 20 * hd + 16;
            const u16* wsrc = (hf ? P.wob : P.woa) + (size_t)row * DM;
            unsigned f[3][8];
            if (bid < 160) {
#pragma unroll
                for (int q = 0; q < 3; ++q) {
                    const int n = tid + 256 * q < 640 ? tid + 256 * q : 639;
#pragma unroll
                    for (int j = 0; j < 8; ++j) f[q][j] = wsrc[j * DM + n];
                }
            }
            DRAIN();
            if (lane == 0) LDS_CNT_ADD(C_WA + 8);
            phm = (u32x4){e[0] | (e[1] << 16), e[2] | (e[3] << 16), e[4] | (e[5] << 16), e[6] | (e[7] << 16)}; pht = e[8] | (e[9] << 16);
            if (bid < 160) {
#pragma unroll
                for (int q = 0; q < 3; ++q) {
                    const int n = tid + 256 * q;
                    const u32x4 v = {f[q][0] | (f[q][1] << 16), f[q][2] | (f[q][3] << 16), f[q][4] | (f[q][5] << 16), f[q][6] | (f[q][7] << 16)};
                    if (n < 640) ST16(v, WS_WT + (((n >> 4) * 40 + hf * 20 + (k0 >> 5)) * 64 + ((k0 >> 3) & 3) * 16 + (n & 15)) * 16);
                }
            }
        }
        const int tokS = qb * 256 + hw * 64, m7 = tokS >> 7, t4 = (tokS >> 5) & 3;
        {
            const unsigned pt3 = (lane >> 1) & 3;
            const unsigned tb = WS_F_G + 2048 + ((((lane & 1) ? nhi : nlo) + (pt3 < 3 ? pt3 : 0) * 5) * 16 + qb * 2 + ((lane >> 3) & 1)) * 8;
            POLL_ONE2(hw == 0, 4, HS, tb, WS_F_SD + (qb * 2 + (lane & 1)) * 8, C_P1);
        }
        const unsigned om = t4 * 1024 + lane * 16, ot = 4096 + (t4 * 32 + r) * 8 + 4 * h;
        u32x4 qf[2][3]; float gS[2];
        {
            const unsigned kb = WS_KV + (NH + head) * 163840 + (2 * (m7 & 7) + (m7 >> 3)) * 10240, qbb = WS_Q + (NH + head) * 81920 + m7 * 5120;
            const unsigned ov = 5120 + t4 * 1280 + h * 16 + (r < 20 ? r : 19) * 32;
            u32x4 e0[2], w0[2], w1[2], eq[2]; unsigned et[2], eqt[2];
#pragma unroll
            for (int i = 0; i < 2; ++i) {
                e0[i] = LD16(kb + om + i * 1024); et[i] = LD4(kb + ot + i * 256); w0[i] = LD16(kb + ov + i * 1280); w1[i] = LD16(kb + ov + i * 1280 + 640);
                eq[i] = LD16(qbb + om + i * 1024); eqt[i] = LD4(qbb + ot + i * 256); gS[i] = __uint_as_float(LD4(WS_G + (tokS + i * 32 + r) * 4));
            }
            u32x4 i0, i1;
#pragma unroll
            for (int j = 0; j < 4; ++j) {
                const int e = r - 8 * h - 2 * j;
                i0[j] = e == 0 ? 0x3f80u : (e == 1 ? 0x3f800000u : 0u);
                i1[j] = e == 16 ? 0x3f80u : (e == 17 ? 0x3f800000u : 0u);
            }
            f32x16 k0 = {}, k1 = {};
#pragma unroll
            for (int i = 0; i < 2; ++i) {
                if (r >= 20) { w0[i] = (u32x4){one, one, one, one}; w1[i] = w0[i]; }
                u32x4 fp, fn, ft;
                feature_map(phm, pht, e0[i], et[i], h, fp, fn, ft);
                f32x16 T0 = MFMA32_16(fp, i0, z16); T0 = MFMA32_16(fn, i1, T0);
                f32x16 T1 = MFMA32_16(ft, i0, z16);
                u32x4 b00, b01, b10, b11;
#pragma unroll
                for (int j = 0; j < 4; ++j) {
                    b00[j] = pk2(T0[2 * j], T0[2 * j + 1]); b01[j] = pk2(T0[8 + 2 * j], T0[9 + 2 * j]);
                    b10[j] = pk2(T1[2 * j], T1[2 * j + 1]); b11[j] = pk2(T1[8 + 2 * j], T1[9 + 2 * j]);
                }
                k0 = MFMA32_16(w0[i], b00, k0); k0 = MFMA32_16(w1[i], b01, k0);
                k1 = MFMA32_16(w0[i], b10, k1); k1 = MFMA32_16(w1[i], b11, k1);
            }
            prog_wait<8>(lds + C_PR, lane, 1);
            *(LDS3 float*)(lds + X_G + (hw * 64 + lane) * 4) = h ? gS[1] : gS[0];
            if (hw * 64 + lane < 144) *(LDS3 u32x4*)(lds + X_KVF + (hw * 64 + lane) * 16) = (u32x4){0u, 0u, 0u, 0u};
            LDS3 char* sp = lds + X_SLOT + hw * 4096;
#pragma unroll
            for (int gq = 0; gq < 3; ++gq) {
                *(LDS3 f32x4*)(sp + gq * 1024 + lane * 16) = (f32x4){k0[4 * gq], k0[4 * gq + 1], k0[4 * gq + 2], k0[4 * gq + 3]};
                if (r < 8) *(LDS3 f32x4*)(sp + 3072 + (gq * 16 + h * 8 + r) * 16) = (f32x4){k1[4 * gq], k1[4 * gq + 1], k1[4 * gq + 2], k1[4 * gq + 3]};
            }
            asm volatile("s_waitcnt vmcnt(0) lgkmcnt(0)" ::: "memory");
            unsigned arr = 0;
            if (lane == 0) arr = LDS_CNT_ADD(C_KV);
            if (__builtin_amdgcn_readfirstlane(arr) == 3) {
                __builtin_amdgcn_fence(__ATOMIC_ACQUIRE, "wavefront");
#pragma unroll
                for (int i = 0; i < 4; ++i) {
                    const int qd = lane + 64 * i;
                    LDS3 const char* s0 = lds + X_SLOT + qd * 16;
                    const f32x4 s = (*(LDS3 const f32x4*)s0 + *(LDS3 const f32x4*)(s0 + 4096)) + (*(LDS3 const f32x4*)(s0 + 8192) + *(LDS3 const f32x4*)(s0 + 12288));
                    if (qd < 240) ST16(__builtin_bit_cast(u32x4, s), WS_KVP + (head * 8 + qb) * 4096 + qd * 16);
                }
                DRAIN();
                if (lane == 0) set_flag(ws, WS_F_KV + (head * 8 + qb) * 8, tag);
            }
#pragma unroll
            for (int i = 0; i < 2; ++i) feature_map(phm, pht, eq[i], eqt[i], h, qf[i][0], qf[i][1], qf[i][2]);
        }
        STAMP(4);
        POLL_ONE2(hw == 0, 4, HS, WS_F_KV + (head * 8 + (lane & 7)) * 8, WS_F_KV + (head * 8 + (lane & 7)) * 8, C_P2);
        {
            const int qd = hw * 60 + (lane < 60 ? lane : 59);
            f32x4 pp[8];
#pragma unroll
            for (int p = 0; p < 8; ++p) pp[p] = __builtin_bit_cast(f32x4, LD16(WS_KVP + (head * 8 + p) * 4096 + qd * 16));
            const f32x4 s4 = ((pp[0] + pp[1]) + (pp[2] + pp[3])) + ((pp[4] + pp[5]) + (pp[6] + pp[7]));
            const int gq = qd < 192 ? qd >> 6 : (qd - 192) >> 4, hs = qd < 192 ? (qd >> 5) & 1 : (qd >> 3) & 1, sg = qd < 192 ? qd & 31 : 32 + (qd & 7);
            const unsigned a = pk2(s4[0], s4[1]), b = pk2(s4[2], s4[3]);
            LDS3 u16* kp = (LDS3 u16*)(lds + X_KVF + (8 * gq + 4 * hs) * 96 + sg * 2);
            kp[0] = (u16)a; kp[48] = (u16)(a >> 16); kp[96] = (u16)b; kp[144] = (u16)(b >> 16);
            asm volatile("s_waitcnt lgkmcnt(0)" ::: "memory");
            if (lane == 0) LDS_CNT_ADD(C_KVF);
            lds_wait<2>(lds + C_KVF, 4);
        }
        {
            LDS3 const char* kp = lds + X_KVF + (r < 24 ? r : 23) * 96 + h * 16;
            const u32x4 a0 = *(LDS3 const u32x4*)kp, a1 = *(LDS3 const u32x4*)(kp + 32), a2 = *(LDS3 const u32x4*)(kp + 64);
#pragma unroll
            for (int i = 0; i < 2; ++i) {
                f32x16 d = MFMA32_16(a0, qf[i][0], z16); d = MFMA32_16(a1, qf[i][1], d); d = MFMA32_16(a2, qf[i][2], d);
                const float den = upper_half(d[8]) + 1e-6f;
                float o[12];
#pragma unroll
                for (int j = 0; j < 12; ++j) o[j] = d[j];
                store_o(rs, o, (1.f - gS[i]) / den, tokS + i * 32 + r, 1, head, h);
            }
            DRAIN();
            unsigned arr = 0;
            if (lane == 0) arr = LDS_CNT_ADD(C_OB);
            if (__builtin_amdgcn_readfirstlane(arr) == 3 && lane == 0) set_flag(ws, WS_F_OB + (qb * 32 + head) * 8, tag);
        }
        STAMP(5);
        f32x4 c[5];
#pragma unroll
        for (int n = 0; n < 5; ++n) c[n] = (f32x4){0.f, 0.f, 0.f, 0.f};
        const char* wsrc = ws + WS_WT + (size_t)ntO * 5 * 40960;
        {
            if (hw == 0) wait_flags<8>(ws, WS_F_KV + lane * 8, WS_F_KV + 512 + lane * 8, tag);
            POLL_ONE2(hw == 0, 8, HS, WS_F_KV + 1024 + lane * 8, WS_F_KV + 1536 + lane * 8, C_P5);
#pragma unroll 1
            for (int t = 0; t < 5; ++t) {
                if (t == 0) prog_wait<2 * HS>(lds + C_PR, lane, 4); else if (t == 3) prog_wait<2 * HS>(lds + C_PR, lane, 5);
                const int ks = hw + 4 * t < 19 ? hw + 4 * t : 18;
#pragma unroll
                for (int n = 0; n < 5; ++n) glds16s(lane * 16, wsrc + (n * 40 + 20 + ks) * 1024, lds0 + 4096 + (ks * 5 + n) * 1024);
            }
            POLL_ONE2(hw == 0, 4, HS, WS_F_OB + ((mtO >> 2) * 32 + r) * 8, WS_F_OB + ((mtO >> 2) * 32 + r) * 8, C_P3);
            const unsigned ab = WS_AO + ((mtO * 4 + hw) * 40 + 20) * 1024;
            u32x4 af[20];
#pragma unroll
            for (int ks = 0; ks < 16; ++ks) af[ks] = LD16(ab + ks * 1024 + lane * 16);
#pragma unroll
            for (int ks = 16; ks < 20; ++ks) LD_TAIL(af[ks], ab + ks * 1024);
            asm volatile("s_waitcnt vmcnt(20)" ::: "memory");
            if (lane == 0) LDS_CNT_ADD(C_BX);
            lds_wait<2>(lds + C_BX, 4);
            u32x4 wf[3][5], wl[5];
            LDS3 const char* wp = lds + 4096 + lane * 16;
#pragma unroll
            for (int ks = 0; ks < 2; ++ks)
#pragma unroll
                for (int n = 0; n < 5; ++n) wf[ks][n] = *(LDS3 const u32x4*)(wp + (ks * 5 + n) * 1024);
#pragma unroll
            for (int ks = 0; ks < 20; ++ks) {
                if (ks + 2 < 19) {
#pragma unroll
                    for (int n = 0; n < 5; ++n) wf[(ks + 2) % 3][n] = *(LDS3 const u32x4*)(wp + ((ks + 2) * 5 + n) * 1024);
                }
                if (ks == 8) {
#pragma unroll
                    for (int n = 0; n < 5; ++n) wl[n] = LD16(WS_WT + ((ntO * 5 + n) * 40 + 39) * 1024 + lane * 16);
                }
                SBAR();
#pragma unroll
                for (int n = 0; n < 5; ++n) c[n] = MFMA16_32(ks < 19 ? wf[ks % 3][n] : wl[n], af[ks], c[n]);
                SBAR();
            }
            if (lane == 0) LDS_CNT_ADD(C_BX);
            lds_wait<2>(lds + C_BX, 8);
        }
        STAMP(6);
        prog_wait<2 * HS>(lds + C_PR, lane, 7);
#pragma unroll 1
        for (int n = 0; n < 5; ++n)
#pragma unroll
            for (int t = 0; t < 5; ++t) { const int ks = hw + 4 * t; glds16s(lane * 16, wsrc + (n * 40 + ks) * 1024, lds0 + L_WA + (n * 20 + ks) * 1024); }
#pragma unroll
        for (int n = 0; n < 3; ++n) *(LDS3 f32x4*)(lds + X_FIN + hw * 3072 + n * 1024 + lane * 16) = c[n];
        DRAIN();
        if (lane == 0) LDS_CNT_ADD(C_WA);
        prog_wait<2 * HS>(lds + C_PR, lane, 8);
#pragma unroll
        for (int n = 3; n < 5; ++n) *(LDS3 f32x4*)(lds + 143360 + hw * 2048 + (n - 3) * 1024 + lane * 16) = c[n];
        asm volatile("s_waitcnt lgkmcnt(0)" ::: "memory");
        if (lane == 0) __hip_atomic_store((LDS3 unsigned*)(lds + C_BP + hw * 4), 1u, __ATOMIC_RELAXED, __HIP_MEMORY_SCOPE_WORKGROUP);
        STAMP(7);
    } else {
        const int qs = w & 3, kh = w >> 2;
        const int q0 = qb * 256 + qs * 64;
        GET_TAG();
        if (bid < 240) {
            const int part = nt / 5, c0 = (nt - part * 5) * 128;
            const int wm = w & 1, wn = w >> 1;
            constexpr int RB = 32768;
            const int xr = wm * 64 + r;
            const unsigned xa = RB + xr * 128 + ((h ^ ((xr >> 1) & 7)) << 4);
            unsigned wt[2];
#pragma unroll
            for (int t = 0; t < 2; ++t) {
                const int q = (lane & 15) >> 2, p = lane & 3, blk = (lane >> 4) & 1, row = 8 * h + 4 * t + q;
                wt[t] = RB + 16384 + row * 256 + (((4 * wn + 2 * blk + (p >> 1)) ^ ((q << 2) | (2 * h + t))) << 4) + 8 * (p & 1);
            }
            auto gbody = [&](auto trc) {
                constexpr bool TR = decltype(trc)::value;
                u32x4 xf[2][8], wf[2][4];
                auto rdk = [&](auto bc, LDS3 const char* sx, LDS3 const char* sb, int ks) {
                    constexpr int B = decltype(bc)::value;
                    xf[B][2 * ks] = *(LDS3 const u32x4*)(sx + (xa ^ (ks * 32)));
                    xf[B][2 * ks + 1] = *(LDS3 const u32x4*)(sx + ((xa + 4096) ^ (ks * 32)));
                    const u32x2 lo = trread(sb + wt[0] + ks * 4096), hi = trread(sb + wt[1] + ks * 4096);
                    wf[B][ks] = (u32x4){lo[0], lo[1], hi[0], hi[1]};
                };
                f32x16 acc0, acc1;
                auto step = [&](auto bc, int s) {
                    constexpr int B = decltype(bc)::value;
                    if (s == GA_PUB) {
                        DRAIN();
                        unsigned arr = 0;
                        if (lane == 0) arr = LDS_CNT_ADD(G_ST);
                        if (__builtin_amdgcn_readfirstlane(arr) == 7 && lane == 0) set_flag(ws, fG, tag);
                    }
                    __builtin_amdgcn_s_waitcnt(0xC07F);
                    if (s < 18) BAR();
                    SBAR();
                    const int s1 = s + 1;
                    LDS3 const char* sb = lds + __builtin_amdgcn_readfirstlane((s1 & 3) * 32768);
                    LDS3 const char* sx = lds + __builtin_amdgcn_readfirstlane(((s1 >= 10 ? 20 - s1 : s1 + 1) & 3) * 32768);
#define GM(A, i) do { if constexpr (TR) A = MFMA32_16(wf[B][(i) >> 1], xf[B][i], A); else A = MFMA32_16(xf[B][i], wf[B][(i) >> 1], A); SBAR(); } while (0)
#define GX(i) do { xf[B ^ 1][i] = *(LDS3 const u32x4*)(sx + ((xa + ((i) & 1) * 4096) ^ (((i) >> 1) * 32))); } while (0)
#define GW(ks) do { const u32x2 lo = trread(sb + wt[0] + (ks) * 4096), hi = trread(sb + wt[1] + (ks) * 4096); wf[B ^ 1][ks] = (u32x4){lo[0], lo[1], hi[0], hi[1]}; } while (0)
                    GM(acc0, 0); GX(0); GW(0); SBAR(); GM(acc1, 1); GX(1); GX(2); SBAR(); GM(acc0, 2); GW(1); GX(3); SBAR(); GM(acc1, 3); GX(4); GW(2); SBAR();
                    GM(acc0, 4); GX(5); GX(6); SBAR(); GM(acc1, 5); GW(3); GX(7); SBAR(); GM(acc0, 6); GM(acc1, 7);
#undef GX
#undef GW
#undef GM
                };
                WAIT_BAR(63);
#pragma unroll
                for (int ks = 0; ks < 4; ++ks) rdk(std::integral_constant<int, 0>{}, lds + 32768, lds, ks);
#pragma unroll 1
                for (int mat = 0; mat < 2; ++mat) {
                    acc0 = (f32x16){}; acc1 = (f32x16){};
#pragma unroll 1
                    for (int kk = 0; kk < 10; kk += 2) {
                        step(std::integral_constant<int, 0>{}, mat * 10 + kk);
                        step(std::integral_constant<int, 1>{}, mat * 10 + kk + 1);
                    }
                    const int cpos = 2 * (mt & 7) + (mt >> 3);
                    if constexpr (TR) {
                        const float sc = (part == 0 && mat == 0) ? QSCALE : 1.f;
                        const unsigned base = part == 0 ? WS_Q + mat * NH * 81920 + mt * 5120 : WS_KV + mat * NH * 163840 + cpos * 10240;
                        const unsigned hs = part == 0 ? 81920 : 163840;
#pragma unroll
                        for (int pr = 0; pr < 2; ++pr) {
                            const int G8 = ((c0 + 32 * wn) >> 3) + 2 * pr + h, pp = G8 / 5, gi = G8 - pp * 5;
                            const unsigned hb0 = base + (2 * pp + (gi > 2)) * hs;
#pragma unroll
                            for (int i = 0; i < 2; ++i) {
                                const f32x16& a = i ? acc1 : acc0;
                                const int tl = wm * 64 + i * 32 + r;
                                const auto s0 = __builtin_amdgcn_permlane32_swap(pk2(a[8 * pr] * sc, a[8 * pr + 1] * sc), pk2(a[8 * pr + 4] * sc, a[8 * pr + 5] * sc), false, false);
                                const auto s1 = __builtin_amdgcn_permlane32_swap(pk2(a[8 * pr + 2] * sc, a[8 * pr + 3] * sc), pk2(a[8 * pr + 6] * sc, a[8 * pr + 7] * sc), false, false);
                                if (gi != 2) { const u32x4 ov = {s0[0], s1[0], s0[1], s1[1]}; ST16(ov, hb0 + (tl >> 5) * 1024 + (gi > 2 ? gi - 3 : gi) * 512 + r * 16); }
                                else { const u32x2 o0 = {s0[0], s1[0]}, o1 = {s0[1], s1[1]}; ST8(o0, hb0 + 4096 + tl * 8); ST8(o1, hb0 + hs + 4096 + tl * 8); }
                            }
                        }
                    } else {
                        const int n = c0 + 32 * wn + r, hd = n / 20, d = n - hd * 20, rho = (hd & 1) ? (d < 4 ? d + 16 : d - 4) : d;
                        const unsigned base = WS_KV + (mat * NH + hd) * 163840 + cpos * 10240 + 5120 + h * 16 + rho * 32;
#pragma unroll
                        for (int i = 0; i < 2; ++i)
#pragma unroll
                            for (int s2 = 0; s2 < 2; ++s2) {
                                const f32x16& a = i ? acc1 : acc0;
                                u32x4 ov = {pk2(a[8 * s2], a[8 * s2 + 1]), pk2(a[8 * s2 + 2], a[8 * s2 + 3]), pk2(a[8 * s2 + 4], a[8 * s2 + 5]), pk2(a[8 * s2 + 6], a[8 * s2 + 7])};
                                ST16(ov, base + (wm * 4 + i * 2 + s2) * 640);
                            }
                    }
                    if (mat == 0) STAMP(1);
                }
            };
            if (part < 2) gbody(std::true_type{}); else gbody(std::false_type{});
        } else {
            const int sj = bid - 240;
            WAIT_BAR(63);
            {
                const u32x4 ga = *(const u32x4*)(P.gw + lane * 8), gb = *(const u32x4*)(P.gw + 512 + (lane & 15) * 8);
                u32x4 xa[16], xb[4];
                const u16* xr = P.x + (size_t)(sj * 128 + w * 16) * DM;
#pragma unroll
                for (int rr = 0; rr < 16; ++rr) xa[rr] = *(const u32x4*)(xr + rr * DM + lane * 8);
#pragma unroll
                for (int i = 0; i < 4; ++i) xb[i] = *(const u32x4*)(xr + (i * 4 + (lane >> 4)) * DM + 512 + (lane & 15) * 8);
                float res = 0.f;
#pragma unroll
                for (int rr = 0; rr < 16; ++rr) {
                    float s = 0.f, s2 = 0.f;
#pragma unroll
                    for (int j = 0; j < 4; ++j) { s += bf_lo(xa[rr][j]) * bf_lo(ga[j]) + bf_hi(xa[rr][j]) * bf_hi(ga[j]); s2 += bf_lo(xb[rr >> 2][j]) * bf_lo(gb[j]) + bf_hi(xb[rr >> 2][j]) * bf_hi(gb[j]); }
                    if ((lane >> 4) == (rr & 3)) s += s2;
#pragma unroll
                    for (int o = 32; o >= 1; o >>= 1) s += __shfl_xor(s, o);
                    if (lane == rr) res = s;
                }
                if (lane < 16) ST4(__float_as_uint(1.f / (1.f + __expf(-res))), WS_G + (sj * 128 + w * 16 + lane) * 4);
            }
            DRAIN();
            unsigned arr = 0;
            if (lane == 0) arr = LDS_CNT_ADD(G_ST);
            if (__builtin_amdgcn_readfirstlane(arr) == 7 && lane == 0) set_flag(ws, WS_F_SD + sj * 8, tag);
        }
        STAMP(2);
        const unsigned hb = lds0 + kh * 10240;
        const unsigned laneK = hb + lane * 16, laneT = hb + 4096 + r * 8 + h * 4, laneV = hb + 5120 + h * 16 + (r < 20 ? r : 19) * 32;
        const u64 vmask = 0x000fffff000fffffull;
        u32x4 v0a = {one, one, one, one}, v0b = v0a, v1 = v0a;
        u32x4 qA, qB, ka; unsigned qtA, qtB, kt;
        {
            lds_wait(lds + G_Q, 4);
            LDS3 const char* qp = lds + L_Q + (qs >> 1) * 5120;
            const unsigned om = (qs & 1) * 2048 + lane * 16, ot = 4096 + ((qs & 1) * 64 + r) * 8 + 4 * h;
            qA = *(LDS3 const u32x4*)(qp + om); qB = *(LDS3 const u32x4*)(qp + om + 1024);
            qtA = *(LDS3 const unsigned*)(qp + ot); qtB = *(LDS3 const unsigned*)(qp + ot + 256);
            DSR128(ka, kh ? laneK : lds0 + L_Q + 10240 + lane * 16, 0); DSR32(kt, laneT, 0); DSR128M2(v0a, laneV, 0, v1, laneV, 640);
            asm volatile("s_waitcnt lgkmcnt(0)" : "+v"(ka), "+v"(kt), "+v"(v0a), "+v"(v1), "+v"(qA), "+v"(qB), "+v"(qtA), "+v"(qtB) :: "memory");
            if (lane == 0) LDS_CNT_ADD(G_RD);
        }
        STAMP(5);
        float mA, mB; u32x2 qTA, qTB; f32x16 sA, sB;
        {
            const u32x2 t0 = {kt, 0x3f80u}, a0 = {qtA, 0u}, b0 = {qtB, 0u};
            const f32x16 s = MFMA32_16(ka, qA, z16);
            const f32x16 s0 = MFMA32_8(t0, a0, s);
            f32x16 s1 = MFMA32_16(ka, qB, z16); s1 = MFMA32_8(t0, b0, s1);
            float x0 = s0[0], x1 = s1[0];
#pragma unroll
            for (int i = 1; i < 16; ++i) { x0 = fmaxf(x0, s0[i]); x1 = fmaxf(x1, s1[i]); }
            x0 = -xhalf_max(x0); x1 = -xhalf_max(x1);
            const unsigned hh = pk2(x0, x1);
            const unsigned ll = pk2(x0 - bf_lo(hh), x1 - bf_hi(hh));
            mA = -(bf_lo(hh) + bf_lo(ll)); mB = -(bf_hi(hh) + bf_hi(ll));
            const unsigned sel = h ? ll : hh;
            qTA = (u32x2){qtA, sel & 0xffffu}; qTB = (u32x2){qtB, sel >> 16};
            sA = MFMA32_8(t0, qTA, s);
        }
        f32x16 oA = {}, oB = {};
        u32x4 pa, pb = {0u, 0u, 0u, 0u};
#define EX4(S, i, Pv, j) do { Pv[j] = pk2(ex2(S[i]), ex2(S[i + 1])); Pv[j + 1] = pk2(ex2(S[i + 2]), ex2(S[i + 3])); PIN(Pv); SBAR(); } while (0)
#define KT2 ((u32x2){kt, 0x3f80u})
#define TILE(V0C, V0N, aK1, oK1, aT1, oT1, oV1, aV0, oV0) do { \
            sB = MFMA32_16(ka, qB, z16); SBAR(); DSR128(ka, aK1, oK1); EX4(sA, 0, pa, 0); \
            oB = MFMA32_16(v1, pb, oB); SBAR(); DSR128M2(v1, aV, oV1, V0N, aV0, oV0); EX4(sA, 4, pa, 2); \
            sB = MFMA32_8(KT2, qTB, sB); SBAR(); DSR32(kt, aT1, oT1); EX4(sA, 8, pb, 0); \
            oA = MFMA32_16(V0C, pa, oA); SBAR(); EX4(sA, 12, pb, 2); \
            LWAIT(1); sA = MFMA32_16(ka, qA, z16); SBAR(); EX4(sB, 0, pa, 0); \
            oA = MFMA32_16(v1, pb, oA); SBAR(); EX4(sB, 4, pa, 2); \
            LWAIT(0); sA = MFMA32_8(KT2, qTA, sA); SBAR(); EX4(sB, 8, pb, 0); \
            oB = MFMA32_16(V0C, pa, oB); SBAR(); EX4(sB, 12, pb, 2); \
        } while (0)
        unsigned aK = laneK, aT = laneT, aV = laneV;
#pragma unroll 1
        for (int c = 0; c < 8; ++c) {
            if (c >= 1 && c <= 3) lds_wait(lds + (c == 1 ? C_KVL : (c == 2 ? C_KVL + 12 : C_WA + 8)), 4);
            if (c >= 1) {
                unsigned pp;
                asm volatile("ds_write_b32 %1, %2\n\tds_read_b32 %0, %3\n\ts_waitcnt lgkmcnt(0)" : "=&v"(pp) : "v"(lds0 + C_PR + w * 4), "v"(c), "v"(lds0 + C_PR + (w ^ 4) * 4) : "memory");
                const int pc = __builtin_amdgcn_readfirstlane(pp);
                if (pc > c || (pc == c && ((c ^ kh) & 1))) __builtin_amdgcn_s_setprio(1); else __builtin_amdgcn_s_setprio(0);
            } else if (kh) __builtin_amdgcn_s_setprio(1);
            const unsigned nx = (c < 7 ? c + 1 : 7) * 20480;
            const unsigned nK = laneK + nx, nT = laneT + nx, nV = laneV + nx;
            SBAR();
            TILE(v0a, v0b, aK, 1024, aT, 256, 640,  aV, 1280);
            TILE(v0b, v0a, aK, 2048, aT, 512, 1920, aV, 2560);
            if (c == 0) {
                DRAIN();
                unsigned arr = 0;
                if (lane == 0) arr = LDS_CNT_ADD(C_GB);
                if (__builtin_amdgcn_readfirstlane(arr) == 7 && lane == 0 && bid < 240) set_flag(ws, fG + 2048, tag);
                asm volatile("s_waitcnt lgkmcnt(0)" ::: "memory");
                SBAR();
            }
            TILE(v0a, v0b, aK, 3072, aT, 768, 3200, aV, 3840);
            TILE(v0b, v0a, nK, 0,    nT, 0,   4480, nV, 0);
            aK = nK; aT = nT; aV = nV;
        }
        oB = MFMA32_16(v1, pb, oB);
#undef TILE
#undef EX4
        if (lane == 0) __hip_atomic_store((LDS3 unsigned*)(lds + C_PR + w * 4), 8u, __ATOMIC_RELAXED, __HIP_MEMORY_SCOPE_WORKGROUP);
        STAMP(6);
        {
            float chk = 0.f;
#pragma unroll
            for (int i = 0; i < 12; ++i) chk += fabsf(oA[i]) * 0.f + fabsf(oB[i]) * 0.f;
            bool bad = !(chk == 0.f);
            if (h) bad = bad || !(oA[8] >= 0x1p-60f && oA[8] <= 0x1p60f && oB[8] >= 0x1p-60f && oB[8] <= 0x1p60f);
            if (__builtin_expect(__any(bad), 0)) {
                mA = -INFINITY; mB = -INFINITY;
#pragma unroll
                for (int i = 0; i < 16; ++i) { oA[i] = 0.f; oB[i] = 0.f; }
                const u32x2 a0 = {qtA, 0u}, b0 = {qtB, 0u};
#pragma unroll 1
                for (int t = 0; t < 32; ++t) {
                    const unsigned kb = WS_KV + head * 163840 + (2 * (t >> 2) + kh) * 10240, t4 = t & 3;
                    const u32x4 k0 = LD16(kb + t4 * 1024 + lane * 16);
                    const u32x2 t0 = {LD4(kb + 4096 + (t4 * 32 + r) * 8 + 4 * h), 0u};
                    u32x4 w0 = LD16(kb + 5120 + t4 * 1280 + h * 16 + (r < 20 ? r : 19) * 32), w1 = LD16(kb + 5120 + t4 * 1280 + 640 + h * 16 + (r < 20 ? r : 19) * 32);
                    if (r >= 20) { w0 = (u32x4){one, one, one, one}; w1 = w0; }
                    f32x16 s0 = MFMA32_16(k0, qA, z16); s0 = MFMA32_8(t0, a0, s0);
                    f32x16 s1 = MFMA32_16(k0, qB, z16); s1 = MFMA32_8(t0, b0, s1);
                    float x0 = s0[0], x1 = s1[0];
#pragma unroll
                    for (int i = 1; i < 16; ++i) { x0 = fmaxf(x0, s0[i]); x1 = fmaxf(x1, s1[i]); }
                    x0 = fmaxf(xhalf_max(x0), mA); x1 = fmaxf(xhalf_max(x1), mB);
                    const float g0 = (mA == x0) ? 1.f : ex2(mA - x0), g1 = (mB == x1) ? 1.f : ex2(mB - x1);
                    mA = x0; mB = x1;
#pragma unroll
                    for (int i = 0; i < 16; ++i) { oA[i] *= g0; oB[i] *= g1; }
#pragma unroll
                    for (int i = 0; i < 4; ++i) { pa[i] = pk2(ex2(s0[2 * i] - x0), ex2(s0[2 * i + 1] - x0)); pb[i] = pk2(ex2(s0[8 + 2 * i] - x0), ex2(s0[9 + 2 * i] - x0)); }
                    oA = MFMA32_16(w0, pa, oA); oA = MFMA32_16(w1, pb, oA);
#pragma unroll
                    for (int i = 0; i < 4; ++i) { pa[i] = pk2(ex2(s1[2 * i] - x1), ex2(s1[2 * i + 1] - x1)); pb[i] = pk2(ex2(s1[8 + 2 * i] - x1), ex2(s1[9 + 2 * i] - x1)); }
                    oB = MFMA32_16(w0, pa, oB); oB = MFMA32_16(w1, pb, oB);
                }
            }
        }
        lds_wait<4>(lds + C_BX, 8);
        const float gO = *(LDS3 const float*)(lds + X_G + (qs * 64 + kh * 32 + r) * 4);
        {
            LDS3 char* xp = lds + X_MRG + w * 2816;
            const f32x16& s = kh ? oA : oB;
            *(LDS3 f32x4*)(xp + lane * 16) = (f32x4){s[0], s[1], s[2], s[3]};
            *(LDS3 f32x4*)(xp + 1024 + lane * 16) = (f32x4){s[4], s[5], s[6], s[7]};
            if (!h) *(LDS3 f32x4*)(xp + 2048 + r * 16) = (f32x4){s[8], s[9], s[10], s[11]};
            else *(LDS3 f32x2*)(xp + 2560 + r * 8) = (f32x2){s[8], kh ? mA : mB};
            asm volatile("s_waitcnt lgkmcnt(0)" ::: "memory");
            if (lane == 0) __hip_atomic_store((LDS3 unsigned*)(lds + C_MRG + w * 4), 1u, __ATOMIC_RELAXED, __HIP_MEMORY_SCOPE_WORKGROUP);
        }
        lds_wait<MS>(lds + C_MRG + (w ^ 4) * 4, 1);
        bool lastw;
        {
            LDS3 const char* xp = lds + X_MRG + (w ^ 4) * 2816;
            const f32x4 p0 = *(LDS3 const f32x4*)(xp + lane * 16), p1 = *(LDS3 const f32x4*)(xp + 1024 + lane * 16), p2 = *(LDS3 const f32x4*)(xp + 2048 + r * 16);
            const f32x2 lm = *(LDS3 const f32x2*)(xp + 2560 + r * 8);
            const f32x16& s = kh ? oB : oA;
            const float mo = kh ? mB : mA, mp = lm[1];
            const float mx = fmaxf(mo, mp);
            const float fo = (mo == mx) ? 1.f : ex2(mo - mx), fq = (mp == mx) ? 1.f : ex2(mp - mx);
            float o[12];
#pragma unroll
            for (int i = 0; i < 4; ++i) { o[i] = s[i] * fo + p0[i] * fq; o[4 + i] = s[4 + i] * fo + p1[i] * fq; o[8 + i] = s[8 + i] * fo + p2[i] * fq; }
            const float l = upper_half(s[8]) * fo + lm[0] * fq;
            store_o(rs, o, gO / l, q0 + kh * 32 + r, 0, head, h);
            DRAIN();
            unsigned arr = 0;
            if (lane == 0) arr = LDS_CNT_ADD(C_OA);
            lastw = __builtin_amdgcn_readfirstlane(arr) == 7;
            if (lastw && lane == 0) set_flag(ws, WS_F_OA + (qb * 32 + head) * 8, tag);
        }
        STAMP(7);
        {
            const int c16 = lane & 15, g = lane >> 4, tt = w & 3, k0 = kh * 10;
            lds_wait(lds + C_WA, 4);
            LDS3 const char* wp = lds + L_WA + k0 * 1024 + lane * 16;
            u32x4 wf[4][5];
#pragma unroll
            for (int ks = 0; ks < 4; ++ks)
#pragma unroll
                for (int n = 0; n < 5; ++n) wf[ks][n] = *(LDS3 const u32x4*)(wp + (n * 20 + ks) * 1024);
            POLL_ONE(lastw, 2, WS_F_OA + ((mtO >> 2) * 32 + r) * 8, WS_F_OA + ((mtO >> 2) * 32 + r) * 8, C_P4);
            STAMP(9);
            const unsigned ab = WS_AO + ((mtO * 4 + tt) * 40 + k0) * 1024;
            u32x4 af[10];
#pragma unroll
            for (int ks = 0; ks < 6; ++ks) af[ks] = LD16(ab + ks * 1024 + lane * 16);
            if (kh) {
#pragma unroll
                for (int ks = 6; ks < 10; ++ks) LD_TAIL(af[ks], ab + ks * 1024);
            } else {
#pragma unroll
                for (int ks = 6; ks < 10; ++ks) af[ks] = LD16(ab + ks * 1024 + lane * 16);
            }
            f32x4 c[5];
#pragma unroll
            for (int n = 0; n < 5; ++n) c[n] = (f32x4){0.f, 0.f, 0.f, 0.f};
            SBAR();
#pragma unroll
            for (int ks = 0; ks < 10; ++ks) {
#pragma unroll
                for (int n = 0; n < 5; ++n) c[n] = MFMA16_32(wf[ks & 3][n], af[ks], c[n]);
                SBAR();
                if (ks + 4 < 10) {
#pragma unroll
                    for (int n = 0; n < 5; ++n) wf[ks & 3][n] = *(LDS3 const u32x4*)(wp + (n * 20 + ks + 4) * 1024);
                }
                SBAR();
            }
            LDS3 char* xp = lds + X_MRG + tt * 5120 + lane * 16;
            if (w < 4) { *(LDS3 f32x4*)(xp + 3072) = c[3]; *(LDS3 f32x4*)(xp + 4096) = c[4]; }
            else { *(LDS3 f32x4*)(xp) = c[0]; *(LDS3 f32x4*)(xp + 1024) = c[1]; *(LDS3 f32x4*)(xp + 2048) = c[2]; }
            asm volatile("s_waitcnt lgkmcnt(0)" ::: "memory");
            if (lane == 0) __hip_atomic_store((LDS3 unsigned*)(lds + C_FIN + w * 4), 1u, __ATOMIC_RELAXED, __HIP_MEMORY_SCOPE_WORKGROUP);
            lds_wait(lds + C_FIN + (w ^ 4) * 4, 1);
            lds_wait(lds + C_BP + tt * 4, 1);
            u16* op = outp + (size_t)(mtO * 64 + tt * 16 + c16) * DM + ntO * 80 + g * 4;
#pragma unroll
            for (int n = 0; n < 5; ++n) {
                if ((w < 4) == (n < 3)) {
                    const f32x4 bp = *(LDS3 const f32x4*)(n < 3 ? lds + X_FIN + tt * 3072 + n * 1024 + lane * 16 : lds + 143360 + tt * 2048 + (n - 3) * 1024 + lane * 16);
                    const f32x4 s = c[n] + *(LDS3 const f32x4*)(xp + n * 1024) + bp;
                    *(u32x2*)(op + n * 16) = (u32x2){pk2(s[0], s[1]), pk2(s[2], s[3])};
                }
            }
            if (bid == 0 && mtid == 0) set_flag(ws, WS_EPOCH, tag);
        }
        STAMP(10);
    }
}

extern "C" void kernel_launch(void* const* d_in, const int* in_sizes, int n_in, void* d_out, int out_size, void* d_ws, size_t ws_size,
                              hipStream_t stream) {
    Params P;
    P.x = (const u16*)d_in[0]; P.wa = (const u16*)d_in[1]; P.woa = (const u16*)d_in[2]; P.wb = (const u16*)d_in[4];
    P.wphi = (const u16*)d_in[5]; P.wob = (const u16*)d_in[6]; P.gw = (const u16*)d_in[7]; P.ws = (char*)d_ws; P.out = (u16*)d_out;
    mega<<<256, 768, 0, stream>>>(P);
}
```
